```python
import torch

def _softmax(x, axis=-1):
    return torch.softmax(x, dim=axis)
def _gelu(x):
    return torch.nn.functional.gelu(x, approximate="tanh")
def _silu(x):
    return torch.nn.functional.silu(x)
def _sigmoid(x):
    return torch.sigmoid(x)
def _layernorm(x, axis=-1, eps=1e-5):
    m = x.mean(dim=axis, keepdim=True)
    v = x.var(dim=axis, keepdim=True, unbiased=False)
    return (x - m) / torch.sqrt(v + eps)
def _scatter_add(tgt, idx, src, axis=0):
    return tgt.clone().index_add_(axis, idx, src)
def _segment_sum(src, seg, n):
    out = torch.zeros((n,) + src.shape[1:], dtype=src.dtype)
    return out.index_add_(0, seg, src)
def _pad(x, pw):
    flat = tuple(v for pair in reversed(pw) for v in pair)
    return torch.nn.functional.pad(x, flat)

kernel_name = 'concat_heads__mha_block_cached__gqa_block_cached__near_ridge'

def setup_inputs():

    _w = (lambda a: (lambda t: t.to(torch.bfloat16) if t.is_floating_point() else t)(torch.as_tensor(a)))
    def _A_setup():
        B, Sq, D = 8, 304, 512
        H, Sk, Dh = 32, 1024, 128
        return {
            "x": _w((lambda *s: torch.randn(*s, dtype=torch.bfloat16) * 0.8)(B, Sq, D)),
            "k_cache": _w((lambda *s: torch.randn(*s, dtype=torch.bfloat16) * 0.8)(B, H, Sk, Dh)),
            "v_cache": _w((lambda *s: torch.randn(*s, dtype=torch.bfloat16) * 0.05)(B, H, Sk, Dh)),
            "wq": _w((lambda *s: torch.randn(*s, dtype=torch.bfloat16) * 0.8)(D, H * Dh) * (D ** -0.5)),
            "wo": _w((lambda *s: torch.randn(*s, dtype=torch.bfloat16) * 0.05)(H * Dh, D) * ((H * Dh) ** -0.5)),
        }
    _A_in = _A_setup()

    def _B_setup():
        B, Sq, D = 8, 304, 512
        Hq, Hkv, Sk, Dh = 32, 32, 1024, 128
        assert Hq % Hkv == 0, (Hq, Hkv)
        return {
            "x": _w((lambda *s: torch.randn(*s, dtype=torch.bfloat16) * 0.8)(B, Sq, D)),
            "k_cache": _w((lambda *s: torch.randn(*s, dtype=torch.bfloat16) * 0.8)(B, Hkv, Sk, Dh)),
            "v_cache": _w((lambda *s: torch.randn(*s, dtype=torch.bfloat16) * 0.05)(B, Hkv, Sk, Dh)),
            "wq": _w((lambda *s: torch.randn(*s, dtype=torch.bfloat16) * 0.8)(D, Hq * Dh) * (D ** -0.5)),
            "wo": _w((lambda *s: torch.randn(*s, dtype=torch.bfloat16) * 0.05)(Hq * Dh, D) * ((Hq * Dh) ** -0.5)),
        }
    _B_in = _B_setup()

    return {
        "A_x": _A_in["x"],
        "A_k_cache": _A_in["k_cache"],
        "A_v_cache": _A_in["v_cache"],
        "A_wq": _A_in["wq"],
        "A_wo": _A_in["wo"],
        "B_x": _B_in["x"],
        "B_k_cache": _B_in["k_cache"],
        "B_v_cache": _B_in["v_cache"],
        "B_wq": _B_in["wq"],
        "B_wo": _B_in["wo"],
    }

def reference(A_x, A_k_cache, A_v_cache, A_wq, A_wo, B_x, B_k_cache, B_v_cache, B_wq, B_wo):
    def _A_ref(x, k_cache, v_cache, wq, wo):
        B, Sq, D = x.shape
        H, Dh = 32, 128
        q = (lambda x, *d: x.permute(*d))((x @ wq).reshape(B, Sq, H, Dh), 0, 2, 1, 3)
        scores = torch.einsum("bhqd,bhkd->bhqk", q, k_cache) * (Dh ** -0.5)
        p = _softmax(scores, axis=-1)
        o = torch.einsum("bhqk,bhkd->bhqd", p, v_cache)
        return (lambda x, *d: x.permute(*d))(o, 0, 2, 1, 3).reshape(B, Sq, H * Dh) @ wo

    def _B_ref(x, k_cache, v_cache, wq, wo):
        B, Sq, D = x.shape
        Hq, Dh = 32, 128
        Hkv = k_cache.shape[1]
        rep = Hq // Hkv
        q = (lambda x, *d: x.permute(*d))((x @ wq).reshape(B, Sq, Hq, Dh), 0, 2, 1, 3)
        k = (lambda x, n, axis: torch.repeat_interleave(x, n, dim=axis))(k_cache, rep, axis=1)
        v = (lambda x, n, axis: torch.repeat_interleave(x, n, dim=axis))(v_cache, rep, axis=1)
        scores = torch.einsum("bhqd,bhkd->bhqk", q, k) * (Dh ** -0.5)
        p = _softmax(scores, axis=-1)
        o = torch.einsum("bhqk,bhkd->bhqd", p, v)
        return (lambda x, *d: x.permute(*d))(o, 0, 2, 1, 3).reshape(B, Sq, Hq * Dh) @ wo

    a = _A_ref(A_x, A_k_cache, A_v_cache, A_wq, A_wo)
    b = _B_ref(B_x, B_k_cache, B_v_cache, B_wq, B_wo)
    return torch.concatenate([a, b], axis=-1)
```

```cpp
#include <hip/hip_runtime.h>
#include <hip/hip_bf16.h>
typedef unsigned short u16;
__device__ __forceinline__ float b2f(u16 x){ return __uint_as_float(((unsigned)x)<<16); }
__device__ __forceinline__ u16 f2b(float f){ __hip_bfloat16 h = __float2bfloat16(f); return *(u16*)&h; }
__global__ void gemm_naive(const u16* A, const u16* B, u16* C, int M, int N, int K, int ldc){
  __shared__ float As[32][33], Bs[32][33];
  int tx=threadIdx.x&31, ty=threadIdx.x>>5;
  int m0=blockIdx.y*32, n0=blockIdx.x*32;
  float acc[4]={0,0,0,0};
  for(int k0=0;k0<K;k0+=32){
    for(int i=0;i<4;i++){ As[ty+8*i][tx]=b2f(A[(long)(m0+ty+8*i)*K+k0+tx]); Bs[ty+8*i][tx]=b2f(B[(long)(k0+ty+8*i)*N+n0+tx]); }
    __syncthreads();
    for(int k=0;k<32;k++){ float b=Bs[k][tx]; for(int i=0;i<4;i++) acc[i]+=As[ty+8*i][k]*b; }
    __syncthreads();
  }
  for(int i=0;i<4;i++) C[(long)(m0+ty+8*i)*ldc+n0+tx]=f2b(acc[i]);
}
__global__ void attn_naive(const u16* Q, const u16* Kc, const u16* Vc, u16* O){
  __shared__ float q[16][128]; __shared__ float s[16][1024];
  int qt=blockIdx.x, h=blockIdx.y, b=blockIdx.z, t=threadIdx.x;
  for(int i=t;i<16*128;i+=256){ int r=i>>7,d=i&127; q[r][d]=b2f(Q[((long)(b*304+qt*16+r))*4096+h*128+d]); }
  __syncthreads();
  const u16* Kh=Kc+((long)(b*32+h))*1024*128; const u16* Vh=Vc+((long)(b*32+h))*1024*128;
  for(int k=t;k<1024;k+=256){ float a[16]; for(int r=0;r<16;r++)a[r]=0;
    for(int d=0;d<128;d++){ float kv=b2f(Kh[k*128+d]); for(int r=0;r<16;r++) a[r]+=q[r][d]*kv; }
    for(int r=0;r<16;r++) s[r][k]=a[r]*0.08838834764831845f; }
  __syncthreads();
  { int r=t>>4, l=t&15; float m=-1e30f; for(int k=l;k<1024;k+=16) m=fmaxf(m,s[r][k]);
    for(int o=8;o;o>>=1) m=fmaxf(m,__shfl_xor(m,o,16));
    float sum=0; for(int k=l;k<1024;k+=16){ float e=__expf(s[r][k]-m); s[r][k]=e; sum+=e; }
    for(int o=8;o;o>>=1) sum+=__shfl_xor(sum,o,16);
    float inv=1.f/sum; for(int k=l;k<1024;k+=16) s[r][k]*=inv; }
  __syncthreads();
  { int d=t&127, g=t>>7; float a[8]; for(int r=0;r<8;r++)a[r]=0;
    for(int k=0;k<1024;k++){ float v=b2f(Vh[k*128+d]); for(int r=0;r<8;r++) a[r]+=s[g*8+r][k]*v; }
    for(int r=0;r<8;r++) O[((long)(b*304+qt*16+g*8+r))*4096+h*128+d]=f2b(a[r]); }
}
extern "C" void kernel_launch(void* const* d_in, const int* in_sizes, int n_in, void* d_out, int out_size, void* d_ws, size_t ws_size, hipStream_t stream){
  u16* Q=(u16*)d_ws; u16* O=Q+(long)2432*4096;
  for(int br=0;br<2;br++){
    const u16* x=(const u16*)d_in[br*5+0]; const u16* k=(const u16*)d_in[br*5+1]; const u16* v=(const u16*)d_in[br*5+2];
    const u16* wq=(const u16*)d_in[br*5+3]; const u16* wo=(const u16*)d_in[br*5+4];
    gemm_naive<<<dim3(4096/32,2432/32),256,0,stream>>>(x,wq,Q,2432,4096,512,4096);
    attn_naive<<<dim3(19,32,8),256,0,stream>>>(Q,k,v,O);
    gemm_naive<<<dim3(512/32,2432/32),256,0,stream>>>(O,wo,(u16*)d_out+br*512,2432,512,4096,1024);
  }
}
```

```cpp
#include <hip/hip_runtime.h>

struct Params { const void* p[2][8]; };

__global__ __launch_bounds__(256, 1) void fused_attn_kernel(Params p) {
  const unsigned w = __builtin_amdgcn_readfirstlane(threadIdx.x >> 6);
  asm volatile(
R"ASM(
.macro M x:vararg
v_mfma_f32_16x16x32_bf16 \x
.endm
.macro T x:vararg
ds_read_b64_tr_b16 \x
.endm
.macro R x:vararg
ds_read_b128 \x
.endm
.macro B x:vararg
buffer_load_dwordx4 \x
.endm
.macro C x:vararg
v_cvt_pk_bf16_f32 \x
.endm
.macro AR x:vararg
v_accvgpr_read_b32 \x
.endm
.macro AW x:vararg
v_accvgpr_write_b32 \x
.endm
.macro E x:vararg
v_exp_f32 \x
.endm
.macro F x:vararg
v_add_f32 \x
.endm
.macro U x:vararg
v_mul_f32 \x
.endm
.macro W x:vararg
s_waitcnt \x
.endm
)ASM"
R"ASM(
LBEGIN_%=:
s_and_b32 s30,%1,7
s_lshr_b32 s35,s30,2
s_lshl_b32 s94,s35,6
s_load_dwordx16 s[8:23],%0,s94
s_mov_b32 s87,%2
s_lshr_b32 s29,%1,3
s_and_b32 s28,s29,15
s_and_b32 s34,s30,3
s_lshl_b32 s34,s34,1
s_lshr_b32 s94,s29,4
s_add_u32 s34,s34,s94
s_lshl_b32 s32,s35,3
s_add_u32 s32,s32,s34
s_mul_i32 s66,s34,304
s_mul_i32 s94,s87,80
s_add_u32 s66,s66,s94
s_lshl_b32 s66,s66,10
s_lshl_b32 s26,s30,1
s_and_b32 s94,s28,0
s_add_u32 s26,s26,s94
s_lshr_b32 s94,s29,4
s_lshl_b32 s94,s94,3
s_add_u32 s26,s26,s94
s_and_b32 s26,s26,7
s_lshl_b32 s83,s26,19
s_lshl_b32 s26,s26,7
s_lshl_b32 s94,s28,8
s_add_u32 s83,s83,s94
s_lshl_b32 s67,s87,13
s_add_u32 s67,s67,0x20000
s_lshl_b32 s82,s34,5
s_add_u32 s82,s82,s28
s_lshl_b32 s82,s82,18
s_mov_b32 s70,0
v_mbcnt_lo_u32_b32 v253,-1,0
v_mbcnt_hi_u32_b32 v253,-1,v253
v_lshrrev_b32 v227,4,v253
v_and_b32 v244,15,v253
v_lshlrev_b32 v245,4,v253
s_getpc_b64 s[36:37]
LPC_%=:
s_sub_u32 s36,s36,LPC_%=-LBEGIN_%=
s_subb_u32 s37,s37,0
s_and_b32 s37,s37,0xffff
s_mov_b32 s38,LEND_%=-LBEGIN_%=
s_mov_b32 s39,0x20000
s_mul_i32 s92,s29,5120
s_cmp_eq_u32 s87,3
s_cselect_b32 s92,s92,0x7fff0000
s_cbranch_scc0 LNTC_%=
v_lshlrev_b32 v250,3,v245
s_mov_b32 exec_hi,0xff
buffer_load_dword v254,v250,s[36:39],s92 offen sc1
s_mov_b32 exec_hi,-1
LNTC_%=:
v_add_u32 v250,0,v227
v_and_b32 v251,7,v253
v_xor_b32 v250,v250,v251
v_lshlrev_b32 v250,4,v250
v_lshrrev_b32 v251,3,v253
v_lshl_add_u32 v212,v251,10,v250
v_add_u32 v250,0,v227
v_lshlrev_b32 v250,1,v250
v_xor_b32 v250,v250,v244
v_lshlrev_b32 v246,4,v250
v_add_u32 v250,4,v227
v_and_b32 v251,7,v253
v_xor_b32 v250,v250,v251
v_lshlrev_b32 v250,4,v250
v_lshrrev_b32 v251,3,v253
v_lshl_add_u32 v213,v251,10,v250
v_add_u32 v213,7168,v213
v_add_u32 v250,4,v227
v_lshlrev_b32 v250,1,v250
v_xor_b32 v250,v250,v244
v_lshlrev_b32 v247,4,v250
s_lshr_b32 s90,s30,1
s_lshl_b32 s90,s90,8
s_add_u32 s90,s90,0x2800000
s_mov_b32 s46,0x400000
s_mov_b32 s47,0x20000
s_mov_b32 s95,8192
v_mad_u32_u24 v218,v227,s95,v246
v_mad_u32_u24 v219,v227,s95,v247
v_add_u32 v219,64512,v219
v_mad_u32_u24 v220,v227,s95,v246
v_add_u32 v220,129024,v220
v_mad_u32_u24 v221,v227,s95,v247
v_add_u32 v221,193536,v221
s_add_u32 s61,s83,0
s_mov_b32 s62,32768
s_mov_b32 s63,229376
s_mov_b32 s75,229376
s_mov_b32 s74,0x3fffff
s_lshr_b32 s94,s87,1
s_lshl_b32 s94,s94,18
s_and_b32 s95,s87,1
s_lshl_b32 s95,s95,15
s_add_u32 s94,s94,s95
s_add_u32 s60,s94,s61
s_lshl_b32 s76,s87,12
W lgkmcnt(0)
s_memtime s[22:23]
s_memrealtime s[72:73]
s_mov_b32 s48,s8
s_and_b32 s49,s9,0xffff
s_mov_b32 s50,0x260000
s_mov_b32 s51,0x20000
s_add_u32 m0,s76,0x10000
s_mov_b32 s44,s14
s_and_b32 s45,s15,0xffff
B v218,s[44:47],s60 offen lds
B v219,s[44:47],s60 offen offset:1024 lds
B v220,s[44:47],s60 offen offset:2048 lds
B v221,s[44:47],s60 offen offset:3072 lds
s_add_u32 m0,s67,0
s_add_u32 s27,s26,0
s_and_b32 s27,s27,0x380
s_add_u32 s27,s27,s66
s_add_u32 s68,s27,0
B v212,s[48:51],s68 offen lds
B v213,s[48:51],s68 offen offset:1024 lds
s_add_u32 m0,s67,2048
s_add_u32 s68,s27,16384
B v212,s[48:51],s68 offen lds
B v213,s[48:51],s68 offen offset:1024 lds
s_load_dwordx2 s[24:25],s[18:19],s90
v_add_u32 v250,0,v227
v_xor_b32 v250,v250,v244
v_lshlrev_b32 v250,4,v250
v_lshl_add_u32 v198,v244,8,v250
v_add_u32 v250,4,v227
v_xor_b32 v250,v250,v244
v_lshlrev_b32 v250,4,v250
v_lshl_add_u32 v199,v244,8,v250
v_add_u32 v250,8,v227
v_xor_b32 v250,v250,v244
v_lshlrev_b32 v250,4,v250
v_lshl_add_u32 v200,v244,8,v250
v_add_u32 v250,12,v227
v_xor_b32 v250,v250,v244
v_lshlrev_b32 v250,4,v250
v_lshl_add_u32 v201,v244,8,v250
v_lshrrev_b32 v250,2,v253
v_and_b32 v251,7,v250
v_lshlrev_b32 v251,5,v251
v_lshl_add_u32 v250,v250,8,v251
v_and_b32 v251,3,v253
v_lshl_add_u32 v250,v251,3,v250
v_add_u32 v202,0x10000,v250
v_xor_b32 v203,32,v202
v_xor_b32 v204,64,v202
v_xor_b32 v205,96,v202
v_xor_b32 v206,128,v202
v_xor_b32 v207,160,v202
v_xor_b32 v208,192,v202
v_xor_b32 v209,224,v202
v_add_u32 v250,0,v227
v_lshrrev_b32 v251,1,v244
v_xor_b32 v250,v250,v251
v_lshlrev_b32 v250,4,v250
v_lshl_add_u32 v250,v244,7,v250
v_add_u32 v210,s67,v250
v_add_u32 v250,4,v227
v_lshrrev_b32 v251,1,v244
v_xor_b32 v250,v250,v251
v_lshlrev_b32 v250,4,v250
v_lshl_add_u32 v250,v244,7,v250
v_add_u32 v211,s67,v250
v_mov_b32 v222,0xff800000
v_mov_b32 v223,0xff800000
v_mov_b32 v224,0xff800000
v_mov_b32 v225,0xff800000
v_mov_b32 v226,0xff800000
s_mov_b32 s64,0x3e0293ee
s_mov_b32 s65,0x3e0293ee
s_mov_b32 s71,0
s_mov_b32 s52,s18
s_and_b32 s53,s19,0xffff
s_mov_b32 s54,0x3270000
s_mov_b32 s55,0x20000
s_mov_b32 s56,s20
s_and_b32 s57,s21,0xffff
s_mov_b32 s58,0x4c0000
s_mov_b32 s59,0x20000
s_lshl_b32 s94,s32,2
s_add_u32 s95,s94,s87
s_mul_i32 s84,s95,655360
s_mul_i32 s95,s28,20480
s_add_u32 s84,s84,s95
s_lshl_b32 s94,s32,10
s_add_u32 s94,s94,0x2801000
s_lshl_b32 s95,s87,7
s_add_u32 s85,s94,s95
s_lshl_b32 s95,s28,3
s_add_u32 s85,s85,s95
s_and_b32 s34,s30,2
s_lshl_b32 s34,s34,1
s_lshr_b32 s94,s29,3
s_add_u32 s34,s34,s94
s_lshl_b32 s32,s35,3
s_add_u32 s32,s32,s34
s_lshr_b32 s91,s29,1
s_and_b32 s91,s91,2
s_and_b32 s4,s30,1
s_lshl_b32 s4,s4,1
s_lshl_b32 s6,s29,3
s_add_u32 s6,s6,s30
s_xor_b32 s7,s6,1
s_xor_b32 s94,s87,3
s_or_b32 s94,s94,s29
s_or_b32 s94,s94,s4
s_cmp_eq_u32 s94,0
s_cselect_b32 s90,s90,0x7fffff00
s_lshl_b32 s94,s32,10
s_lshl_b32 s95,s91,7
s_add_u32 s94,s94,s95
s_add_u32 s86,s94,0x2801000
s_lshr_b32 s94,s87,1
s_add_u32 s91,s91,s94
s_and_b32 s94,s87,1
s_add_u32 s4,s4,s94
s_and_b32 s88,s28,3
s_lshl_b32 s88,s88,8
s_lshl_b32 s94,s32,2
s_add_u32 s95,s94,s91
s_mul_i32 s95,s95,655360
s_add_u32 s96,s18,s95
s_addc_u32 s97,s19,0
s_and_b32 s97,s97,0xffff
s_mov_b32 s98,655360
s_mov_b32 s99,0x20000
s_mul_i32 s94,s34,304
s_mul_i32 s95,s91,80
s_add_u32 s94,s94,s95
s_lshl_b32 s94,s94,11
s_lshl_b32 s95,s35,10
s_add_u32 s94,s94,s95
s_add_u32 s94,s94,s88
s_and_b32 s95,s4,1
s_lshl_b32 s95,s95,7
s_add_u32 s94,s94,s95
s_and_b32 s95,s4,2
s_lshl_b32 s95,s95,5
s_add_u32 s89,s94,s95
s_lshl_b32 s95,s95,11
s_add_u32 s88,s88,s95
s_lshl_b32 s6,s6,2
s_add_u32 s6,s6,s87
s_mul_i32 s6,s6,10368
s_add_u32 s6,s6,0x2850000
s_lshl_b32 s7,s7,2
s_add_u32 s7,s7,s87
s_mul_i32 s7,s7,10368
s_add_u32 s7,s7,0x2850000
s_and_b32 s94,s87,1
s_lshl_b32 s78,s94,16
s_lshr_b32 s95,s87,1
s_lshl_b32 s80,s95,15
s_add_u32 s80,s80,s78
s_add_u32 s80,s80,s88
s_add_u32 s80,s80,786432
s_lshl_b32 s95,s95,13
s_add_u32 s78,s78,s95
s_mov_b32 s5,49152
s_cmp_eq_u32 s91,3
s_cselect_b32 s35,0x70000000,0
v_lshrrev_b32 v251,1,v245
v_xor_b32 v251,v251,v245
v_and_b32 v251,24,v251
v_and_b32 v252,32,v245
v_or_b32 v251,v251,v252
v_xor_b32 v184,v251,v202
v_xor_b32 v185,v251,v204
v_xor_b32 v186,v251,v206
v_xor_b32 v187,v251,v208
s_add_u32 m0,s67,4096
s_add_u32 s68,s27,32768
B v212,s[48:51],s68 offen lds
B v213,s[48:51],s68 offen offset:1024 lds
s_add_u32 m0,s67,6144
s_add_u32 s68,s27,49152
B v212,s[48:51],s68 offen lds
B v213,s[48:51],s68 offen offset:1024 lds
s_cmp_lg_u32 s87,3
s_cbranch_scc1 LCOLDa_%=
)ASM"
R"ASM(
LCOLDb_%=:
s_mov_b32 s40,s10
s_and_b32 s41,s11,0xffff
s_mov_b32 s42,0x4000000
s_mov_b32 s43,0x20000
v_add_u32 v250,0,v227
v_xor_b32 v250,v250,v244
v_lshlrev_b32 v250,4,v250
v_lshl_add_u32 v214,v227,8,v250
v_add_u32 v250,4,v227
v_xor_b32 v250,v250,v244
v_lshlrev_b32 v250,4,v250
v_lshl_add_u32 v215,v227,8,v250
v_add_u32 v250,8,v227
v_xor_b32 v250,v250,v244
v_lshlrev_b32 v250,4,v250
v_lshl_add_u32 v216,v227,8,v250
v_add_u32 v250,12,v227
v_xor_b32 v250,v250,v244
v_lshlrev_b32 v250,4,v250
v_lshl_add_u32 v217,v227,8,v250
s_add_u32 s60,s82,0
s_mov_b32 s76,4096
s_mov_b32 s77,4096
v_subrev_u32 v68,0x18000,v210
v_subrev_u32 v69,0x18000,v211
v_lshlrev_b32 v250,6,v245
s_mul_i32 s95,s28,19456
s_add_u32 s95,s95,s66
s_sub_u32 s95,s95,245632
s_add_u32 s95,s95,s26
s_mov_b64 exec,0x7ffff
buffer_load_dword v254,v250,s[48:51],s95 offen sc1
s_mov_b64 exec,-1
s_add_u32 s61,s61,524288
s_and_b32 s61,s61,s74
s_mov_b32 m0,0x14000
s_nop 0
B v218,s[44:47],s61 offen lds
B v219,s[44:47],s61 offen offset:1024 lds
B v220,s[44:47],s61 offen offset:2048 lds
B v221,s[44:47],s61 offen offset:3072 lds
s_add_u32 s61,s61,s62
s_mov_b32 m0,0x15000
s_nop 0
B v218,s[44:47],s61 offen lds
B v219,s[44:47],s61 offen offset:1024 lds
B v220,s[44:47],s61 offen offset:2048 lds
B v221,s[44:47],s61 offen offset:3072 lds
s_add_u32 s61,s61,s63
s_mov_b32 m0,0x16000
s_nop 0
B v218,s[44:47],s61 offen lds
B v219,s[44:47],s61 offen offset:1024 lds
B v220,s[44:47],s61 offen offset:2048 lds
B v221,s[44:47],s61 offen offset:3072 lds
s_add_u32 s61,s61,s62
s_mov_b32 m0,0x17000
s_nop 0
B v218,s[44:47],s61 offen lds
B v219,s[44:47],s61 offen offset:1024 lds
B v220,s[44:47],s61 offen offset:2048 lds
B v221,s[44:47],s61 offen offset:3072 lds
s_add_u32 s61,s61,s75
s_and_b32 s61,s61,s74
W vmcnt(17)
s_memtime s[18:19]
s_memrealtime s[20:21]
W lgkmcnt(0)
s_sub_u32 s20,s20,s72
W lgkmcnt(0)
s_mul_i32 s94,s24,0x5851f42d
s_mul_hi_u32 s95,s24,0x4c957f2d
s_add_u32 s94,s94,s95
s_mul_i32 s95,s25,0x4c957f2d
s_add_u32 s94,s94,s95
s_mul_i32 s95,s24,0x4c957f2d
s_add_u32 s72,s95,0xf767814f
s_addc_u32 s73,s94,0x14057b7e
s_sub_u32 s9,s18,s22
s_mul_i32 s94,s20,0
s_mul_i32 s95,s9,10
s_cmp_gt_u32 s95,s94
s_cselect_b32 s81,s9,0
R a[224:227],v210
R a[228:231],v211
R a[232:235],v210 offset:2048
R a[236:239],v211 offset:2048
R a[240:243],v210 offset:4096
R a[244:247],v211 offset:4096
R a[248:251],v210 offset:6144
R a[252:255],v211 offset:6144
s_barrier
T a[192:193],v184
T a[194:195],v184 offset:4096
T a[196:197],v184 offset:8192
T a[198:199],v184 offset:12288
T a[200:201],v184 offset:8
T a[202:203],v184 offset:4104
T a[204:205],v184 offset:8200
T a[206:207],v184 offset:12296
s_mul_i32 s20,s20,234
s_mul_i32 s21,s9,10
s_cmp_gt_u32 s21,s20
s_cselect_b32 s9,s9,0
s_cmp_lt_u32 s9,2300
s_cbranch_scc1 LQ0bw_%=
s_branch LQ0b_%=
)ASM"
R"ASM(
LCOLDa_%=:
s_lshl_b32 s94,s87,11
s_add_u32 m0,s94,49152
s_add_u32 s68,s27,65536
B v212,s[48:51],s68 offen lds
B v213,s[48:51],s68 offen offset:1024 lds
s_mov_b32 s44,s16
s_and_b32 s45,s17,0xffff
s_mov_b32 s46,0x400000
s_mov_b32 s47,0x20000
s_mov_b32 s95,1024
v_mad_u32_u24 v218,v227,s95,v246
v_mad_u32_u24 v219,v227,s95,v247
v_add_u32 v219,3072,v219
v_mad_u32_u24 v220,v227,s95,v246
v_add_u32 v220,6144,v220
v_mad_u32_u24 v221,v227,s95,v247
v_add_u32 v221,9216,v221
s_mul_i32 s95,s87,6144
s_add_u32 s95,s95,81920
v_subrev_u32 v250,s95,v210
v_subrev_u32 v251,s95,v211
W vmcnt(0)
W lgkmcnt(0)
s_mul_i32 s94,s24,0x5851f42d
s_mul_hi_u32 s95,s24,0x4c957f2d
s_add_u32 s94,s94,s95
s_mul_i32 s95,s25,0x4c957f2d
s_add_u32 s94,s94,s95
s_mul_i32 s95,s24,0x4c957f2d
s_add_u32 s72,s95,0xf767814f
s_addc_u32 s73,s94,0x14057b7e
R a[224:227],v210
R a[228:231],v211
R a[232:235],v210 offset:2048
R a[236:239],v211 offset:2048
R a[240:243],v210 offset:4096
R a[244:247],v211 offset:4096
R a[248:251],v210 offset:6144
R a[252:255],v211 offset:6144
R v[228:231],v250
R v[232:235],v251
s_barrier
T a[192:193],v184
T a[194:195],v184 offset:4096
T a[196:197],v184 offset:8192
T a[198:199],v184 offset:12288
T a[200:201],v184 offset:8
T a[202:203],v184 offset:4104
T a[204:205],v184 offset:8200
T a[206:207],v184 offset:12296
)ASM"
R"ASM(
LQ0a_%=:
W lgkmcnt(4)
s_add_u32 m0,s67,2048
s_add_u32 s27,s26,128
s_and_b32 s27,s27,0x380
s_add_u32 s27,s27,s66
s_add_u32 s68,s27,0
M v[0:3],a[192:195],a[224:227],0
B v212,s[48:51],s68 offen lds
M v[0:3],a[196:199],a[228:231],v[0:3]
T a[208:209],v185
M v[16:19],a[196:199],a[236:239],0
B v213,s[48:51],s68 offen offset:1024 lds
M v[16:19],a[192:195],a[232:235],v[16:19]
s_add_u32 m0,s67,4096
s_add_u32 s68,s27,16384
T a[210:211],v185 offset:4096
M v[32:35],a[192:195],a[240:243],0
B v212,s[48:51],s68 offen lds
M v[32:35],a[196:199],a[244:247],v[32:35]
T a[212:213],v185 offset:8192
M v[48:51],a[196:199],a[252:255],0
W lgkmcnt(15)
B v213,s[48:51],s68 offen offset:1024 lds
M v[48:51],a[192:195],a[248:251],v[48:51]
s_add_u32 m0,s67,6144
s_add_u32 s68,s27,32768
T a[214:215],v185 offset:12288
M v[64:67],a[192:195],v[228:231],0
B v212,s[48:51],s68 offen lds
M v[64:67],a[196:199],v[232:235],v[64:67]
W lgkmcnt(4)
T a[216:217],v185 offset:8
M v[80:83],a[204:207],a[228:231],0
B v213,s[48:51],s68 offen offset:1024 lds
M v[80:83],a[200:203],a[224:227],v[80:83]
s_add_u32 m0,s67,0
s_add_u32 s68,s27,49152
T a[218:219],v185 offset:4104
M v[96:99],a[200:203],a[232:235],0
B v212,s[48:51],s68 offen lds
M v[96:99],a[204:207],a[236:239],v[96:99]
T a[220:221],v185 offset:8200
M v[112:115],a[204:207],a[244:247],0
B v213,s[48:51],s68 offen offset:1024 lds
M v[112:115],a[200:203],a[240:243],v[112:115]
T a[222:223],v185 offset:12296
M v[128:131],a[200:203],a[248:251],0
M v[128:131],a[204:207],a[252:255],v[128:131]
M v[144:147],a[204:207],v[232:235],0
M v[144:147],a[200:203],v[228:231],v[144:147]
W lgkmcnt(4)
M v[4:7],a[208:211],a[224:227],0
M v[4:7],a[212:215],a[228:231],v[4:7]
T a[192:193],v186
M v[20:23],a[212:215],a[236:239],0
M v[20:23],a[208:211],a[232:235],v[20:23]
T a[194:195],v186 offset:4096
M v[36:39],a[208:211],a[240:243],0
M v[36:39],a[212:215],a[244:247],v[36:39]
T a[196:197],v186 offset:8192
M v[52:55],a[212:215],a[252:255],0
M v[52:55],a[208:211],a[248:251],v[52:55]
T a[198:199],v186 offset:12288
M v[68:71],a[208:211],v[228:231],0
M v[68:71],a[212:215],v[232:235],v[68:71]
W lgkmcnt(4)
T a[200:201],v186 offset:8
M v[84:87],a[220:223],a[228:231],0
M v[84:87],a[216:219],a[224:227],v[84:87]
W vmcnt(6)
T a[202:203],v186 offset:4104
M v[100:103],a[216:219],a[232:235],0
R a[160:163],v210 offset:2048
M v[100:103],a[220:223],a[236:239],v[100:103]
T a[204:205],v186 offset:8200
M v[116:119],a[220:223],a[244:247],0
R a[164:167],v211 offset:2048
M v[116:119],a[216:219],a[240:243],v[116:119]
T a[206:207],v186 offset:12296
M v[132:135],a[216:219],a[248:251],0
M v[132:135],a[220:223],a[252:255],v[132:135]
W lgkmcnt(1)
s_add_u32 m0,s67,2048
s_add_u32 s68,s27,65536
M v[148:151],a[220:223],v[232:235],0
B v212,s[48:51],s68 offen lds
M v[148:151],a[216:219],v[228:231],v[148:151]
W lgkmcnt(15)
W vmcnt(5)
B v213,s[48:51],s68 offen offset:1024 lds
M v[8:11],a[192:195],a[224:227],0
R a[168:171],v210 offset:4096
M v[8:11],a[196:199],a[228:231],v[8:11]
T a[208:209],v187
M v[24:27],a[196:199],a[236:239],0
R a[172:175],v211 offset:4096
M v[24:27],a[192:195],a[232:235],v[24:27]
T a[210:211],v187 offset:4096
M v[40:43],a[192:195],a[240:243],0
M v[40:43],a[196:199],a[244:247],v[40:43]
W lgkmcnt(1)
s_add_u32 m0,s67,4096
s_add_u32 s27,s26,256
s_and_b32 s27,s27,0x380
s_add_u32 s27,s27,s66
s_add_u32 s68,s27,0
T a[212:213],v187 offset:8192
M v[56:59],a[196:199],a[252:255],0
B v212,s[48:51],s68 offen lds
M v[56:59],a[192:195],a[248:251],v[56:59]
W vmcnt(5)
T a[214:215],v187 offset:12288
M v[72:75],a[192:195],v[228:231],0
B v213,s[48:51],s68 offen offset:1024 lds
M v[72:75],a[196:199],v[232:235],v[72:75]
W lgkmcnt(15)
T a[216:217],v187 offset:8
M v[88:91],a[204:207],a[228:231],0
R a[176:179],v210 offset:6144
M v[88:91],a[200:203],a[224:227],v[88:91]
T a[218:219],v187 offset:4104
M v[104:107],a[200:203],a[232:235],0
R a[180:183],v211 offset:6144
M v[104:107],a[204:207],a[236:239],v[104:107]
W lgkmcnt(0)
s_add_u32 m0,s67,6144
s_add_u32 s68,s27,16384
T a[220:221],v187 offset:8200
M v[120:123],a[204:207],a[244:247],0
B v212,s[48:51],s68 offen lds
M v[120:123],a[200:203],a[240:243],v[120:123]
W vmcnt(5)
T a[222:223],v187 offset:12296
M v[136:139],a[200:203],a[248:251],0
B v213,s[48:51],s68 offen offset:1024 lds
M v[136:139],a[204:207],a[252:255],v[136:139]
R a[184:187],v210
M v[152:155],a[204:207],v[232:235],0
R a[188:191],v211
M v[152:155],a[200:203],v[228:231],v[152:155]
W lgkmcnt(15)
M v[12:15],a[208:211],a[224:227],0
s_barrier
M v[12:15],a[212:215],a[228:231],v[12:15]
W lgkmcnt(0)
s_add_u32 m0,s67,0
s_add_u32 s68,s27,32768
T a[192:193],v184 offset:16384
M v[28:31],a[212:215],a[236:239],0
B v212,s[48:51],s68 offen lds
M v[28:31],a[208:211],a[232:235],v[28:31]
T a[194:195],v184 offset:20480
M v[44:47],a[208:211],a[240:243],0
B v213,s[48:51],s68 offen offset:1024 lds
M v[44:47],a[212:215],a[244:247],v[44:47]
T a[196:197],v184 offset:24576
M v[60:63],a[212:215],a[252:255],0
M v[60:63],a[208:211],a[248:251],v[60:63]
T a[198:199],v184 offset:28672
M v[76:79],a[208:211],v[228:231],0
M v[76:79],a[212:215],v[232:235],v[76:79]
W lgkmcnt(15)
W vmcnt(6)
T a[200:201],v184 offset:16392
M v[92:95],a[220:223],a[228:231],0
R v[236:239],v210 offset:2048
M v[92:95],a[216:219],a[224:227],v[92:95]
T a[202:203],v184 offset:20488
M v[108:111],a[216:219],a[232:235],0
R v[240:243],v211 offset:2048
M v[108:111],a[220:223],a[236:239],v[108:111]
T a[204:205],v184 offset:24584
M v[124:127],a[220:223],a[244:247],0
M v[124:127],a[216:219],a[240:243],v[124:127]
W lgkmcnt(1)
s_add_u32 m0,s67,2048
s_add_u32 s68,s27,49152
T a[206:207],v184 offset:28680
M v[140:143],a[216:219],a[248:251],0
B v212,s[48:51],s68 offen lds
M v[140:143],a[220:223],a[252:255],v[140:143]
B v213,s[48:51],s68 offen offset:1024 lds
M v[156:159],a[220:223],v[232:235],0
M v[156:159],a[216:219],v[228:231],v[156:159]
W lgkmcnt(15)
M v[0:3],a[192:195],a[160:163],v[0:3]
M v[0:3],a[196:199],a[164:167],v[0:3]
W vmcnt(6)
T a[208:209],v185 offset:16384
M v[16:19],a[196:199],a[172:175],v[16:19]
R a[224:227],v210 offset:4096
M v[16:19],a[192:195],a[168:171],v[16:19]
T a[210:211],v185 offset:20480
M v[32:35],a[192:195],a[176:179],v[32:35]
R a[228:231],v211 offset:4096
M v[32:35],a[196:199],a[180:183],v[32:35]
T a[212:213],v185 offset:24576
M v[48:51],a[196:199],a[188:191],v[48:51]
M v[48:51],a[192:195],a[184:187],v[48:51]
T a[214:215],v185 offset:28672
M v[64:67],a[192:195],v[236:239],v[64:67]
M v[64:67],a[196:199],v[240:243],v[64:67]
W lgkmcnt(6)
W lgkmcnt(2)
T a[216:217],v185 offset:16392
M v[80:83],a[204:207],a[164:167],v[80:83]
M v[80:83],a[200:203],a[160:163],v[80:83]
s_add_u32 m0,s67,4096
s_add_u32 s68,s27,65536
T a[218:219],v185 offset:20488
M v[96:99],a[200:203],a[168:171],v[96:99]
B v212,s[48:51],s68 offen lds
M v[96:99],a[204:207],a[172:175],v[96:99]
T a[220:221],v185 offset:24584
M v[112:115],a[204:207],a[180:183],v[112:115]
B v213,s[48:51],s68 offen offset:1024 lds
M v[112:115],a[200:203],a[176:179],v[112:115]
T a[222:223],v185 offset:28680
M v[128:131],a[200:203],a[184:187],v[128:131]
M v[128:131],a[204:207],a[188:191],v[128:131]
W vmcnt(6)
M v[144:147],a[204:207],v[240:243],v[144:147]
R a[232:235],v210 offset:6144
M v[144:147],a[200:203],v[236:239],v[144:147]
W lgkmcnt(5)
M v[4:7],a[208:211],a[160:163],v[4:7]
R a[236:239],v211 offset:6144
M v[4:7],a[212:215],a[164:167],v[4:7]
T a[192:193],v186 offset:16384
M v[20:23],a[212:215],a[172:175],v[20:23]
M v[20:23],a[208:211],a[168:171],v[20:23]
T a[194:195],v186 offset:20480
M v[36:39],a[208:211],a[176:179],v[36:39]
M v[36:39],a[212:215],a[180:183],v[36:39]
W lgkmcnt(2)
T a[196:197],v186 offset:24576
M v[52:55],a[212:215],a[188:191],v[52:55]
M v[52:55],a[208:211],a[184:187],v[52:55]
s_add_u32 m0,s67,6144
s_add_u32 s27,s26,384
s_and_b32 s27,s27,0x380
s_add_u32 s27,s27,s66
s_add_u32 s68,s27,0
T a[198:199],v186 offset:28672
M v[68:71],a[208:211],v[236:239],v[68:71]
B v212,s[48:51],s68 offen lds
M v[68:71],a[212:215],v[240:243],v[68:71]
W lgkmcnt(15)
T a[200:201],v186 offset:16392
M v[84:87],a[220:223],a[164:167],v[84:87]
B v213,s[48:51],s68 offen offset:1024 lds
M v[84:87],a[216:219],a[160:163],v[84:87]
T a[202:203],v186 offset:20488
M v[100:103],a[216:219],a[168:171],v[100:103]
M v[100:103],a[220:223],a[172:175],v[100:103]
W vmcnt(6)
T a[204:205],v186 offset:24584
M v[116:119],a[220:223],a[180:183],v[116:119]
R a[240:243],v210
M v[116:119],a[216:219],a[176:179],v[116:119]
T a[206:207],v186 offset:28680
M v[132:135],a[216:219],a[184:187],v[132:135]
R a[244:247],v211
M v[132:135],a[220:223],a[188:191],v[132:135]
M v[148:151],a[220:223],v[240:243],v[148:151]
M v[148:151],a[216:219],v[236:239],v[148:151]
W lgkmcnt(6)
M v[8:11],a[192:195],a[160:163],v[8:11]
M v[8:11],a[196:199],a[164:167],v[8:11]
W lgkmcnt(0)
T a[208:209],v187 offset:16384
M v[24:27],a[196:199],a[172:175],v[24:27]
M v[24:27],a[192:195],a[168:171],v[24:27]
s_add_u32 m0,s67,0
s_add_u32 s68,s27,16384
T a[210:211],v187 offset:20480
M v[40:43],a[192:195],a[176:179],v[40:43]
B v212,s[48:51],s68 offen lds
M v[40:43],a[196:199],a[180:183],v[40:43]
T a[212:213],v187 offset:24576
M v[56:59],a[196:199],a[188:191],v[56:59]
B v213,s[48:51],s68 offen offset:1024 lds
M v[56:59],a[192:195],a[184:187],v[56:59]
T a[214:215],v187 offset:28672
M v[72:75],a[192:195],v[236:239],v[72:75]
M v[72:75],a[196:199],v[240:243],v[72:75]
W lgkmcnt(15)
W vmcnt(6)
T a[216:217],v187 offset:16392
M v[88:91],a[204:207],a[164:167],v[88:91]
R a[248:251],v210 offset:2048
M v[88:91],a[200:203],a[160:163],v[88:91]
T a[218:219],v187 offset:20488
M v[104:107],a[200:203],a[168:171],v[104:107]
R a[252:255],v211 offset:2048
M v[104:107],a[204:207],a[172:175],v[104:107]
T a[220:221],v187 offset:24584
M v[120:123],a[204:207],a[180:183],v[120:123]
M v[120:123],a[200:203],a[176:179],v[120:123]
)ASM"
R"ASM(
T a[222:223],v187 offset:28680
M v[136:139],a[200:203],a[184:187],v[136:139]
M v[136:139],a[204:207],a[188:191],v[136:139]
W lgkmcnt(2)
s_add_u32 m0,s67,2048
s_add_u32 s68,s27,32768
M v[152:155],a[204:207],v[240:243],v[152:155]
B v212,s[48:51],s68 offen lds
M v[152:155],a[200:203],v[236:239],v[152:155]
W lgkmcnt(15)
M v[12:15],a[208:211],a[160:163],v[12:15]
s_barrier
M v[12:15],a[212:215],a[164:167],v[12:15]
T a[192:193],v184 offset:32768
M v[28:31],a[212:215],a[172:175],v[28:31]
B v213,s[48:51],s68 offen offset:1024 lds
M v[28:31],a[208:211],a[168:171],v[28:31]
T a[194:195],v184 offset:36864
M v[44:47],a[208:211],a[176:179],v[44:47]
M v[44:47],a[212:215],a[180:183],v[44:47]
W vmcnt(6)
T a[196:197],v184 offset:40960
M v[60:63],a[212:215],a[188:191],v[60:63]
R v[228:231],v210 offset:4096
M v[60:63],a[208:211],a[184:187],v[60:63]
T a[198:199],v184 offset:45056
M v[76:79],a[208:211],v[236:239],v[76:79]
R v[232:235],v211 offset:4096
M v[76:79],a[212:215],v[240:243],v[76:79]
W lgkmcnt(6)
T a[200:201],v184 offset:32776
M v[92:95],a[220:223],a[164:167],v[92:95]
M v[92:95],a[216:219],a[160:163],v[92:95]
T a[202:203],v184 offset:36872
M v[108:111],a[216:219],a[168:171],v[108:111]
M v[108:111],a[220:223],a[172:175],v[108:111]
W lgkmcnt(2)
T a[204:205],v184 offset:40968
M v[124:127],a[220:223],a[180:183],v[124:127]
M v[124:127],a[216:219],a[176:179],v[124:127]
s_add_u32 m0,s67,4096
s_add_u32 s68,s27,49152
T a[206:207],v184 offset:45064
M v[140:143],a[216:219],a[184:187],v[140:143]
B v212,s[48:51],s68 offen lds
M v[140:143],a[220:223],a[188:191],v[140:143]
M v[156:159],a[220:223],v[240:243],v[156:159]
B v213,s[48:51],s68 offen offset:1024 lds
M v[156:159],a[216:219],v[236:239],v[156:159]
W lgkmcnt(15)
M v[0:3],a[192:195],a[224:227],v[0:3]
M v[0:3],a[196:199],a[228:231],v[0:3]
W vmcnt(6)
T a[208:209],v185 offset:32768
M v[16:19],a[196:199],a[236:239],v[16:19]
R a[160:163],v210 offset:6144
M v[16:19],a[192:195],a[232:235],v[16:19]
T a[210:211],v185 offset:36864
M v[32:35],a[192:195],a[240:243],v[32:35]
R a[164:167],v211 offset:6144
M v[32:35],a[196:199],a[244:247],v[32:35]
T a[212:213],v185 offset:40960
M v[48:51],a[196:199],a[252:255],v[48:51]
M v[48:51],a[192:195],a[248:251],v[48:51]
T a[214:215],v185 offset:45056
M v[64:67],a[192:195],v[228:231],v[64:67]
M v[64:67],a[196:199],v[232:235],v[64:67]
W lgkmcnt(6)
W lgkmcnt(2)
T a[216:217],v185 offset:32776
M v[80:83],a[204:207],a[228:231],v[80:83]
M v[80:83],a[200:203],a[224:227],v[80:83]
s_add_u32 m0,s67,6144
s_add_u32 s68,s27,65536
T a[218:219],v185 offset:36872
M v[96:99],a[200:203],a[232:235],v[96:99]
B v212,s[48:51],s68 offen lds
M v[96:99],a[204:207],a[236:239],v[96:99]
T a[220:221],v185 offset:40968
M v[112:115],a[204:207],a[244:247],v[112:115]
B v213,s[48:51],s68 offen offset:1024 lds
M v[112:115],a[200:203],a[240:243],v[112:115]
T a[222:223],v185 offset:45064
M v[128:131],a[200:203],a[248:251],v[128:131]
M v[128:131],a[204:207],a[252:255],v[128:131]
W vmcnt(6)
M v[144:147],a[204:207],v[232:235],v[144:147]
R a[168:171],v210
M v[144:147],a[200:203],v[228:231],v[144:147]
W lgkmcnt(5)
M v[4:7],a[208:211],a[224:227],v[4:7]
R a[172:175],v211
M v[4:7],a[212:215],a[228:231],v[4:7]
T a[192:193],v186 offset:32768
M v[20:23],a[212:215],a[236:239],v[20:23]
M v[20:23],a[208:211],a[232:235],v[20:23]
T a[194:195],v186 offset:36864
M v[36:39],a[208:211],a[240:243],v[36:39]
M v[36:39],a[212:215],a[244:247],v[36:39]
W lgkmcnt(2)
T a[196:197],v186 offset:40960
M v[52:55],a[212:215],a[252:255],v[52:55]
M v[52:55],a[208:211],a[248:251],v[52:55]
s_add_u32 m0,s67,0
s_add_u32 s27,s26,512
s_and_b32 s27,s27,0x380
s_add_u32 s27,s27,s66
s_add_u32 s68,s27,0
T a[198:199],v186 offset:45056
M v[68:71],a[208:211],v[228:231],v[68:71]
B v212,s[48:51],s68 offen lds
M v[68:71],a[212:215],v[232:235],v[68:71]
W lgkmcnt(15)
T a[200:201],v186 offset:32776
M v[84:87],a[220:223],a[228:231],v[84:87]
B v213,s[48:51],s68 offen offset:1024 lds
M v[84:87],a[216:219],a[224:227],v[84:87]
T a[202:203],v186 offset:36872
M v[100:103],a[216:219],a[232:235],v[100:103]
M v[100:103],a[220:223],a[236:239],v[100:103]
W vmcnt(6)
T a[204:205],v186 offset:40968
M v[116:119],a[220:223],a[244:247],v[116:119]
R a[176:179],v210 offset:2048
M v[116:119],a[216:219],a[240:243],v[116:119]
T a[206:207],v186 offset:45064
M v[132:135],a[216:219],a[248:251],v[132:135]
R a[180:183],v211 offset:2048
M v[132:135],a[220:223],a[252:255],v[132:135]
M v[148:151],a[220:223],v[232:235],v[148:151]
M v[148:151],a[216:219],v[228:231],v[148:151]
W lgkmcnt(6)
M v[8:11],a[192:195],a[224:227],v[8:11]
M v[8:11],a[196:199],a[228:231],v[8:11]
W lgkmcnt(0)
T a[208:209],v187 offset:32768
M v[24:27],a[196:199],a[236:239],v[24:27]
M v[24:27],a[192:195],a[232:235],v[24:27]
s_add_u32 m0,s67,2048
s_add_u32 s68,s27,16384
T a[210:211],v187 offset:36864
M v[40:43],a[192:195],a[240:243],v[40:43]
B v212,s[48:51],s68 offen lds
M v[40:43],a[196:199],a[244:247],v[40:43]
T a[212:213],v187 offset:40960
M v[56:59],a[196:199],a[252:255],v[56:59]
B v213,s[48:51],s68 offen offset:1024 lds
M v[56:59],a[192:195],a[248:251],v[56:59]
T a[214:215],v187 offset:45056
M v[72:75],a[192:195],v[228:231],v[72:75]
M v[72:75],a[196:199],v[232:235],v[72:75]
W lgkmcnt(15)
W vmcnt(6)
T a[216:217],v187 offset:32776
M v[88:91],a[204:207],a[228:231],v[88:91]
R a[184:187],v210 offset:4096
M v[88:91],a[200:203],a[224:227],v[88:91]
T a[218:219],v187 offset:36872
M v[104:107],a[200:203],a[232:235],v[104:107]
R a[188:191],v211 offset:4096
M v[104:107],a[204:207],a[236:239],v[104:107]
T a[220:221],v187 offset:40968
M v[120:123],a[204:207],a[244:247],v[120:123]
M v[120:123],a[200:203],a[240:243],v[120:123]
T a[222:223],v187 offset:45064
M v[136:139],a[200:203],a[248:251],v[136:139]
M v[136:139],a[204:207],a[252:255],v[136:139]
W lgkmcnt(2)
s_add_u32 m0,s67,4096
s_add_u32 s68,s27,32768
M v[152:155],a[204:207],v[232:235],v[152:155]
B v212,s[48:51],s68 offen lds
M v[152:155],a[200:203],v[228:231],v[152:155]
W lgkmcnt(15)
M v[12:15],a[208:211],a[224:227],v[12:15]
s_barrier
M v[12:15],a[212:215],a[228:231],v[12:15]
T a[192:193],v184 offset:49152
M v[28:31],a[212:215],a[236:239],v[28:31]
B v213,s[48:51],s68 offen offset:1024 lds
M v[28:31],a[208:211],a[232:235],v[28:31]
T a[194:195],v184 offset:53248
M v[44:47],a[208:211],a[240:243],v[44:47]
M v[44:47],a[212:215],a[244:247],v[44:47]
W vmcnt(6)
T a[196:197],v184 offset:57344
M v[60:63],a[212:215],a[252:255],v[60:63]
R v[236:239],v210 offset:6144
M v[60:63],a[208:211],a[248:251],v[60:63]
T a[198:199],v184 offset:61440
M v[76:79],a[208:211],v[228:231],v[76:79]
R v[240:243],v211 offset:6144
M v[76:79],a[212:215],v[232:235],v[76:79]
W lgkmcnt(6)
T a[200:201],v184 offset:49160
M v[92:95],a[220:223],a[228:231],v[92:95]
M v[92:95],a[216:219],a[224:227],v[92:95]
T a[202:203],v184 offset:53256
M v[108:111],a[216:219],a[232:235],v[108:111]
M v[108:111],a[220:223],a[236:239],v[108:111]
W lgkmcnt(2)
T a[204:205],v184 offset:57352
M v[124:127],a[220:223],a[244:247],v[124:127]
M v[124:127],a[216:219],a[240:243],v[124:127]
s_add_u32 m0,s67,6144
s_add_u32 s68,s27,49152
T a[206:207],v184 offset:61448
M v[140:143],a[216:219],a[248:251],v[140:143]
B v212,s[48:51],s68 offen lds
M v[140:143],a[220:223],a[252:255],v[140:143]
M v[156:159],a[220:223],v[232:235],v[156:159]
B v213,s[48:51],s68 offen offset:1024 lds
M v[156:159],a[216:219],v[228:231],v[156:159]
W lgkmcnt(15)
M v[0:3],a[192:195],a[160:163],v[0:3]
M v[0:3],a[196:199],a[164:167],v[0:3]
W vmcnt(6)
T a[208:209],v185 offset:49152
M v[16:19],a[196:199],a[172:175],v[16:19]
R a[224:227],v210
M v[16:19],a[192:195],a[168:171],v[16:19]
T a[210:211],v185 offset:53248
M v[32:35],a[192:195],a[176:179],v[32:35]
R a[228:231],v211
M v[32:35],a[196:199],a[180:183],v[32:35]
T a[212:213],v185 offset:57344
M v[48:51],a[196:199],a[188:191],v[48:51]
M v[48:51],a[192:195],a[184:187],v[48:51]
T a[214:215],v185 offset:61440
M v[64:67],a[192:195],v[236:239],v[64:67]
M v[64:67],a[196:199],v[240:243],v[64:67]
W lgkmcnt(6)
W lgkmcnt(2)
T a[216:217],v185 offset:49160
M v[80:83],a[204:207],a[164:167],v[80:83]
M v[80:83],a[200:203],a[160:163],v[80:83]
s_add_u32 m0,s67,0
s_add_u32 s68,s27,65536
T a[218:219],v185 offset:53256
M v[96:99],a[200:203],a[168:171],v[96:99]
B v212,s[48:51],s68 offen lds
M v[96:99],a[204:207],a[172:175],v[96:99]
T a[220:221],v185 offset:57352
M v[112:115],a[204:207],a[180:183],v[112:115]
B v213,s[48:51],s68 offen offset:1024 lds
M v[112:115],a[200:203],a[176:179],v[112:115]
T a[222:223],v185 offset:61448
M v[128:131],a[200:203],a[184:187],v[128:131]
M v[128:131],a[204:207],a[188:191],v[128:131]
W vmcnt(6)
M v[144:147],a[204:207],v[240:243],v[144:147]
R a[232:235],v210 offset:2048
M v[144:147],a[200:203],v[236:239],v[144:147]
W lgkmcnt(5)
M v[4:7],a[208:211],a[160:163],v[4:7]
R a[236:239],v211 offset:2048
M v[4:7],a[212:215],a[164:167],v[4:7]
T a[192:193],v186 offset:49152
M v[20:23],a[212:215],a[172:175],v[20:23]
M v[20:23],a[208:211],a[168:171],v[20:23]
T a[194:195],v186 offset:53248
M v[36:39],a[208:211],a[176:179],v[36:39]
M v[36:39],a[212:215],a[180:183],v[36:39]
W lgkmcnt(2)
T a[196:197],v186 offset:57344
M v[52:55],a[212:215],a[188:191],v[52:55]
M v[52:55],a[208:211],a[184:187],v[52:55]
s_add_u32 m0,s67,2048
s_add_u32 s27,s26,640
s_and_b32 s27,s27,0x380
s_add_u32 s27,s27,s66
s_add_u32 s68,s27,0
T a[198:199],v186 offset:61440
M v[68:71],a[208:211],v[236:239],v[68:71]
B v212,s[48:51],s68 offen lds
M v[68:71],a[212:215],v[240:243],v[68:71]
W lgkmcnt(15)
T a[200:201],v186 offset:49160
M v[84:87],a[220:223],a[164:167],v[84:87]
B v213,s[48:51],s68 offen offset:1024 lds
M v[84:87],a[216:219],a[160:163],v[84:87]
T a[202:203],v186 offset:53256
M v[100:103],a[216:219],a[168:171],v[100:103]
M v[100:103],a[220:223],a[172:175],v[100:103]
W vmcnt(6)
T a[204:205],v186 offset:57352
M v[116:119],a[220:223],a[180:183],v[116:119]
R a[240:243],v210 offset:4096
M v[116:119],a[216:219],a[176:179],v[116:119]
T a[206:207],v186 offset:61448
M v[132:135],a[216:219],a[184:187],v[132:135]
R a[244:247],v211 offset:4096
M v[132:135],a[220:223],a[188:191],v[132:135]
M v[148:151],a[220:223],v[240:243],v[148:151]
M v[148:151],a[216:219],v[236:239],v[148:151]
W lgkmcnt(6)
M v[8:11],a[192:195],a[160:163],v[8:11]
M v[8:11],a[196:199],a[164:167],v[8:11]
W lgkmcnt(0)
T a[208:209],v187 offset:49152
)ASM"
R"ASM(
M v[24:27],a[196:199],a[172:175],v[24:27]
M v[24:27],a[192:195],a[168:171],v[24:27]
s_add_u32 m0,s67,4096
s_add_u32 s68,s27,16384
T a[210:211],v187 offset:53248
M v[40:43],a[192:195],a[176:179],v[40:43]
B v212,s[48:51],s68 offen lds
M v[40:43],a[196:199],a[180:183],v[40:43]
T a[212:213],v187 offset:57344
M v[56:59],a[196:199],a[188:191],v[56:59]
B v213,s[48:51],s68 offen offset:1024 lds
M v[56:59],a[192:195],a[184:187],v[56:59]
T a[214:215],v187 offset:61440
M v[72:75],a[192:195],v[236:239],v[72:75]
M v[72:75],a[196:199],v[240:243],v[72:75]
W lgkmcnt(15)
W vmcnt(6)
T a[216:217],v187 offset:49160
M v[88:91],a[204:207],a[164:167],v[88:91]
R a[248:251],v210 offset:6144
M v[88:91],a[200:203],a[160:163],v[88:91]
T a[218:219],v187 offset:53256
M v[104:107],a[200:203],a[168:171],v[104:107]
R a[252:255],v211 offset:6144
M v[104:107],a[204:207],a[172:175],v[104:107]
T a[220:221],v187 offset:57352
M v[120:123],a[204:207],a[180:183],v[120:123]
M v[120:123],a[200:203],a[176:179],v[120:123]
T a[222:223],v187 offset:61448
M v[136:139],a[200:203],a[184:187],v[136:139]
M v[136:139],a[204:207],a[188:191],v[136:139]
W lgkmcnt(2)
s_add_u32 m0,s67,6144
s_add_u32 s68,s27,32768
M v[152:155],a[204:207],v[240:243],v[152:155]
B v212,s[48:51],s68 offen lds
M v[152:155],a[200:203],v[236:239],v[152:155]
W lgkmcnt(15)
M v[12:15],a[208:211],a[160:163],v[12:15]
s_barrier
M v[12:15],a[212:215],a[164:167],v[12:15]
T a[192:193],v184
M v[28:31],a[212:215],a[172:175],v[28:31]
B v213,s[48:51],s68 offen offset:1024 lds
M v[28:31],a[208:211],a[168:171],v[28:31]
T a[194:195],v184 offset:4096
M v[44:47],a[208:211],a[176:179],v[44:47]
M v[44:47],a[212:215],a[180:183],v[44:47]
W vmcnt(6)
T a[196:197],v184 offset:8192
M v[60:63],a[212:215],a[188:191],v[60:63]
R v[228:231],v210
M v[60:63],a[208:211],a[184:187],v[60:63]
T a[198:199],v184 offset:12288
M v[76:79],a[208:211],v[236:239],v[76:79]
R v[232:235],v211
M v[76:79],a[212:215],v[240:243],v[76:79]
W lgkmcnt(6)
T a[200:201],v184 offset:8
M v[92:95],a[220:223],a[164:167],v[92:95]
M v[92:95],a[216:219],a[160:163],v[92:95]
T a[202:203],v184 offset:4104
M v[108:111],a[216:219],a[168:171],v[108:111]
M v[108:111],a[220:223],a[172:175],v[108:111]
W lgkmcnt(2)
T a[204:205],v184 offset:8200
M v[124:127],a[220:223],a[180:183],v[124:127]
M v[124:127],a[216:219],a[176:179],v[124:127]
s_add_u32 m0,s67,0
s_add_u32 s68,s27,49152
T a[206:207],v184 offset:12296
M v[140:143],a[216:219],a[184:187],v[140:143]
B v212,s[48:51],s68 offen lds
M v[140:143],a[220:223],a[188:191],v[140:143]
M v[156:159],a[220:223],v[240:243],v[156:159]
B v213,s[48:51],s68 offen offset:1024 lds
M v[156:159],a[216:219],v[236:239],v[156:159]
W lgkmcnt(15)
M v[0:3],a[192:195],a[224:227],v[0:3]
M v[0:3],a[196:199],a[228:231],v[0:3]
W vmcnt(6)
T a[208:209],v185
M v[16:19],a[196:199],a[236:239],v[16:19]
R a[160:163],v210 offset:2048
M v[16:19],a[192:195],a[232:235],v[16:19]
T a[210:211],v185 offset:4096
M v[32:35],a[192:195],a[240:243],v[32:35]
R a[164:167],v211 offset:2048
M v[32:35],a[196:199],a[244:247],v[32:35]
T a[212:213],v185 offset:8192
M v[48:51],a[196:199],a[252:255],v[48:51]
M v[48:51],a[192:195],a[248:251],v[48:51]
T a[214:215],v185 offset:12288
M v[64:67],a[192:195],v[228:231],v[64:67]
M v[64:67],a[196:199],v[232:235],v[64:67]
W lgkmcnt(6)
W lgkmcnt(2)
T a[216:217],v185 offset:8
M v[80:83],a[204:207],a[228:231],v[80:83]
M v[80:83],a[200:203],a[224:227],v[80:83]
s_add_u32 m0,s67,2048
s_add_u32 s68,s27,65536
T a[218:219],v185 offset:4104
M v[96:99],a[200:203],a[232:235],v[96:99]
B v212,s[48:51],s68 offen lds
M v[96:99],a[204:207],a[236:239],v[96:99]
T a[220:221],v185 offset:8200
M v[112:115],a[204:207],a[244:247],v[112:115]
B v213,s[48:51],s68 offen offset:1024 lds
M v[112:115],a[200:203],a[240:243],v[112:115]
T a[222:223],v185 offset:12296
M v[128:131],a[200:203],a[248:251],v[128:131]
M v[128:131],a[204:207],a[252:255],v[128:131]
W vmcnt(6)
M v[144:147],a[204:207],v[232:235],v[144:147]
R a[168:171],v210 offset:4096
M v[144:147],a[200:203],v[228:231],v[144:147]
W lgkmcnt(5)
M v[4:7],a[208:211],a[224:227],v[4:7]
R a[172:175],v211 offset:4096
M v[4:7],a[212:215],a[228:231],v[4:7]
T a[192:193],v186
M v[20:23],a[212:215],a[236:239],v[20:23]
M v[20:23],a[208:211],a[232:235],v[20:23]
T a[194:195],v186 offset:4096
M v[36:39],a[208:211],a[240:243],v[36:39]
M v[36:39],a[212:215],a[244:247],v[36:39]
W lgkmcnt(2)
T a[196:197],v186 offset:8192
M v[52:55],a[212:215],a[252:255],v[52:55]
M v[52:55],a[208:211],a[248:251],v[52:55]
s_add_u32 m0,s67,4096
s_add_u32 s27,s26,768
s_and_b32 s27,s27,0x380
s_add_u32 s27,s27,s66
s_add_u32 s68,s27,0
T a[198:199],v186 offset:12288
M v[68:71],a[208:211],v[228:231],v[68:71]
B v212,s[48:51],s68 offen lds
M v[68:71],a[212:215],v[232:235],v[68:71]
W lgkmcnt(15)
T a[200:201],v186 offset:8
M v[84:87],a[220:223],a[228:231],v[84:87]
B v213,s[48:51],s68 offen offset:1024 lds
M v[84:87],a[216:219],a[224:227],v[84:87]
T a[202:203],v186 offset:4104
M v[100:103],a[216:219],a[232:235],v[100:103]
M v[100:103],a[220:223],a[236:239],v[100:103]
W vmcnt(6)
T a[204:205],v186 offset:8200
M v[116:119],a[220:223],a[244:247],v[116:119]
R a[176:179],v210 offset:6144
M v[116:119],a[216:219],a[240:243],v[116:119]
T a[206:207],v186 offset:12296
M v[132:135],a[216:219],a[248:251],v[132:135]
R a[180:183],v211 offset:6144
M v[132:135],a[220:223],a[252:255],v[132:135]
M v[148:151],a[220:223],v[232:235],v[148:151]
M v[148:151],a[216:219],v[228:231],v[148:151]
W lgkmcnt(6)
M v[8:11],a[192:195],a[224:227],v[8:11]
M v[8:11],a[196:199],a[228:231],v[8:11]
W lgkmcnt(0)
T a[208:209],v187
M v[24:27],a[196:199],a[236:239],v[24:27]
M v[24:27],a[192:195],a[232:235],v[24:27]
s_add_u32 m0,s67,6144
s_add_u32 s68,s27,16384
T a[210:211],v187 offset:4096
M v[40:43],a[192:195],a[240:243],v[40:43]
B v212,s[48:51],s68 offen lds
M v[40:43],a[196:199],a[244:247],v[40:43]
T a[212:213],v187 offset:8192
M v[56:59],a[196:199],a[252:255],v[56:59]
B v213,s[48:51],s68 offen offset:1024 lds
M v[56:59],a[192:195],a[248:251],v[56:59]
T a[214:215],v187 offset:12288
M v[72:75],a[192:195],v[228:231],v[72:75]
M v[72:75],a[196:199],v[232:235],v[72:75]
W lgkmcnt(15)
W vmcnt(6)
T a[216:217],v187 offset:8
M v[88:91],a[204:207],a[228:231],v[88:91]
R a[184:187],v210
M v[88:91],a[200:203],a[224:227],v[88:91]
T a[218:219],v187 offset:4104
M v[104:107],a[200:203],a[232:235],v[104:107]
R a[188:191],v211
M v[104:107],a[204:207],a[236:239],v[104:107]
T a[220:221],v187 offset:8200
M v[120:123],a[204:207],a[244:247],v[120:123]
M v[120:123],a[200:203],a[240:243],v[120:123]
T a[222:223],v187 offset:12296
M v[136:139],a[200:203],a[248:251],v[136:139]
M v[136:139],a[204:207],a[252:255],v[136:139]
W lgkmcnt(2)
s_add_u32 m0,s67,0
s_add_u32 s68,s27,32768
M v[152:155],a[204:207],v[232:235],v[152:155]
B v212,s[48:51],s68 offen lds
M v[152:155],a[200:203],v[228:231],v[152:155]
W lgkmcnt(15)
M v[12:15],a[208:211],a[224:227],v[12:15]
s_barrier
M v[12:15],a[212:215],a[228:231],v[12:15]
T a[192:193],v184 offset:16384
M v[28:31],a[212:215],a[236:239],v[28:31]
B v213,s[48:51],s68 offen offset:1024 lds
M v[28:31],a[208:211],a[232:235],v[28:31]
T a[194:195],v184 offset:20480
M v[44:47],a[208:211],a[240:243],v[44:47]
M v[44:47],a[212:215],a[244:247],v[44:47]
W vmcnt(6)
T a[196:197],v184 offset:24576
M v[60:63],a[212:215],a[252:255],v[60:63]
R v[236:239],v210 offset:2048
M v[60:63],a[208:211],a[248:251],v[60:63]
T a[198:199],v184 offset:28672
M v[76:79],a[208:211],v[228:231],v[76:79]
R v[240:243],v211 offset:2048
M v[76:79],a[212:215],v[232:235],v[76:79]
W lgkmcnt(6)
T a[200:201],v184 offset:16392
M v[92:95],a[220:223],a[228:231],v[92:95]
M v[92:95],a[216:219],a[224:227],v[92:95]
T a[202:203],v184 offset:20488
M v[108:111],a[216:219],a[232:235],v[108:111]
M v[108:111],a[220:223],a[236:239],v[108:111]
W lgkmcnt(2)
T a[204:205],v184 offset:24584
M v[124:127],a[220:223],a[244:247],v[124:127]
M v[124:127],a[216:219],a[240:243],v[124:127]
s_add_u32 m0,s67,2048
s_add_u32 s68,s27,49152
T a[206:207],v184 offset:28680
M v[140:143],a[216:219],a[248:251],v[140:143]
B v212,s[48:51],s68 offen lds
M v[140:143],a[220:223],a[252:255],v[140:143]
M v[156:159],a[220:223],v[232:235],v[156:159]
B v213,s[48:51],s68 offen offset:1024 lds
M v[156:159],a[216:219],v[228:231],v[156:159]
W lgkmcnt(15)
M v[0:3],a[192:195],a[160:163],v[0:3]
M v[0:3],a[196:199],a[164:167],v[0:3]
W vmcnt(6)
T a[208:209],v185 offset:16384
M v[16:19],a[196:199],a[172:175],v[16:19]
R a[224:227],v210 offset:4096
M v[16:19],a[192:195],a[168:171],v[16:19]
T a[210:211],v185 offset:20480
M v[32:35],a[192:195],a[176:179],v[32:35]
R a[228:231],v211 offset:4096
M v[32:35],a[196:199],a[180:183],v[32:35]
T a[212:213],v185 offset:24576
M v[48:51],a[196:199],a[188:191],v[48:51]
M v[48:51],a[192:195],a[184:187],v[48:51]
T a[214:215],v185 offset:28672
M v[64:67],a[192:195],v[236:239],v[64:67]
M v[64:67],a[196:199],v[240:243],v[64:67]
W lgkmcnt(6)
W lgkmcnt(2)
T a[216:217],v185 offset:16392
M v[80:83],a[204:207],a[164:167],v[80:83]
M v[80:83],a[200:203],a[160:163],v[80:83]
s_add_u32 m0,s67,4096
s_add_u32 s68,s27,65536
T a[218:219],v185 offset:20488
M v[96:99],a[200:203],a[168:171],v[96:99]
B v212,s[48:51],s68 offen lds
M v[96:99],a[204:207],a[172:175],v[96:99]
T a[220:221],v185 offset:24584
M v[112:115],a[204:207],a[180:183],v[112:115]
B v213,s[48:51],s68 offen offset:1024 lds
M v[112:115],a[200:203],a[176:179],v[112:115]
T a[222:223],v185 offset:28680
M v[128:131],a[200:203],a[184:187],v[128:131]
M v[128:131],a[204:207],a[188:191],v[128:131]
W vmcnt(6)
M v[144:147],a[204:207],v[240:243],v[144:147]
R a[232:235],v210 offset:6144
M v[144:147],a[200:203],v[236:239],v[144:147]
W lgkmcnt(5)
M v[4:7],a[208:211],a[160:163],v[4:7]
R a[236:239],v211 offset:6144
M v[4:7],a[212:215],a[164:167],v[4:7]
T a[192:193],v186 offset:16384
M v[20:23],a[212:215],a[172:175],v[20:23]
M v[20:23],a[208:211],a[168:171],v[20:23]
T a[194:195],v186 offset:20480
M v[36:39],a[208:211],a[176:179],v[36:39]
M v[36:39],a[212:215],a[180:183],v[36:39]
W lgkmcnt(2)
T a[196:197],v186 offset:24576
M v[52:55],a[212:215],a[188:191],v[52:55]
M v[52:55],a[208:211],a[184:187],v[52:55]
s_add_u32 m0,s67,6144
s_add_u32 s27,s26,896
s_and_b32 s27,s27,0x380
s_add_u32 s27,s27,s66
s_add_u32 s68,s27,0
)ASM"
R"ASM(
T a[198:199],v186 offset:28672
M v[68:71],a[208:211],v[236:239],v[68:71]
B v212,s[48:51],s68 offen lds
M v[68:71],a[212:215],v[240:243],v[68:71]
W lgkmcnt(15)
T a[200:201],v186 offset:16392
M v[84:87],a[220:223],a[164:167],v[84:87]
B v213,s[48:51],s68 offen offset:1024 lds
M v[84:87],a[216:219],a[160:163],v[84:87]
T a[202:203],v186 offset:20488
M v[100:103],a[216:219],a[168:171],v[100:103]
M v[100:103],a[220:223],a[172:175],v[100:103]
W vmcnt(6)
T a[204:205],v186 offset:24584
M v[116:119],a[220:223],a[180:183],v[116:119]
R a[240:243],v210
M v[116:119],a[216:219],a[176:179],v[116:119]
T a[206:207],v186 offset:28680
M v[132:135],a[216:219],a[184:187],v[132:135]
R a[244:247],v211
M v[132:135],a[220:223],a[188:191],v[132:135]
M v[148:151],a[220:223],v[240:243],v[148:151]
M v[148:151],a[216:219],v[236:239],v[148:151]
W lgkmcnt(6)
M v[8:11],a[192:195],a[160:163],v[8:11]
M v[8:11],a[196:199],a[164:167],v[8:11]
W lgkmcnt(0)
T a[208:209],v187 offset:16384
M v[24:27],a[196:199],a[172:175],v[24:27]
M v[24:27],a[192:195],a[168:171],v[24:27]
s_add_u32 m0,s67,0
s_add_u32 s68,s27,16384
T a[210:211],v187 offset:20480
M v[40:43],a[192:195],a[176:179],v[40:43]
B v212,s[48:51],s68 offen lds
M v[40:43],a[196:199],a[180:183],v[40:43]
T a[212:213],v187 offset:24576
M v[56:59],a[196:199],a[188:191],v[56:59]
B v213,s[48:51],s68 offen offset:1024 lds
M v[56:59],a[192:195],a[184:187],v[56:59]
T a[214:215],v187 offset:28672
M v[72:75],a[192:195],v[236:239],v[72:75]
M v[72:75],a[196:199],v[240:243],v[72:75]
W lgkmcnt(15)
W vmcnt(6)
T a[216:217],v187 offset:16392
M v[88:91],a[204:207],a[164:167],v[88:91]
R a[248:251],v210 offset:2048
M v[88:91],a[200:203],a[160:163],v[88:91]
T a[218:219],v187 offset:20488
M v[104:107],a[200:203],a[168:171],v[104:107]
R a[252:255],v211 offset:2048
M v[104:107],a[204:207],a[172:175],v[104:107]
T a[220:221],v187 offset:24584
M v[120:123],a[204:207],a[180:183],v[120:123]
M v[120:123],a[200:203],a[176:179],v[120:123]
T a[222:223],v187 offset:28680
M v[136:139],a[200:203],a[184:187],v[136:139]
M v[136:139],a[204:207],a[188:191],v[136:139]
W lgkmcnt(2)
s_add_u32 m0,s67,2048
s_add_u32 s68,s27,32768
M v[152:155],a[204:207],v[240:243],v[152:155]
B v212,s[48:51],s68 offen lds
M v[152:155],a[200:203],v[236:239],v[152:155]
W lgkmcnt(15)
M v[12:15],a[208:211],a[160:163],v[12:15]
s_barrier
M v[12:15],a[212:215],a[164:167],v[12:15]
T a[192:193],v184 offset:32768
M v[28:31],a[212:215],a[172:175],v[28:31]
B v213,s[48:51],s68 offen offset:1024 lds
M v[28:31],a[208:211],a[168:171],v[28:31]
T a[194:195],v184 offset:36864
M v[44:47],a[208:211],a[176:179],v[44:47]
M v[44:47],a[212:215],a[180:183],v[44:47]
W vmcnt(6)
T a[196:197],v184 offset:40960
M v[60:63],a[212:215],a[188:191],v[60:63]
R v[228:231],v210 offset:4096
M v[60:63],a[208:211],a[184:187],v[60:63]
T a[198:199],v184 offset:45056
M v[76:79],a[208:211],v[236:239],v[76:79]
R v[232:235],v211 offset:4096
M v[76:79],a[212:215],v[240:243],v[76:79]
W lgkmcnt(6)
T a[200:201],v184 offset:32776
M v[92:95],a[220:223],a[164:167],v[92:95]
M v[92:95],a[216:219],a[160:163],v[92:95]
T a[202:203],v184 offset:36872
M v[108:111],a[216:219],a[168:171],v[108:111]
M v[108:111],a[220:223],a[172:175],v[108:111]
W lgkmcnt(2)
T a[204:205],v184 offset:40968
M v[124:127],a[220:223],a[180:183],v[124:127]
M v[124:127],a[216:219],a[176:179],v[124:127]
s_add_u32 m0,s67,4096
s_add_u32 s68,s27,49152
T a[206:207],v184 offset:45064
M v[140:143],a[216:219],a[184:187],v[140:143]
B v212,s[48:51],s68 offen lds
M v[140:143],a[220:223],a[188:191],v[140:143]
M v[156:159],a[220:223],v[240:243],v[156:159]
B v213,s[48:51],s68 offen offset:1024 lds
M v[156:159],a[216:219],v[236:239],v[156:159]
W lgkmcnt(15)
M v[0:3],a[192:195],a[224:227],v[0:3]
M v[0:3],a[196:199],a[228:231],v[0:3]
W vmcnt(6)
T a[208:209],v185 offset:32768
M v[16:19],a[196:199],a[236:239],v[16:19]
R a[160:163],v210 offset:6144
M v[16:19],a[192:195],a[232:235],v[16:19]
T a[210:211],v185 offset:36864
M v[32:35],a[192:195],a[240:243],v[32:35]
R a[164:167],v211 offset:6144
M v[32:35],a[196:199],a[244:247],v[32:35]
T a[212:213],v185 offset:40960
M v[48:51],a[196:199],a[252:255],v[48:51]
M v[48:51],a[192:195],a[248:251],v[48:51]
T a[214:215],v185 offset:45056
M v[64:67],a[192:195],v[228:231],v[64:67]
M v[64:67],a[196:199],v[232:235],v[64:67]
W lgkmcnt(6)
W lgkmcnt(2)
T a[216:217],v185 offset:32776
M v[80:83],a[204:207],a[228:231],v[80:83]
M v[80:83],a[200:203],a[224:227],v[80:83]
s_add_u32 m0,s67,6144
s_add_u32 s68,s27,65536
T a[218:219],v185 offset:36872
M v[96:99],a[200:203],a[232:235],v[96:99]
B v212,s[48:51],s68 offen lds
M v[96:99],a[204:207],a[236:239],v[96:99]
T a[220:221],v185 offset:40968
M v[112:115],a[204:207],a[244:247],v[112:115]
B v213,s[48:51],s68 offen offset:1024 lds
M v[112:115],a[200:203],a[240:243],v[112:115]
T a[222:223],v185 offset:45064
M v[128:131],a[200:203],a[248:251],v[128:131]
M v[128:131],a[204:207],a[252:255],v[128:131]
W vmcnt(6)
M v[144:147],a[204:207],v[232:235],v[144:147]
R a[168:171],v210
M v[144:147],a[200:203],v[228:231],v[144:147]
W lgkmcnt(5)
M v[4:7],a[208:211],a[224:227],v[4:7]
R a[172:175],v211
M v[4:7],a[212:215],a[228:231],v[4:7]
T a[192:193],v186 offset:32768
M v[20:23],a[212:215],a[236:239],v[20:23]
M v[20:23],a[208:211],a[232:235],v[20:23]
T a[194:195],v186 offset:36864
M v[36:39],a[208:211],a[240:243],v[36:39]
M v[36:39],a[212:215],a[244:247],v[36:39]
W lgkmcnt(2)
T a[196:197],v186 offset:40960
M v[52:55],a[212:215],a[252:255],v[52:55]
M v[52:55],a[208:211],a[248:251],v[52:55]
s_add_u32 m0,s67,0
s_add_u32 s27,s26,0
s_and_b32 s27,s27,0x380
s_add_u32 s27,s27,s66
s_add_u32 s68,s27,0
T a[198:199],v186 offset:45056
M v[68:71],a[208:211],v[228:231],v[68:71]
B v212,s[48:51],s68 offen lds
M v[68:71],a[212:215],v[232:235],v[68:71]
W lgkmcnt(15)
T a[200:201],v186 offset:32776
M v[84:87],a[220:223],a[228:231],v[84:87]
B v213,s[48:51],s68 offen offset:1024 lds
M v[84:87],a[216:219],a[224:227],v[84:87]
T a[202:203],v186 offset:36872
M v[100:103],a[216:219],a[232:235],v[100:103]
M v[100:103],a[220:223],a[236:239],v[100:103]
W vmcnt(6)
T a[204:205],v186 offset:40968
M v[116:119],a[220:223],a[244:247],v[116:119]
R a[176:179],v210 offset:2048
M v[116:119],a[216:219],a[240:243],v[116:119]
T a[206:207],v186 offset:45064
M v[132:135],a[216:219],a[248:251],v[132:135]
R a[180:183],v211 offset:2048
M v[132:135],a[220:223],a[252:255],v[132:135]
M v[148:151],a[220:223],v[232:235],v[148:151]
M v[148:151],a[216:219],v[228:231],v[148:151]
W lgkmcnt(6)
M v[8:11],a[192:195],a[224:227],v[8:11]
M v[8:11],a[196:199],a[228:231],v[8:11]
W lgkmcnt(0)
T a[208:209],v187 offset:32768
M v[24:27],a[196:199],a[236:239],v[24:27]
M v[24:27],a[192:195],a[232:235],v[24:27]
s_add_u32 m0,s67,2048
s_add_u32 s68,s27,16384
T a[210:211],v187 offset:36864
M v[40:43],a[192:195],a[240:243],v[40:43]
B v212,s[48:51],s68 offen lds
M v[40:43],a[196:199],a[244:247],v[40:43]
T a[212:213],v187 offset:40960
M v[56:59],a[196:199],a[252:255],v[56:59]
B v213,s[48:51],s68 offen offset:1024 lds
M v[56:59],a[192:195],a[248:251],v[56:59]
T a[214:215],v187 offset:45056
M v[72:75],a[192:195],v[228:231],v[72:75]
M v[72:75],a[196:199],v[232:235],v[72:75]
W lgkmcnt(15)
W vmcnt(6)
T a[216:217],v187 offset:32776
M v[88:91],a[204:207],a[228:231],v[88:91]
R a[184:187],v210 offset:4096
M v[88:91],a[200:203],a[224:227],v[88:91]
T a[218:219],v187 offset:36872
M v[104:107],a[200:203],a[232:235],v[104:107]
R a[188:191],v211 offset:4096
M v[104:107],a[204:207],a[236:239],v[104:107]
T a[220:221],v187 offset:40968
M v[120:123],a[204:207],a[244:247],v[120:123]
M v[120:123],a[200:203],a[240:243],v[120:123]
T a[222:223],v187 offset:45064
M v[136:139],a[200:203],a[248:251],v[136:139]
M v[136:139],a[204:207],a[252:255],v[136:139]
W lgkmcnt(2)
s_add_u32 m0,s67,4096
s_add_u32 s68,s27,32768
M v[152:155],a[204:207],v[232:235],v[152:155]
B v212,s[48:51],s68 offen lds
M v[152:155],a[200:203],v[228:231],v[152:155]
W lgkmcnt(15)
M v[12:15],a[208:211],a[224:227],v[12:15]
s_barrier
M v[12:15],a[212:215],a[228:231],v[12:15]
T a[192:193],v184 offset:49152
M v[28:31],a[212:215],a[236:239],v[28:31]
B v213,s[48:51],s68 offen offset:1024 lds
M v[28:31],a[208:211],a[232:235],v[28:31]
T a[194:195],v184 offset:53248
M v[44:47],a[208:211],a[240:243],v[44:47]
M v[44:47],a[212:215],a[244:247],v[44:47]
W vmcnt(6)
T a[196:197],v184 offset:57344
M v[60:63],a[212:215],a[252:255],v[60:63]
R v[236:239],v210 offset:6144
M v[60:63],a[208:211],a[248:251],v[60:63]
T a[198:199],v184 offset:61440
M v[76:79],a[208:211],v[228:231],v[76:79]
R v[240:243],v211 offset:6144
M v[76:79],a[212:215],v[232:235],v[76:79]
W lgkmcnt(6)
T a[200:201],v184 offset:49160
M v[92:95],a[220:223],a[228:231],v[92:95]
M v[92:95],a[216:219],a[224:227],v[92:95]
T a[202:203],v184 offset:53256
M v[108:111],a[216:219],a[232:235],v[108:111]
M v[108:111],a[220:223],a[236:239],v[108:111]
W lgkmcnt(2)
T a[204:205],v184 offset:57352
M v[124:127],a[220:223],a[244:247],v[124:127]
M v[124:127],a[216:219],a[240:243],v[124:127]
s_add_u32 m0,s67,6144
s_add_u32 s68,s27,49152
T a[206:207],v184 offset:61448
M v[140:143],a[216:219],a[248:251],v[140:143]
B v212,s[48:51],s68 offen lds
M v[140:143],a[220:223],a[252:255],v[140:143]
M v[156:159],a[220:223],v[232:235],v[156:159]
B v213,s[48:51],s68 offen offset:1024 lds
M v[156:159],a[216:219],v[228:231],v[156:159]
W lgkmcnt(15)
M v[0:3],a[192:195],a[160:163],v[0:3]
M v[0:3],a[196:199],a[164:167],v[0:3]
T a[208:209],v185 offset:49152
M v[16:19],a[196:199],a[172:175],v[16:19]
M v[16:19],a[192:195],a[168:171],v[16:19]
T a[210:211],v185 offset:53248
M v[32:35],a[192:195],a[176:179],v[32:35]
M v[32:35],a[196:199],a[180:183],v[32:35]
T a[212:213],v185 offset:57344
M v[48:51],a[196:199],a[188:191],v[48:51]
M v[48:51],a[192:195],a[184:187],v[48:51]
T a[214:215],v185 offset:61440
M v[64:67],a[192:195],v[236:239],v[64:67]
M v[64:67],a[196:199],v[240:243],v[64:67]
W lgkmcnt(4)
T a[216:217],v185 offset:49160
M v[80:83],a[204:207],a[164:167],v[80:83]
M v[80:83],a[200:203],a[160:163],v[80:83]
T a[218:219],v185 offset:53256
M v[96:99],a[200:203],a[168:171],v[96:99]
M v[96:99],a[204:207],a[172:175],v[96:99]
T a[220:221],v185 offset:57352
M v[112:115],a[204:207],a[180:183],v[112:115]
M v[112:115],a[200:203],a[176:179],v[112:115]
T a[222:223],v185 offset:61448
M v[128:131],a[200:203],a[184:187],v[128:131]
M v[128:131],a[204:207],a[188:191],v[128:131]
U v0,s64,v0
U v1,s64,v1
M v[144:147],a[204:207],v[240:243],v[144:147]
)ASM"
R"ASM(
U v2,s64,v2
U v3,s64,v3
M v[144:147],a[200:203],v[236:239],v[144:147]
W lgkmcnt(4)
U v80,s64,v80
U v81,s64,v81
M v[4:7],a[208:211],a[160:163],v[4:7]
U v82,s64,v82
U v83,s64,v83
M v[4:7],a[212:215],a[164:167],v[4:7]
T a[192:193],v186 offset:49152
C v0,v0,v1
M v[20:23],a[212:215],a[172:175],v[20:23]
C v1,v2,v3
C v2,v80,v81
M v[20:23],a[208:211],a[168:171],v[20:23]
T a[194:195],v186 offset:53248
C v3,v82,v83
M v[36:39],a[208:211],a[176:179],v[36:39]
U v16,s64,v16
U v17,s64,v17
M v[36:39],a[212:215],a[180:183],v[36:39]
T a[196:197],v186 offset:57344
U v18,s64,v18
M v[52:55],a[212:215],a[188:191],v[52:55]
U v19,s64,v19
U v96,s64,v96
M v[52:55],a[208:211],a[184:187],v[52:55]
T a[198:199],v186 offset:61440
U v97,s64,v97
M v[68:71],a[208:211],v[236:239],v[68:71]
U v98,s64,v98
U v99,s64,v99
M v[68:71],a[212:215],v[240:243],v[68:71]
W lgkmcnt(4)
T a[200:201],v186 offset:49160
M v[84:87],a[220:223],a[164:167],v[84:87]
C v16,v16,v17
C v17,v18,v19
M v[84:87],a[216:219],a[160:163],v[84:87]
T a[202:203],v186 offset:53256
C v18,v96,v97
M v[100:103],a[216:219],a[168:171],v[100:103]
C v19,v98,v99
U v32,s64,v32
M v[100:103],a[220:223],a[172:175],v[100:103]
T a[204:205],v186 offset:57352
U v33,s64,v33
M v[116:119],a[220:223],a[180:183],v[116:119]
U v34,s64,v34
U v35,s64,v35
M v[116:119],a[216:219],a[176:179],v[116:119]
T a[206:207],v186 offset:61448
U v112,s64,v112
M v[132:135],a[216:219],a[184:187],v[132:135]
U v113,s64,v113
U v114,s64,v114
M v[132:135],a[220:223],a[188:191],v[132:135]
U v115,s64,v115
C v32,v32,v33
M v[148:151],a[220:223],v[240:243],v[148:151]
C v33,v34,v35
C v34,v112,v113
M v[148:151],a[216:219],v[236:239],v[148:151]
W lgkmcnt(4)
C v35,v114,v115
U v48,s64,v48
M v[8:11],a[192:195],a[160:163],v[8:11]
U v49,s64,v49
U v50,s64,v50
M v[8:11],a[196:199],a[164:167],v[8:11]
T a[208:209],v187 offset:49152
U v51,s64,v51
M v[24:27],a[196:199],a[172:175],v[24:27]
U v128,s64,v128
U v129,s64,v129
M v[24:27],a[192:195],a[168:171],v[24:27]
T a[210:211],v187 offset:53248
U v130,s64,v130
M v[40:43],a[192:195],a[176:179],v[40:43]
U v131,s64,v131
C v48,v48,v49
M v[40:43],a[196:199],a[180:183],v[40:43]
T a[212:213],v187 offset:57344
C v49,v50,v51
M v[56:59],a[196:199],a[188:191],v[56:59]
C v50,v128,v129
C v51,v130,v131
M v[56:59],a[192:195],a[184:187],v[56:59]
T a[214:215],v187 offset:61440
U v64,s64,v64
M v[72:75],a[192:195],v[236:239],v[72:75]
U v65,s64,v65
U v66,s64,v66
M v[72:75],a[196:199],v[240:243],v[72:75]
W lgkmcnt(4)
T a[216:217],v187 offset:49160
M v[88:91],a[204:207],a[164:167],v[88:91]
U v67,s64,v67
U v144,s64,v144
M v[88:91],a[200:203],a[160:163],v[88:91]
T a[218:219],v187 offset:53256
U v145,s64,v145
M v[104:107],a[200:203],a[168:171],v[104:107]
U v146,s64,v146
U v147,s64,v147
M v[104:107],a[204:207],a[172:175],v[104:107]
T a[220:221],v187 offset:57352
C v64,v64,v65
M v[120:123],a[204:207],a[180:183],v[120:123]
C v65,v66,v67
C v66,v144,v145
M v[120:123],a[200:203],a[176:179],v[120:123]
T a[222:223],v187 offset:61448
C v67,v146,v147
M v[136:139],a[200:203],a[184:187],v[136:139]
U v4,s64,v4
U v5,s64,v5
M v[136:139],a[204:207],a[188:191],v[136:139]
U v6,s64,v6
U v7,s64,v7
M v[152:155],a[204:207],v[240:243],v[152:155]
U v84,s64,v84
U v85,s64,v85
M v[152:155],a[200:203],v[236:239],v[152:155]
W lgkmcnt(4)
U v86,s64,v86
U v87,s64,v87
M v[12:15],a[208:211],a[160:163],v[12:15]
C v4,v4,v5
C v5,v6,v7
M v[12:15],a[212:215],a[164:167],v[12:15]
C v6,v84,v85
C v7,v86,v87
M v[28:31],a[212:215],a[172:175],v[28:31]
U v20,s64,v20
U v21,s64,v21
M v[28:31],a[208:211],a[168:171],v[28:31]
U v22,s64,v22
U v23,s64,v23
M v[44:47],a[208:211],a[176:179],v[44:47]
U v100,s64,v100
U v101,s64,v101
M v[44:47],a[212:215],a[180:183],v[44:47]
U v102,s64,v102
U v103,s64,v103
M v[60:63],a[212:215],a[188:191],v[60:63]
C v20,v20,v21
C v21,v22,v23
M v[60:63],a[208:211],a[184:187],v[60:63]
C v22,v100,v101
C v23,v102,v103
M v[76:79],a[208:211],v[236:239],v[76:79]
U v36,s64,v36
U v37,s64,v37
M v[76:79],a[212:215],v[240:243],v[76:79]
W lgkmcnt(0)
U v38,s64,v38
U v39,s64,v39
M v[92:95],a[220:223],a[164:167],v[92:95]
U v116,s64,v116
U v117,s64,v117
M v[92:95],a[216:219],a[160:163],v[92:95]
U v118,s64,v118
U v119,s64,v119
M v[108:111],a[216:219],a[168:171],v[108:111]
C v36,v36,v37
C v37,v38,v39
M v[108:111],a[220:223],a[172:175],v[108:111]
C v38,v116,v117
C v39,v118,v119
M v[124:127],a[220:223],a[180:183],v[124:127]
U v52,s64,v52
U v53,s64,v53
M v[124:127],a[216:219],a[176:179],v[124:127]
U v54,s64,v54
U v55,s64,v55
M v[140:143],a[216:219],a[184:187],v[140:143]
U v132,s64,v132
U v133,s64,v133
M v[140:143],a[220:223],a[188:191],v[140:143]
U v134,s64,v134
U v135,s64,v135
M v[156:159],a[220:223],v[240:243],v[156:159]
C v52,v52,v53
C v53,v54,v55
M v[156:159],a[216:219],v[236:239],v[156:159]
C v54,v132,v133
C v55,v134,v135
s_barrier
R a[160:163],v198
R a[164:167],v199
R a[168:171],v200
R a[172:175],v201
s_nop 1
v_pk_mul_f32 v[68:69],v[68:69],s[64:65]
v_pk_mul_f32 v[70:71],v[70:71],s[64:65]
v_pk_mul_f32 v[148:149],v[148:149],s[64:65]
v_pk_mul_f32 v[150:151],v[150:151],s[64:65]
C v68,v68,v69
C v69,v70,v71
C v70,v148,v149
C v71,v150,v151
v_pk_mul_f32 v[8:9],v[8:9],s[64:65]
v_pk_mul_f32 v[10:11],v[10:11],s[64:65]
v_pk_mul_f32 v[88:89],v[88:89],s[64:65]
v_pk_mul_f32 v[90:91],v[90:91],s[64:65]
C v8,v8,v9
C v9,v10,v11
C v10,v88,v89
C v11,v90,v91
v_pk_mul_f32 v[24:25],v[24:25],s[64:65]
v_pk_mul_f32 v[26:27],v[26:27],s[64:65]
v_pk_mul_f32 v[104:105],v[104:105],s[64:65]
v_pk_mul_f32 v[106:107],v[106:107],s[64:65]
C v24,v24,v25
C v25,v26,v27
C v26,v104,v105
C v27,v106,v107
v_pk_mul_f32 v[40:41],v[40:41],s[64:65]
v_pk_mul_f32 v[42:43],v[42:43],s[64:65]
v_pk_mul_f32 v[120:121],v[120:121],s[64:65]
v_pk_mul_f32 v[122:123],v[122:123],s[64:65]
C v40,v40,v41
C v41,v42,v43
C v42,v120,v121
C v43,v122,v123
v_pk_mul_f32 v[56:57],v[56:57],s[64:65]
v_pk_mul_f32 v[58:59],v[58:59],s[64:65]
v_pk_mul_f32 v[136:137],v[136:137],s[64:65]
v_pk_mul_f32 v[138:139],v[138:139],s[64:65]
C v56,v56,v57
C v57,v58,v59
C v58,v136,v137
C v59,v138,v139
v_pk_mul_f32 v[72:73],v[72:73],s[64:65]
v_pk_mul_f32 v[74:75],v[74:75],s[64:65]
v_pk_mul_f32 v[152:153],v[152:153],s[64:65]
v_pk_mul_f32 v[154:155],v[154:155],s[64:65]
C v72,v72,v73
C v73,v74,v75
C v74,v152,v153
C v75,v154,v155
v_pk_mul_f32 v[12:13],v[12:13],s[64:65]
v_pk_mul_f32 v[14:15],v[14:15],s[64:65]
v_pk_mul_f32 v[92:93],v[92:93],s[64:65]
v_pk_mul_f32 v[94:95],v[94:95],s[64:65]
C v12,v12,v13
C v13,v14,v15
C v14,v92,v93
C v15,v94,v95
v_pk_mul_f32 v[28:29],v[28:29],s[64:65]
v_pk_mul_f32 v[30:31],v[30:31],s[64:65]
v_pk_mul_f32 v[108:109],v[108:109],s[64:65]
v_pk_mul_f32 v[110:111],v[110:111],s[64:65]
C v28,v28,v29
C v29,v30,v31
C v30,v108,v109
C v31,v110,v111
v_pk_mul_f32 v[44:45],v[44:45],s[64:65]
v_pk_mul_f32 v[46:47],v[46:47],s[64:65]
v_pk_mul_f32 v[124:125],v[124:125],s[64:65]
v_pk_mul_f32 v[126:127],v[126:127],s[64:65]
C v44,v44,v45
C v45,v46,v47
C v46,v124,v125
C v47,v126,v127
v_pk_mul_f32 v[60:61],v[60:61],s[64:65]
v_pk_mul_f32 v[62:63],v[62:63],s[64:65]
v_pk_mul_f32 v[140:141],v[140:141],s[64:65]
v_pk_mul_f32 v[142:143],v[142:143],s[64:65]
C v60,v60,v61
C v61,v62,v63
C v62,v140,v141
C v63,v142,v143
v_pk_mul_f32 v[76:77],v[76:77],s[64:65]
v_pk_mul_f32 v[78:79],v[78:79],s[64:65]
v_pk_mul_f32 v[156:157],v[156:157],s[64:65]
v_pk_mul_f32 v[158:159],v[158:159],s[64:65]
C v76,v76,v77
C v77,v78,v79
C v78,v156,v157
C v79,v158,v159
)ASM"
R"ASM(
LPREa_%=:
W lgkmcnt(0)
s_mov_b32 s69,0
M v[120:123],a[160:163],v[0:3],0
v_cmp_eq_u32 vcc,1,v244
v_mov_b32 v250,0x44444444
v_mov_b32 v251,0xeeeeeeee
M v[120:123],a[164:167],v[4:7],v[120:123]
R a[176:179],v198 offset:4096
v_cndmask_b32 v253,v250,v251,vcc
v_cmp_gt_u32 vcc,2,v244
M v[120:123],a[168:171],v[8:11],v[120:123]
R a[180:183],v199 offset:4096
v_mov_b32 v251,0x3f803f80
AW a224,0
M v[120:123],a[172:175],v[12:15],v[120:123]
R a[184:187],v200 offset:4096
v_cndmask_b32 v250,0,v251,vcc
AW a244,v250
M v[124:127],a[160:163],v[16:19],0
R a[188:191],v201 offset:4096
AW a245,v250
AW a246,v250
M v[124:127],a[164:167],v[20:23],v[124:127]
AW a247,v250
AW a225,0
M v[124:127],a[168:171],v[24:27],v[124:127]
AW a228,0
AW a229,0
M v[124:127],a[172:175],v[28:31],v[124:127]
AW a232,0
AW a233,0
M v[128:131],a[160:163],v[32:35],0
AW a236,0
AW a237,0
M v[128:131],a[164:167],v[36:39],v[128:131]
AW a240,0
AW a241,0
M v[128:131],a[168:171],v[40:43],v[128:131]
v_max3_f32 v160,v120,v121,v222
v_max3_f32 v160,v122,v123,v160
M v[128:131],a[172:175],v[44:47],v[128:131]
v_mov_b32 v161,v160
M v[132:135],a[160:163],v[48:51],0
M v[132:135],a[164:167],v[52:55],v[132:135]
v_permlane32_swap_b32 v160,v161
v_max_f32 v160,v160,v161
M v[132:135],a[168:171],v[56:59],v[132:135]
v_max3_f32 v164,v124,v125,v223
v_mov_b32 v161,v160
M v[132:135],a[172:175],v[60:63],v[132:135]
v_max3_f32 v164,v126,v127,v164
v_permlane16_swap_b32 v160,v161
M v[136:139],a[160:163],v[64:67],0
v_mov_b32 v165,v164
v_max_f32 v160,v160,v161
M v[136:139],a[164:167],v[68:71],v[136:139]
v_permlane32_swap_b32 v164,v165
v_sub_f32 v161,0xc1400000,v160
M v[136:139],a[168:171],v[72:75],v[136:139]
v_max3_f32 v168,v128,v129,v224
v_max_f32 v164,v164,v165
M v[136:139],a[172:175],v[76:79],v[136:139]
W lgkmcnt(0)
v_max3_f32 v168,v130,v131,v168
v_mov_b32 v165,v164
v_mov_b32 v169,v168
v_sub_f32 v162,0xc1400000,v160
v_max3_f32 v172,v132,v133,v225
v_permlane32_swap_b32 v168,v169
v_max3_f32 v172,v134,v135,v172
v_max_f32 v168,v168,v169
v_mov_b32 v173,v172
v_mov_b32 v169,v168
v_max3_f32 v176,v136,v137,v226
v_permlane32_swap_b32 v172,v173
v_max3_f32 v176,v138,v139,v176
v_max_f32 v172,v172,v173
v_mov_b32 v177,v176
v_mov_b32 v173,v172
v_permlane16_swap_b32 v164,v165
v_permlane32_swap_b32 v176,v177
v_permlane16_swap_b32 v168,v169
v_max_f32 v176,v176,v177
v_permlane16_swap_b32 v172,v173
v_mov_b32 v177,v176
v_max_f32 v164,v164,v165
v_max_f32 v168,v168,v169
v_permlane16_swap_b32 v176,v177
v_max_f32 v172,v172,v173
v_max_f32 v176,v176,v177
v_sub_f32 v165,0xc1400000,v164
v_sub_f32 v169,0xc1400000,v168
v_sub_f32 v173,0xc1400000,v172
v_sub_f32 v177,0xc1400000,v176
v_sub_f32 v166,0xc1400000,v164
v_sub_f32 v170,0xc1400000,v168
v_sub_f32 v174,0xc1400000,v172
v_sub_f32 v178,0xc1400000,v176
v_sub_f32 v163,0xc1400000,v160
v_sub_f32 v167,0xc1400000,v164
v_sub_f32 v171,0xc1400000,v168
v_sub_f32 v175,0xc1400000,v172
v_sub_f32 v179,0xc1400000,v176
v_sub_f32 v160,0xc1400000,v160
v_sub_f32 v164,0xc1400000,v164
v_sub_f32 v168,0xc1400000,v168
v_sub_f32 v172,0xc1400000,v172
v_sub_f32 v176,0xc1400000,v176
F v120,v120,v160
F v124,v124,v164
F v128,v128,v168
F v132,v132,v172
F v136,v136,v176
F v121,v121,v160
F v125,v125,v164
F v129,v129,v168
F v133,v133,v172
F v137,v137,v176
F v122,v122,v160
F v126,v126,v164
F v130,v130,v168
F v134,v134,v172
F v138,v138,v176
F v123,v123,v160
F v127,v127,v164
F v131,v131,v168
F v135,v135,v172
F v139,v139,v176
M v[140:143],a[176:179],v[0:3],v[160:163]
E v120,v120
E v121,v121
M v[140:143],a[180:183],v[4:7],v[140:143]
R a[160:163],v198 offset:8192
E v122,v122
E v123,v123
M v[140:143],a[184:187],v[8:11],v[140:143]
R a[164:167],v199 offset:8192
C v80,v120,v121
C v81,v122,v123
M v[140:143],a[188:191],v[12:15],v[140:143]
R a[168:171],v200 offset:8192
E v124,v124
E v125,v125
M v[144:147],a[176:179],v[16:19],v[164:167]
R a[172:175],v201 offset:8192
E v126,v126
E v127,v127
M v[144:147],a[180:183],v[20:23],v[144:147]
C v88,v124,v125
C v89,v126,v127
M v[144:147],a[184:187],v[24:27],v[144:147]
E v128,v128
E v129,v129
M v[144:147],a[188:191],v[28:31],v[144:147]
E v130,v130
E v131,v131
M v[148:151],a[176:179],v[32:35],v[168:171]
C v96,v128,v129
C v97,v130,v131
M v[148:151],a[180:183],v[36:39],v[148:151]
E v132,v132
M v[148:151],a[184:187],v[40:43],v[148:151]
E v133,v133
E v134,v134
M v[148:151],a[188:191],v[44:47],v[148:151]
E v135,v135
C v104,v132,v133
M v[152:155],a[176:179],v[48:51],v[172:175]
C v105,v134,v135
E v136,v136
M v[152:155],a[180:183],v[52:55],v[152:155]
E v137,v137
E v138,v138
M v[152:155],a[184:187],v[56:59],v[152:155]
E v139,v139
C v112,v136,v137
M v[152:155],a[188:191],v[60:63],v[152:155]
C v113,v138,v139
E v140,v140
M v[156:159],a[176:179],v[64:67],v[176:179]
E v141,v141
E v142,v142
M v[156:159],a[180:183],v[68:71],v[156:159]
E v143,v143
C v82,v140,v141
M v[156:159],a[184:187],v[72:75],v[156:159]
C v83,v142,v143
E v144,v144
M v[156:159],a[188:191],v[76:79],v[156:159]
W lgkmcnt(0)
E v145,v145
M v[120:123],a[160:163],v[0:3],v[160:163]
E v146,v146
E v147,v147
M v[120:123],a[164:167],v[4:7],v[120:123]
R a[176:179],v198 offset:12288
C v90,v144,v145
C v91,v146,v147
M v[120:123],a[168:171],v[8:11],v[120:123]
R a[180:183],v199 offset:12288
E v148,v148
E v149,v149
M v[120:123],a[172:175],v[12:15],v[120:123]
R a[184:187],v200 offset:12288
E v150,v150
E v151,v151
M v[124:127],a[160:163],v[16:19],v[164:167]
R a[188:191],v201 offset:12288
C v98,v148,v149
C v99,v150,v151
M v[124:127],a[164:167],v[20:23],v[124:127]
E v152,v152
E v153,v153
M v[124:127],a[168:171],v[24:27],v[124:127]
E v154,v154
E v155,v155
M v[124:127],a[172:175],v[28:31],v[124:127]
C v106,v152,v153
C v107,v154,v155
M v[128:131],a[160:163],v[32:35],v[168:171]
E v156,v156
E v157,v157
M v[128:131],a[164:167],v[36:39],v[128:131]
E v158,v158
M v[128:131],a[168:171],v[40:43],v[128:131]
E v159,v159
C v114,v156,v157
M v[128:131],a[172:175],v[44:47],v[128:131]
C v115,v158,v159
E v120,v120
M v[132:135],a[160:163],v[48:51],v[172:175]
E v121,v121
E v122,v122
M v[132:135],a[164:167],v[52:55],v[132:135]
E v123,v123
C v84,v120,v121
M v[132:135],a[168:171],v[56:59],v[132:135]
C v85,v122,v123
E v124,v124
M v[132:135],a[172:175],v[60:63],v[132:135]
E v125,v125
E v126,v126
M v[136:139],a[160:163],v[64:67],v[176:179]
E v127,v127
C v92,v124,v125
M v[136:139],a[164:167],v[68:71],v[136:139]
C v93,v126,v127
E v128,v128
M v[136:139],a[168:171],v[72:75],v[136:139]
E v129,v129
E v130,v130
M v[136:139],a[172:175],v[76:79],v[136:139]
W lgkmcnt(0)
E v131,v131
M v[140:143],a[176:179],v[0:3],v[160:163]
s_barrier
C v100,v128,v129
C v101,v130,v131
M v[140:143],a[180:183],v[4:7],v[140:143]
T a[192:193],v202
E v132,v132
E v133,v133
M v[140:143],a[184:187],v[8:11],v[140:143]
T a[194:195],v202 offset:4096
E v134,v134
E v135,v135
M v[140:143],a[188:191],v[12:15],v[140:143]
T a[196:197],v202 offset:8192
C v108,v132,v133
C v109,v134,v135
M v[144:147],a[176:179],v[16:19],v[164:167]
T a[198:199],v202 offset:12288
E v136,v136
E v137,v137
M v[144:147],a[180:183],v[20:23],v[144:147]
R a[160:163],v198 offset:16384
E v138,v138
E v139,v139
M v[144:147],a[184:187],v[24:27],v[144:147]
R a[164:167],v199 offset:16384
C v116,v136,v137
C v117,v138,v139
M v[144:147],a[188:191],v[28:31],v[144:147]
R a[168:171],v200 offset:16384
E v140,v140
E v141,v141
M v[148:151],a[176:179],v[32:35],v[168:171]
R a[172:175],v201 offset:16384
E v142,v142
E v143,v143
M v[148:151],a[180:183],v[36:39],v[148:151]
T a[200:201],v203
C v86,v140,v141
M v[148:151],a[184:187],v[40:43],v[148:151]
T a[202:203],v203 offset:4096
C v87,v142,v143
M v[148:151],a[188:191],v[44:47],v[148:151]
T a[204:205],v203 offset:8192
E v144,v144
)ASM"
R"ASM(
E v145,v145
E v146,v146
M v[152:155],a[176:179],v[48:51],v[172:175]
T a[206:207],v203 offset:12288
E v147,v147
C v94,v144,v145
M v[152:155],a[180:183],v[52:55],v[152:155]
C v95,v146,v147
M v[152:155],a[184:187],v[56:59],v[152:155]
M v[152:155],a[188:191],v[60:63],v[152:155]
E v148,v148
E v149,v149
E v150,v150
E v151,v151
C v102,v148,v149
M v[184:187],a[176:179],v[64:67],v[176:179]
C v103,v150,v151
M v[184:187],a[180:183],v[68:71],v[184:187]
M v[184:187],a[184:187],v[72:75],v[184:187]
M v[184:187],a[188:191],v[76:79],v[184:187]
E v152,v152
E v153,v153
E v154,v154
E v155,v155
C v110,v152,v153
C v111,v154,v155
s_cmp_eq_u32 s70,0
s_cbranch_scc1 LNOPUBa_%=
W vmcnt(0)
v_mov_b32 v248,s72
v_mov_b32 v249,s73
s_mov_b64 exec,1
buffer_store_dwordx2 v[248:249],off,s[52:55],s85 sc1
s_mov_b64 exec,-1
LNOPUBa_%=:
s_nop 0
W lgkmcnt(4)
)ASM"
R"ASM(
M v[180:183],a[160:163],v[0:3],v[160:163]
E v184,v184
M v[180:183],a[164:167],v[4:7],v[180:183]
E v185,v185
M v[180:183],a[168:171],v[8:11],v[180:183]
T a[208:209],v204
M v[180:183],a[172:175],v[12:15],v[180:183]
E v186,v186
M a[0:3],a[192:195],v[80:83],0
T a[210:211],v204 offset:4096
C v118,v184,v185
M a[0:3],a[196:199],v[84:87],a[0:3]
E v187,v187
M a[4:7],a[196:199],v[92:95],0
T a[212:213],v204 offset:8192
M a[4:7],a[192:195],v[88:91],a[4:7]
C v119,v186,v187
M v[184:187],a[160:163],v[16:19],v[164:167]
W lgkmcnt(3)
E v180,v180
M v[184:187],a[164:167],v[20:23],v[184:187]
E v181,v181
M v[184:187],a[168:171],v[24:27],v[184:187]
T a[214:215],v204 offset:12288
M v[184:187],a[172:175],v[28:31],v[184:187]
E v182,v182
M a[8:11],a[192:195],v[96:99],0
C v120,v180,v181
M a[8:11],a[196:199],v[100:103],a[8:11]
E v183,v183
M a[12:15],a[196:199],v[108:111],0
R a[176:179],v198 offset:20480
M a[12:15],a[192:195],v[104:107],a[12:15]
C v121,v182,v183
M v[180:183],a[160:163],v[32:35],v[168:171]
E v184,v184
M v[180:183],a[164:167],v[36:39],v[180:183]
E v185,v185
M v[180:183],a[168:171],v[40:43],v[180:183]
R a[180:183],v199 offset:20480
M v[180:183],a[172:175],v[44:47],v[180:183]
E v186,v186
M a[16:19],a[192:195],v[112:115],0
R a[184:187],v200 offset:20480
C v128,v184,v185
M a[16:19],a[196:199],v[116:119],a[16:19]
E v187,v187
M a[20:23],a[204:207],v[84:87],0
R a[188:191],v201 offset:20480
M a[20:23],a[200:203],v[80:83],a[20:23]
C v129,v186,v187
v_smfmac_f32_16x16x64_bf16 a[224:227],a[244:247],v[80:87],v253
M v[184:187],a[160:163],v[48:51],v[172:175]
E v180,v180
M v[184:187],a[164:167],v[52:55],v[184:187]
E v181,v181
M v[184:187],a[168:171],v[56:59],v[184:187]
T a[216:217],v205
M v[184:187],a[172:175],v[60:63],v[184:187]
E v182,v182
M a[24:27],a[200:203],v[88:91],0
C v136,v180,v181
M a[24:27],a[204:207],v[92:95],a[24:27]
E v183,v183
M a[28:31],a[204:207],v[100:103],0
T a[218:219],v205 offset:4096
M a[28:31],a[200:203],v[96:99],a[28:31]
C v137,v182,v183
M v[180:183],a[160:163],v[64:67],v[176:179]
E v184,v184
M v[180:183],a[164:167],v[68:71],v[180:183]
E v185,v185
M v[180:183],a[168:171],v[72:75],v[180:183]
T a[220:221],v205 offset:8192
M v[180:183],a[172:175],v[76:79],v[180:183]
E v186,v186
M a[32:35],a[200:203],v[104:107],0
C v144,v184,v185
M a[32:35],a[204:207],v[108:111],a[32:35]
W lgkmcnt(3)
E v187,v187
M a[36:39],a[204:207],v[116:119],0
T a[222:223],v205 offset:12288
M a[36:39],a[200:203],v[112:115],a[36:39]
C v145,v186,v187
M v[184:187],a[176:179],v[0:3],v[160:163]
E v180,v180
M v[184:187],a[180:183],v[4:7],v[184:187]
E v181,v181
M v[184:187],a[184:187],v[8:11],v[184:187]
T a[192:193],v206
M v[184:187],a[188:191],v[12:15],v[184:187]
E v182,v182
M a[40:43],a[208:211],v[80:83],0
T a[194:195],v206 offset:4096
C v152,v180,v181
M a[40:43],a[212:215],v[84:87],a[40:43]
E v183,v183
M a[44:47],a[212:215],v[92:95],0
T a[196:197],v206 offset:8192
M a[44:47],a[208:211],v[88:91],a[44:47]
C v153,v182,v183
M v[180:183],a[176:179],v[16:19],v[164:167]
W lgkmcnt(3)
E v184,v184
M v[180:183],a[180:183],v[20:23],v[180:183]
E v185,v185
M v[180:183],a[184:187],v[24:27],v[180:183]
T a[198:199],v206 offset:12288
M v[180:183],a[188:191],v[28:31],v[180:183]
E v186,v186
M a[48:51],a[208:211],v[96:99],0
C v122,v184,v185
M a[48:51],a[212:215],v[100:103],a[48:51]
E v187,v187
M a[52:55],a[212:215],v[108:111],0
R a[160:163],v198 offset:24576
M a[52:55],a[208:211],v[104:107],a[52:55]
C v123,v186,v187
v_smfmac_f32_16x16x64_bf16 a[236:239],a[244:247],v[104:111],v253
M v[184:187],a[176:179],v[32:35],v[168:171]
E v180,v180
M v[184:187],a[180:183],v[36:39],v[184:187]
E v181,v181
M v[184:187],a[184:187],v[40:43],v[184:187]
R a[164:167],v199 offset:24576
M v[184:187],a[188:191],v[44:47],v[184:187]
E v182,v182
M a[56:59],a[208:211],v[112:115],0
R a[168:171],v200 offset:24576
C v130,v180,v181
M a[56:59],a[212:215],v[116:119],a[56:59]
E v183,v183
M a[60:63],a[220:223],v[84:87],0
R a[172:175],v201 offset:24576
M a[60:63],a[216:219],v[80:83],a[60:63]
C v131,v182,v183
M v[180:183],a[176:179],v[48:51],v[172:175]
E v184,v184
M v[180:183],a[180:183],v[52:55],v[180:183]
E v185,v185
M v[180:183],a[184:187],v[56:59],v[180:183]
T a[200:201],v207
M v[180:183],a[188:191],v[60:63],v[180:183]
E v186,v186
M a[64:67],a[216:219],v[88:91],0
C v138,v184,v185
M a[64:67],a[220:223],v[92:95],a[64:67]
E v187,v187
M a[68:71],a[220:223],v[100:103],0
T a[202:203],v207 offset:4096
M a[68:71],a[216:219],v[96:99],a[68:71]
C v139,v186,v187
M v[184:187],a[176:179],v[64:67],v[176:179]
E v180,v180
M v[184:187],a[180:183],v[68:71],v[184:187]
E v181,v181
M v[184:187],a[184:187],v[72:75],v[184:187]
T a[204:205],v207 offset:8192
M v[184:187],a[188:191],v[76:79],v[184:187]
E v182,v182
M a[72:75],a[216:219],v[104:107],0
C v146,v180,v181
M a[72:75],a[220:223],v[108:111],a[72:75]
W lgkmcnt(3)
E v183,v183
M a[76:79],a[220:223],v[116:119],0
T a[206:207],v207 offset:12288
M a[76:79],a[216:219],v[112:115],a[76:79]
C v147,v182,v183
M v[180:183],a[160:163],v[0:3],v[160:163]
E v184,v184
M v[180:183],a[164:167],v[4:7],v[180:183]
E v185,v185
M v[180:183],a[168:171],v[8:11],v[180:183]
T a[208:209],v208
M v[180:183],a[172:175],v[12:15],v[180:183]
E v186,v186
M a[80:83],a[192:195],v[80:83],0
T a[210:211],v208 offset:4096
C v154,v184,v185
M a[80:83],a[196:199],v[84:87],a[80:83]
E v187,v187
M a[84:87],a[196:199],v[92:95],0
T a[212:213],v208 offset:8192
M a[84:87],a[192:195],v[88:91],a[84:87]
C v155,v186,v187
v_smfmac_f32_16x16x64_bf16 a[228:231],a[244:247],v[88:95],v253
M v[184:187],a[160:163],v[16:19],v[164:167]
W lgkmcnt(3)
E v180,v180
M v[184:187],a[164:167],v[20:23],v[184:187]
E v181,v181
M v[184:187],a[168:171],v[24:27],v[184:187]
T a[214:215],v208 offset:12288
M v[184:187],a[172:175],v[28:31],v[184:187]
E v182,v182
M a[88:91],a[192:195],v[96:99],0
C v124,v180,v181
M a[88:91],a[196:199],v[100:103],a[88:91]
E v183,v183
M a[92:95],a[196:199],v[108:111],0
R a[176:179],v198 offset:28672
M a[92:95],a[192:195],v[104:107],a[92:95]
C v125,v182,v183
M v[180:183],a[160:163],v[32:35],v[168:171]
E v184,v184
M v[180:183],a[164:167],v[36:39],v[180:183]
E v185,v185
M v[180:183],a[168:171],v[40:43],v[180:183]
R a[180:183],v199 offset:28672
M v[180:183],a[172:175],v[44:47],v[180:183]
E v186,v186
M a[96:99],a[192:195],v[112:115],0
R a[184:187],v200 offset:28672
C v132,v184,v185
M a[96:99],a[196:199],v[116:119],a[96:99]
E v187,v187
M a[100:103],a[204:207],v[84:87],0
R a[188:191],v201 offset:28672
M a[100:103],a[200:203],v[80:83],a[100:103]
C v133,v186,v187
M v[184:187],a[160:163],v[48:51],v[172:175]
E v180,v180
M v[184:187],a[164:167],v[52:55],v[184:187]
E v181,v181
M v[184:187],a[168:171],v[56:59],v[184:187]
T a[216:217],v209
M v[184:187],a[172:175],v[60:63],v[184:187]
E v182,v182
M a[104:107],a[200:203],v[88:91],0
C v140,v180,v181
M a[104:107],a[204:207],v[92:95],a[104:107]
E v183,v183
M a[108:111],a[204:207],v[100:103],0
T a[218:219],v209 offset:4096
M a[108:111],a[200:203],v[96:99],a[108:111]
C v141,v182,v183
M v[180:183],a[160:163],v[64:67],v[176:179]
E v184,v184
M v[180:183],a[164:167],v[68:71],v[180:183]
E v185,v185
M v[180:183],a[168:171],v[72:75],v[180:183]
T a[220:221],v209 offset:8192
M v[180:183],a[172:175],v[76:79],v[180:183]
E v186,v186
M a[112:115],a[200:203],v[104:107],0
C v148,v184,v185
M a[112:115],a[204:207],v[108:111],a[112:115]
W lgkmcnt(3)
E v187,v187
M a[116:119],a[204:207],v[116:119],0
T a[222:223],v209 offset:12288
M a[116:119],a[200:203],v[112:115],a[116:119]
C v149,v186,v187
v_smfmac_f32_16x16x64_bf16 a[240:243],a[244:247],v[112:119],v253
M v[184:187],a[176:179],v[0:3],v[160:163]
s_barrier
E v180,v180
M v[184:187],a[180:183],v[4:7],v[184:187]
E v181,v181
M v[184:187],a[184:187],v[8:11],v[184:187]
T a[192:193],v202 offset:16384
M v[184:187],a[188:191],v[12:15],v[184:187]
E v182,v182
M a[120:123],a[208:211],v[80:83],0
T a[194:195],v202 offset:20480
C v156,v180,v181
M a[120:123],a[212:215],v[84:87],a[120:123]
E v183,v183
M a[124:127],a[212:215],v[92:95],0
T a[196:197],v202 offset:24576
M a[124:127],a[208:211],v[88:91],a[124:127]
C v157,v182,v183
M v[180:183],a[176:179],v[16:19],v[164:167]
W lgkmcnt(3)
E v184,v184
M v[180:183],a[180:183],v[20:23],v[180:183]
E v185,v185
M v[180:183],a[184:187],v[24:27],v[180:183]
T a[198:199],v202 offset:28672
M v[180:183],a[188:191],v[28:31],v[180:183]
E v186,v186
M a[128:131],a[208:211],v[96:99],0
C v126,v184,v185
M a[128:131],a[212:215],v[100:103],a[128:131]
E v187,v187
M a[132:135],a[212:215],v[108:111],0
R a[160:163],v198 offset:32768
M a[132:135],a[208:211],v[104:107],a[132:135]
C v127,v186,v187
M v[184:187],a[176:179],v[32:35],v[168:171]
E v180,v180
M v[184:187],a[180:183],v[36:39],v[184:187]
E v181,v181
M v[184:187],a[184:187],v[40:43],v[184:187]
R a[164:167],v199 offset:32768
M v[184:187],a[188:191],v[44:47],v[184:187]
E v182,v182
M a[136:139],a[208:211],v[112:115],0
)ASM"
R"ASM(
R a[168:171],v200 offset:32768
C v134,v180,v181
M a[136:139],a[212:215],v[116:119],a[136:139]
E v183,v183
M a[140:143],a[220:223],v[84:87],0
R a[172:175],v201 offset:32768
M a[140:143],a[216:219],v[80:83],a[140:143]
C v135,v182,v183
M v[180:183],a[176:179],v[48:51],v[172:175]
E v184,v184
M v[180:183],a[180:183],v[52:55],v[180:183]
E v185,v185
M v[180:183],a[184:187],v[56:59],v[180:183]
T a[200:201],v203 offset:16384
M v[180:183],a[188:191],v[60:63],v[180:183]
E v186,v186
M a[144:147],a[216:219],v[88:91],0
C v142,v184,v185
M a[144:147],a[220:223],v[92:95],a[144:147]
E v187,v187
M a[148:151],a[220:223],v[100:103],0
T a[202:203],v203 offset:20480
M a[148:151],a[216:219],v[96:99],a[148:151]
C v143,v186,v187
v_smfmac_f32_16x16x64_bf16 a[232:235],a[244:247],v[96:103],v253
M v[184:187],a[176:179],v[64:67],v[176:179]
E v180,v180
M v[184:187],a[180:183],v[68:71],v[184:187]
E v181,v181
M v[184:187],a[184:187],v[72:75],v[184:187]
T a[204:205],v203 offset:24576
M v[184:187],a[188:191],v[76:79],v[184:187]
E v182,v182
M a[152:155],a[216:219],v[104:107],0
C v150,v180,v181
M a[152:155],a[220:223],v[108:111],a[152:155]
W lgkmcnt(3)
E v183,v183
M a[156:159],a[220:223],v[116:119],0
T a[206:207],v203 offset:28672
M a[156:159],a[216:219],v[112:115],a[156:159]
C v151,v182,v183
s_branch LIT1a_%=
)ASM"
R"ASM(
.p2align 4
LLOOPa_%=:
)ASM"
R"ASM(
M v[180:183],a[160:163],v[0:3],v[160:163]
E v184,v184
M v[180:183],a[164:167],v[4:7],v[180:183]
E v185,v185
M v[180:183],a[168:171],v[8:11],v[180:183]
T a[208:209],v204
M v[180:183],a[172:175],v[12:15],v[180:183]
E v186,v186
M a[0:3],a[192:195],v[80:83],a[0:3]
T a[210:211],v204 offset:4096
C v118,v184,v185
M a[0:3],a[196:199],v[84:87],a[0:3]
E v187,v187
M a[4:7],a[196:199],v[92:95],a[4:7]
T a[212:213],v204 offset:8192
M a[4:7],a[192:195],v[88:91],a[4:7]
C v119,v186,v187
M v[184:187],a[160:163],v[16:19],v[164:167]
W lgkmcnt(3)
E v180,v180
M v[184:187],a[164:167],v[20:23],v[184:187]
E v181,v181
M v[184:187],a[168:171],v[24:27],v[184:187]
T a[214:215],v204 offset:12288
M v[184:187],a[172:175],v[28:31],v[184:187]
E v182,v182
M a[8:11],a[192:195],v[96:99],a[8:11]
C v120,v180,v181
M a[8:11],a[196:199],v[100:103],a[8:11]
E v183,v183
M a[12:15],a[196:199],v[108:111],a[12:15]
R a[176:179],v198 offset:20480
M a[12:15],a[192:195],v[104:107],a[12:15]
C v121,v182,v183
M v[180:183],a[160:163],v[32:35],v[168:171]
E v184,v184
M v[180:183],a[164:167],v[36:39],v[180:183]
E v185,v185
M v[180:183],a[168:171],v[40:43],v[180:183]
R a[180:183],v199 offset:20480
M v[180:183],a[172:175],v[44:47],v[180:183]
E v186,v186
M a[16:19],a[192:195],v[112:115],a[16:19]
R a[184:187],v200 offset:20480
C v128,v184,v185
M a[16:19],a[196:199],v[116:119],a[16:19]
E v187,v187
M a[20:23],a[204:207],v[84:87],a[20:23]
R a[188:191],v201 offset:20480
M a[20:23],a[200:203],v[80:83],a[20:23]
C v129,v186,v187
v_smfmac_f32_16x16x64_bf16 a[224:227],a[244:247],v[80:87],v253
M v[184:187],a[160:163],v[48:51],v[172:175]
E v180,v180
M v[184:187],a[164:167],v[52:55],v[184:187]
E v181,v181
M v[184:187],a[168:171],v[56:59],v[184:187]
T a[216:217],v205
M v[184:187],a[172:175],v[60:63],v[184:187]
E v182,v182
M a[24:27],a[200:203],v[88:91],a[24:27]
C v136,v180,v181
M a[24:27],a[204:207],v[92:95],a[24:27]
E v183,v183
M a[28:31],a[204:207],v[100:103],a[28:31]
T a[218:219],v205 offset:4096
M a[28:31],a[200:203],v[96:99],a[28:31]
C v137,v182,v183
M v[180:183],a[160:163],v[64:67],v[176:179]
E v184,v184
M v[180:183],a[164:167],v[68:71],v[180:183]
E v185,v185
M v[180:183],a[168:171],v[72:75],v[180:183]
T a[220:221],v205 offset:8192
M v[180:183],a[172:175],v[76:79],v[180:183]
E v186,v186
M a[32:35],a[200:203],v[104:107],a[32:35]
C v144,v184,v185
M a[32:35],a[204:207],v[108:111],a[32:35]
W lgkmcnt(3)
E v187,v187
M a[36:39],a[204:207],v[116:119],a[36:39]
T a[222:223],v205 offset:12288
M a[36:39],a[200:203],v[112:115],a[36:39]
C v145,v186,v187
M v[184:187],a[176:179],v[0:3],v[160:163]
E v180,v180
M v[184:187],a[180:183],v[4:7],v[184:187]
E v181,v181
M v[184:187],a[184:187],v[8:11],v[184:187]
T a[192:193],v206
M v[184:187],a[188:191],v[12:15],v[184:187]
E v182,v182
M a[40:43],a[208:211],v[80:83],a[40:43]
T a[194:195],v206 offset:4096
C v152,v180,v181
M a[40:43],a[212:215],v[84:87],a[40:43]
E v183,v183
M a[44:47],a[212:215],v[92:95],a[44:47]
T a[196:197],v206 offset:8192
M a[44:47],a[208:211],v[88:91],a[44:47]
C v153,v182,v183
M v[180:183],a[176:179],v[16:19],v[164:167]
W lgkmcnt(3)
E v184,v184
M v[180:183],a[180:183],v[20:23],v[180:183]
E v185,v185
M v[180:183],a[184:187],v[24:27],v[180:183]
T a[198:199],v206 offset:12288
M v[180:183],a[188:191],v[28:31],v[180:183]
E v186,v186
M a[48:51],a[208:211],v[96:99],a[48:51]
C v122,v184,v185
M a[48:51],a[212:215],v[100:103],a[48:51]
E v187,v187
M a[52:55],a[212:215],v[108:111],a[52:55]
R a[160:163],v198 offset:24576
M a[52:55],a[208:211],v[104:107],a[52:55]
C v123,v186,v187
v_smfmac_f32_16x16x64_bf16 a[236:239],a[244:247],v[104:111],v253
M v[184:187],a[176:179],v[32:35],v[168:171]
E v180,v180
M v[184:187],a[180:183],v[36:39],v[184:187]
E v181,v181
M v[184:187],a[184:187],v[40:43],v[184:187]
R a[164:167],v199 offset:24576
M v[184:187],a[188:191],v[44:47],v[184:187]
E v182,v182
M a[56:59],a[208:211],v[112:115],a[56:59]
R a[168:171],v200 offset:24576
C v130,v180,v181
M a[56:59],a[212:215],v[116:119],a[56:59]
E v183,v183
M a[60:63],a[220:223],v[84:87],a[60:63]
R a[172:175],v201 offset:24576
M a[60:63],a[216:219],v[80:83],a[60:63]
C v131,v182,v183
M v[180:183],a[176:179],v[48:51],v[172:175]
E v184,v184
M v[180:183],a[180:183],v[52:55],v[180:183]
E v185,v185
M v[180:183],a[184:187],v[56:59],v[180:183]
T a[200:201],v207
M v[180:183],a[188:191],v[60:63],v[180:183]
E v186,v186
M a[64:67],a[216:219],v[88:91],a[64:67]
C v138,v184,v185
M a[64:67],a[220:223],v[92:95],a[64:67]
E v187,v187
M a[68:71],a[220:223],v[100:103],a[68:71]
T a[202:203],v207 offset:4096
M a[68:71],a[216:219],v[96:99],a[68:71]
C v139,v186,v187
M v[184:187],a[176:179],v[64:67],v[176:179]
E v180,v180
M v[184:187],a[180:183],v[68:71],v[184:187]
E v181,v181
M v[184:187],a[184:187],v[72:75],v[184:187]
T a[204:205],v207 offset:8192
M v[184:187],a[188:191],v[76:79],v[184:187]
E v182,v182
M a[72:75],a[216:219],v[104:107],a[72:75]
C v146,v180,v181
M a[72:75],a[220:223],v[108:111],a[72:75]
W lgkmcnt(3)
E v183,v183
M a[76:79],a[220:223],v[116:119],a[76:79]
T a[206:207],v207 offset:12288
M a[76:79],a[216:219],v[112:115],a[76:79]
C v147,v182,v183
M v[180:183],a[160:163],v[0:3],v[160:163]
E v184,v184
M v[180:183],a[164:167],v[4:7],v[180:183]
E v185,v185
M v[180:183],a[168:171],v[8:11],v[180:183]
T a[208:209],v208
M v[180:183],a[172:175],v[12:15],v[180:183]
E v186,v186
M a[80:83],a[192:195],v[80:83],a[80:83]
T a[210:211],v208 offset:4096
C v154,v184,v185
M a[80:83],a[196:199],v[84:87],a[80:83]
E v187,v187
M a[84:87],a[196:199],v[92:95],a[84:87]
T a[212:213],v208 offset:8192
M a[84:87],a[192:195],v[88:91],a[84:87]
C v155,v186,v187
v_smfmac_f32_16x16x64_bf16 a[228:231],a[244:247],v[88:95],v253
M v[184:187],a[160:163],v[16:19],v[164:167]
W lgkmcnt(3)
E v180,v180
M v[184:187],a[164:167],v[20:23],v[184:187]
E v181,v181
M v[184:187],a[168:171],v[24:27],v[184:187]
T a[214:215],v208 offset:12288
M v[184:187],a[172:175],v[28:31],v[184:187]
E v182,v182
M a[88:91],a[192:195],v[96:99],a[88:91]
C v124,v180,v181
M a[88:91],a[196:199],v[100:103],a[88:91]
E v183,v183
M a[92:95],a[196:199],v[108:111],a[92:95]
R a[176:179],v198 offset:28672
M a[92:95],a[192:195],v[104:107],a[92:95]
C v125,v182,v183
M v[180:183],a[160:163],v[32:35],v[168:171]
E v184,v184
M v[180:183],a[164:167],v[36:39],v[180:183]
E v185,v185
M v[180:183],a[168:171],v[40:43],v[180:183]
R a[180:183],v199 offset:28672
M v[180:183],a[172:175],v[44:47],v[180:183]
E v186,v186
M a[96:99],a[192:195],v[112:115],a[96:99]
R a[184:187],v200 offset:28672
C v132,v184,v185
M a[96:99],a[196:199],v[116:119],a[96:99]
E v187,v187
M a[100:103],a[204:207],v[84:87],a[100:103]
R a[188:191],v201 offset:28672
M a[100:103],a[200:203],v[80:83],a[100:103]
C v133,v186,v187
M v[184:187],a[160:163],v[48:51],v[172:175]
E v180,v180
M v[184:187],a[164:167],v[52:55],v[184:187]
E v181,v181
M v[184:187],a[168:171],v[56:59],v[184:187]
T a[216:217],v209
M v[184:187],a[172:175],v[60:63],v[184:187]
E v182,v182
M a[104:107],a[200:203],v[88:91],a[104:107]
C v140,v180,v181
M a[104:107],a[204:207],v[92:95],a[104:107]
E v183,v183
M a[108:111],a[204:207],v[100:103],a[108:111]
T a[218:219],v209 offset:4096
M a[108:111],a[200:203],v[96:99],a[108:111]
C v141,v182,v183
M v[180:183],a[160:163],v[64:67],v[176:179]
E v184,v184
M v[180:183],a[164:167],v[68:71],v[180:183]
E v185,v185
M v[180:183],a[168:171],v[72:75],v[180:183]
T a[220:221],v209 offset:8192
M v[180:183],a[172:175],v[76:79],v[180:183]
E v186,v186
M a[112:115],a[200:203],v[104:107],a[112:115]
C v148,v184,v185
M a[112:115],a[204:207],v[108:111],a[112:115]
W lgkmcnt(3)
E v187,v187
M a[116:119],a[204:207],v[116:119],a[116:119]
T a[222:223],v209 offset:12288
M a[116:119],a[200:203],v[112:115],a[116:119]
C v149,v186,v187
v_smfmac_f32_16x16x64_bf16 a[240:243],a[244:247],v[112:119],v253
M v[184:187],a[176:179],v[0:3],v[160:163]
s_barrier
E v180,v180
M v[184:187],a[180:183],v[4:7],v[184:187]
E v181,v181
M v[184:187],a[184:187],v[8:11],v[184:187]
T a[192:193],v202 offset:16384
M v[184:187],a[188:191],v[12:15],v[184:187]
E v182,v182
M a[120:123],a[208:211],v[80:83],a[120:123]
T a[194:195],v202 offset:20480
C v156,v180,v181
M a[120:123],a[212:215],v[84:87],a[120:123]
E v183,v183
M a[124:127],a[212:215],v[92:95],a[124:127]
T a[196:197],v202 offset:24576
M a[124:127],a[208:211],v[88:91],a[124:127]
C v157,v182,v183
M v[180:183],a[176:179],v[16:19],v[164:167]
W lgkmcnt(3)
E v184,v184
M v[180:183],a[180:183],v[20:23],v[180:183]
E v185,v185
M v[180:183],a[184:187],v[24:27],v[180:183]
T a[198:199],v202 offset:28672
M v[180:183],a[188:191],v[28:31],v[180:183]
E v186,v186
M a[128:131],a[208:211],v[96:99],a[128:131]
C v126,v184,v185
M a[128:131],a[212:215],v[100:103],a[128:131]
E v187,v187
M a[132:135],a[212:215],v[108:111],a[132:135]
R a[160:163],v198 offset:32768
M a[132:135],a[208:211],v[104:107],a[132:135]
C v127,v186,v187
M v[184:187],a[176:179],v[32:35],v[168:171]
E v180,v180
M v[184:187],a[180:183],v[36:39],v[184:187]
E v181,v181
M v[184:187],a[184:187],v[40:43],v[184:187]
R a[164:167],v199 offset:32768
M v[184:187],a[188:191],v[44:47],v[184:187]
E v182,v182
M a[136:139],a[208:211],v[112:115],a[136:139]
)ASM"
R"ASM(
R a[168:171],v200 offset:32768
C v134,v180,v181
M a[136:139],a[212:215],v[116:119],a[136:139]
E v183,v183
M a[140:143],a[220:223],v[84:87],a[140:143]
R a[172:175],v201 offset:32768
M a[140:143],a[216:219],v[80:83],a[140:143]
C v135,v182,v183
M v[180:183],a[176:179],v[48:51],v[172:175]
E v184,v184
M v[180:183],a[180:183],v[52:55],v[180:183]
E v185,v185
M v[180:183],a[184:187],v[56:59],v[180:183]
T a[200:201],v203 offset:16384
M v[180:183],a[188:191],v[60:63],v[180:183]
E v186,v186
M a[144:147],a[216:219],v[88:91],a[144:147]
C v142,v184,v185
M a[144:147],a[220:223],v[92:95],a[144:147]
E v187,v187
M a[148:151],a[220:223],v[100:103],a[148:151]
T a[202:203],v203 offset:20480
M a[148:151],a[216:219],v[96:99],a[148:151]
C v143,v186,v187
v_smfmac_f32_16x16x64_bf16 a[232:235],a[244:247],v[96:103],v253
M v[184:187],a[176:179],v[64:67],v[176:179]
E v180,v180
M v[184:187],a[180:183],v[68:71],v[184:187]
E v181,v181
M v[184:187],a[184:187],v[72:75],v[184:187]
T a[204:205],v203 offset:24576
M v[184:187],a[188:191],v[76:79],v[184:187]
E v182,v182
M a[152:155],a[216:219],v[104:107],a[152:155]
C v150,v180,v181
M a[152:155],a[220:223],v[108:111],a[152:155]
W lgkmcnt(3)
E v183,v183
M a[156:159],a[220:223],v[116:119],a[156:159]
T a[206:207],v203 offset:28672
M a[156:159],a[216:219],v[112:115],a[156:159]
C v151,v182,v183
)ASM"
R"ASM(
LIT1a_%=:
M v[180:183],a[160:163],v[0:3],v[160:163]
E v184,v184
M v[180:183],a[164:167],v[4:7],v[180:183]
E v185,v185
M v[180:183],a[168:171],v[8:11],v[180:183]
T a[208:209],v204 offset:16384
M v[180:183],a[172:175],v[12:15],v[180:183]
E v186,v186
M a[0:3],a[192:195],v[120:123],a[0:3]
T a[210:211],v204 offset:20480
C v158,v184,v185
M a[0:3],a[196:199],v[124:127],a[0:3]
E v187,v187
M a[4:7],a[196:199],v[132:135],a[4:7]
T a[212:213],v204 offset:24576
M a[4:7],a[192:195],v[128:131],a[4:7]
C v159,v186,v187
M v[184:187],a[160:163],v[16:19],v[164:167]
W lgkmcnt(3)
E v180,v180
M v[184:187],a[164:167],v[20:23],v[184:187]
E v181,v181
M v[184:187],a[168:171],v[24:27],v[184:187]
T a[214:215],v204 offset:28672
M v[184:187],a[172:175],v[28:31],v[184:187]
E v182,v182
M a[8:11],a[192:195],v[136:139],a[8:11]
C v80,v180,v181
M a[8:11],a[196:199],v[140:143],a[8:11]
E v183,v183
M a[12:15],a[196:199],v[148:151],a[12:15]
R a[176:179],v198 offset:36864
M a[12:15],a[192:195],v[144:147],a[12:15]
C v81,v182,v183
M v[180:183],a[160:163],v[32:35],v[168:171]
E v184,v184
M v[180:183],a[164:167],v[36:39],v[180:183]
E v185,v185
M v[180:183],a[168:171],v[40:43],v[180:183]
R a[180:183],v199 offset:36864
M v[180:183],a[172:175],v[44:47],v[180:183]
E v186,v186
M a[16:19],a[192:195],v[152:155],a[16:19]
R a[184:187],v200 offset:36864
C v88,v184,v185
M a[16:19],a[196:199],v[156:159],a[16:19]
E v187,v187
M a[20:23],a[204:207],v[124:127],a[20:23]
R a[188:191],v201 offset:36864
M a[20:23],a[200:203],v[120:123],a[20:23]
C v89,v186,v187
v_smfmac_f32_16x16x64_bf16 a[224:227],a[244:247],v[120:127],v253
M v[184:187],a[160:163],v[48:51],v[172:175]
E v180,v180
M v[184:187],a[164:167],v[52:55],v[184:187]
E v181,v181
M v[184:187],a[168:171],v[56:59],v[184:187]
T a[216:217],v205 offset:16384
M v[184:187],a[172:175],v[60:63],v[184:187]
E v182,v182
M a[24:27],a[200:203],v[128:131],a[24:27]
C v96,v180,v181
M a[24:27],a[204:207],v[132:135],a[24:27]
E v183,v183
M a[28:31],a[204:207],v[140:143],a[28:31]
T a[218:219],v205 offset:20480
M a[28:31],a[200:203],v[136:139],a[28:31]
C v97,v182,v183
M v[180:183],a[160:163],v[64:67],v[176:179]
E v184,v184
M v[180:183],a[164:167],v[68:71],v[180:183]
E v185,v185
M v[180:183],a[168:171],v[72:75],v[180:183]
T a[220:221],v205 offset:24576
M v[180:183],a[172:175],v[76:79],v[180:183]
E v186,v186
M a[32:35],a[200:203],v[144:147],a[32:35]
C v104,v184,v185
M a[32:35],a[204:207],v[148:151],a[32:35]
W lgkmcnt(3)
E v187,v187
M a[36:39],a[204:207],v[156:159],a[36:39]
T a[222:223],v205 offset:28672
M a[36:39],a[200:203],v[152:155],a[36:39]
C v105,v186,v187
M v[184:187],a[176:179],v[0:3],v[160:163]
E v180,v180
M v[184:187],a[180:183],v[4:7],v[184:187]
E v181,v181
M v[184:187],a[184:187],v[8:11],v[184:187]
T a[192:193],v206 offset:16384
M v[184:187],a[188:191],v[12:15],v[184:187]
E v182,v182
M a[40:43],a[208:211],v[120:123],a[40:43]
T a[194:195],v206 offset:20480
C v112,v180,v181
M a[40:43],a[212:215],v[124:127],a[40:43]
E v183,v183
M a[44:47],a[212:215],v[132:135],a[44:47]
T a[196:197],v206 offset:24576
M a[44:47],a[208:211],v[128:131],a[44:47]
C v113,v182,v183
M v[180:183],a[176:179],v[16:19],v[164:167]
W lgkmcnt(3)
E v184,v184
M v[180:183],a[180:183],v[20:23],v[180:183]
E v185,v185
M v[180:183],a[184:187],v[24:27],v[180:183]
T a[198:199],v206 offset:28672
M v[180:183],a[188:191],v[28:31],v[180:183]
E v186,v186
M a[48:51],a[208:211],v[136:139],a[48:51]
C v82,v184,v185
M a[48:51],a[212:215],v[140:143],a[48:51]
E v187,v187
M a[52:55],a[212:215],v[148:151],a[52:55]
R a[160:163],v198 offset:40960
M a[52:55],a[208:211],v[144:147],a[52:55]
C v83,v186,v187
v_smfmac_f32_16x16x64_bf16 a[236:239],a[244:247],v[144:151],v253
M v[184:187],a[176:179],v[32:35],v[168:171]
E v180,v180
M v[184:187],a[180:183],v[36:39],v[184:187]
E v181,v181
M v[184:187],a[184:187],v[40:43],v[184:187]
R a[164:167],v199 offset:40960
M v[184:187],a[188:191],v[44:47],v[184:187]
E v182,v182
M a[56:59],a[208:211],v[152:155],a[56:59]
R a[168:171],v200 offset:40960
C v90,v180,v181
M a[56:59],a[212:215],v[156:159],a[56:59]
E v183,v183
M a[60:63],a[220:223],v[124:127],a[60:63]
R a[172:175],v201 offset:40960
M a[60:63],a[216:219],v[120:123],a[60:63]
C v91,v182,v183
M v[180:183],a[176:179],v[48:51],v[172:175]
E v184,v184
M v[180:183],a[180:183],v[52:55],v[180:183]
E v185,v185
M v[180:183],a[184:187],v[56:59],v[180:183]
T a[200:201],v207 offset:16384
M v[180:183],a[188:191],v[60:63],v[180:183]
E v186,v186
M a[64:67],a[216:219],v[128:131],a[64:67]
C v98,v184,v185
M a[64:67],a[220:223],v[132:135],a[64:67]
E v187,v187
M a[68:71],a[220:223],v[140:143],a[68:71]
T a[202:203],v207 offset:20480
M a[68:71],a[216:219],v[136:139],a[68:71]
C v99,v186,v187
M v[184:187],a[176:179],v[64:67],v[176:179]
E v180,v180
M v[184:187],a[180:183],v[68:71],v[184:187]
E v181,v181
M v[184:187],a[184:187],v[72:75],v[184:187]
T a[204:205],v207 offset:24576
M v[184:187],a[188:191],v[76:79],v[184:187]
E v182,v182
M a[72:75],a[216:219],v[144:147],a[72:75]
C v106,v180,v181
M a[72:75],a[220:223],v[148:151],a[72:75]
W lgkmcnt(3)
E v183,v183
M a[76:79],a[220:223],v[156:159],a[76:79]
T a[206:207],v207 offset:28672
M a[76:79],a[216:219],v[152:155],a[76:79]
C v107,v182,v183
)ASM"
R"ASM(
M v[180:183],a[160:163],v[0:3],v[160:163]
E v184,v184
M v[180:183],a[164:167],v[4:7],v[180:183]
E v185,v185
M v[180:183],a[168:171],v[8:11],v[180:183]
T a[208:209],v208 offset:16384
M v[180:183],a[172:175],v[12:15],v[180:183]
E v186,v186
M a[80:83],a[192:195],v[120:123],a[80:83]
T a[210:211],v208 offset:20480
C v114,v184,v185
M a[80:83],a[196:199],v[124:127],a[80:83]
E v187,v187
M a[84:87],a[196:199],v[132:135],a[84:87]
T a[212:213],v208 offset:24576
M a[84:87],a[192:195],v[128:131],a[84:87]
C v115,v186,v187
v_smfmac_f32_16x16x64_bf16 a[228:231],a[244:247],v[128:135],v253
M v[184:187],a[160:163],v[16:19],v[164:167]
W lgkmcnt(3)
E v180,v180
M v[184:187],a[164:167],v[20:23],v[184:187]
E v181,v181
M v[184:187],a[168:171],v[24:27],v[184:187]
T a[214:215],v208 offset:28672
M v[184:187],a[172:175],v[28:31],v[184:187]
E v182,v182
M a[88:91],a[192:195],v[136:139],a[88:91]
C v84,v180,v181
M a[88:91],a[196:199],v[140:143],a[88:91]
E v183,v183
M a[92:95],a[196:199],v[148:151],a[92:95]
R a[176:179],v198 offset:45056
M a[92:95],a[192:195],v[144:147],a[92:95]
C v85,v182,v183
M v[180:183],a[160:163],v[32:35],v[168:171]
E v184,v184
M v[180:183],a[164:167],v[36:39],v[180:183]
E v185,v185
M v[180:183],a[168:171],v[40:43],v[180:183]
R a[180:183],v199 offset:45056
M v[180:183],a[172:175],v[44:47],v[180:183]
E v186,v186
M a[96:99],a[192:195],v[152:155],a[96:99]
R a[184:187],v200 offset:45056
C v92,v184,v185
M a[96:99],a[196:199],v[156:159],a[96:99]
E v187,v187
M a[100:103],a[204:207],v[124:127],a[100:103]
R a[188:191],v201 offset:45056
M a[100:103],a[200:203],v[120:123],a[100:103]
C v93,v186,v187
M v[184:187],a[160:163],v[48:51],v[172:175]
E v180,v180
M v[184:187],a[164:167],v[52:55],v[184:187]
E v181,v181
M v[184:187],a[168:171],v[56:59],v[184:187]
T a[216:217],v209 offset:16384
M v[184:187],a[172:175],v[60:63],v[184:187]
E v182,v182
M a[104:107],a[200:203],v[128:131],a[104:107]
C v100,v180,v181
M a[104:107],a[204:207],v[132:135],a[104:107]
E v183,v183
M a[108:111],a[204:207],v[140:143],a[108:111]
T a[218:219],v209 offset:20480
M a[108:111],a[200:203],v[136:139],a[108:111]
C v101,v182,v183
M v[180:183],a[160:163],v[64:67],v[176:179]
E v184,v184
M v[180:183],a[164:167],v[68:71],v[180:183]
E v185,v185
M v[180:183],a[168:171],v[72:75],v[180:183]
T a[220:221],v209 offset:24576
M v[180:183],a[172:175],v[76:79],v[180:183]
E v186,v186
M a[112:115],a[200:203],v[144:147],a[112:115]
C v108,v184,v185
M a[112:115],a[204:207],v[148:151],a[112:115]
W lgkmcnt(3)
E v187,v187
M a[116:119],a[204:207],v[156:159],a[116:119]
T a[222:223],v209 offset:28672
M a[116:119],a[200:203],v[152:155],a[116:119]
C v109,v186,v187
v_smfmac_f32_16x16x64_bf16 a[240:243],a[244:247],v[152:159],v253
M v[184:187],a[176:179],v[0:3],v[160:163]
s_barrier
E v180,v180
M v[184:187],a[180:183],v[4:7],v[184:187]
E v181,v181
M v[184:187],a[184:187],v[8:11],v[184:187]
T a[192:193],v202 offset:32768
M v[184:187],a[188:191],v[12:15],v[184:187]
E v182,v182
M a[120:123],a[208:211],v[120:123],a[120:123]
T a[194:195],v202 offset:36864
C v116,v180,v181
M a[120:123],a[212:215],v[124:127],a[120:123]
E v183,v183
M a[124:127],a[212:215],v[132:135],a[124:127]
T a[196:197],v202 offset:40960
M a[124:127],a[208:211],v[128:131],a[124:127]
C v117,v182,v183
M v[180:183],a[176:179],v[16:19],v[164:167]
W lgkmcnt(3)
E v184,v184
M v[180:183],a[180:183],v[20:23],v[180:183]
E v185,v185
M v[180:183],a[184:187],v[24:27],v[180:183]
T a[198:199],v202 offset:45056
M v[180:183],a[188:191],v[28:31],v[180:183]
E v186,v186
M a[128:131],a[208:211],v[136:139],a[128:131]
C v86,v184,v185
M a[128:131],a[212:215],v[140:143],a[128:131]
E v187,v187
M a[132:135],a[212:215],v[148:151],a[132:135]
R a[160:163],v198 offset:49152
M a[132:135],a[208:211],v[144:147],a[132:135]
C v87,v186,v187
M v[184:187],a[176:179],v[32:35],v[168:171]
E v180,v180
M v[184:187],a[180:183],v[36:39],v[184:187]
E v181,v181
M v[184:187],a[184:187],v[40:43],v[184:187]
R a[164:167],v199 offset:49152
M v[184:187],a[188:191],v[44:47],v[184:187]
E v182,v182
M a[136:139],a[208:211],v[152:155],a[136:139]
R a[168:171],v200 offset:49152
C v94,v180,v181
M a[136:139],a[212:215],v[156:159],a[136:139]
E v183,v183
M a[140:143],a[220:223],v[124:127],a[140:143]
R a[172:175],v201 offset:49152
M a[140:143],a[216:219],v[120:123],a[140:143]
C v95,v182,v183
M v[180:183],a[176:179],v[48:51],v[172:175]
E v184,v184
M v[180:183],a[180:183],v[52:55],v[180:183]
E v185,v185
M v[180:183],a[184:187],v[56:59],v[180:183]
T a[200:201],v203 offset:32768
M v[180:183],a[188:191],v[60:63],v[180:183]
E v186,v186
M a[144:147],a[216:219],v[128:131],a[144:147]
C v102,v184,v185
M a[144:147],a[220:223],v[132:135],a[144:147]
E v187,v187
M a[148:151],a[220:223],v[140:143],a[148:151]
T a[202:203],v203 offset:36864
M a[148:151],a[216:219],v[136:139],a[148:151]
C v103,v186,v187
v_smfmac_f32_16x16x64_bf16 a[232:235],a[244:247],v[136:143],v253
M v[184:187],a[176:179],v[64:67],v[176:179]
E v180,v180
M v[184:187],a[180:183],v[68:71],v[184:187]
E v181,v181
M v[184:187],a[184:187],v[72:75],v[184:187]
T a[204:205],v203 offset:40960
M v[184:187],a[188:191],v[76:79],v[184:187]
E v182,v182
M a[152:155],a[216:219],v[144:147],a[152:155]
C v110,v180,v181
M a[152:155],a[220:223],v[148:151],a[152:155]
W lgkmcnt(3)
E v183,v183
M a[156:159],a[220:223],v[156:159],a[156:159]
T a[206:207],v203 offset:45056
M a[156:159],a[216:219],v[152:155],a[156:159]
C v111,v182,v183
)ASM"
R"ASM(
M v[180:183],a[160:163],v[0:3],v[160:163]
E v184,v184
M v[180:183],a[164:167],v[4:7],v[180:183]
E v185,v185
M v[180:183],a[168:171],v[8:11],v[180:183]
T a[208:209],v204 offset:32768
M v[180:183],a[172:175],v[12:15],v[180:183]
E v186,v186
M a[0:3],a[192:195],v[80:83],a[0:3]
T a[210:211],v204 offset:36864
C v118,v184,v185
M a[0:3],a[196:199],v[84:87],a[0:3]
E v187,v187
M a[4:7],a[196:199],v[92:95],a[4:7]
T a[212:213],v204 offset:40960
M a[4:7],a[192:195],v[88:91],a[4:7]
C v119,v186,v187
M v[184:187],a[160:163],v[16:19],v[164:167]
W lgkmcnt(3)
E v180,v180
M v[184:187],a[164:167],v[20:23],v[184:187]
E v181,v181
M v[184:187],a[168:171],v[24:27],v[184:187]
T a[214:215],v204 offset:45056
M v[184:187],a[172:175],v[28:31],v[184:187]
E v182,v182
M a[8:11],a[192:195],v[96:99],a[8:11]
C v120,v180,v181
M a[8:11],a[196:199],v[100:103],a[8:11]
E v183,v183
M a[12:15],a[196:199],v[108:111],a[12:15]
R a[176:179],v198 offset:53248
M a[12:15],a[192:195],v[104:107],a[12:15]
C v121,v182,v183
M v[180:183],a[160:163],v[32:35],v[168:171]
E v184,v184
M v[180:183],a[164:167],v[36:39],v[180:183]
E v185,v185
M v[180:183],a[168:171],v[40:43],v[180:183]
R a[180:183],v199 offset:53248
M v[180:183],a[172:175],v[44:47],v[180:183]
E v186,v186
M a[16:19],a[192:195],v[112:115],a[16:19]
R a[184:187],v200 offset:53248
C v128,v184,v185
M a[16:19],a[196:199],v[116:119],a[16:19]
E v187,v187
M a[20:23],a[204:207],v[84:87],a[20:23]
R a[188:191],v201 offset:53248
M a[20:23],a[200:203],v[80:83],a[20:23]
C v129,v186,v187
v_smfmac_f32_16x16x64_bf16 a[224:227],a[244:247],v[80:87],v253
M v[184:187],a[160:163],v[48:51],v[172:175]
E v180,v180
M v[184:187],a[164:167],v[52:55],v[184:187]
E v181,v181
M v[184:187],a[168:171],v[56:59],v[184:187]
T a[216:217],v205 offset:32768
M v[184:187],a[172:175],v[60:63],v[184:187]
E v182,v182
M a[24:27],a[200:203],v[88:91],a[24:27]
C v136,v180,v181
M a[24:27],a[204:207],v[92:95],a[24:27]
E v183,v183
M a[28:31],a[204:207],v[100:103],a[28:31]
T a[218:219],v205 offset:36864
M a[28:31],a[200:203],v[96:99],a[28:31]
C v137,v182,v183
M v[180:183],a[160:163],v[64:67],v[176:179]
E v184,v184
M v[180:183],a[164:167],v[68:71],v[180:183]
E v185,v185
M v[180:183],a[168:171],v[72:75],v[180:183]
T a[220:221],v205 offset:40960
M v[180:183],a[172:175],v[76:79],v[180:183]
E v186,v186
M a[32:35],a[200:203],v[104:107],a[32:35]
C v144,v184,v185
M a[32:35],a[204:207],v[108:111],a[32:35]
W lgkmcnt(3)
E v187,v187
M a[36:39],a[204:207],v[116:119],a[36:39]
T a[222:223],v205 offset:45056
M a[36:39],a[200:203],v[112:115],a[36:39]
C v145,v186,v187
M v[184:187],a[176:179],v[0:3],v[160:163]
E v180,v180
M v[184:187],a[180:183],v[4:7],v[184:187]
E v181,v181
M v[184:187],a[184:187],v[8:11],v[184:187]
T a[192:193],v206 offset:32768
M v[184:187],a[188:191],v[12:15],v[184:187]
E v182,v182
M a[40:43],a[208:211],v[80:83],a[40:43]
T a[194:195],v206 offset:36864
C v152,v180,v181
M a[40:43],a[212:215],v[84:87],a[40:43]
E v183,v183
M a[44:47],a[212:215],v[92:95],a[44:47]
T a[196:197],v206 offset:40960
M a[44:47],a[208:211],v[88:91],a[44:47]
C v153,v182,v183
M v[180:183],a[176:179],v[16:19],v[164:167]
W lgkmcnt(3)
E v184,v184
M v[180:183],a[180:183],v[20:23],v[180:183]
E v185,v185
M v[180:183],a[184:187],v[24:27],v[180:183]
T a[198:199],v206 offset:45056
M v[180:183],a[188:191],v[28:31],v[180:183]
E v186,v186
M a[48:51],a[208:211],v[96:99],a[48:51]
C v122,v184,v185
M a[48:51],a[212:215],v[100:103],a[48:51]
E v187,v187
M a[52:55],a[212:215],v[108:111],a[52:55]
R a[160:163],v198 offset:57344
M a[52:55],a[208:211],v[104:107],a[52:55]
C v123,v186,v187
v_smfmac_f32_16x16x64_bf16 a[236:239],a[244:247],v[104:111],v253
M v[184:187],a[176:179],v[32:35],v[168:171]
E v180,v180
M v[184:187],a[180:183],v[36:39],v[184:187]
E v181,v181
M v[184:187],a[184:187],v[40:43],v[184:187]
R a[164:167],v199 offset:57344
M v[184:187],a[188:191],v[44:47],v[184:187]
E v182,v182
M a[56:59],a[208:211],v[112:115],a[56:59]
R a[168:171],v200 offset:57344
C v130,v180,v181
M a[56:59],a[212:215],v[116:119],a[56:59]
E v183,v183
M a[60:63],a[220:223],v[84:87],a[60:63]
R a[172:175],v201 offset:57344
M a[60:63],a[216:219],v[80:83],a[60:63]
C v131,v182,v183
M v[180:183],a[176:179],v[48:51],v[172:175]
E v184,v184
M v[180:183],a[180:183],v[52:55],v[180:183]
E v185,v185
M v[180:183],a[184:187],v[56:59],v[180:183]
T a[200:201],v207 offset:32768
M v[180:183],a[188:191],v[60:63],v[180:183]
E v186,v186
M a[64:67],a[216:219],v[88:91],a[64:67]
C v138,v184,v185
M a[64:67],a[220:223],v[92:95],a[64:67]
E v187,v187
M a[68:71],a[220:223],v[100:103],a[68:71]
T a[202:203],v207 offset:36864
M a[68:71],a[216:219],v[96:99],a[68:71]
C v139,v186,v187
M v[184:187],a[176:179],v[64:67],v[176:179]
E v180,v180
M v[184:187],a[180:183],v[68:71],v[184:187]
E v181,v181
M v[184:187],a[184:187],v[72:75],v[184:187]
T a[204:205],v207 offset:40960
M v[184:187],a[188:191],v[76:79],v[184:187]
E v182,v182
M a[72:75],a[216:219],v[104:107],a[72:75]
C v146,v180,v181
M a[72:75],a[220:223],v[108:111],a[72:75]
W lgkmcnt(3)
E v183,v183
M a[76:79],a[220:223],v[116:119],a[76:79]
T a[206:207],v207 offset:45056
M a[76:79],a[216:219],v[112:115],a[76:79]
C v147,v182,v183
M v[180:183],a[160:163],v[0:3],v[160:163]
E v184,v184
M v[180:183],a[164:167],v[4:7],v[180:183]
E v185,v185
M v[180:183],a[168:171],v[8:11],v[180:183]
T a[208:209],v208 offset:32768
M v[180:183],a[172:175],v[12:15],v[180:183]
E v186,v186
M a[80:83],a[192:195],v[80:83],a[80:83]
T a[210:211],v208 offset:36864
C v154,v184,v185
M a[80:83],a[196:199],v[84:87],a[80:83]
E v187,v187
M a[84:87],a[196:199],v[92:95],a[84:87]
T a[212:213],v208 offset:40960
M a[84:87],a[192:195],v[88:91],a[84:87]
C v155,v186,v187
v_smfmac_f32_16x16x64_bf16 a[228:231],a[244:247],v[88:95],v253
M v[184:187],a[160:163],v[16:19],v[164:167]
W lgkmcnt(3)
E v180,v180
M v[184:187],a[164:167],v[20:23],v[184:187]
E v181,v181
M v[184:187],a[168:171],v[24:27],v[184:187]
T a[214:215],v208 offset:45056
M v[184:187],a[172:175],v[28:31],v[184:187]
E v182,v182
M a[88:91],a[192:195],v[96:99],a[88:91]
C v124,v180,v181
M a[88:91],a[196:199],v[100:103],a[88:91]
E v183,v183
M a[92:95],a[196:199],v[108:111],a[92:95]
R a[176:179],v198 offset:61440
M a[92:95],a[192:195],v[104:107],a[92:95]
C v125,v182,v183
M v[180:183],a[160:163],v[32:35],v[168:171]
E v184,v184
M v[180:183],a[164:167],v[36:39],v[180:183]
E v185,v185
M v[180:183],a[168:171],v[40:43],v[180:183]
R a[180:183],v199 offset:61440
M v[180:183],a[172:175],v[44:47],v[180:183]
E v186,v186
M a[96:99],a[192:195],v[112:115],a[96:99]
R a[184:187],v200 offset:61440
C v132,v184,v185
M a[96:99],a[196:199],v[116:119],a[96:99]
E v187,v187
M a[100:103],a[204:207],v[84:87],a[100:103]
R a[188:191],v201 offset:61440
M a[100:103],a[200:203],v[80:83],a[100:103]
C v133,v186,v187
M v[184:187],a[160:163],v[48:51],v[172:175]
E v180,v180
M v[184:187],a[164:167],v[52:55],v[184:187]
E v181,v181
M v[184:187],a[168:171],v[56:59],v[184:187]
T a[216:217],v209 offset:32768
M v[184:187],a[172:175],v[60:63],v[184:187]
E v182,v182
M a[104:107],a[200:203],v[88:91],a[104:107]
C v140,v180,v181
M a[104:107],a[204:207],v[92:95],a[104:107]
E v183,v183
M a[108:111],a[204:207],v[100:103],a[108:111]
T a[218:219],v209 offset:36864
M a[108:111],a[200:203],v[96:99],a[108:111]
C v141,v182,v183
M v[180:183],a[160:163],v[64:67],v[176:179]
E v184,v184
M v[180:183],a[164:167],v[68:71],v[180:183]
E v185,v185
M v[180:183],a[168:171],v[72:75],v[180:183]
T a[220:221],v209 offset:40960
M v[180:183],a[172:175],v[76:79],v[180:183]
E v186,v186
M a[112:115],a[200:203],v[104:107],a[112:115]
C v148,v184,v185
M a[112:115],a[204:207],v[108:111],a[112:115]
W lgkmcnt(3)
E v187,v187
M a[116:119],a[204:207],v[116:119],a[116:119]
T a[222:223],v209 offset:45056
M a[116:119],a[200:203],v[112:115],a[116:119]
C v149,v186,v187
v_smfmac_f32_16x16x64_bf16 a[240:243],a[244:247],v[112:119],v253
M v[184:187],a[176:179],v[0:3],v[160:163]
s_barrier
E v180,v180
M v[184:187],a[180:183],v[4:7],v[184:187]
E v181,v181
M v[184:187],a[184:187],v[8:11],v[184:187]
T a[192:193],v202 offset:49152
M v[184:187],a[188:191],v[12:15],v[184:187]
E v182,v182
M a[120:123],a[208:211],v[80:83],a[120:123]
T a[194:195],v202 offset:53248
C v156,v180,v181
M a[120:123],a[212:215],v[84:87],a[120:123]
E v183,v183
M a[124:127],a[212:215],v[92:95],a[124:127]
T a[196:197],v202 offset:57344
M a[124:127],a[208:211],v[88:91],a[124:127]
C v157,v182,v183
M v[180:183],a[176:179],v[16:19],v[164:167]
W lgkmcnt(3)
E v184,v184
M v[180:183],a[180:183],v[20:23],v[180:183]
E v185,v185
M v[180:183],a[184:187],v[24:27],v[180:183]
T a[198:199],v202 offset:61440
M v[180:183],a[188:191],v[28:31],v[180:183]
E v186,v186
M a[128:131],a[208:211],v[96:99],a[128:131]
C v126,v184,v185
M a[128:131],a[212:215],v[100:103],a[128:131]
E v187,v187
M a[132:135],a[212:215],v[108:111],a[132:135]
R a[160:163],v198
M a[132:135],a[208:211],v[104:107],a[132:135]
C v127,v186,v187
M v[184:187],a[176:179],v[32:35],v[168:171]
E v180,v180
M v[184:187],a[180:183],v[36:39],v[184:187]
E v181,v181
M v[184:187],a[184:187],v[40:43],v[184:187]
R a[164:167],v199
M v[184:187],a[188:191],v[44:47],v[184:187]
E v182,v182
M a[136:139],a[208:211],v[112:115],a[136:139]
)ASM"
R"ASM(
R a[168:171],v200
C v134,v180,v181
M a[136:139],a[212:215],v[116:119],a[136:139]
E v183,v183
M a[140:143],a[220:223],v[84:87],a[140:143]
R a[172:175],v201
M a[140:143],a[216:219],v[80:83],a[140:143]
C v135,v182,v183
M v[180:183],a[176:179],v[48:51],v[172:175]
E v184,v184
M v[180:183],a[180:183],v[52:55],v[180:183]
E v185,v185
M v[180:183],a[184:187],v[56:59],v[180:183]
T a[200:201],v203 offset:49152
M v[180:183],a[188:191],v[60:63],v[180:183]
E v186,v186
M a[144:147],a[216:219],v[88:91],a[144:147]
C v142,v184,v185
M a[144:147],a[220:223],v[92:95],a[144:147]
E v187,v187
M a[148:151],a[220:223],v[100:103],a[148:151]
T a[202:203],v203 offset:53248
M a[148:151],a[216:219],v[96:99],a[148:151]
C v143,v186,v187
v_smfmac_f32_16x16x64_bf16 a[232:235],a[244:247],v[96:103],v253
M v[184:187],a[176:179],v[64:67],v[176:179]
E v180,v180
M v[184:187],a[180:183],v[68:71],v[184:187]
E v181,v181
M v[184:187],a[184:187],v[72:75],v[184:187]
T a[204:205],v203 offset:57344
M v[184:187],a[188:191],v[76:79],v[184:187]
E v182,v182
M a[152:155],a[216:219],v[104:107],a[152:155]
C v150,v180,v181
M a[152:155],a[220:223],v[108:111],a[152:155]
W lgkmcnt(3)
E v183,v183
M a[156:159],a[220:223],v[116:119],a[156:159]
T a[206:207],v203 offset:61440
M a[156:159],a[216:219],v[112:115],a[156:159]
s_cmp_eq_u32 s69,3
C v151,v182,v183
s_cbranch_scc1 LEXITa_%=
)ASM"
R"ASM(
M v[180:183],a[160:163],v[0:3],v[160:163]
E v184,v184
M v[180:183],a[164:167],v[4:7],v[180:183]
E v185,v185
M v[180:183],a[168:171],v[8:11],v[180:183]
T a[208:209],v204 offset:49152
M v[180:183],a[172:175],v[12:15],v[180:183]
E v186,v186
M a[0:3],a[192:195],v[120:123],a[0:3]
T a[210:211],v204 offset:53248
C v158,v184,v185
M a[0:3],a[196:199],v[124:127],a[0:3]
E v187,v187
M a[4:7],a[196:199],v[132:135],a[4:7]
T a[212:213],v204 offset:57344
M a[4:7],a[192:195],v[128:131],a[4:7]
C v159,v186,v187
M v[184:187],a[160:163],v[16:19],v[164:167]
W lgkmcnt(3)
E v180,v180
M v[184:187],a[164:167],v[20:23],v[184:187]
E v181,v181
M v[184:187],a[168:171],v[24:27],v[184:187]
T a[214:215],v204 offset:61440
M v[184:187],a[172:175],v[28:31],v[184:187]
E v182,v182
M a[8:11],a[192:195],v[136:139],a[8:11]
C v80,v180,v181
M a[8:11],a[196:199],v[140:143],a[8:11]
E v183,v183
M a[12:15],a[196:199],v[148:151],a[12:15]
R a[176:179],v198 offset:4096
M a[12:15],a[192:195],v[144:147],a[12:15]
C v81,v182,v183
M v[180:183],a[160:163],v[32:35],v[168:171]
E v184,v184
M v[180:183],a[164:167],v[36:39],v[180:183]
E v185,v185
M v[180:183],a[168:171],v[40:43],v[180:183]
R a[180:183],v199 offset:4096
M v[180:183],a[172:175],v[44:47],v[180:183]
E v186,v186
M a[16:19],a[192:195],v[152:155],a[16:19]
R a[184:187],v200 offset:4096
C v88,v184,v185
M a[16:19],a[196:199],v[156:159],a[16:19]
E v187,v187
M a[20:23],a[204:207],v[124:127],a[20:23]
R a[188:191],v201 offset:4096
M a[20:23],a[200:203],v[120:123],a[20:23]
C v89,v186,v187
v_smfmac_f32_16x16x64_bf16 a[224:227],a[244:247],v[120:127],v253
M v[184:187],a[160:163],v[48:51],v[172:175]
E v180,v180
M v[184:187],a[164:167],v[52:55],v[184:187]
E v181,v181
M v[184:187],a[168:171],v[56:59],v[184:187]
T a[216:217],v205 offset:49152
M v[184:187],a[172:175],v[60:63],v[184:187]
E v182,v182
M a[24:27],a[200:203],v[128:131],a[24:27]
C v96,v180,v181
M a[24:27],a[204:207],v[132:135],a[24:27]
E v183,v183
M a[28:31],a[204:207],v[140:143],a[28:31]
T a[218:219],v205 offset:53248
M a[28:31],a[200:203],v[136:139],a[28:31]
C v97,v182,v183
M v[180:183],a[160:163],v[64:67],v[176:179]
E v184,v184
M v[180:183],a[164:167],v[68:71],v[180:183]
E v185,v185
M v[180:183],a[168:171],v[72:75],v[180:183]
T a[220:221],v205 offset:57344
M v[180:183],a[172:175],v[76:79],v[180:183]
E v186,v186
M a[32:35],a[200:203],v[144:147],a[32:35]
C v104,v184,v185
M a[32:35],a[204:207],v[148:151],a[32:35]
W lgkmcnt(3)
E v187,v187
M a[36:39],a[204:207],v[156:159],a[36:39]
T a[222:223],v205 offset:61440
M a[36:39],a[200:203],v[152:155],a[36:39]
C v105,v186,v187
M v[184:187],a[176:179],v[0:3],v[160:163]
E v180,v180
M v[184:187],a[180:183],v[4:7],v[184:187]
E v181,v181
M v[184:187],a[184:187],v[8:11],v[184:187]
T a[192:193],v206 offset:49152
M v[184:187],a[188:191],v[12:15],v[184:187]
E v182,v182
M a[40:43],a[208:211],v[120:123],a[40:43]
T a[194:195],v206 offset:53248
C v112,v180,v181
M a[40:43],a[212:215],v[124:127],a[40:43]
E v183,v183
M a[44:47],a[212:215],v[132:135],a[44:47]
T a[196:197],v206 offset:57344
M a[44:47],a[208:211],v[128:131],a[44:47]
C v113,v182,v183
M v[180:183],a[176:179],v[16:19],v[164:167]
W lgkmcnt(3)
E v184,v184
M v[180:183],a[180:183],v[20:23],v[180:183]
E v185,v185
M v[180:183],a[184:187],v[24:27],v[180:183]
T a[198:199],v206 offset:61440
M v[180:183],a[188:191],v[28:31],v[180:183]
E v186,v186
M a[48:51],a[208:211],v[136:139],a[48:51]
C v82,v184,v185
M a[48:51],a[212:215],v[140:143],a[48:51]
E v187,v187
M a[52:55],a[212:215],v[148:151],a[52:55]
R a[160:163],v198 offset:8192
M a[52:55],a[208:211],v[144:147],a[52:55]
C v83,v186,v187
v_smfmac_f32_16x16x64_bf16 a[236:239],a[244:247],v[144:151],v253
M v[184:187],a[176:179],v[32:35],v[168:171]
E v180,v180
M v[184:187],a[180:183],v[36:39],v[184:187]
E v181,v181
M v[184:187],a[184:187],v[40:43],v[184:187]
R a[164:167],v199 offset:8192
M v[184:187],a[188:191],v[44:47],v[184:187]
E v182,v182
M a[56:59],a[208:211],v[152:155],a[56:59]
R a[168:171],v200 offset:8192
C v90,v180,v181
M a[56:59],a[212:215],v[156:159],a[56:59]
E v183,v183
M a[60:63],a[220:223],v[124:127],a[60:63]
R a[172:175],v201 offset:8192
M a[60:63],a[216:219],v[120:123],a[60:63]
C v91,v182,v183
M v[180:183],a[176:179],v[48:51],v[172:175]
E v184,v184
M v[180:183],a[180:183],v[52:55],v[180:183]
E v185,v185
M v[180:183],a[184:187],v[56:59],v[180:183]
T a[200:201],v207 offset:49152
M v[180:183],a[188:191],v[60:63],v[180:183]
E v186,v186
M a[64:67],a[216:219],v[128:131],a[64:67]
C v98,v184,v185
M a[64:67],a[220:223],v[132:135],a[64:67]
E v187,v187
M a[68:71],a[220:223],v[140:143],a[68:71]
T a[202:203],v207 offset:53248
M a[68:71],a[216:219],v[136:139],a[68:71]
C v99,v186,v187
M v[184:187],a[176:179],v[64:67],v[176:179]
E v180,v180
M v[184:187],a[180:183],v[68:71],v[184:187]
E v181,v181
M v[184:187],a[184:187],v[72:75],v[184:187]
T a[204:205],v207 offset:57344
M v[184:187],a[188:191],v[76:79],v[184:187]
E v182,v182
M a[72:75],a[216:219],v[144:147],a[72:75]
C v106,v180,v181
M a[72:75],a[220:223],v[148:151],a[72:75]
W lgkmcnt(3)
E v183,v183
M a[76:79],a[220:223],v[156:159],a[76:79]
T a[206:207],v207 offset:61440
M a[76:79],a[216:219],v[152:155],a[76:79]
C v107,v182,v183
M v[180:183],a[160:163],v[0:3],v[160:163]
E v184,v184
M v[180:183],a[164:167],v[4:7],v[180:183]
E v185,v185
M v[180:183],a[168:171],v[8:11],v[180:183]
T a[208:209],v208 offset:49152
M v[180:183],a[172:175],v[12:15],v[180:183]
E v186,v186
M a[80:83],a[192:195],v[120:123],a[80:83]
T a[210:211],v208 offset:53248
C v114,v184,v185
M a[80:83],a[196:199],v[124:127],a[80:83]
E v187,v187
M a[84:87],a[196:199],v[132:135],a[84:87]
T a[212:213],v208 offset:57344
M a[84:87],a[192:195],v[128:131],a[84:87]
C v115,v186,v187
v_smfmac_f32_16x16x64_bf16 a[228:231],a[244:247],v[128:135],v253
M v[184:187],a[160:163],v[16:19],v[164:167]
W lgkmcnt(3)
E v180,v180
M v[184:187],a[164:167],v[20:23],v[184:187]
E v181,v181
M v[184:187],a[168:171],v[24:27],v[184:187]
T a[214:215],v208 offset:61440
M v[184:187],a[172:175],v[28:31],v[184:187]
E v182,v182
M a[88:91],a[192:195],v[136:139],a[88:91]
C v84,v180,v181
M a[88:91],a[196:199],v[140:143],a[88:91]
E v183,v183
M a[92:95],a[196:199],v[148:151],a[92:95]
R a[176:179],v198 offset:12288
M a[92:95],a[192:195],v[144:147],a[92:95]
C v85,v182,v183
M v[180:183],a[160:163],v[32:35],v[168:171]
E v184,v184
M v[180:183],a[164:167],v[36:39],v[180:183]
E v185,v185
M v[180:183],a[168:171],v[40:43],v[180:183]
R a[180:183],v199 offset:12288
M v[180:183],a[172:175],v[44:47],v[180:183]
E v186,v186
M a[96:99],a[192:195],v[152:155],a[96:99]
R a[184:187],v200 offset:12288
C v92,v184,v185
M a[96:99],a[196:199],v[156:159],a[96:99]
E v187,v187
M a[100:103],a[204:207],v[124:127],a[100:103]
R a[188:191],v201 offset:12288
M a[100:103],a[200:203],v[120:123],a[100:103]
C v93,v186,v187
M v[184:187],a[160:163],v[48:51],v[172:175]
E v180,v180
M v[184:187],a[164:167],v[52:55],v[184:187]
E v181,v181
M v[184:187],a[168:171],v[56:59],v[184:187]
T a[216:217],v209 offset:49152
M v[184:187],a[172:175],v[60:63],v[184:187]
E v182,v182
M a[104:107],a[200:203],v[128:131],a[104:107]
C v100,v180,v181
M a[104:107],a[204:207],v[132:135],a[104:107]
E v183,v183
M a[108:111],a[204:207],v[140:143],a[108:111]
T a[218:219],v209 offset:53248
M a[108:111],a[200:203],v[136:139],a[108:111]
C v101,v182,v183
M v[180:183],a[160:163],v[64:67],v[176:179]
E v184,v184
M v[180:183],a[164:167],v[68:71],v[180:183]
E v185,v185
M v[180:183],a[168:171],v[72:75],v[180:183]
T a[220:221],v209 offset:57344
M v[180:183],a[172:175],v[76:79],v[180:183]
E v186,v186
M a[112:115],a[200:203],v[144:147],a[112:115]
C v108,v184,v185
M a[112:115],a[204:207],v[148:151],a[112:115]
W lgkmcnt(3)
E v187,v187
M a[116:119],a[204:207],v[156:159],a[116:119]
T a[222:223],v209 offset:61440
M a[116:119],a[200:203],v[152:155],a[116:119]
C v109,v186,v187
v_smfmac_f32_16x16x64_bf16 a[240:243],a[244:247],v[152:159],v253
M v[184:187],a[176:179],v[0:3],v[160:163]
s_barrier
E v180,v180
M v[184:187],a[180:183],v[4:7],v[184:187]
E v181,v181
M v[184:187],a[184:187],v[8:11],v[184:187]
T a[192:193],v202
M v[184:187],a[188:191],v[12:15],v[184:187]
E v182,v182
M a[120:123],a[208:211],v[120:123],a[120:123]
T a[194:195],v202 offset:4096
C v116,v180,v181
M a[120:123],a[212:215],v[124:127],a[120:123]
E v183,v183
M a[124:127],a[212:215],v[132:135],a[124:127]
T a[196:197],v202 offset:8192
M a[124:127],a[208:211],v[128:131],a[124:127]
C v117,v182,v183
M v[180:183],a[176:179],v[16:19],v[164:167]
W lgkmcnt(3)
E v184,v184
M v[180:183],a[180:183],v[20:23],v[180:183]
E v185,v185
M v[180:183],a[184:187],v[24:27],v[180:183]
T a[198:199],v202 offset:12288
M v[180:183],a[188:191],v[28:31],v[180:183]
E v186,v186
M a[128:131],a[208:211],v[136:139],a[128:131]
C v86,v184,v185
M a[128:131],a[212:215],v[140:143],a[128:131]
E v187,v187
M a[132:135],a[212:215],v[148:151],a[132:135]
R a[160:163],v198 offset:16384
M a[132:135],a[208:211],v[144:147],a[132:135]
C v87,v186,v187
M v[184:187],a[176:179],v[32:35],v[168:171]
E v180,v180
M v[184:187],a[180:183],v[36:39],v[184:187]
E v181,v181
M v[184:187],a[184:187],v[40:43],v[184:187]
R a[164:167],v199 offset:16384
M v[184:187],a[188:191],v[44:47],v[184:187]
E v182,v182
M a[136:139],a[208:211],v[152:155],a[136:139]
)ASM"
R"ASM(
R a[168:171],v200 offset:16384
C v94,v180,v181
M a[136:139],a[212:215],v[156:159],a[136:139]
E v183,v183
M a[140:143],a[220:223],v[124:127],a[140:143]
R a[172:175],v201 offset:16384
M a[140:143],a[216:219],v[120:123],a[140:143]
C v95,v182,v183
M v[180:183],a[176:179],v[48:51],v[172:175]
E v184,v184
M v[180:183],a[180:183],v[52:55],v[180:183]
E v185,v185
M v[180:183],a[184:187],v[56:59],v[180:183]
T a[200:201],v203
M v[180:183],a[188:191],v[60:63],v[180:183]
E v186,v186
M a[144:147],a[216:219],v[128:131],a[144:147]
C v102,v184,v185
M a[144:147],a[220:223],v[132:135],a[144:147]
E v187,v187
M a[148:151],a[220:223],v[140:143],a[148:151]
T a[202:203],v203 offset:4096
M a[148:151],a[216:219],v[136:139],a[148:151]
C v103,v186,v187
v_smfmac_f32_16x16x64_bf16 a[232:235],a[244:247],v[136:143],v253
M v[184:187],a[176:179],v[64:67],v[176:179]
E v180,v180
M v[184:187],a[180:183],v[68:71],v[184:187]
E v181,v181
M v[184:187],a[184:187],v[72:75],v[184:187]
T a[204:205],v203 offset:8192
M v[184:187],a[188:191],v[76:79],v[184:187]
E v182,v182
M a[152:155],a[216:219],v[144:147],a[152:155]
C v110,v180,v181
M a[152:155],a[220:223],v[148:151],a[152:155]
W lgkmcnt(3)
E v183,v183
M a[156:159],a[220:223],v[156:159],a[156:159]
T a[206:207],v203 offset:12288
M a[156:159],a[216:219],v[152:155],a[156:159]
s_add_u32 s69,s69,1
C v111,v182,v183
s_branch LLOOPa_%=
)ASM"
R"ASM(
LEXITa_%=:
s_cmp_eq_u32 s70,1
s_cbranch_scc1 LDRAIN1a_%=
)ASM"
R"ASM(
LDRAIN0a_%=:
v_smfmac_f32_16x16x64_bf16 a[224:227],a[244:247],v[120:127],v253
E v184,v184
v_smfmac_f32_16x16x64_bf16 a[228:231],a[244:247],v[128:135],v253
E v185,v185
v_smfmac_f32_16x16x64_bf16 a[232:235],a[244:247],v[136:143],v253
E v186,v186
v_smfmac_f32_16x16x64_bf16 a[236:239],a[244:247],v[144:151],v253
E v187,v187
s_lshl_b32 s94,s87,11
M a[0:3],a[192:195],v[120:123],a[0:3]
C v158,v184,v185
C v159,v186,v187
M a[0:3],a[196:199],v[124:127],a[0:3]
T a[208:209],v204 offset:49152
M a[4:7],a[196:199],v[132:135],a[4:7]
AR v188,a224
AR v189,a225
v_smfmac_f32_16x16x64_bf16 a[240:243],a[244:247],v[152:159],v253
M a[4:7],a[192:195],v[128:131],a[4:7]
T a[210:211],v204 offset:53248
AR v190,a228
AR v191,a229
M a[8:11],a[192:195],v[136:139],a[8:11]
AR v192,a232
AR v193,a233
M a[8:11],a[196:199],v[140:143],a[8:11]
s_add_u32 m0,s94,49152
s_add_u32 s27,s26,s66
s_add_u32 s68,s27,65536
T a[212:213],v204 offset:57344
AR v194,a236
AR v195,a237
M a[12:15],a[196:199],v[148:151],a[12:15]
B v212,s[48:51],s68 offen lds
M a[12:15],a[192:195],v[144:147],a[12:15]
T a[214:215],v204 offset:61440
AR v196,a240
AR v197,a241
M a[16:19],a[192:195],v[152:155],a[16:19]
B v213,s[48:51],s68 offen offset:1024 lds
F v188,v188,v189
M a[16:19],a[196:199],v[156:159],a[16:19]
W lgkmcnt(4)
W vmcnt(2)
T a[216:217],v205 offset:49152
F v190,v190,v191
M a[20:23],a[204:207],v[124:127],a[20:23]
R a[224:227],v210
F v192,v192,v193
M a[20:23],a[200:203],v[120:123],a[20:23]
T a[218:219],v205 offset:53248
F v194,v194,v195
M a[24:27],a[200:203],v[128:131],a[24:27]
R a[228:231],v211
F v196,v196,v197
M a[24:27],a[204:207],v[132:135],a[24:27]
T a[220:221],v205 offset:57344
v_mov_b32 v189,v188
M a[28:31],a[204:207],v[140:143],a[28:31]
R a[232:235],v210 offset:2048
v_mov_b32 v191,v190
M a[28:31],a[200:203],v[136:139],a[28:31]
T a[222:223],v205 offset:61440
v_mov_b32 v193,v192
M a[32:35],a[200:203],v[144:147],a[32:35]
R a[236:239],v211 offset:2048
v_mov_b32 v195,v194
M a[32:35],a[204:207],v[148:151],a[32:35]
v_mov_b32 v197,v196
v_permlane32_swap_b32 v188,v189
M a[36:39],a[204:207],v[156:159],a[36:39]
R a[240:243],v210 offset:4096
v_permlane32_swap_b32 v190,v191
M a[36:39],a[200:203],v[152:155],a[36:39]
W lgkmcnt(9)
v_permlane32_swap_b32 v192,v193
v_permlane32_swap_b32 v194,v195
M a[40:43],a[208:211],v[120:123],a[40:43]
R a[244:247],v211 offset:4096
v_permlane32_swap_b32 v196,v197
M a[40:43],a[212:215],v[124:127],a[40:43]
T a[192:193],v206 offset:49152
F v188,v188,v189
M a[44:47],a[212:215],v[132:135],a[44:47]
R a[248:251],v210 offset:6144
F v190,v190,v191
M a[44:47],a[208:211],v[128:131],a[44:47]
T a[194:195],v206 offset:53248
F v192,v192,v193
M a[48:51],a[208:211],v[136:139],a[48:51]
R a[252:255],v211 offset:6144
F v194,v194,v195
M a[48:51],a[212:215],v[140:143],a[48:51]
T a[196:197],v206 offset:57344
F v196,v196,v197
M a[52:55],a[212:215],v[148:151],a[52:55]
v_mov_b32 v189,v188
v_mov_b32 v191,v190
M a[52:55],a[208:211],v[144:147],a[52:55]
T a[198:199],v206 offset:61440
v_mov_b32 v193,v192
M a[56:59],a[208:211],v[152:155],a[56:59]
v_mov_b32 v195,v194
v_mov_b32 v197,v196
M a[56:59],a[212:215],v[156:159],a[56:59]
W lgkmcnt(9)
T a[200:201],v207 offset:49152
v_permlane16_swap_b32 v188,v189
M a[60:63],a[220:223],v[124:127],a[60:63]
v_permlane16_swap_b32 v190,v191
v_permlane16_swap_b32 v192,v193
M a[60:63],a[216:219],v[120:123],a[60:63]
W lgkmcnt(3)
T a[202:203],v207 offset:53248
v_permlane16_swap_b32 v194,v195
M a[64:67],a[216:219],v[128:131],a[64:67]
v_permlane16_swap_b32 v196,v197
F v188,v188,v189
M a[64:67],a[220:223],v[132:135],a[64:67]
s_add_u32 m0,s67,2048
s_add_u32 s27,s26,128
s_and_b32 s27,s27,0x380
s_add_u32 s27,s27,s66
s_add_u32 s68,s27,0
T a[204:205],v207 offset:57344
F v190,v190,v191
M a[68:71],a[220:223],v[140:143],a[68:71]
B v212,s[48:51],s68 offen lds
F v192,v192,v193
M a[68:71],a[216:219],v[136:139],a[68:71]
T a[206:207],v207 offset:61440
F v194,v194,v195
M a[72:75],a[216:219],v[144:147],a[72:75]
B v213,s[48:51],s68 offen offset:1024 lds
F v196,v196,v197
M a[72:75],a[220:223],v[148:151],a[72:75]
s_add_u32 m0,s67,4096
s_add_u32 s68,s27,16384
v_lshrrev_b32 v251,1,v245
v_xor_b32 v251,v251,v245
M a[76:79],a[220:223],v[156:159],a[76:79]
B v212,s[48:51],s68 offen lds
v_and_b32 v251,24,v251
M a[76:79],a[216:219],v[152:155],a[76:79]
W lgkmcnt(4)
v_and_b32 v252,32,v245
v_or_b32 v251,v251,v252
M a[80:83],a[192:195],v[120:123],a[80:83]
B v213,s[48:51],s68 offen offset:1024 lds
v_xor_b32 v184,v251,v202
M a[80:83],a[196:199],v[124:127],a[80:83]
s_add_u32 m0,s67,6144
s_add_u32 s68,s27,32768
T a[208:209],v208 offset:49152
v_xor_b32 v185,v251,v204
M a[84:87],a[196:199],v[132:135],a[84:87]
B v212,s[48:51],s68 offen lds
v_xor_b32 v186,v251,v206
M a[84:87],a[192:195],v[128:131],a[84:87]
T a[210:211],v208 offset:53248
v_xor_b32 v187,v251,v208
M a[88:91],a[192:195],v[136:139],a[88:91]
B v213,s[48:51],s68 offen offset:1024 lds
F v250,v188,v190
M a[88:91],a[196:199],v[140:143],a[88:91]
s_add_u32 m0,s67,0
s_add_u32 s68,s27,49152
T a[212:213],v208 offset:57344
F v251,v192,v194
M a[92:95],a[196:199],v[148:151],a[92:95]
B v212,s[48:51],s68 offen lds
F v250,v250,v196
M a[92:95],a[192:195],v[144:147],a[92:95]
T a[214:215],v208 offset:61440
F v250,v250,v251
M a[96:99],a[192:195],v[152:155],a[96:99]
B v213,s[48:51],s68 offen offset:1024 lds
v_mov_b32 v252,s87
M a[96:99],a[196:199],v[156:159],a[96:99]
W lgkmcnt(4)
T a[216:217],v209 offset:49152
v_cmp_ngt_f32 vcc,0x5f800000,v250
M a[100:103],a[204:207],v[124:127],a[100:103]
v_lshlrev_b32 v252,2,v252
M a[100:103],a[200:203],v[120:123],a[100:103]
T a[218:219],v209 offset:53248
s_or_b32 s95,vcc_lo,vcc_hi
M a[104:107],a[200:203],v[128:131],a[104:107]
v_add_u32 v252,0x18000,v252
v_mov_b32 v251,s95
M a[104:107],a[204:207],v[132:135],a[104:107]
T a[220:221],v209 offset:57344
ds_write_b32 v252,v251
v_mov_b32 v253,0x18000
M a[108:111],a[204:207],v[140:143],a[108:111]
v_rcp_f32 v188,v188
M a[108:111],a[200:203],v[136:139],a[108:111]
T a[222:223],v209 offset:61440
v_rcp_f32 v190,v190
M a[112:115],a[200:203],v[144:147],a[112:115]
v_rcp_f32 v192,v192
M a[112:115],a[204:207],v[148:151],a[112:115]
v_rcp_f32 v194,v194
M a[116:119],a[204:207],v[156:159],a[116:119]
v_rcp_f32 v196,v196
M a[116:119],a[200:203],v[152:155],a[116:119]
W lgkmcnt(5)
M a[120:123],a[208:211],v[120:123],a[120:123]
W lgkmcnt(0)
s_barrier
R v[180:183],v253
M a[120:123],a[212:215],v[124:127],a[120:123]
s_mul_i32 s94,s87,6144
T a[192:193],v184
M a[124:127],a[212:215],v[132:135],a[124:127]
s_add_u32 s94,s94,81920
M a[124:127],a[208:211],v[128:131],a[124:127]
T a[194:195],v184 offset:4096
M a[128:131],a[208:211],v[136:139],a[128:131]
v_subrev_u32 v251,s94,v210
v_subrev_u32 v252,s94,v211
M a[128:131],a[212:215],v[140:143],a[128:131]
T a[196:197],v184 offset:8192
M a[132:135],a[212:215],v[148:151],a[132:135]
W lgkmcnt(3)
s_barrier
v_or3_b32 v250,v180,v181,v182
M a[132:135],a[208:211],v[144:147],a[132:135]
T a[198:199],v184 offset:12288
v_or_b32 v250,v250,v183
M a[136:139],a[208:211],v[152:155],a[136:139]
v_readfirstlane_b32 s95,v250
M a[136:139],a[212:215],v[156:159],a[136:139]
W lgkmcnt(15)
T a[200:201],v184 offset:8
M a[140:143],a[220:223],v[124:127],a[140:143]
s_cmp_eq_u32 s71,0
s_cselect_b32 s95,s95,0
M a[140:143],a[216:219],v[120:123],a[140:143]
T a[202:203],v184 offset:4104
M a[144:147],a[216:219],v[128:131],a[144:147]
M a[144:147],a[220:223],v[132:135],a[144:147]
T a[204:205],v184 offset:8200
M a[148:151],a[220:223],v[140:143],a[148:151]
M a[148:151],a[216:219],v[136:139],a[148:151]
T a[206:207],v184 offset:12296
M a[152:155],a[216:219],v[144:147],a[152:155]
W vmcnt(8)
R v[228:231],v251
M a[152:155],a[220:223],v[148:151],a[152:155]
R v[232:235],v252
M a[156:159],a[220:223],v[156:159],a[156:159]
M a[156:159],a[216:219],v[152:155],a[156:159]
s_cmp_lg_u32 s95,0
s_cbranch_scc1 LFBa_%=
)ASM"
R"ASM(
s_mov_b32 s71,0
s_mov_b32 s70,1
s_add_u32 s82,s82,4194304
)ASM"
R"ASM(
LQ1a_%=:
W lgkmcnt(6)
M v[0:3],a[192:195],a[224:227],0
M v[0:3],a[196:199],a[228:231],v[0:3]
W vmcnt(6)
T a[208:209],v185
M v[16:19],a[196:199],a[236:239],0
R a[160:163],v210 offset:2048
M v[16:19],a[192:195],a[232:235],v[16:19]
T a[210:211],v185 offset:4096
M v[32:35],a[192:195],a[240:243],0
R a[164:167],v211 offset:2048
AR v160,a0
M v[32:35],a[196:199],a[244:247],v[32:35]
T a[212:213],v185 offset:8192
AR v161,a1
M v[48:51],a[196:199],a[252:255],0
W lgkmcnt(5)
AR v162,a2
AR v163,a3
M v[48:51],a[192:195],a[248:251],v[48:51]
T a[214:215],v185 offset:12288
AR v164,a20
M v[64:67],a[192:195],v[228:231],0
AR v165,a21
AR v166,a22
M v[64:67],a[196:199],v[232:235],v[64:67]
W lgkmcnt(15)
W lgkmcnt(2)
T a[216:217],v185 offset:8
M v[80:83],a[204:207],a[228:231],0
AR v167,a23
U v160,v160,v188
M v[80:83],a[200:203],a[224:227],v[80:83]
s_add_u32 m0,s67,2048
s_add_u32 s68,s27,65536
T a[218:219],v185 offset:4104
M v[96:99],a[200:203],a[232:235],0
B v212,s[48:51],s68 offen lds
M v[96:99],a[204:207],a[236:239],v[96:99]
T a[220:221],v185 offset:8200
U v161,v161,v188
M v[112:115],a[204:207],a[244:247],0
B v213,s[48:51],s68 offen offset:1024 lds
M v[112:115],a[200:203],a[240:243],v[112:115]
T a[222:223],v185 offset:12296
U v162,v162,v188
M v[128:131],a[200:203],a[248:251],0
U v163,v163,v188
U v164,v164,v188
M v[128:131],a[204:207],a[252:255],v[128:131]
W vmcnt(6)
U v165,v165,v188
U v166,v166,v188
M v[144:147],a[204:207],v[232:235],0
R a[168:171],v210 offset:4096
U v167,v167,v188
M v[144:147],a[200:203],v[228:231],v[144:147]
W lgkmcnt(5)
C v168,v160,v161
C v169,v162,v163
M v[4:7],a[208:211],a[224:227],0
R a[172:175],v211 offset:4096
C v170,v164,v165
M v[4:7],a[212:215],a[228:231],v[4:7]
T a[192:193],v186
C v171,v166,v167
M v[20:23],a[212:215],a[236:239],0
M v[20:23],a[208:211],a[232:235],v[20:23]
s_add_u32 s94,s84,0
T a[194:195],v186 offset:4096
M v[36:39],a[208:211],a[240:243],0
buffer_store_dwordx4 v[168:171],v245,s[52:55],s94 offen sc1
M v[36:39],a[212:215],a[244:247],v[36:39]
W lgkmcnt(2)
T a[196:197],v186 offset:8192
M v[52:55],a[212:215],a[252:255],0
AR v160,a4
AR v161,a5
M v[52:55],a[208:211],a[248:251],v[52:55]
s_add_u32 m0,s67,4096
s_add_u32 s27,s26,256
s_and_b32 s27,s27,0x380
s_add_u32 s27,s27,s66
s_add_u32 s68,s27,0
T a[198:199],v186 offset:12288
M v[68:71],a[208:211],v[228:231],0
B v212,s[48:51],s68 offen lds
M v[68:71],a[212:215],v[232:235],v[68:71]
W lgkmcnt(15)
T a[200:201],v186 offset:8
M v[84:87],a[220:223],a[228:231],0
B v213,s[48:51],s68 offen offset:1024 lds
M v[84:87],a[216:219],a[224:227],v[84:87]
T a[202:203],v186 offset:4104
AR v162,a6
M v[100:103],a[216:219],a[232:235],0
AR v163,a7
AR v164,a24
M v[100:103],a[220:223],a[236:239],v[100:103]
W vmcnt(7)
T a[204:205],v186 offset:8200
M v[116:119],a[220:223],a[244:247],0
R a[176:179],v210 offset:6144
AR v165,a25
M v[116:119],a[216:219],a[240:243],v[116:119]
T a[206:207],v186 offset:12296
AR v166,a26
M v[132:135],a[216:219],a[248:251],0
R a[180:183],v211 offset:6144
AR v167,a27
M v[132:135],a[220:223],a[252:255],v[132:135]
U v160,v160,v190
U v161,v161,v190
M v[148:151],a[220:223],v[232:235],0
U v162,v162,v190
U v163,v163,v190
M v[148:151],a[216:219],v[228:231],v[148:151]
W lgkmcnt(6)
U v164,v164,v190
U v165,v165,v190
M v[8:11],a[192:195],a[224:227],0
U v166,v166,v190
U v167,v167,v190
M v[8:11],a[196:199],a[228:231],v[8:11]
W lgkmcnt(0)
T a[208:209],v187
M v[24:27],a[196:199],a[236:239],0
C v172,v160,v161
C v173,v162,v163
M v[24:27],a[192:195],a[232:235],v[24:27]
s_add_u32 m0,s67,6144
s_add_u32 s68,s27,16384
T a[210:211],v187 offset:4096
M v[40:43],a[192:195],a[240:243],0
B v212,s[48:51],s68 offen lds
M v[40:43],a[196:199],a[244:247],v[40:43]
T a[212:213],v187 offset:8192
C v174,v164,v165
M v[56:59],a[196:199],a[252:255],0
B v213,s[48:51],s68 offen offset:1024 lds
M v[56:59],a[192:195],a[248:251],v[56:59]
T a[214:215],v187 offset:12288
C v175,v166,v167
M v[72:75],a[192:195],v[228:231],0
M v[72:75],a[196:199],v[232:235],v[72:75]
W lgkmcnt(15)
W vmcnt(7)
T a[216:217],v187 offset:8
M v[88:91],a[204:207],a[228:231],0
R a[184:187],v210
M v[88:91],a[200:203],a[224:227],v[88:91]
T a[218:219],v187 offset:4104
M v[104:107],a[200:203],a[232:235],0
R a[188:191],v211
AR v160,a8
M v[104:107],a[204:207],a[236:239],v[104:107]
T a[220:221],v187 offset:8200
AR v161,a9
M v[120:123],a[204:207],a[244:247],0
AR v162,a10
AR v163,a11
M v[120:123],a[200:203],a[240:243],v[120:123]
T a[222:223],v187 offset:12296
AR v164,a28
M v[136:139],a[200:203],a[248:251],0
AR v165,a29
AR v166,a30
M v[136:139],a[204:207],a[252:255],v[136:139]
W lgkmcnt(2)
s_add_u32 m0,s67,0
s_add_u32 s68,s27,32768
M v[152:155],a[204:207],v[232:235],0
B v212,s[48:51],s68 offen lds
M v[152:155],a[200:203],v[228:231],v[152:155]
W lgkmcnt(15)
AR v167,a31
U v160,v160,v192
M v[12:15],a[208:211],a[224:227],0
s_barrier
U v161,v161,v192
U v162,v162,v192
M v[12:15],a[212:215],a[228:231],v[12:15]
T a[192:193],v184 offset:16384
U v163,v163,v192
M v[28:31],a[212:215],a[236:239],0
B v213,s[48:51],s68 offen offset:1024 lds
M v[28:31],a[208:211],a[232:235],v[28:31]
s_add_u32 s94,s84,1024
T a[194:195],v184 offset:20480
M v[44:47],a[208:211],a[240:243],0
buffer_store_dwordx4 v[172:175],v245,s[52:55],s94 offen sc1
M v[44:47],a[212:215],a[244:247],v[44:47]
W vmcnt(8)
T a[196:197],v184 offset:24576
M v[60:63],a[212:215],a[252:255],0
R v[236:239],v210 offset:2048
U v164,v164,v192
M v[60:63],a[208:211],a[248:251],v[60:63]
T a[198:199],v184 offset:28672
U v165,v165,v192
M v[76:79],a[208:211],v[228:231],0
R v[240:243],v211 offset:2048
U v166,v166,v192
M v[76:79],a[212:215],v[232:235],v[76:79]
W lgkmcnt(6)
T a[200:201],v184 offset:16392
M v[92:95],a[220:223],a[228:231],0
U v167,v167,v192
C v176,v160,v161
M v[92:95],a[216:219],a[224:227],v[92:95]
T a[202:203],v184 offset:20488
C v177,v162,v163
M v[108:111],a[216:219],a[232:235],0
C v178,v164,v165
C v179,v166,v167
M v[108:111],a[220:223],a[236:239],v[108:111]
W lgkmcnt(2)
T a[204:205],v184 offset:24584
M v[124:127],a[220:223],a[244:247],0
M v[124:127],a[216:219],a[240:243],v[124:127]
s_add_u32 m0,s67,2048
s_add_u32 s68,s27,49152
T a[206:207],v184 offset:28680
M v[140:143],a[216:219],a[248:251],0
B v212,s[48:51],s68 offen lds
M v[140:143],a[220:223],a[252:255],v[140:143]
M v[156:159],a[220:223],v[232:235],0
B v213,s[48:51],s68 offen offset:1024 lds
M v[156:159],a[216:219],v[228:231],v[156:159]
W lgkmcnt(15)
M v[0:3],a[192:195],a[160:163],v[0:3]
AR v160,a12
AR v161,a13
M v[0:3],a[196:199],a[164:167],v[0:3]
W vmcnt(7)
T a[208:209],v185 offset:16384
M v[16:19],a[196:199],a[172:175],v[16:19]
R a[224:227],v210 offset:4096
AR v162,a14
M v[16:19],a[192:195],a[168:171],v[16:19]
T a[210:211],v185 offset:20480
AR v163,a15
M v[32:35],a[192:195],a[176:179],v[32:35]
R a[228:231],v211 offset:4096
AR v164,a32
M v[32:35],a[196:199],a[180:183],v[32:35]
T a[212:213],v185 offset:24576
AR v165,a33
M v[48:51],a[196:199],a[188:191],v[48:51]
AR v166,a34
AR v167,a35
M v[48:51],a[192:195],a[184:187],v[48:51]
T a[214:215],v185 offset:28672
U v160,v160,v194
M v[64:67],a[192:195],v[236:239],v[64:67]
U v161,v161,v194
U v162,v162,v194
M v[64:67],a[196:199],v[240:243],v[64:67]
W lgkmcnt(6)
W lgkmcnt(2)
T a[216:217],v185 offset:16392
M v[80:83],a[204:207],a[164:167],v[80:83]
U v163,v163,v194
U v164,v164,v194
M v[80:83],a[200:203],a[160:163],v[80:83]
s_add_u32 m0,s67,4096
s_add_u32 s68,s27,65536
T a[218:219],v185 offset:20488
M v[96:99],a[200:203],a[168:171],v[96:99]
B v212,s[48:51],s68 offen lds
M v[96:99],a[204:207],a[172:175],v[96:99]
T a[220:221],v185 offset:24584
U v165,v165,v194
M v[112:115],a[204:207],a[180:183],v[112:115]
B v213,s[48:51],s68 offen offset:1024 lds
M v[112:115],a[200:203],a[176:179],v[112:115]
T a[222:223],v185 offset:28680
U v166,v166,v194
M v[128:131],a[200:203],a[184:187],v[128:131]
U v167,v167,v194
C v180,v160,v161
M v[128:131],a[204:207],a[188:191],v[128:131]
W vmcnt(7)
C v181,v162,v163
C v182,v164,v165
M v[144:147],a[204:207],v[240:243],v[144:147]
R a[232:235],v210 offset:6144
C v183,v166,v167
M v[144:147],a[200:203],v[236:239],v[144:147]
W lgkmcnt(5)
M v[4:7],a[208:211],a[160:163],v[4:7]
R a[236:239],v211 offset:6144
M v[4:7],a[212:215],a[164:167],v[4:7]
s_add_u32 s94,s84,2048
T a[192:193],v186 offset:16384
M v[20:23],a[212:215],a[172:175],v[20:23]
buffer_store_dwordx4 v[176:179],v245,s[52:55],s94 offen sc1
M v[20:23],a[208:211],a[168:171],v[20:23]
T a[194:195],v186 offset:20480
)ASM"
R"ASM(
M v[36:39],a[208:211],a[176:179],v[36:39]
M v[36:39],a[212:215],a[180:183],v[36:39]
W lgkmcnt(2)
T a[196:197],v186 offset:24576
M v[52:55],a[212:215],a[188:191],v[52:55]
M v[52:55],a[208:211],a[184:187],v[52:55]
s_add_u32 m0,s67,6144
s_add_u32 s27,s26,384
s_and_b32 s27,s27,0x380
s_add_u32 s27,s27,s66
s_add_u32 s68,s27,0
T a[198:199],v186 offset:28672
M v[68:71],a[208:211],v[236:239],v[68:71]
B v212,s[48:51],s68 offen lds
M v[68:71],a[212:215],v[240:243],v[68:71]
W lgkmcnt(15)
T a[200:201],v186 offset:16392
M v[84:87],a[220:223],a[164:167],v[84:87]
B v213,s[48:51],s68 offen offset:1024 lds
M v[84:87],a[216:219],a[160:163],v[84:87]
T a[202:203],v186 offset:20488
AR v160,a16
M v[100:103],a[216:219],a[168:171],v[100:103]
AR v161,a17
AR v162,a18
M v[100:103],a[220:223],a[172:175],v[100:103]
W vmcnt(8)
T a[204:205],v186 offset:24584
M v[116:119],a[220:223],a[180:183],v[116:119]
R a[240:243],v210
AR v163,a19
M v[116:119],a[216:219],a[176:179],v[116:119]
T a[206:207],v186 offset:28680
AR v164,a36
M v[132:135],a[216:219],a[184:187],v[132:135]
R a[244:247],v211
AR v165,a37
M v[132:135],a[220:223],a[188:191],v[132:135]
AR v166,a38
AR v167,a39
M v[148:151],a[220:223],v[240:243],v[148:151]
U v160,v160,v196
U v161,v161,v196
M v[148:151],a[216:219],v[236:239],v[148:151]
W lgkmcnt(6)
U v162,v162,v196
U v163,v163,v196
M v[8:11],a[192:195],a[160:163],v[8:11]
U v164,v164,v196
U v165,v165,v196
M v[8:11],a[196:199],a[164:167],v[8:11]
W lgkmcnt(0)
T a[208:209],v187 offset:16384
M v[24:27],a[196:199],a[172:175],v[24:27]
U v166,v166,v196
U v167,v167,v196
M v[24:27],a[192:195],a[168:171],v[24:27]
s_add_u32 m0,s67,0
s_add_u32 s68,s27,16384
T a[210:211],v187 offset:20480
M v[40:43],a[192:195],a[176:179],v[40:43]
B v212,s[48:51],s68 offen lds
M v[40:43],a[196:199],a[180:183],v[40:43]
T a[212:213],v187 offset:24576
C v214,v160,v161
M v[56:59],a[196:199],a[188:191],v[56:59]
B v213,s[48:51],s68 offen offset:1024 lds
M v[56:59],a[192:195],a[184:187],v[56:59]
T a[214:215],v187 offset:28672
C v215,v162,v163
M v[72:75],a[192:195],v[236:239],v[72:75]
C v216,v164,v165
C v217,v166,v167
M v[72:75],a[196:199],v[240:243],v[72:75]
W lgkmcnt(15)
W vmcnt(7)
T a[216:217],v187 offset:16392
M v[88:91],a[204:207],a[164:167],v[88:91]
R a[248:251],v210 offset:2048
M v[88:91],a[200:203],a[160:163],v[88:91]
T a[218:219],v187 offset:20488
M v[104:107],a[200:203],a[168:171],v[104:107]
R a[252:255],v211 offset:2048
M v[104:107],a[204:207],a[172:175],v[104:107]
T a[220:221],v187 offset:24584
M v[120:123],a[204:207],a[180:183],v[120:123]
M v[120:123],a[200:203],a[176:179],v[120:123]
T a[222:223],v187 offset:28680
M v[136:139],a[200:203],a[184:187],v[136:139]
AR v160,a40
AR v161,a41
M v[136:139],a[204:207],a[188:191],v[136:139]
W lgkmcnt(2)
s_add_u32 m0,s67,2048
s_add_u32 s68,s27,32768
M v[152:155],a[204:207],v[240:243],v[152:155]
B v212,s[48:51],s68 offen lds
s_add_u32 s94,s84,3072
M v[152:155],a[200:203],v[236:239],v[152:155]
W lgkmcnt(15)
buffer_store_dwordx4 v[180:183],v245,s[52:55],s94 offen sc1
M v[12:15],a[208:211],a[160:163],v[12:15]
s_barrier
AR v162,a42
AR v163,a43
M v[12:15],a[212:215],a[164:167],v[12:15]
T a[192:193],v184 offset:32768
AR v164,a60
M v[28:31],a[212:215],a[172:175],v[28:31]
B v213,s[48:51],s68 offen offset:1024 lds
M v[28:31],a[208:211],a[168:171],v[28:31]
T a[194:195],v184 offset:36864
AR v165,a61
M v[44:47],a[208:211],a[176:179],v[44:47]
AR v166,a62
AR v167,a63
M v[44:47],a[212:215],a[180:183],v[44:47]
W vmcnt(8)
T a[196:197],v184 offset:40960
M v[60:63],a[212:215],a[188:191],v[60:63]
R v[228:231],v210 offset:4096
U v160,v160,v188
M v[60:63],a[208:211],a[184:187],v[60:63]
T a[198:199],v184 offset:45056
U v161,v161,v188
M v[76:79],a[208:211],v[236:239],v[76:79]
R v[232:235],v211 offset:4096
U v162,v162,v188
M v[76:79],a[212:215],v[240:243],v[76:79]
W lgkmcnt(6)
T a[200:201],v184 offset:32776
M v[92:95],a[220:223],a[164:167],v[92:95]
U v163,v163,v188
U v164,v164,v188
M v[92:95],a[216:219],a[160:163],v[92:95]
T a[202:203],v184 offset:36872
U v165,v165,v188
M v[108:111],a[216:219],a[168:171],v[108:111]
U v166,v166,v188
U v167,v167,v188
M v[108:111],a[220:223],a[172:175],v[108:111]
W lgkmcnt(2)
T a[204:205],v184 offset:40968
M v[124:127],a[220:223],a[180:183],v[124:127]
C v168,v160,v161
C v169,v162,v163
M v[124:127],a[216:219],a[176:179],v[124:127]
s_add_u32 m0,s67,4096
s_add_u32 s68,s27,49152
T a[206:207],v184 offset:45064
M v[140:143],a[216:219],a[184:187],v[140:143]
B v212,s[48:51],s68 offen lds
M v[140:143],a[220:223],a[188:191],v[140:143]
C v170,v164,v165
C v171,v166,v167
M v[156:159],a[220:223],v[240:243],v[156:159]
B v213,s[48:51],s68 offen offset:1024 lds
M v[156:159],a[216:219],v[236:239],v[156:159]
W lgkmcnt(15)
M v[0:3],a[192:195],a[224:227],v[0:3]
M v[0:3],a[196:199],a[228:231],v[0:3]
W vmcnt(7)
T a[208:209],v185 offset:32768
M v[16:19],a[196:199],a[236:239],v[16:19]
R a[160:163],v210 offset:6144
M v[16:19],a[192:195],a[232:235],v[16:19]
T a[210:211],v185 offset:36864
M v[32:35],a[192:195],a[240:243],v[32:35]
R a[164:167],v211 offset:6144
AR v160,a44
M v[32:35],a[196:199],a[244:247],v[32:35]
T a[212:213],v185 offset:40960
AR v161,a45
M v[48:51],a[196:199],a[252:255],v[48:51]
AR v162,a46
AR v163,a47
M v[48:51],a[192:195],a[248:251],v[48:51]
T a[214:215],v185 offset:45056
AR v164,a64
M v[64:67],a[192:195],v[228:231],v[64:67]
AR v165,a65
AR v166,a66
M v[64:67],a[196:199],v[232:235],v[64:67]
W lgkmcnt(6)
W lgkmcnt(2)
T a[216:217],v185 offset:32776
M v[80:83],a[204:207],a[228:231],v[80:83]
AR v167,a67
U v160,v160,v190
M v[80:83],a[200:203],a[224:227],v[80:83]
s_add_u32 m0,s67,6144
s_add_u32 s68,s27,65536
T a[218:219],v185 offset:36872
M v[96:99],a[200:203],a[232:235],v[96:99]
B v212,s[48:51],s68 offen lds
M v[96:99],a[204:207],a[236:239],v[96:99]
T a[220:221],v185 offset:40968
U v161,v161,v190
M v[112:115],a[204:207],a[244:247],v[112:115]
B v213,s[48:51],s68 offen offset:1024 lds
M v[112:115],a[200:203],a[240:243],v[112:115]
s_add_u32 s94,s84,4096
T a[222:223],v185 offset:45064
M v[128:131],a[200:203],a[248:251],v[128:131]
buffer_store_dwordx4 v[214:217],v245,s[52:55],s94 offen sc1
M v[128:131],a[204:207],a[252:255],v[128:131]
W vmcnt(8)
U v162,v162,v190
U v163,v163,v190
M v[144:147],a[204:207],v[232:235],v[144:147]
R a[168:171],v210
U v164,v164,v190
M v[144:147],a[200:203],v[228:231],v[144:147]
W lgkmcnt(5)
U v165,v165,v190
U v166,v166,v190
M v[4:7],a[208:211],a[224:227],v[4:7]
R a[172:175],v211
U v167,v167,v190
M v[4:7],a[212:215],a[228:231],v[4:7]
T a[192:193],v186 offset:32768
C v172,v160,v161
M v[20:23],a[212:215],a[236:239],v[20:23]
C v173,v162,v163
C v174,v164,v165
M v[20:23],a[208:211],a[232:235],v[20:23]
T a[194:195],v186 offset:36864
C v175,v166,v167
M v[36:39],a[208:211],a[240:243],v[36:39]
M v[36:39],a[212:215],a[244:247],v[36:39]
W lgkmcnt(2)
T a[196:197],v186 offset:40960
M v[52:55],a[212:215],a[252:255],v[52:55]
M v[52:55],a[208:211],a[248:251],v[52:55]
s_add_u32 m0,s67,0
s_add_u32 s27,s26,512
s_and_b32 s27,s27,0x380
s_add_u32 s27,s27,s66
s_add_u32 s68,s27,0
T a[198:199],v186 offset:45056
M v[68:71],a[208:211],v[228:231],v[68:71]
B v212,s[48:51],s68 offen lds
M v[68:71],a[212:215],v[232:235],v[68:71]
W lgkmcnt(15)
T a[200:201],v186 offset:32776
M v[84:87],a[220:223],a[228:231],v[84:87]
B v213,s[48:51],s68 offen offset:1024 lds
M v[84:87],a[216:219],a[224:227],v[84:87]
T a[202:203],v186 offset:36872
M v[100:103],a[216:219],a[232:235],v[100:103]
AR v160,a48
AR v161,a49
M v[100:103],a[220:223],a[236:239],v[100:103]
W vmcnt(7)
T a[204:205],v186 offset:40968
M v[116:119],a[220:223],a[244:247],v[116:119]
R a[176:179],v210 offset:2048
AR v162,a50
M v[116:119],a[216:219],a[240:243],v[116:119]
T a[206:207],v186 offset:45064
AR v163,a51
M v[132:135],a[216:219],a[248:251],v[132:135]
R a[180:183],v211 offset:2048
AR v164,a68
M v[132:135],a[220:223],a[252:255],v[132:135]
AR v165,a69
AR v166,a70
M v[148:151],a[220:223],v[232:235],v[148:151]
AR v167,a71
U v160,v160,v192
M v[148:151],a[216:219],v[228:231],v[148:151]
W lgkmcnt(6)
U v161,v161,v192
U v162,v162,v192
M v[8:11],a[192:195],a[224:227],v[8:11]
U v163,v163,v192
U v164,v164,v192
M v[8:11],a[196:199],a[228:231],v[8:11]
W lgkmcnt(0)
T a[208:209],v187 offset:32768
M v[24:27],a[196:199],a[236:239],v[24:27]
U v165,v165,v192
U v166,v166,v192
M v[24:27],a[192:195],a[232:235],v[24:27]
s_add_u32 m0,s67,2048
s_add_u32 s68,s27,16384
T a[210:211],v187 offset:36864
M v[40:43],a[192:195],a[240:243],v[40:43]
B v212,s[48:51],s68 offen lds
M v[40:43],a[196:199],a[244:247],v[40:43]
T a[212:213],v187 offset:40960
U v167,v167,v192
M v[56:59],a[196:199],a[252:255],v[56:59]
B v213,s[48:51],s68 offen offset:1024 lds
M v[56:59],a[192:195],a[248:251],v[56:59]
T a[214:215],v187 offset:45056
C v176,v160,v161
M v[72:75],a[192:195],v[228:231],v[72:75]
C v177,v162,v163
C v178,v164,v165
)ASM"
R"ASM(
M v[72:75],a[196:199],v[232:235],v[72:75]
W lgkmcnt(15)
W vmcnt(7)
T a[216:217],v187 offset:32776
M v[88:91],a[204:207],a[228:231],v[88:91]
R a[184:187],v210 offset:4096
C v179,v166,v167
M v[88:91],a[200:203],a[224:227],v[88:91]
T a[218:219],v187 offset:36872
M v[104:107],a[200:203],a[232:235],v[104:107]
R a[188:191],v211 offset:4096
M v[104:107],a[204:207],a[236:239],v[104:107]
s_add_u32 s94,s84,5120
T a[220:221],v187 offset:40968
M v[120:123],a[204:207],a[244:247],v[120:123]
buffer_store_dwordx4 v[168:171],v245,s[52:55],s94 offen sc1
M v[120:123],a[200:203],a[240:243],v[120:123]
T a[222:223],v187 offset:45064
M v[136:139],a[200:203],a[248:251],v[136:139]
M v[136:139],a[204:207],a[252:255],v[136:139]
W lgkmcnt(2)
s_add_u32 m0,s67,4096
s_add_u32 s68,s27,32768
M v[152:155],a[204:207],v[232:235],v[152:155]
B v212,s[48:51],s68 offen lds
M v[152:155],a[200:203],v[228:231],v[152:155]
W lgkmcnt(15)
M v[12:15],a[208:211],a[224:227],v[12:15]
s_barrier
AR v160,a52
AR v161,a53
M v[12:15],a[212:215],a[228:231],v[12:15]
T a[192:193],v184 offset:49152
AR v162,a54
M v[28:31],a[212:215],a[236:239],v[28:31]
B v213,s[48:51],s68 offen offset:1024 lds
M v[28:31],a[208:211],a[232:235],v[28:31]
T a[194:195],v184 offset:53248
AR v163,a55
M v[44:47],a[208:211],a[240:243],v[44:47]
AR v164,a72
AR v165,a73
M v[44:47],a[212:215],a[244:247],v[44:47]
W vmcnt(8)
T a[196:197],v184 offset:57344
M v[60:63],a[212:215],a[252:255],v[60:63]
R v[236:239],v210 offset:6144
AR v166,a74
M v[60:63],a[208:211],a[248:251],v[60:63]
T a[198:199],v184 offset:61440
AR v167,a75
M v[76:79],a[208:211],v[228:231],v[76:79]
R v[240:243],v211 offset:6144
U v160,v160,v194
M v[76:79],a[212:215],v[232:235],v[76:79]
W lgkmcnt(6)
T a[200:201],v184 offset:49160
M v[92:95],a[220:223],a[228:231],v[92:95]
U v161,v161,v194
U v162,v162,v194
M v[92:95],a[216:219],a[224:227],v[92:95]
T a[202:203],v184 offset:53256
U v163,v163,v194
M v[108:111],a[216:219],a[232:235],v[108:111]
U v164,v164,v194
U v165,v165,v194
M v[108:111],a[220:223],a[236:239],v[108:111]
W lgkmcnt(2)
T a[204:205],v184 offset:57352
M v[124:127],a[220:223],a[244:247],v[124:127]
U v166,v166,v194
U v167,v167,v194
M v[124:127],a[216:219],a[240:243],v[124:127]
s_add_u32 m0,s67,6144
s_add_u32 s68,s27,49152
T a[206:207],v184 offset:61448
M v[140:143],a[216:219],a[248:251],v[140:143]
B v212,s[48:51],s68 offen lds
M v[140:143],a[220:223],a[252:255],v[140:143]
C v180,v160,v161
C v181,v162,v163
M v[156:159],a[220:223],v[232:235],v[156:159]
B v213,s[48:51],s68 offen offset:1024 lds
M v[156:159],a[216:219],v[228:231],v[156:159]
W lgkmcnt(15)
C v182,v164,v165
C v183,v166,v167
M v[0:3],a[192:195],a[160:163],v[0:3]
M v[0:3],a[196:199],a[164:167],v[0:3]
W vmcnt(7)
T a[208:209],v185 offset:49152
M v[16:19],a[196:199],a[172:175],v[16:19]
R a[224:227],v210
M v[16:19],a[192:195],a[168:171],v[16:19]
T a[210:211],v185 offset:53248
M v[32:35],a[192:195],a[176:179],v[32:35]
R a[228:231],v211
M v[32:35],a[196:199],a[180:183],v[32:35]
T a[212:213],v185 offset:57344
M v[48:51],a[196:199],a[188:191],v[48:51]
M v[48:51],a[192:195],a[184:187],v[48:51]
T a[214:215],v185 offset:61440
M v[64:67],a[192:195],v[236:239],v[64:67]
AR v160,a56
AR v161,a57
M v[64:67],a[196:199],v[240:243],v[64:67]
W lgkmcnt(6)
W lgkmcnt(2)
T a[216:217],v185 offset:49160
M v[80:83],a[204:207],a[164:167],v[80:83]
AR v162,a58
AR v163,a59
M v[80:83],a[200:203],a[160:163],v[80:83]
s_add_u32 m0,s67,0
s_add_u32 s68,s27,65536
T a[218:219],v185 offset:53256
M v[96:99],a[200:203],a[168:171],v[96:99]
B v212,s[48:51],s68 offen lds
M v[96:99],a[204:207],a[172:175],v[96:99]
T a[220:221],v185 offset:57352
AR v164,a76
M v[112:115],a[204:207],a[180:183],v[112:115]
B v213,s[48:51],s68 offen offset:1024 lds
M v[112:115],a[200:203],a[176:179],v[112:115]
s_add_u32 s94,s84,6144
T a[222:223],v185 offset:61448
M v[128:131],a[200:203],a[184:187],v[128:131]
buffer_store_dwordx4 v[172:175],v245,s[52:55],s94 offen sc1
M v[128:131],a[204:207],a[188:191],v[128:131]
W vmcnt(8)
AR v165,a77
AR v166,a78
M v[144:147],a[204:207],v[240:243],v[144:147]
R a[232:235],v210 offset:2048
AR v167,a79
M v[144:147],a[200:203],v[236:239],v[144:147]
W lgkmcnt(5)
U v160,v160,v196
U v161,v161,v196
M v[4:7],a[208:211],a[160:163],v[4:7]
R a[236:239],v211 offset:2048
U v162,v162,v196
M v[4:7],a[212:215],a[164:167],v[4:7]
T a[192:193],v186 offset:49152
U v163,v163,v196
M v[20:23],a[212:215],a[172:175],v[20:23]
U v164,v164,v196
U v165,v165,v196
M v[20:23],a[208:211],a[168:171],v[20:23]
T a[194:195],v186 offset:53248
U v166,v166,v196
M v[36:39],a[208:211],a[176:179],v[36:39]
U v167,v167,v196
C v214,v160,v161
M v[36:39],a[212:215],a[180:183],v[36:39]
W lgkmcnt(2)
T a[196:197],v186 offset:57344
M v[52:55],a[212:215],a[188:191],v[52:55]
C v215,v162,v163
C v216,v164,v165
M v[52:55],a[208:211],a[184:187],v[52:55]
s_add_u32 m0,s67,2048
s_add_u32 s27,s26,640
s_and_b32 s27,s27,0x380
s_add_u32 s27,s27,s66
s_add_u32 s68,s27,0
T a[198:199],v186 offset:61440
M v[68:71],a[208:211],v[236:239],v[68:71]
B v212,s[48:51],s68 offen lds
M v[68:71],a[212:215],v[240:243],v[68:71]
W lgkmcnt(15)
T a[200:201],v186 offset:49160
M v[84:87],a[220:223],a[164:167],v[84:87]
B v213,s[48:51],s68 offen offset:1024 lds
M v[84:87],a[216:219],a[160:163],v[84:87]
T a[202:203],v186 offset:53256
C v217,v166,v167
M v[100:103],a[216:219],a[168:171],v[100:103]
M v[100:103],a[220:223],a[172:175],v[100:103]
W vmcnt(7)
T a[204:205],v186 offset:57352
M v[116:119],a[220:223],a[180:183],v[116:119]
R a[240:243],v210 offset:4096
M v[116:119],a[216:219],a[176:179],v[116:119]
T a[206:207],v186 offset:61448
M v[132:135],a[216:219],a[184:187],v[132:135]
R a[244:247],v211 offset:4096
AR v160,a80
M v[132:135],a[220:223],a[188:191],v[132:135]
AR v161,a81
AR v162,a82
M v[148:151],a[220:223],v[240:243],v[148:151]
AR v163,a83
AR v164,a100
M v[148:151],a[216:219],v[236:239],v[148:151]
W lgkmcnt(6)
AR v165,a101
AR v166,a102
M v[8:11],a[192:195],a[160:163],v[8:11]
AR v167,a103
U v160,v160,v188
M v[8:11],a[196:199],a[164:167],v[8:11]
W lgkmcnt(0)
T a[208:209],v187 offset:49152
M v[24:27],a[196:199],a[172:175],v[24:27]
U v161,v161,v188
U v162,v162,v188
M v[24:27],a[192:195],a[168:171],v[24:27]
s_add_u32 m0,s67,4096
s_add_u32 s68,s27,16384
T a[210:211],v187 offset:53248
M v[40:43],a[192:195],a[176:179],v[40:43]
B v212,s[48:51],s68 offen lds
M v[40:43],a[196:199],a[180:183],v[40:43]
T a[212:213],v187 offset:57344
U v163,v163,v188
M v[56:59],a[196:199],a[188:191],v[56:59]
B v213,s[48:51],s68 offen offset:1024 lds
M v[56:59],a[192:195],a[184:187],v[56:59]
T a[214:215],v187 offset:61440
U v164,v164,v188
M v[72:75],a[192:195],v[236:239],v[72:75]
U v165,v165,v188
U v166,v166,v188
M v[72:75],a[196:199],v[240:243],v[72:75]
W lgkmcnt(15)
W vmcnt(7)
T a[216:217],v187 offset:49160
M v[88:91],a[204:207],a[164:167],v[88:91]
R a[248:251],v210 offset:6144
U v167,v167,v188
M v[88:91],a[200:203],a[160:163],v[88:91]
T a[218:219],v187 offset:53256
C v168,v160,v161
M v[104:107],a[200:203],a[168:171],v[104:107]
R a[252:255],v211 offset:6144
C v169,v162,v163
M v[104:107],a[204:207],a[172:175],v[104:107]
s_add_u32 s94,s84,7168
T a[220:221],v187 offset:57352
M v[120:123],a[204:207],a[180:183],v[120:123]
buffer_store_dwordx4 v[176:179],v245,s[52:55],s94 offen sc1
M v[120:123],a[200:203],a[176:179],v[120:123]
T a[222:223],v187 offset:61448
C v170,v164,v165
M v[136:139],a[200:203],a[184:187],v[136:139]
C v171,v166,v167
M v[136:139],a[204:207],a[188:191],v[136:139]
W lgkmcnt(2)
s_add_u32 m0,s67,6144
s_add_u32 s68,s27,32768
M v[152:155],a[204:207],v[240:243],v[152:155]
B v212,s[48:51],s68 offen lds
M v[152:155],a[200:203],v[236:239],v[152:155]
W lgkmcnt(15)
M v[12:15],a[208:211],a[160:163],v[12:15]
s_barrier
M v[12:15],a[212:215],a[164:167],v[12:15]
T a[192:193],v184
M v[28:31],a[212:215],a[172:175],v[28:31]
B v213,s[48:51],s68 offen offset:1024 lds
M v[28:31],a[208:211],a[168:171],v[28:31]
T a[194:195],v184 offset:4096
M v[44:47],a[208:211],a[176:179],v[44:47]
AR v160,a84
AR v161,a85
M v[44:47],a[212:215],a[180:183],v[44:47]
W vmcnt(8)
T a[196:197],v184 offset:8192
M v[60:63],a[212:215],a[188:191],v[60:63]
R v[228:231],v210
AR v162,a86
M v[60:63],a[208:211],a[184:187],v[60:63]
T a[198:199],v184 offset:12288
AR v163,a87
M v[76:79],a[208:211],v[236:239],v[76:79]
R v[232:235],v211
AR v164,a104
M v[76:79],a[212:215],v[240:243],v[76:79]
W lgkmcnt(6)
T a[200:201],v184 offset:8
M v[92:95],a[220:223],a[164:167],v[92:95]
AR v165,a105
AR v166,a106
M v[92:95],a[216:219],a[160:163],v[92:95]
T a[202:203],v184 offset:4104
AR v167,a107
M v[108:111],a[216:219],a[168:171],v[108:111]
U v160,v160,v190
U v161,v161,v190
M v[108:111],a[220:223],a[172:175],v[108:111]
W lgkmcnt(2)
T a[204:205],v184 offset:8200
M v[124:127],a[220:223],a[180:183],v[124:127]
U v162,v162,v190
U v163,v163,v190
M v[124:127],a[216:219],a[176:179],v[124:127]
s_add_u32 m0,s67,0
s_add_u32 s68,s27,49152
T a[206:207],v184 offset:12296
)ASM"
R"ASM(
M v[140:143],a[216:219],a[184:187],v[140:143]
B v212,s[48:51],s68 offen lds
M v[140:143],a[220:223],a[188:191],v[140:143]
U v164,v164,v190
U v165,v165,v190
M v[156:159],a[220:223],v[240:243],v[156:159]
B v213,s[48:51],s68 offen offset:1024 lds
M v[156:159],a[216:219],v[236:239],v[156:159]
W lgkmcnt(15)
U v166,v166,v190
U v167,v167,v190
M v[0:3],a[192:195],a[224:227],v[0:3]
C v172,v160,v161
C v173,v162,v163
M v[0:3],a[196:199],a[228:231],v[0:3]
W vmcnt(7)
T a[208:209],v185
M v[16:19],a[196:199],a[236:239],v[16:19]
R a[160:163],v210 offset:2048
C v174,v164,v165
M v[16:19],a[192:195],a[232:235],v[16:19]
T a[210:211],v185 offset:4096
C v175,v166,v167
M v[32:35],a[192:195],a[240:243],v[32:35]
R a[164:167],v211 offset:2048
M v[32:35],a[196:199],a[244:247],v[32:35]
T a[212:213],v185 offset:8192
M v[48:51],a[196:199],a[252:255],v[48:51]
M v[48:51],a[192:195],a[248:251],v[48:51]
s_add_u32 s94,s84,8192
T a[214:215],v185 offset:12288
M v[64:67],a[192:195],v[228:231],v[64:67]
buffer_store_dwordx4 v[180:183],v245,s[52:55],s94 offen sc1
M v[64:67],a[196:199],v[232:235],v[64:67]
W lgkmcnt(6)
W lgkmcnt(2)
T a[216:217],v185 offset:8
M v[80:83],a[204:207],a[228:231],v[80:83]
M v[80:83],a[200:203],a[224:227],v[80:83]
s_add_u32 m0,s67,2048
s_add_u32 s68,s27,65536
T a[218:219],v185 offset:4104
M v[96:99],a[200:203],a[232:235],v[96:99]
B v212,s[48:51],s68 offen lds
M v[96:99],a[204:207],a[236:239],v[96:99]
T a[220:221],v185 offset:8200
AR v160,a88
M v[112:115],a[204:207],a[244:247],v[112:115]
B v213,s[48:51],s68 offen offset:1024 lds
M v[112:115],a[200:203],a[240:243],v[112:115]
T a[222:223],v185 offset:12296
AR v161,a89
M v[128:131],a[200:203],a[248:251],v[128:131]
AR v162,a90
AR v163,a91
M v[128:131],a[204:207],a[252:255],v[128:131]
W vmcnt(8)
AR v164,a108
AR v165,a109
M v[144:147],a[204:207],v[232:235],v[144:147]
R a[168:171],v210 offset:4096
AR v166,a110
M v[144:147],a[200:203],v[228:231],v[144:147]
W lgkmcnt(5)
AR v167,a111
U v160,v160,v192
M v[4:7],a[208:211],a[224:227],v[4:7]
R a[172:175],v211 offset:4096
U v161,v161,v192
M v[4:7],a[212:215],a[228:231],v[4:7]
T a[192:193],v186
U v162,v162,v192
M v[20:23],a[212:215],a[236:239],v[20:23]
U v163,v163,v192
U v164,v164,v192
M v[20:23],a[208:211],a[232:235],v[20:23]
T a[194:195],v186 offset:4096
U v165,v165,v192
M v[36:39],a[208:211],a[240:243],v[36:39]
U v166,v166,v192
U v167,v167,v192
M v[36:39],a[212:215],a[244:247],v[36:39]
W lgkmcnt(2)
T a[196:197],v186 offset:8192
M v[52:55],a[212:215],a[252:255],v[52:55]
C v176,v160,v161
C v177,v162,v163
M v[52:55],a[208:211],a[248:251],v[52:55]
s_add_u32 m0,s67,4096
s_add_u32 s27,s26,768
s_and_b32 s27,s27,0x380
s_add_u32 s27,s27,s66
s_add_u32 s68,s27,0
T a[198:199],v186 offset:12288
M v[68:71],a[208:211],v[228:231],v[68:71]
B v212,s[48:51],s68 offen lds
M v[68:71],a[212:215],v[232:235],v[68:71]
W lgkmcnt(15)
T a[200:201],v186 offset:8
M v[84:87],a[220:223],a[228:231],v[84:87]
B v213,s[48:51],s68 offen offset:1024 lds
M v[84:87],a[216:219],a[224:227],v[84:87]
T a[202:203],v186 offset:4104
C v178,v164,v165
M v[100:103],a[216:219],a[232:235],v[100:103]
C v179,v166,v167
M v[100:103],a[220:223],a[236:239],v[100:103]
W vmcnt(7)
T a[204:205],v186 offset:8200
M v[116:119],a[220:223],a[244:247],v[116:119]
R a[176:179],v210 offset:6144
M v[116:119],a[216:219],a[240:243],v[116:119]
T a[206:207],v186 offset:12296
M v[132:135],a[216:219],a[248:251],v[132:135]
R a[180:183],v211 offset:6144
M v[132:135],a[220:223],a[252:255],v[132:135]
M v[148:151],a[220:223],v[232:235],v[148:151]
M v[148:151],a[216:219],v[228:231],v[148:151]
W lgkmcnt(6)
M v[8:11],a[192:195],a[224:227],v[8:11]
AR v160,a92
AR v161,a93
M v[8:11],a[196:199],a[228:231],v[8:11]
W lgkmcnt(0)
T a[208:209],v187
M v[24:27],a[196:199],a[236:239],v[24:27]
AR v162,a94
AR v163,a95
M v[24:27],a[192:195],a[232:235],v[24:27]
s_add_u32 m0,s67,6144
s_add_u32 s68,s27,16384
T a[210:211],v187 offset:4096
M v[40:43],a[192:195],a[240:243],v[40:43]
B v212,s[48:51],s68 offen lds
M v[40:43],a[196:199],a[244:247],v[40:43]
T a[212:213],v187 offset:8192
AR v164,a112
M v[56:59],a[196:199],a[252:255],v[56:59]
B v213,s[48:51],s68 offen offset:1024 lds
M v[56:59],a[192:195],a[248:251],v[56:59]
s_add_u32 s94,s84,9216
T a[214:215],v187 offset:12288
M v[72:75],a[192:195],v[228:231],v[72:75]
buffer_store_dwordx4 v[214:217],v245,s[52:55],s94 offen sc1
M v[72:75],a[196:199],v[232:235],v[72:75]
W lgkmcnt(15)
W vmcnt(8)
T a[216:217],v187 offset:8
M v[88:91],a[204:207],a[228:231],v[88:91]
R a[184:187],v210
AR v165,a113
M v[88:91],a[200:203],a[224:227],v[88:91]
T a[218:219],v187 offset:4104
AR v166,a114
M v[104:107],a[200:203],a[232:235],v[104:107]
R a[188:191],v211
AR v167,a115
M v[104:107],a[204:207],a[236:239],v[104:107]
T a[220:221],v187 offset:8200
U v160,v160,v194
M v[120:123],a[204:207],a[244:247],v[120:123]
U v161,v161,v194
U v162,v162,v194
M v[120:123],a[200:203],a[240:243],v[120:123]
T a[222:223],v187 offset:12296
U v163,v163,v194
M v[136:139],a[200:203],a[248:251],v[136:139]
U v164,v164,v194
U v165,v165,v194
M v[136:139],a[204:207],a[252:255],v[136:139]
W lgkmcnt(2)
s_add_u32 m0,s67,0
s_add_u32 s68,s27,32768
M v[152:155],a[204:207],v[232:235],v[152:155]
B v212,s[48:51],s68 offen lds
M v[152:155],a[200:203],v[228:231],v[152:155]
W lgkmcnt(15)
U v166,v166,v194
U v167,v167,v194
M v[12:15],a[208:211],a[224:227],v[12:15]
s_barrier
C v180,v160,v161
C v181,v162,v163
M v[12:15],a[212:215],a[228:231],v[12:15]
T a[192:193],v184 offset:16384
C v182,v164,v165
M v[28:31],a[212:215],a[236:239],v[28:31]
B v213,s[48:51],s68 offen offset:1024 lds
M v[28:31],a[208:211],a[232:235],v[28:31]
T a[194:195],v184 offset:20480
C v183,v166,v167
M v[44:47],a[208:211],a[240:243],v[44:47]
M v[44:47],a[212:215],a[244:247],v[44:47]
W vmcnt(7)
T a[196:197],v184 offset:24576
M v[60:63],a[212:215],a[252:255],v[60:63]
R v[236:239],v210 offset:2048
M v[60:63],a[208:211],a[248:251],v[60:63]
T a[198:199],v184 offset:28672
M v[76:79],a[208:211],v[228:231],v[76:79]
R v[240:243],v211 offset:2048
AR v160,a96
M v[76:79],a[212:215],v[232:235],v[76:79]
W lgkmcnt(6)
T a[200:201],v184 offset:16392
M v[92:95],a[220:223],a[228:231],v[92:95]
AR v161,a97
AR v162,a98
M v[92:95],a[216:219],a[224:227],v[92:95]
T a[202:203],v184 offset:20488
AR v163,a99
M v[108:111],a[216:219],a[232:235],v[108:111]
AR v164,a116
AR v165,a117
M v[108:111],a[220:223],a[236:239],v[108:111]
W lgkmcnt(2)
T a[204:205],v184 offset:24584
M v[124:127],a[220:223],a[244:247],v[124:127]
AR v166,a118
AR v167,a119
M v[124:127],a[216:219],a[240:243],v[124:127]
s_add_u32 m0,s67,2048
s_add_u32 s68,s27,49152
T a[206:207],v184 offset:28680
M v[140:143],a[216:219],a[248:251],v[140:143]
B v212,s[48:51],s68 offen lds
M v[140:143],a[220:223],a[252:255],v[140:143]
U v160,v160,v196
U v161,v161,v196
M v[156:159],a[220:223],v[232:235],v[156:159]
B v213,s[48:51],s68 offen offset:1024 lds
M v[156:159],a[216:219],v[228:231],v[156:159]
W lgkmcnt(15)
U v162,v162,v196
U v163,v163,v196
M v[0:3],a[192:195],a[160:163],v[0:3]
U v164,v164,v196
U v165,v165,v196
M v[0:3],a[196:199],a[164:167],v[0:3]
W vmcnt(7)
T a[208:209],v185 offset:16384
M v[16:19],a[196:199],a[172:175],v[16:19]
R a[224:227],v210 offset:4096
U v166,v166,v196
M v[16:19],a[192:195],a[168:171],v[16:19]
T a[210:211],v185 offset:20480
U v167,v167,v196
M v[32:35],a[192:195],a[176:179],v[32:35]
R a[228:231],v211 offset:4096
C v214,v160,v161
M v[32:35],a[196:199],a[180:183],v[32:35]
s_add_u32 s94,s84,10240
T a[212:213],v185 offset:24576
M v[48:51],a[196:199],a[188:191],v[48:51]
buffer_store_dwordx4 v[168:171],v245,s[52:55],s94 offen sc1
M v[48:51],a[192:195],a[184:187],v[48:51]
T a[214:215],v185 offset:28672
C v215,v162,v163
M v[64:67],a[192:195],v[236:239],v[64:67]
C v216,v164,v165
C v217,v166,v167
M v[64:67],a[196:199],v[240:243],v[64:67]
W lgkmcnt(6)
W lgkmcnt(2)
T a[216:217],v185 offset:16392
M v[80:83],a[204:207],a[164:167],v[80:83]
M v[80:83],a[200:203],a[160:163],v[80:83]
s_add_u32 m0,s67,4096
s_add_u32 s68,s27,65536
T a[218:219],v185 offset:20488
M v[96:99],a[200:203],a[168:171],v[96:99]
B v212,s[48:51],s68 offen lds
M v[96:99],a[204:207],a[172:175],v[96:99]
T a[220:221],v185 offset:24584
M v[112:115],a[204:207],a[180:183],v[112:115]
B v213,s[48:51],s68 offen offset:1024 lds
M v[112:115],a[200:203],a[176:179],v[112:115]
T a[222:223],v185 offset:28680
M v[128:131],a[200:203],a[184:187],v[128:131]
AR v160,a120
AR v161,a121
M v[128:131],a[204:207],a[188:191],v[128:131]
W vmcnt(8)
AR v162,a122
AR v163,a123
M v[144:147],a[204:207],v[240:243],v[144:147]
R a[232:235],v210 offset:6144
AR v164,a140
M v[144:147],a[200:203],v[236:239],v[144:147]
W lgkmcnt(5)
AR v165,a141
AR v166,a142
M v[4:7],a[208:211],a[160:163],v[4:7]
R a[236:239],v211 offset:6144
AR v167,a143
M v[4:7],a[212:215],a[164:167],v[4:7]
T a[192:193],v186 offset:16384
U v160,v160,v188
M v[20:23],a[212:215],a[172:175],v[20:23]
U v161,v161,v188
)ASM"
R"ASM(
U v162,v162,v188
M v[20:23],a[208:211],a[168:171],v[20:23]
T a[194:195],v186 offset:20480
U v163,v163,v188
M v[36:39],a[208:211],a[176:179],v[36:39]
U v164,v164,v188
U v165,v165,v188
M v[36:39],a[212:215],a[180:183],v[36:39]
W lgkmcnt(2)
T a[196:197],v186 offset:24576
M v[52:55],a[212:215],a[188:191],v[52:55]
U v166,v166,v188
U v167,v167,v188
M v[52:55],a[208:211],a[184:187],v[52:55]
s_add_u32 m0,s67,6144
s_add_u32 s27,s26,896
s_and_b32 s27,s27,0x380
s_add_u32 s27,s27,s66
s_add_u32 s68,s27,0
T a[198:199],v186 offset:28672
M v[68:71],a[208:211],v[236:239],v[68:71]
B v212,s[48:51],s68 offen lds
M v[68:71],a[212:215],v[240:243],v[68:71]
W lgkmcnt(15)
T a[200:201],v186 offset:16392
M v[84:87],a[220:223],a[164:167],v[84:87]
B v213,s[48:51],s68 offen offset:1024 lds
M v[84:87],a[216:219],a[160:163],v[84:87]
T a[202:203],v186 offset:20488
C v168,v160,v161
M v[100:103],a[216:219],a[168:171],v[100:103]
C v169,v162,v163
C v170,v164,v165
M v[100:103],a[220:223],a[172:175],v[100:103]
W vmcnt(7)
T a[204:205],v186 offset:24584
M v[116:119],a[220:223],a[180:183],v[116:119]
R a[240:243],v210
C v171,v166,v167
M v[116:119],a[216:219],a[176:179],v[116:119]
T a[206:207],v186 offset:28680
M v[132:135],a[216:219],a[184:187],v[132:135]
R a[244:247],v211
M v[132:135],a[220:223],a[188:191],v[132:135]
M v[148:151],a[220:223],v[240:243],v[148:151]
M v[148:151],a[216:219],v[236:239],v[148:151]
W lgkmcnt(6)
M v[8:11],a[192:195],a[160:163],v[8:11]
M v[8:11],a[196:199],a[164:167],v[8:11]
W lgkmcnt(0)
s_add_u32 s94,s84,11264
T a[208:209],v187 offset:16384
M v[24:27],a[196:199],a[172:175],v[24:27]
buffer_store_dwordx4 v[172:175],v245,s[52:55],s94 offen sc1
M v[24:27],a[192:195],a[168:171],v[24:27]
s_add_u32 m0,s67,0
s_add_u32 s68,s27,16384
T a[210:211],v187 offset:20480
M v[40:43],a[192:195],a[176:179],v[40:43]
B v212,s[48:51],s68 offen lds
M v[40:43],a[196:199],a[180:183],v[40:43]
T a[212:213],v187 offset:24576
AR v160,a124
M v[56:59],a[196:199],a[188:191],v[56:59]
B v213,s[48:51],s68 offen offset:1024 lds
M v[56:59],a[192:195],a[184:187],v[56:59]
T a[214:215],v187 offset:28672
AR v161,a125
M v[72:75],a[192:195],v[236:239],v[72:75]
AR v162,a126
AR v163,a127
M v[72:75],a[196:199],v[240:243],v[72:75]
W lgkmcnt(15)
W vmcnt(8)
T a[216:217],v187 offset:16392
M v[88:91],a[204:207],a[164:167],v[88:91]
R a[248:251],v210 offset:2048
AR v164,a144
M v[88:91],a[200:203],a[160:163],v[88:91]
T a[218:219],v187 offset:20488
AR v165,a145
M v[104:107],a[200:203],a[168:171],v[104:107]
R a[252:255],v211 offset:2048
AR v166,a146
M v[104:107],a[204:207],a[172:175],v[104:107]
T a[220:221],v187 offset:24584
AR v167,a147
M v[120:123],a[204:207],a[180:183],v[120:123]
U v160,v160,v190
U v161,v161,v190
M v[120:123],a[200:203],a[176:179],v[120:123]
T a[222:223],v187 offset:28680
U v162,v162,v190
M v[136:139],a[200:203],a[184:187],v[136:139]
U v163,v163,v190
U v164,v164,v190
M v[136:139],a[204:207],a[188:191],v[136:139]
W lgkmcnt(2)
s_add_u32 m0,s67,2048
s_add_u32 s68,s27,32768
M v[152:155],a[204:207],v[240:243],v[152:155]
B v212,s[48:51],s68 offen lds
M v[152:155],a[200:203],v[236:239],v[152:155]
W lgkmcnt(15)
U v165,v165,v190
U v166,v166,v190
M v[12:15],a[208:211],a[160:163],v[12:15]
s_barrier
U v167,v167,v190
C v172,v160,v161
M v[12:15],a[212:215],a[164:167],v[12:15]
T a[192:193],v184 offset:32768
C v173,v162,v163
M v[28:31],a[212:215],a[172:175],v[28:31]
B v213,s[48:51],s68 offen offset:1024 lds
M v[28:31],a[208:211],a[168:171],v[28:31]
T a[194:195],v184 offset:36864
C v174,v164,v165
M v[44:47],a[208:211],a[176:179],v[44:47]
C v175,v166,v167
M v[44:47],a[212:215],a[180:183],v[44:47]
W vmcnt(7)
T a[196:197],v184 offset:40960
M v[60:63],a[212:215],a[188:191],v[60:63]
R v[228:231],v210 offset:4096
M v[60:63],a[208:211],a[184:187],v[60:63]
T a[198:199],v184 offset:45056
M v[76:79],a[208:211],v[236:239],v[76:79]
R v[232:235],v211 offset:4096
M v[76:79],a[212:215],v[240:243],v[76:79]
W lgkmcnt(6)
T a[200:201],v184 offset:32776
M v[92:95],a[220:223],a[164:167],v[92:95]
M v[92:95],a[216:219],a[160:163],v[92:95]
T a[202:203],v184 offset:36872
M v[108:111],a[216:219],a[168:171],v[108:111]
M v[108:111],a[220:223],a[172:175],v[108:111]
W lgkmcnt(2)
T a[204:205],v184 offset:40968
M v[124:127],a[220:223],a[180:183],v[124:127]
M v[124:127],a[216:219],a[176:179],v[124:127]
s_add_u32 m0,s67,4096
s_add_u32 s68,s27,49152
T a[206:207],v184 offset:45064
M v[140:143],a[216:219],a[184:187],v[140:143]
B v212,s[48:51],s68 offen lds
M v[140:143],a[220:223],a[188:191],v[140:143]
M v[156:159],a[220:223],v[240:243],v[156:159]
B v213,s[48:51],s68 offen offset:1024 lds
M v[156:159],a[216:219],v[236:239],v[156:159]
W lgkmcnt(15)
s_add_u32 s94,s84,12288
AR v160,a128
M v[0:3],a[192:195],a[224:227],v[0:3]
buffer_store_dwordx4 v[176:179],v245,s[52:55],s94 offen sc1
M v[0:3],a[196:199],a[228:231],v[0:3]
W vmcnt(8)
T a[208:209],v185 offset:32768
M v[16:19],a[196:199],a[236:239],v[16:19]
R a[160:163],v210 offset:6144
AR v161,a129
M v[16:19],a[192:195],a[232:235],v[16:19]
T a[210:211],v185 offset:36864
AR v162,a130
M v[32:35],a[192:195],a[240:243],v[32:35]
R a[164:167],v211 offset:6144
AR v163,a131
M v[32:35],a[196:199],a[244:247],v[32:35]
T a[212:213],v185 offset:40960
AR v164,a148
M v[48:51],a[196:199],a[252:255],v[48:51]
AR v165,a149
AR v166,a150
M v[48:51],a[192:195],a[248:251],v[48:51]
T a[214:215],v185 offset:45056
AR v167,a151
M v[64:67],a[192:195],v[228:231],v[64:67]
U v160,v160,v192
U v161,v161,v192
M v[64:67],a[196:199],v[232:235],v[64:67]
W lgkmcnt(6)
W lgkmcnt(2)
T a[216:217],v185 offset:32776
M v[80:83],a[204:207],a[228:231],v[80:83]
U v162,v162,v192
U v163,v163,v192
M v[80:83],a[200:203],a[224:227],v[80:83]
s_add_u32 m0,s67,6144
s_add_u32 s68,s27,65536
T a[218:219],v185 offset:36872
M v[96:99],a[200:203],a[232:235],v[96:99]
B v212,s[48:51],s68 offen lds
M v[96:99],a[204:207],a[236:239],v[96:99]
T a[220:221],v185 offset:40968
U v164,v164,v192
M v[112:115],a[204:207],a[244:247],v[112:115]
B v213,s[48:51],s68 offen offset:1024 lds
M v[112:115],a[200:203],a[240:243],v[112:115]
s_add_u32 s94,s84,13312
T a[222:223],v185 offset:45064
M v[128:131],a[200:203],a[248:251],v[128:131]
buffer_store_dwordx4 v[180:183],v245,s[52:55],s94 offen sc1
M v[128:131],a[204:207],a[252:255],v[128:131]
W vmcnt(8)
U v165,v165,v192
U v166,v166,v192
M v[144:147],a[204:207],v[232:235],v[144:147]
R a[168:171],v210
U v167,v167,v192
M v[144:147],a[200:203],v[228:231],v[144:147]
W lgkmcnt(5)
C v176,v160,v161
C v177,v162,v163
M v[4:7],a[208:211],a[224:227],v[4:7]
R a[172:175],v211
C v178,v164,v165
M v[4:7],a[212:215],a[228:231],v[4:7]
T a[192:193],v186 offset:32768
C v179,v166,v167
M v[20:23],a[212:215],a[236:239],v[20:23]
M v[20:23],a[208:211],a[232:235],v[20:23]
T a[194:195],v186 offset:36864
M v[36:39],a[208:211],a[240:243],v[36:39]
M v[36:39],a[212:215],a[244:247],v[36:39]
T a[196:197],v186 offset:40960
AR v160,a132
M v[52:55],a[212:215],a[252:255],v[52:55]
AR v161,a133
AR v162,a134
M v[52:55],a[208:211],a[248:251],v[52:55]
T a[198:199],v186 offset:45056
AR v163,a135
M v[68:71],a[208:211],v[228:231],v[68:71]
AR v164,a152
AR v165,a153
M v[68:71],a[212:215],v[232:235],v[68:71]
W lgkmcnt(6)
T a[200:201],v186 offset:32776
M v[84:87],a[220:223],a[228:231],v[84:87]
AR v166,a154
AR v167,a155
M v[84:87],a[216:219],a[224:227],v[84:87]
s_add_u32 s94,s84,14336
T a[202:203],v186 offset:36872
M v[100:103],a[216:219],a[232:235],v[100:103]
buffer_store_dwordx4 v[214:217],v245,s[52:55],s94 offen sc1
M v[100:103],a[220:223],a[236:239],v[100:103]
W vmcnt(7)
T a[204:205],v186 offset:40968
M v[116:119],a[220:223],a[244:247],v[116:119]
R a[176:179],v210 offset:2048
U v160,v160,v194
M v[116:119],a[216:219],a[240:243],v[116:119]
T a[206:207],v186 offset:45064
U v161,v161,v194
M v[132:135],a[216:219],a[248:251],v[132:135]
R a[180:183],v211 offset:2048
U v162,v162,v194
M v[132:135],a[220:223],a[252:255],v[132:135]
U v163,v163,v194
U v164,v164,v194
M v[148:151],a[220:223],v[232:235],v[148:151]
U v165,v165,v194
U v166,v166,v194
M v[148:151],a[216:219],v[228:231],v[148:151]
W lgkmcnt(6)
U v167,v167,v194
C v180,v160,v161
M v[8:11],a[192:195],a[224:227],v[8:11]
C v181,v162,v163
C v182,v164,v165
M v[8:11],a[196:199],a[228:231],v[8:11]
T a[208:209],v187 offset:32768
C v183,v166,v167
M v[24:27],a[196:199],a[236:239],v[24:27]
M v[24:27],a[192:195],a[232:235],v[24:27]
T a[210:211],v187 offset:36864
M v[40:43],a[192:195],a[240:243],v[40:43]
M v[40:43],a[196:199],a[244:247],v[40:43]
T a[212:213],v187 offset:40960
M v[56:59],a[196:199],a[252:255],v[56:59]
M v[56:59],a[192:195],a[248:251],v[56:59]
s_add_u32 s94,s84,15360
T a[214:215],v187 offset:45056
M v[72:75],a[192:195],v[228:231],v[72:75]
buffer_store_dwordx4 v[168:171],v245,s[52:55],s94 offen sc1
M v[72:75],a[196:199],v[232:235],v[72:75]
W lgkmcnt(5)
W vmcnt(6)
T a[216:217],v187 offset:32776
M v[88:91],a[204:207],a[228:231],v[88:91]
R a[184:187],v210 offset:4096
M v[88:91],a[200:203],a[224:227],v[88:91]
T a[218:219],v187 offset:36872
M v[104:107],a[200:203],a[232:235],v[104:107]
R a[188:191],v211 offset:4096
AR v160,a136
M v[104:107],a[204:207],a[236:239],v[104:107]
T a[220:221],v187 offset:40968
AR v161,a137
)ASM"
R"ASM(
M v[120:123],a[204:207],a[244:247],v[120:123]
AR v162,a138
AR v163,a139
M v[120:123],a[200:203],a[240:243],v[120:123]
T a[222:223],v187 offset:45064
AR v164,a156
M v[136:139],a[200:203],a[248:251],v[136:139]
AR v165,a157
AR v166,a158
M v[136:139],a[204:207],a[252:255],v[136:139]
AR v167,a159
U v160,v160,v196
M v[152:155],a[204:207],v[232:235],v[152:155]
U v161,v161,v196
U v162,v162,v196
M v[152:155],a[200:203],v[228:231],v[152:155]
W lgkmcnt(6)
U v163,v163,v196
U v164,v164,v196
M v[12:15],a[208:211],a[224:227],v[12:15]
s_barrier
U v165,v165,v196
U v166,v166,v196
M v[12:15],a[212:215],a[228:231],v[12:15]
T a[192:193],v184 offset:49152
U v167,v167,v196
M v[28:31],a[212:215],a[236:239],v[28:31]
C v214,v160,v161
C v215,v162,v163
M v[28:31],a[208:211],a[232:235],v[28:31]
T a[194:195],v184 offset:53248
C v216,v164,v165
M v[44:47],a[208:211],a[240:243],v[44:47]
C v217,v166,v167
M v[44:47],a[212:215],a[244:247],v[44:47]
W vmcnt(3)
T a[196:197],v184 offset:57344
M v[60:63],a[212:215],a[252:255],v[60:63]
R v[236:239],v210 offset:6144
M v[60:63],a[208:211],a[248:251],v[60:63]
T a[198:199],v184 offset:61440
M v[76:79],a[208:211],v[228:231],v[76:79]
R v[240:243],v211 offset:6144
M v[76:79],a[212:215],v[232:235],v[76:79]
W lgkmcnt(6)
s_add_u32 s94,s84,16384
T a[200:201],v184 offset:49160
M v[92:95],a[220:223],a[228:231],v[92:95]
buffer_store_dwordx4 v[172:175],v245,s[52:55],s94 offen sc1
M v[92:95],a[216:219],a[224:227],v[92:95]
T a[202:203],v184 offset:53256
M v[108:111],a[216:219],a[232:235],v[108:111]
M v[108:111],a[220:223],a[236:239],v[108:111]
T a[204:205],v184 offset:57352
M v[124:127],a[220:223],a[244:247],v[124:127]
M v[124:127],a[216:219],a[240:243],v[124:127]
T a[206:207],v184 offset:61448
M v[140:143],a[216:219],a[248:251],v[140:143]
M v[140:143],a[220:223],a[252:255],v[140:143]
M v[156:159],a[220:223],v[232:235],v[156:159]
M v[156:159],a[216:219],v[228:231],v[156:159]
W lgkmcnt(4)
M v[0:3],a[192:195],a[160:163],v[0:3]
v_mov_b32 v222,0xff800000
v_mov_b32 v223,0xff800000
M v[0:3],a[196:199],a[164:167],v[0:3]
s_add_u32 s94,s84,17408
T a[208:209],v185 offset:49152
M v[16:19],a[196:199],a[172:175],v[16:19]
buffer_store_dwordx4 v[176:179],v245,s[52:55],s94 offen sc1
M v[16:19],a[192:195],a[168:171],v[16:19]
T a[210:211],v185 offset:53248
v_mov_b32 v224,0xff800000
M v[32:35],a[192:195],a[176:179],v[32:35]
v_mov_b32 v225,0xff800000
v_mov_b32 v226,0xff800000
M v[32:35],a[196:199],a[180:183],v[32:35]
T a[212:213],v185 offset:57344
M v[48:51],a[196:199],a[188:191],v[48:51]
M v[48:51],a[192:195],a[184:187],v[48:51]
T a[214:215],v185 offset:61440
M v[64:67],a[192:195],v[236:239],v[64:67]
M v[64:67],a[196:199],v[240:243],v[64:67]
W lgkmcnt(4)
T a[216:217],v185 offset:49160
M v[80:83],a[204:207],a[164:167],v[80:83]
M v[80:83],a[200:203],a[160:163],v[80:83]
T a[218:219],v185 offset:53256
M v[96:99],a[200:203],a[168:171],v[96:99]
M v[96:99],a[204:207],a[172:175],v[96:99]
T a[220:221],v185 offset:57352
M v[112:115],a[204:207],a[180:183],v[112:115]
M v[112:115],a[200:203],a[176:179],v[112:115]
T a[222:223],v185 offset:61448
M v[128:131],a[200:203],a[184:187],v[128:131]
s_add_u32 s94,s84,18432
M v[128:131],a[204:207],a[188:191],v[128:131]
buffer_store_dwordx4 v[180:183],v245,s[52:55],s94 offen sc1
M v[144:147],a[204:207],v[240:243],v[144:147]
U v0,s64,v0
U v1,s64,v1
M v[144:147],a[200:203],v[236:239],v[144:147]
W lgkmcnt(4)
U v2,s64,v2
U v3,s64,v3
M v[4:7],a[208:211],a[160:163],v[4:7]
U v80,s64,v80
U v81,s64,v81
M v[4:7],a[212:215],a[164:167],v[4:7]
T a[192:193],v186 offset:49152
U v82,s64,v82
M v[20:23],a[212:215],a[172:175],v[20:23]
U v83,s64,v83
C v0,v0,v1
M v[20:23],a[208:211],a[168:171],v[20:23]
T a[194:195],v186 offset:53248
C v1,v2,v3
M v[36:39],a[208:211],a[176:179],v[36:39]
C v2,v80,v81
C v3,v82,v83
M v[36:39],a[212:215],a[180:183],v[36:39]
T a[196:197],v186 offset:57344
U v16,s64,v16
M v[52:55],a[212:215],a[188:191],v[52:55]
U v17,s64,v17
U v18,s64,v18
M v[52:55],a[208:211],a[184:187],v[52:55]
T a[198:199],v186 offset:61440
U v19,s64,v19
M v[68:71],a[208:211],v[236:239],v[68:71]
U v96,s64,v96
U v97,s64,v97
M v[68:71],a[212:215],v[240:243],v[68:71]
W lgkmcnt(4)
T a[200:201],v186 offset:49160
M v[84:87],a[220:223],a[164:167],v[84:87]
U v98,s64,v98
U v99,s64,v99
M v[84:87],a[216:219],a[160:163],v[84:87]
T a[202:203],v186 offset:53256
C v16,v16,v17
M v[100:103],a[216:219],a[168:171],v[100:103]
C v17,v18,v19
C v18,v96,v97
M v[100:103],a[220:223],a[172:175],v[100:103]
s_add_u32 s94,s84,19456
T a[204:205],v186 offset:57352
M v[116:119],a[220:223],a[180:183],v[116:119]
buffer_store_dwordx4 v[214:217],v245,s[52:55],s94 offen sc1
M v[116:119],a[216:219],a[176:179],v[116:119]
T a[206:207],v186 offset:61448
C v19,v98,v99
M v[132:135],a[216:219],a[184:187],v[132:135]
U v32,s64,v32
U v33,s64,v33
M v[132:135],a[220:223],a[188:191],v[132:135]
U v34,s64,v34
U v35,s64,v35
M v[148:151],a[220:223],v[240:243],v[148:151]
U v112,s64,v112
U v113,s64,v113
M v[148:151],a[216:219],v[236:239],v[148:151]
W lgkmcnt(4)
U v114,s64,v114
U v115,s64,v115
M v[8:11],a[192:195],a[160:163],v[8:11]
C v32,v32,v33
C v33,v34,v35
M v[8:11],a[196:199],a[164:167],v[8:11]
T a[208:209],v187 offset:49152
C v34,v112,v113
M v[24:27],a[196:199],a[172:175],v[24:27]
C v35,v114,v115
U v48,s64,v48
M v[24:27],a[192:195],a[168:171],v[24:27]
T a[210:211],v187 offset:53248
U v49,s64,v49
M v[40:43],a[192:195],a[176:179],v[40:43]
U v50,s64,v50
U v51,s64,v51
M v[40:43],a[196:199],a[180:183],v[40:43]
T a[212:213],v187 offset:57344
U v128,s64,v128
M v[56:59],a[196:199],a[188:191],v[56:59]
U v129,s64,v129
U v130,s64,v130
M v[56:59],a[192:195],a[184:187],v[56:59]
T a[214:215],v187 offset:61440
U v131,s64,v131
M v[72:75],a[192:195],v[236:239],v[72:75]
C v48,v48,v49
C v49,v50,v51
M v[72:75],a[196:199],v[240:243],v[72:75]
W lgkmcnt(4)
T a[216:217],v187 offset:49160
M v[88:91],a[204:207],a[164:167],v[88:91]
C v50,v128,v129
C v51,v130,v131
M v[88:91],a[200:203],a[160:163],v[88:91]
T a[218:219],v187 offset:53256
U v64,s64,v64
M v[104:107],a[200:203],a[168:171],v[104:107]
U v65,s64,v65
U v66,s64,v66
M v[104:107],a[204:207],a[172:175],v[104:107]
T a[220:221],v187 offset:57352
U v67,s64,v67
M v[120:123],a[204:207],a[180:183],v[120:123]
U v144,s64,v144
U v145,s64,v145
M v[120:123],a[200:203],a[176:179],v[120:123]
T a[222:223],v187 offset:61448
U v146,s64,v146
M v[136:139],a[200:203],a[184:187],v[136:139]
U v147,s64,v147
C v64,v64,v65
M v[136:139],a[204:207],a[188:191],v[136:139]
C v65,v66,v67
C v66,v144,v145
M v[152:155],a[204:207],v[240:243],v[152:155]
C v67,v146,v147
U v4,s64,v4
M v[152:155],a[200:203],v[236:239],v[152:155]
W lgkmcnt(4)
U v5,s64,v5
U v6,s64,v6
M v[12:15],a[208:211],a[160:163],v[12:15]
U v7,s64,v7
U v84,s64,v84
M v[12:15],a[212:215],a[164:167],v[12:15]
U v85,s64,v85
U v86,s64,v86
M v[28:31],a[212:215],a[172:175],v[28:31]
U v87,s64,v87
C v4,v4,v5
M v[28:31],a[208:211],a[168:171],v[28:31]
C v5,v6,v7
C v6,v84,v85
M v[44:47],a[208:211],a[176:179],v[44:47]
C v7,v86,v87
U v20,s64,v20
M v[44:47],a[212:215],a[180:183],v[44:47]
U v21,s64,v21
U v22,s64,v22
M v[60:63],a[212:215],a[188:191],v[60:63]
U v23,s64,v23
U v100,s64,v100
M v[60:63],a[208:211],a[184:187],v[60:63]
U v101,s64,v101
U v102,s64,v102
M v[76:79],a[208:211],v[236:239],v[76:79]
U v103,s64,v103
C v20,v20,v21
M v[76:79],a[212:215],v[240:243],v[76:79]
W lgkmcnt(0)
C v21,v22,v23
C v22,v100,v101
M v[92:95],a[220:223],a[164:167],v[92:95]
C v23,v102,v103
U v36,s64,v36
M v[92:95],a[216:219],a[160:163],v[92:95]
U v37,s64,v37
U v38,s64,v38
M v[108:111],a[216:219],a[168:171],v[108:111]
U v39,s64,v39
U v116,s64,v116
M v[108:111],a[220:223],a[172:175],v[108:111]
U v117,s64,v117
U v118,s64,v118
M v[124:127],a[220:223],a[180:183],v[124:127]
U v119,s64,v119
C v36,v36,v37
M v[124:127],a[216:219],a[176:179],v[124:127]
C v37,v38,v39
C v38,v116,v117
M v[140:143],a[216:219],a[184:187],v[140:143]
C v39,v118,v119
M v[140:143],a[220:223],a[188:191],v[140:143]
M v[156:159],a[220:223],v[240:243],v[156:159]
M v[156:159],a[216:219],v[236:239],v[156:159]
s_add_u32 s84,s84,327680
s_barrier
R a[160:163],v198
R a[164:167],v199
R a[168:171],v200
R a[172:175],v201
s_nop 1
v_pk_mul_f32 v[52:53],v[52:53],s[64:65]
v_pk_mul_f32 v[54:55],v[54:55],s[64:65]
v_pk_mul_f32 v[132:133],v[132:133],s[64:65]
v_pk_mul_f32 v[134:135],v[134:135],s[64:65]
C v52,v52,v53
C v53,v54,v55
C v54,v132,v133
C v55,v134,v135
v_pk_mul_f32 v[68:69],v[68:69],s[64:65]
v_pk_mul_f32 v[70:71],v[70:71],s[64:65]
v_pk_mul_f32 v[148:149],v[148:149],s[64:65]
v_pk_mul_f32 v[150:151],v[150:151],s[64:65]
C v68,v68,v69
)ASM"
R"ASM(
C v69,v70,v71
C v70,v148,v149
C v71,v150,v151
v_pk_mul_f32 v[8:9],v[8:9],s[64:65]
v_pk_mul_f32 v[10:11],v[10:11],s[64:65]
v_pk_mul_f32 v[88:89],v[88:89],s[64:65]
v_pk_mul_f32 v[90:91],v[90:91],s[64:65]
C v8,v8,v9
C v9,v10,v11
C v10,v88,v89
C v11,v90,v91
v_pk_mul_f32 v[24:25],v[24:25],s[64:65]
v_pk_mul_f32 v[26:27],v[26:27],s[64:65]
v_pk_mul_f32 v[104:105],v[104:105],s[64:65]
v_pk_mul_f32 v[106:107],v[106:107],s[64:65]
C v24,v24,v25
C v25,v26,v27
C v26,v104,v105
C v27,v106,v107
v_pk_mul_f32 v[40:41],v[40:41],s[64:65]
v_pk_mul_f32 v[42:43],v[42:43],s[64:65]
v_pk_mul_f32 v[120:121],v[120:121],s[64:65]
v_pk_mul_f32 v[122:123],v[122:123],s[64:65]
C v40,v40,v41
C v41,v42,v43
C v42,v120,v121
C v43,v122,v123
v_pk_mul_f32 v[56:57],v[56:57],s[64:65]
v_pk_mul_f32 v[58:59],v[58:59],s[64:65]
v_pk_mul_f32 v[136:137],v[136:137],s[64:65]
v_pk_mul_f32 v[138:139],v[138:139],s[64:65]
C v56,v56,v57
C v57,v58,v59
C v58,v136,v137
C v59,v138,v139
v_pk_mul_f32 v[72:73],v[72:73],s[64:65]
v_pk_mul_f32 v[74:75],v[74:75],s[64:65]
v_pk_mul_f32 v[152:153],v[152:153],s[64:65]
v_pk_mul_f32 v[154:155],v[154:155],s[64:65]
C v72,v72,v73
C v73,v74,v75
C v74,v152,v153
C v75,v154,v155
v_pk_mul_f32 v[12:13],v[12:13],s[64:65]
v_pk_mul_f32 v[14:15],v[14:15],s[64:65]
v_pk_mul_f32 v[92:93],v[92:93],s[64:65]
v_pk_mul_f32 v[94:95],v[94:95],s[64:65]
C v12,v12,v13
C v13,v14,v15
C v14,v92,v93
C v15,v94,v95
v_pk_mul_f32 v[28:29],v[28:29],s[64:65]
v_pk_mul_f32 v[30:31],v[30:31],s[64:65]
v_pk_mul_f32 v[108:109],v[108:109],s[64:65]
v_pk_mul_f32 v[110:111],v[110:111],s[64:65]
C v28,v28,v29
C v29,v30,v31
C v30,v108,v109
C v31,v110,v111
v_pk_mul_f32 v[44:45],v[44:45],s[64:65]
v_pk_mul_f32 v[46:47],v[46:47],s[64:65]
v_pk_mul_f32 v[124:125],v[124:125],s[64:65]
v_pk_mul_f32 v[126:127],v[126:127],s[64:65]
C v44,v44,v45
C v45,v46,v47
C v46,v124,v125
C v47,v126,v127
v_pk_mul_f32 v[60:61],v[60:61],s[64:65]
v_pk_mul_f32 v[62:63],v[62:63],s[64:65]
v_pk_mul_f32 v[140:141],v[140:141],s[64:65]
v_pk_mul_f32 v[142:143],v[142:143],s[64:65]
C v60,v60,v61
C v61,v62,v63
C v62,v140,v141
C v63,v142,v143
v_pk_mul_f32 v[76:77],v[76:77],s[64:65]
v_pk_mul_f32 v[78:79],v[78:79],s[64:65]
v_pk_mul_f32 v[156:157],v[156:157],s[64:65]
v_pk_mul_f32 v[158:159],v[158:159],s[64:65]
C v76,v76,v77
C v77,v78,v79
C v78,v156,v157
C v79,v158,v159
s_branch LPREa_%=
)ASM"
R"ASM(
LDRAIN1a_%=:
v_smfmac_f32_16x16x64_bf16 a[224:227],a[244:247],v[120:127],v253
E v184,v184
v_smfmac_f32_16x16x64_bf16 a[228:231],a[244:247],v[128:135],v253
E v185,v185
v_smfmac_f32_16x16x64_bf16 a[232:235],a[244:247],v[136:143],v253
E v186,v186
v_smfmac_f32_16x16x64_bf16 a[236:239],a[244:247],v[144:151],v253
E v187,v187
M a[0:3],a[192:195],v[120:123],a[0:3]
s_mul_i32 s79,s4,10240
C v158,v184,v185
C v159,v186,v187
M a[0:3],a[196:199],v[124:127],a[0:3]
s_mov_b32 s33,s84
T a[208:209],v204 offset:49152
M a[4:7],a[196:199],v[132:135],a[4:7]
AR v188,a224
AR v189,a225
v_add_u32 v240,4096,v245
v_smfmac_f32_16x16x64_bf16 a[240:243],a[244:247],v[152:159],v253
M a[4:7],a[192:195],v[128:131],a[4:7]
T a[210:211],v204 offset:53248
AR v190,a228
AR v191,a229
M a[8:11],a[192:195],v[136:139],a[8:11]
AR v192,a232
AR v193,a233
v_add_u32 v241,8192,v245
M a[8:11],a[196:199],v[140:143],a[8:11]
T a[212:213],v204 offset:57344
AR v194,a236
AR v195,a237
M a[12:15],a[196:199],v[148:151],a[12:15]
B a[224:227],v245,s[96:99],s79 offen sc1
M a[12:15],a[192:195],v[144:147],a[12:15]
T a[214:215],v204 offset:61440
AR v196,a240
AR v197,a241
M a[16:19],a[192:195],v[152:155],a[16:19]
F v188,v188,v189
F v190,v190,v191
M a[16:19],a[196:199],v[156:159],a[16:19]
W lgkmcnt(4)
T a[216:217],v205 offset:49152
F v192,v192,v193
M a[20:23],a[204:207],v[124:127],a[20:23]
B a[228:231],v240,s[96:99],s79 offen offset:1024 sc1
F v194,v194,v195
M a[20:23],a[200:203],v[120:123],a[20:23]
T a[218:219],v205 offset:53248
F v196,v196,v197
M a[24:27],a[200:203],v[128:131],a[24:27]
v_mov_b32 v189,v188
v_mov_b32 v191,v190
M a[24:27],a[204:207],v[132:135],a[24:27]
T a[220:221],v205 offset:57344
v_mov_b32 v193,v192
M a[28:31],a[204:207],v[140:143],a[28:31]
B a[232:235],v245,s[96:99],s79 offen offset:1024 sc1
v_mov_b32 v195,v194
M a[28:31],a[200:203],v[136:139],a[28:31]
T a[222:223],v205 offset:61440
v_mov_b32 v197,v196
M a[32:35],a[200:203],v[144:147],a[32:35]
v_permlane32_swap_b32 v188,v189
v_permlane32_swap_b32 v190,v191
M a[32:35],a[204:207],v[148:151],a[32:35]
B a[236:239],v240,s[96:99],s79 offen offset:2048 sc1
v_permlane32_swap_b32 v192,v193
M a[36:39],a[204:207],v[156:159],a[36:39]
v_permlane32_swap_b32 v194,v195
v_permlane32_swap_b32 v196,v197
M a[36:39],a[200:203],v[152:155],a[36:39]
W lgkmcnt(4)
F v188,v188,v189
F v190,v190,v191
M a[40:43],a[208:211],v[120:123],a[40:43]
B a[240:243],v245,s[96:99],s79 offen offset:2048 sc1
F v192,v192,v193
M a[40:43],a[212:215],v[124:127],a[40:43]
T a[192:193],v206 offset:49152
F v194,v194,v195
M a[44:47],a[212:215],v[132:135],a[44:47]
F v196,v196,v197
v_mov_b32 v189,v188
M a[44:47],a[208:211],v[128:131],a[44:47]
T a[194:195],v206 offset:53248
v_mov_b32 v191,v190
M a[48:51],a[208:211],v[136:139],a[48:51]
B a[244:247],v240,s[96:99],s79 offen offset:3072 sc1
v_mov_b32 v193,v192
M a[48:51],a[212:215],v[140:143],a[48:51]
T a[196:197],v206 offset:57344
v_mov_b32 v195,v194
M a[52:55],a[212:215],v[148:151],a[52:55]
v_mov_b32 v197,v196
v_permlane16_swap_b32 v188,v189
M a[52:55],a[208:211],v[144:147],a[52:55]
T a[198:199],v206 offset:61440
v_permlane16_swap_b32 v190,v191
M a[56:59],a[208:211],v[152:155],a[56:59]
B a[248:251],v245,s[96:99],s79 offen offset:3072 sc1
v_permlane16_swap_b32 v192,v193
M a[56:59],a[212:215],v[156:159],a[56:59]
W lgkmcnt(4)
T a[200:201],v207 offset:49152
v_permlane16_swap_b32 v194,v195
M a[60:63],a[220:223],v[124:127],a[60:63]
v_permlane16_swap_b32 v196,v197
F v188,v188,v189
M a[60:63],a[216:219],v[120:123],a[60:63]
T a[202:203],v207 offset:53248
F v190,v190,v191
M a[64:67],a[216:219],v[128:131],a[64:67]
B a[252:255],v241,s[96:99],s79 offen sc1
F v192,v192,v193
M a[64:67],a[220:223],v[132:135],a[64:67]
T a[204:205],v207 offset:57344
F v194,v194,v195
M a[68:71],a[220:223],v[140:143],a[68:71]
F v196,v196,v197
F v250,v188,v190
M a[68:71],a[216:219],v[136:139],a[68:71]
s_or_b32 s34,s79,s35
T a[206:207],v207 offset:61440
F v251,v192,v194
M a[72:75],a[216:219],v[144:147],a[72:75]
B v[228:231],v240,s[96:99],s34 offen sc1
F v250,v250,v196
M a[72:75],a[220:223],v[148:151],a[72:75]
F v250,v250,v251
v_mov_b32 v252,s87
M a[76:79],a[220:223],v[156:159],a[76:79]
v_cmp_ngt_f32 vcc,0x5f800000,v250
v_lshlrev_b32 v252,2,v252
M a[76:79],a[216:219],v[152:155],a[76:79]
W lgkmcnt(4)
B v[232:235],v241,s[96:99],s34 offen offset:1024 sc1
s_add_u32 s79,s79,40960
s_or_b32 s95,vcc_lo,vcc_hi
M a[80:83],a[192:195],v[120:123],a[80:83]
v_add_u32 v252,0x27000,v252
v_mov_b32 v251,s95
M a[80:83],a[196:199],v[124:127],a[80:83]
T a[208:209],v208 offset:49152
ds_write_b32 v252,v251
v_mov_b32 v253,0x27000
M a[84:87],a[196:199],v[132:135],a[84:87]
B v[80:83],v245,s[96:99],s79 offen sc1
v_rcp_f32 v188,v188
M a[84:87],a[192:195],v[128:131],a[84:87]
T a[210:211],v208 offset:53248
v_rcp_f32 v190,v190
M a[88:91],a[192:195],v[136:139],a[88:91]
v_rcp_f32 v192,v192
M a[88:91],a[196:199],v[140:143],a[88:91]
T a[212:213],v208 offset:57344
v_rcp_f32 v194,v194
M a[92:95],a[196:199],v[148:151],a[92:95]
B v[84:87],v240,s[96:99],s79 offen offset:1024 sc1
v_rcp_f32 v196,v196
M a[92:95],a[192:195],v[144:147],a[92:95]
T a[214:215],v208 offset:61440
M a[96:99],a[192:195],v[152:155],a[96:99]
M a[96:99],a[196:199],v[156:159],a[96:99]
W lgkmcnt(5)
T a[216:217],v209 offset:49152
M a[100:103],a[204:207],v[124:127],a[100:103]
B v[88:91],v245,s[96:99],s79 offen offset:1024 sc1
M a[100:103],a[200:203],v[120:123],a[100:103]
T a[218:219],v209 offset:53248
M a[104:107],a[200:203],v[128:131],a[104:107]
M a[104:107],a[204:207],v[132:135],a[104:107]
T a[220:221],v209 offset:57344
M a[108:111],a[204:207],v[140:143],a[108:111]
B v[92:95],v240,s[96:99],s79 offen offset:2048 sc1
M a[108:111],a[200:203],v[136:139],a[108:111]
T a[222:223],v209 offset:61440
M a[112:115],a[200:203],v[144:147],a[112:115]
M a[112:115],a[204:207],v[148:151],a[112:115]
B v[96:99],v245,s[96:99],s79 offen offset:2048 sc1
M a[116:119],a[204:207],v[156:159],a[116:119]
M a[116:119],a[200:203],v[152:155],a[116:119]
W lgkmcnt(4)
M a[120:123],a[208:211],v[120:123],a[120:123]
W lgkmcnt(0)
s_barrier
R v[180:183],v253
M a[120:123],a[212:215],v[124:127],a[120:123]
s_and_b32 s94,s4,1
s_mul_i32 s94,s94,65664
B v[100:103],v240,s[96:99],s79 offen offset:3072 sc1
M a[124:127],a[212:215],v[132:135],a[124:127]
s_and_b32 s81,s4,2
s_lshl_b32 s81,s81,5
M a[124:127],a[208:211],v[128:131],a[124:127]
s_xor_b32 s81,s81,s94
s_xor_b32 s81,s81,0x10000
M a[128:131],a[208:211],v[136:139],a[128:131]
B v[104:107],v245,s[96:99],s79 offen offset:3072 sc1
v_xor_b32 v160,s81,v202
M a[128:131],a[212:215],v[140:143],a[128:131]
v_xor_b32 v161,s81,v203
M a[132:135],a[212:215],v[148:151],a[132:135]
W lgkmcnt(0)
v_xor_b32 v162,s81,v204
v_or3_b32 v250,v180,v181,v182
M a[132:135],a[208:211],v[144:147],a[132:135]
B v[108:111],v241,s[96:99],s79 offen sc1
v_xor_b32 v163,s81,v205
v_or_b32 v250,v250,v183
M a[136:139],a[208:211],v[152:155],a[136:139]
v_xor_b32 v164,s81,v206
v_readfirstlane_b32 s95,v250
M a[136:139],a[212:215],v[156:159],a[136:139]
W lgkmcnt(15)
s_or_b32 s34,s79,s35
v_xor_b32 v165,s81,v207
M a[140:143],a[220:223],v[124:127],a[140:143]
s_cmp_eq_u32 s71,0
s_cselect_b32 s95,s95,0
B v[112:115],v240,s[96:99],s34 offen sc1
v_xor_b32 v166,s81,v208
M a[140:143],a[216:219],v[120:123],a[140:143]
v_xor_b32 v167,s81,v209
M a[144:147],a[216:219],v[128:131],a[144:147]
M a[144:147],a[220:223],v[132:135],a[144:147]
B v[116:119],v241,s[96:99],s34 offen offset:1024 sc1
s_add_u32 s79,s79,40960
M a[148:151],a[220:223],v[140:143],a[148:151]
M a[148:151],a[216:219],v[136:139],a[148:151]
M a[152:155],a[216:219],v[144:147],a[152:155]
M a[152:155],a[220:223],v[148:151],a[152:155]
M a[156:159],a[220:223],v[156:159],a[156:159]
M a[156:159],a[216:219],v[152:155],a[156:159]
s_cmp_lg_u32 s95,0
s_cbranch_scc1 LFBa_%=
)ASM"
R"ASM(
s_cmp_eq_u32 s91,3
s_cbranch_scc1 LOP4_%=
s_branch LOP5_%=
)ASM"
R"ASM(
LFBa_%=:
s_mov_b32 s71,1
W vmcnt(0)
W lgkmcnt(0)
s_barrier
s_mov_b32 s69,0
)ASM"
R"ASM(
LFBLa_%=:
s_barrier
R a[160:163],v198
R a[164:167],v199
R a[168:171],v200
R a[172:175],v201
W lgkmcnt(0)
M v[120:123],a[160:163],v[0:3],0
M v[120:123],a[164:167],v[4:7],v[120:123]
M v[120:123],a[168:171],v[8:11],v[120:123]
M v[120:123],a[172:175],v[12:15],v[120:123]
M v[124:127],a[160:163],v[16:19],0
M v[124:127],a[164:167],v[20:23],v[124:127]
M v[124:127],a[168:171],v[24:27],v[124:127]
M v[124:127],a[172:175],v[28:31],v[124:127]
M v[128:131],a[160:163],v[32:35],0
M v[128:131],a[164:167],v[36:39],v[128:131]
M v[128:131],a[168:171],v[40:43],v[128:131]
M v[128:131],a[172:175],v[44:47],v[128:131]
M v[132:135],a[160:163],v[48:51],0
M v[132:135],a[164:167],v[52:55],v[132:135]
M v[132:135],a[168:171],v[56:59],v[132:135]
M v[132:135],a[172:175],v[60:63],v[132:135]
M v[136:139],a[160:163],v[64:67],0
M v[136:139],a[164:167],v[68:71],v[136:139]
M v[136:139],a[168:171],v[72:75],v[136:139]
M v[136:139],a[172:175],v[76:79],v[136:139]
s_nop 7
v_max3_f32 v222,v120,v121,v222
v_max3_f32 v222,v122,v123,v222
v_max3_f32 v223,v124,v125,v223
v_max3_f32 v223,v126,v127,v223
v_max3_f32 v224,v128,v129,v224
v_max3_f32 v224,v130,v131,v224
v_max3_f32 v225,v132,v133,v225
v_max3_f32 v225,v134,v135,v225
v_max3_f32 v226,v136,v137,v226
v_max3_f32 v226,v138,v139,v226
R a[176:179],v198 offset:4096
R a[180:183],v199 offset:4096
R a[184:187],v200 offset:4096
R a[188:191],v201 offset:4096
W lgkmcnt(0)
M v[120:123],a[176:179],v[0:3],0
M v[120:123],a[180:183],v[4:7],v[120:123]
M v[120:123],a[184:187],v[8:11],v[120:123]
M v[120:123],a[188:191],v[12:15],v[120:123]
M v[124:127],a[176:179],v[16:19],0
M v[124:127],a[180:183],v[20:23],v[124:127]
M v[124:127],a[184:187],v[24:27],v[124:127]
M v[124:127],a[188:191],v[28:31],v[124:127]
M v[128:131],a[176:179],v[32:35],0
M v[128:131],a[180:183],v[36:39],v[128:131]
M v[128:131],a[184:187],v[40:43],v[128:131]
M v[128:131],a[188:191],v[44:47],v[128:131]
M v[132:135],a[176:179],v[48:51],0
M v[132:135],a[180:183],v[52:55],v[132:135]
M v[132:135],a[184:187],v[56:59],v[132:135]
M v[132:135],a[188:191],v[60:63],v[132:135]
M v[136:139],a[176:179],v[64:67],0
M v[136:139],a[180:183],v[68:71],v[136:139]
M v[136:139],a[184:187],v[72:75],v[136:139]
M v[136:139],a[188:191],v[76:79],v[136:139]
s_nop 7
v_max3_f32 v222,v120,v121,v222
v_max3_f32 v222,v122,v123,v222
v_max3_f32 v223,v124,v125,v223
v_max3_f32 v223,v126,v127,v223
v_max3_f32 v224,v128,v129,v224
v_max3_f32 v224,v130,v131,v224
v_max3_f32 v225,v132,v133,v225
v_max3_f32 v225,v134,v135,v225
v_max3_f32 v226,v136,v137,v226
v_max3_f32 v226,v138,v139,v226
R a[160:163],v198 offset:8192
R a[164:167],v199 offset:8192
R a[168:171],v200 offset:8192
R a[172:175],v201 offset:8192
W lgkmcnt(0)
M v[120:123],a[160:163],v[0:3],0
M v[120:123],a[164:167],v[4:7],v[120:123]
M v[120:123],a[168:171],v[8:11],v[120:123]
M v[120:123],a[172:175],v[12:15],v[120:123]
M v[124:127],a[160:163],v[16:19],0
M v[124:127],a[164:167],v[20:23],v[124:127]
M v[124:127],a[168:171],v[24:27],v[124:127]
M v[124:127],a[172:175],v[28:31],v[124:127]
M v[128:131],a[160:163],v[32:35],0
M v[128:131],a[164:167],v[36:39],v[128:131]
M v[128:131],a[168:171],v[40:43],v[128:131]
M v[128:131],a[172:175],v[44:47],v[128:131]
M v[132:135],a[160:163],v[48:51],0
M v[132:135],a[164:167],v[52:55],v[132:135]
M v[132:135],a[168:171],v[56:59],v[132:135]
M v[132:135],a[172:175],v[60:63],v[132:135]
M v[136:139],a[160:163],v[64:67],0
M v[136:139],a[164:167],v[68:71],v[136:139]
M v[136:139],a[168:171],v[72:75],v[136:139]
M v[136:139],a[172:175],v[76:79],v[136:139]
s_nop 7
v_max3_f32 v222,v120,v121,v222
v_max3_f32 v222,v122,v123,v222
v_max3_f32 v223,v124,v125,v223
v_max3_f32 v223,v126,v127,v223
v_max3_f32 v224,v128,v129,v224
v_max3_f32 v224,v130,v131,v224
v_max3_f32 v225,v132,v133,v225
v_max3_f32 v225,v134,v135,v225
v_max3_f32 v226,v136,v137,v226
v_max3_f32 v226,v138,v139,v226
R a[176:179],v198 offset:12288
R a[180:183],v199 offset:12288
R a[184:187],v200 offset:12288
R a[188:191],v201 offset:12288
W lgkmcnt(0)
M v[120:123],a[176:179],v[0:3],0
M v[120:123],a[180:183],v[4:7],v[120:123]
M v[120:123],a[184:187],v[8:11],v[120:123]
M v[120:123],a[188:191],v[12:15],v[120:123]
M v[124:127],a[176:179],v[16:19],0
M v[124:127],a[180:183],v[20:23],v[124:127]
M v[124:127],a[184:187],v[24:27],v[124:127]
M v[124:127],a[188:191],v[28:31],v[124:127]
M v[128:131],a[176:179],v[32:35],0
M v[128:131],a[180:183],v[36:39],v[128:131]
M v[128:131],a[184:187],v[40:43],v[128:131]
M v[128:131],a[188:191],v[44:47],v[128:131]
M v[132:135],a[176:179],v[48:51],0
M v[132:135],a[180:183],v[52:55],v[132:135]
M v[132:135],a[184:187],v[56:59],v[132:135]
M v[132:135],a[188:191],v[60:63],v[132:135]
M v[136:139],a[176:179],v[64:67],0
M v[136:139],a[180:183],v[68:71],v[136:139]
M v[136:139],a[184:187],v[72:75],v[136:139]
M v[136:139],a[188:191],v[76:79],v[136:139]
s_nop 7
v_max3_f32 v222,v120,v121,v222
v_max3_f32 v222,v122,v123,v222
v_max3_f32 v223,v124,v125,v223
v_max3_f32 v223,v126,v127,v223
v_max3_f32 v224,v128,v129,v224
v_max3_f32 v224,v130,v131,v224
v_max3_f32 v225,v132,v133,v225
v_max3_f32 v225,v134,v135,v225
v_max3_f32 v226,v136,v137,v226
v_max3_f32 v226,v138,v139,v226
s_barrier
s_add_u32 s69,s69,1
s_cmp_lt_u32 s69,16
s_cbranch_scc1 LFBLa_%=
)ASM"
R"ASM(
s_cmp_eq_u32 s70,1
s_cbranch_scc1 LFBX_%=
s_add_u32 m0,s67,0
s_add_u32 s27,s26,0
s_and_b32 s27,s27,0x380
s_add_u32 s27,s27,s66
s_add_u32 s68,s27,0
B v212,s[48:51],s68 offen lds
B v213,s[48:51],s68 offen offset:1024 lds
s_add_u32 m0,s67,2048
s_add_u32 s68,s27,16384
B v212,s[48:51],s68 offen lds
B v213,s[48:51],s68 offen offset:1024 lds
s_add_u32 m0,s67,4096
s_add_u32 s68,s27,32768
B v212,s[48:51],s68 offen lds
B v213,s[48:51],s68 offen offset:1024 lds
s_add_u32 m0,s67,6144
s_add_u32 s68,s27,49152
B v212,s[48:51],s68 offen lds
B v213,s[48:51],s68 offen offset:1024 lds
LFBX_%=:
s_barrier
R a[160:163],v198
R a[164:167],v199
R a[168:171],v200
R a[172:175],v201
s_branch LPREa_%=
)ASM"
R"ASM(
LQ0bw_%=:
W lgkmcnt(4)
s_mov_b32 m0,0xe000
s_add_u32 s27,s26,128
s_and_b32 s27,s27,0x380
s_add_u32 s27,s27,s66
s_add_u32 s68,s27,0
M v[0:3],a[192:195],a[224:227],0
B v212,s[48:51],s68 offen lds
M v[0:3],a[196:199],a[228:231],v[0:3]
T a[208:209],v185
M v[16:19],a[196:199],a[236:239],0
B v213,s[48:51],s68 offen offset:1024 lds
M v[16:19],a[192:195],a[232:235],v[16:19]
s_mov_b32 m0,0xe800
s_add_u32 s68,s27,16384
T a[210:211],v185 offset:4096
M v[32:35],a[192:195],a[240:243],0
B v212,s[48:51],s68 offen lds
M v[32:35],a[196:199],a[244:247],v[32:35]
T a[212:213],v185 offset:8192
M v[48:51],a[196:199],a[252:255],0
B v213,s[48:51],s68 offen offset:1024 lds
M v[48:51],a[192:195],a[248:251],v[48:51]
W lgkmcnt(3)
s_mov_b32 m0,0xf000
s_add_u32 s68,s27,32768
T a[214:215],v185 offset:12288
M v[80:83],a[200:203],a[224:227],0
B v212,s[48:51],s68 offen lds
M v[80:83],a[204:207],a[228:231],v[80:83]
T a[216:217],v185 offset:8
M v[96:99],a[204:207],a[236:239],0
B v213,s[48:51],s68 offen offset:1024 lds
M v[96:99],a[200:203],a[232:235],v[96:99]
T a[218:219],v185 offset:4104
s_cmp_lt_u32 s81,2300
s_cbranch_scc1 LTX0w_%=
v_lshlrev_b32 v252,3,v245
s_mul_i32 s95,s28,19456
s_add_u32 s95,s95,s66
s_sub_u32 s95,s95,245760
s_mov_b64 exec,-1
buffer_load_dword v254,v252,s[48:51],s95 offen sc1
s_mov_b64 exec,-1
LTX0w_%=:
s_mov_b32 m0,0xf800
s_add_u32 s68,s27,49152
M v[112:115],a[200:203],a[240:243],0
B v212,s[48:51],s68 offen lds
M v[112:115],a[204:207],a[244:247],v[112:115]
T a[220:221],v185 offset:8200
M v[128:131],a[204:207],a[252:255],0
B v213,s[48:51],s68 offen offset:1024 lds
M v[128:131],a[200:203],a[248:251],v[128:131]
W lgkmcnt(3)
s_add_u32 m0,s67,0
s_add_u32 s27,s26,256
s_and_b32 s27,s27,0x380
s_add_u32 s27,s27,s66
s_add_u32 s68,s27,0
T a[222:223],v185 offset:12296
M v[4:7],a[208:211],a[224:227],0
B v212,s[48:51],s68 offen lds
M v[4:7],a[212:215],a[228:231],v[4:7]
T a[192:193],v186
M v[20:23],a[212:215],a[236:239],0
B v213,s[48:51],s68 offen offset:1024 lds
M v[20:23],a[208:211],a[232:235],v[20:23]
s_add_u32 m0,s67,2048
s_add_u32 s68,s27,16384
T a[194:195],v186 offset:4096
M v[36:39],a[208:211],a[240:243],0
B v212,s[48:51],s68 offen lds
M v[36:39],a[212:215],a[244:247],v[36:39]
T a[196:197],v186 offset:8192
s_cmp_lt_u32 s81,2300
s_cbranch_scc1 LTX1w_%=
v_lshlrev_b32 v252,3,v245
s_mul_i32 s95,s28,19456
s_add_u32 s95,s95,s66
s_sub_u32 s95,s95,237568
s_mov_b64 exec,-1
buffer_load_dword v254,v252,s[48:51],s95 offen sc1
s_mov_b64 exec,-1
LTX1w_%=:
M v[52:55],a[212:215],a[252:255],0
B v213,s[48:51],s68 offen offset:1024 lds
M v[52:55],a[208:211],a[248:251],v[52:55]
W lgkmcnt(3)
s_add_u32 m0,s67,4096
s_add_u32 s68,s27,32768
T a[198:199],v186 offset:12288
M v[84:87],a[216:219],a[224:227],0
B v212,s[48:51],s68 offen lds
M v[84:87],a[220:223],a[228:231],v[84:87]
T a[200:201],v186 offset:8
M v[100:103],a[220:223],a[236:239],0
B v213,s[48:51],s68 offen offset:1024 lds
M v[100:103],a[216:219],a[232:235],v[100:103]
s_add_u32 m0,s67,6144
s_add_u32 s68,s27,49152
T a[202:203],v186 offset:4104
M v[116:119],a[216:219],a[240:243],0
B v212,s[48:51],s68 offen lds
M v[116:119],a[220:223],a[244:247],v[116:119]
T a[204:205],v186 offset:8200
M v[132:135],a[220:223],a[252:255],0
B v213,s[48:51],s68 offen offset:1024 lds
s_mov_b32 m0,0x18000
M v[132:135],a[216:219],a[248:251],v[132:135]
W lgkmcnt(3)
W vmcnt(14)
T a[206:207],v186 offset:12296
R a[16:19],v68
B v218,s[44:47],s61 offen lds
s_cmp_lt_u32 s81,2300
s_cbranch_scc1 LTX2w_%=
v_lshlrev_b32 v252,3,v245
s_mul_i32 s95,s28,19456
s_add_u32 s95,s95,s66
s_sub_u32 s95,s95,229376
s_mov_b64 exec,0xffffff
buffer_load_dword v254,v252,s[48:51],s95 offen sc1
s_mov_b64 exec,-1
LTX2w_%=:
M v[8:11],a[192:195],a[224:227],0
M v[8:11],a[196:199],a[228:231],v[8:11]
T a[208:209],v187
R a[36:39],v69
M v[24:27],a[196:199],a[236:239],0
B v219,s[44:47],s61 offen offset:1024 lds
M v[24:27],a[192:195],a[232:235],v[24:27]
T a[210:211],v187 offset:4096
M v[40:43],a[192:195],a[240:243],0
B v220,s[44:47],s61 offen offset:2048 lds
M v[40:43],a[196:199],a[244:247],v[40:43]
W lgkmcnt(1)
W vmcnt(15)
s_mov_b32 m0,0xe000
s_add_u32 s27,s26,384
s_and_b32 s27,s27,0x380
s_add_u32 s27,s27,s66
s_add_u32 s68,s27,0
T a[212:213],v187 offset:8192
R a[56:59],v68 offset:2048
M v[56:59],a[196:199],a[252:255],0
B v212,s[48:51],s68 offen lds
s_mov_b32 m0,0x18000
M v[56:59],a[192:195],a[248:251],v[56:59]
W lgkmcnt(15)
T a[214:215],v187 offset:12288
R a[76:79],v69 offset:2048
B v221,s[44:47],s61 offen offset:3072 lds
s_add_u32 s61,s61,s62
s_mov_b32 m0,0xe000
M v[88:91],a[200:203],a[224:227],0
B v213,s[48:51],s68 offen offset:1024 lds
M v[88:91],a[204:207],a[228:231],v[88:91]
s_mov_b32 m0,0x19000
T a[216:217],v187 offset:8
M v[104:107],a[204:207],a[236:239],0
B v218,s[44:47],s61 offen lds
M v[104:107],a[200:203],a[232:235],v[104:107]
W lgkmcnt(1)
W vmcnt(17)
s_mov_b32 m0,0xe800
s_add_u32 s68,s27,16384
T a[218:219],v187 offset:4104
R a[96:99],v68 offset:4096
M v[120:123],a[200:203],a[240:243],0
B v212,s[48:51],s68 offen lds
s_mov_b32 m0,0x19000
M v[120:123],a[204:207],a[244:247],v[120:123]
T a[220:221],v187 offset:8200
R a[116:119],v69 offset:4096
B v219,s[44:47],s61 offen offset:1024 lds
s_mov_b32 m0,0xe800
M v[136:139],a[204:207],a[252:255],0
B v213,s[48:51],s68 offen offset:1024 lds
M v[136:139],a[200:203],a[248:251],v[136:139]
W lgkmcnt(15)
T a[222:223],v187 offset:12296
M v[12:15],a[208:211],a[224:227],0
W vmcnt(63)
s_barrier
s_mov_b32 m0,0x19000
M v[12:15],a[212:215],a[228:231],v[12:15]
W lgkmcnt(1)
T a[192:193],v184 offset:16384
B v220,s[44:47],s61 offen offset:2048 lds
s_mov_b32 m0,0xf000
s_add_u32 s68,s27,32768
M v[28:31],a[212:215],a[236:239],0
B v212,s[48:51],s68 offen lds
s_mov_b32 m0,0x19000
M v[28:31],a[208:211],a[232:235],v[28:31]
W vmcnt(20)
T a[194:195],v184 offset:20480
R a[136:139],v68 offset:6144
B v221,s[44:47],s61 offen offset:3072 lds
s_add_u32 s61,s61,s63
s_mov_b32 m0,0xf000
M v[44:47],a[208:211],a[240:243],0
B v213,s[48:51],s68 offen offset:1024 lds
M v[44:47],a[212:215],a[244:247],v[44:47]
s_mov_b32 m0,0x1a000
T a[196:197],v184 offset:24576
R a[156:159],v69 offset:6144
M v[60:63],a[212:215],a[252:255],0
B v218,s[44:47],s61 offen lds
M v[60:63],a[208:211],a[248:251],v[60:63]
W lgkmcnt(5)
T a[198:199],v184 offset:28672
M v[92:95],a[216:219],a[224:227],0
M v[92:95],a[220:223],a[228:231],v[92:95]
W lgkmcnt(1)
T a[200:201],v184 offset:16392
B v219,s[44:47],s61 offen offset:1024 lds
s_mov_b32 m0,0xf800
s_add_u32 s68,s27,49152
M v[108:111],a[220:223],a[236:239],0
B v212,s[48:51],s68 offen lds
M v[108:111],a[216:219],a[232:235],v[108:111]
T a[202:203],v184 offset:20488
M v[124:127],a[216:219],a[240:243],0
B v213,s[48:51],s68 offen offset:1024 lds
s_mov_b32 m0,0x1a000
M v[124:127],a[220:223],a[244:247],v[124:127]
T a[204:205],v184 offset:24584
B v220,s[44:47],s61 offen offset:2048 lds
M v[140:143],a[220:223],a[252:255],0
M v[140:143],a[216:219],a[248:251],v[140:143]
W lgkmcnt(3)
T a[206:207],v184 offset:28680
M v[0:3],a[192:195],a[16:19],v[0:3]
B v221,s[44:47],s61 offen offset:3072 lds
s_add_u32 s61,s61,s62
M v[0:3],a[196:199],a[36:39],v[0:3]
W vmcnt(26)
s_mov_b32 m0,0x1b000
T a[208:209],v185 offset:16384
M v[16:19],a[196:199],a[76:79],v[16:19]
R a[224:227],v210
B v218,s[44:47],s61 offen lds
M v[16:19],a[192:195],a[56:59],v[16:19]
T a[210:211],v185 offset:20480
M v[32:35],a[192:195],a[96:99],v[32:35]
R a[228:231],v211
M v[32:35],a[196:199],a[116:119],v[32:35]
T a[212:213],v185 offset:24576
M v[48:51],a[196:199],a[156:159],v[48:51]
B v219,s[44:47],s61 offen offset:1024 lds
M v[48:51],a[192:195],a[136:139],v[48:51]
W lgkmcnt(5)
T a[214:215],v185 offset:28672
M v[80:83],a[200:203],a[16:19],v[80:83]
B v220,s[44:47],s61 offen offset:2048 lds
M v[80:83],a[204:207],a[36:39],v[80:83]
W lgkmcnt(2)
T a[216:217],v185 offset:16392
M v[96:99],a[204:207],a[76:79],v[96:99]
M v[96:99],a[200:203],a[56:59],v[96:99]
T a[218:219],v185 offset:20488
B v221,s[44:47],s61 offen offset:3072 lds
s_add_u32 s61,s61,s75
s_and_b32 s61,s61,s74
s_add_u32 m0,s67,0
s_add_u32 s27,s26,512
s_and_b32 s27,s27,0x380
s_add_u32 s27,s27,s66
s_add_u32 s68,s27,0
M v[112:115],a[200:203],a[96:99],v[112:115]
B v212,s[48:51],s68 offen lds
s_mov_b32 m0,0x1c000
M v[112:115],a[204:207],a[116:119],v[112:115]
T a[220:221],v185 offset:24584
B v218,s[44:47],s61 offen lds
s_add_u32 m0,s67,0
M v[128:131],a[204:207],a[156:159],v[128:131]
B v213,s[48:51],s68 offen offset:1024 lds
M v[128:131],a[200:203],a[136:139],v[128:131]
W lgkmcnt(3)
s_mov_b32 m0,0x1c000
T a[222:223],v185 offset:28680
M v[4:7],a[208:211],a[16:19],v[4:7]
B v219,s[44:47],s61 offen offset:1024 lds
M v[4:7],a[212:215],a[36:39],v[4:7]
W vmcnt(32)
T a[192:193],v186 offset:16384
B v220,s[44:47],s61 offen offset:2048 lds
M v[20:23],a[212:215],a[76:79],v[20:23]
R a[232:235],v210 offset:2048
M v[20:23],a[208:211],a[56:59],v[20:23]
T a[194:195],v186 offset:20480
B v221,s[44:47],s61 offen offset:3072 lds
s_add_u32 s61,s61,s62
M v[36:39],a[208:211],a[96:99],v[36:39]
R a[236:239],v211 offset:2048
M v[36:39],a[212:215],a[116:119],v[36:39]
)ASM"
R"ASM(
s_mov_b32 m0,0x1d000
T a[196:197],v186 offset:24576
M v[52:55],a[212:215],a[156:159],v[52:55]
B v218,s[44:47],s61 offen lds
M v[52:55],a[208:211],a[136:139],v[52:55]
W lgkmcnt(5)
T a[198:199],v186 offset:28672
M v[84:87],a[216:219],a[16:19],v[84:87]
B v219,s[44:47],s61 offen offset:1024 lds
M v[84:87],a[220:223],a[36:39],v[84:87]
W lgkmcnt(2)
T a[200:201],v186 offset:16392
M v[100:103],a[220:223],a[76:79],v[100:103]
B v220,s[44:47],s61 offen offset:2048 lds
M v[100:103],a[216:219],a[56:59],v[100:103]
T a[202:203],v186 offset:20488
B v221,s[44:47],s61 offen offset:3072 lds
s_add_u32 s61,s61,s63
s_add_u32 m0,s67,2048
s_add_u32 s68,s27,16384
M v[116:119],a[216:219],a[96:99],v[116:119]
B v212,s[48:51],s68 offen lds
s_mov_b32 m0,0x1e000
M v[116:119],a[220:223],a[116:119],v[116:119]
T a[204:205],v186 offset:24584
B v218,s[44:47],s61 offen lds
s_add_u32 m0,s67,2048
M v[132:135],a[220:223],a[156:159],v[132:135]
B v213,s[48:51],s68 offen offset:1024 lds
M v[132:135],a[216:219],a[136:139],v[132:135]
W lgkmcnt(3)
s_mov_b32 m0,0x1e000
T a[206:207],v186 offset:28680
M v[8:11],a[192:195],a[16:19],v[8:11]
B v219,s[44:47],s61 offen offset:1024 lds
M v[8:11],a[196:199],a[36:39],v[8:11]
W vmcnt(40)
T a[208:209],v187 offset:16384
B v220,s[44:47],s61 offen offset:2048 lds
M v[24:27],a[196:199],a[76:79],v[24:27]
R a[240:243],v210 offset:4096
M v[24:27],a[192:195],a[56:59],v[24:27]
T a[210:211],v187 offset:20480
B v221,s[44:47],s61 offen offset:3072 lds
s_add_u32 s61,s61,s62
M v[40:43],a[192:195],a[96:99],v[40:43]
R a[244:247],v211 offset:4096
M v[40:43],a[196:199],a[116:119],v[40:43]
s_mov_b32 m0,0x1f000
T a[212:213],v187 offset:24576
M v[56:59],a[196:199],a[156:159],v[56:59]
B v218,s[44:47],s61 offen lds
M v[56:59],a[192:195],a[136:139],v[56:59]
W lgkmcnt(5)
T a[214:215],v187 offset:28672
M v[88:91],a[200:203],a[16:19],v[88:91]
B v219,s[44:47],s61 offen offset:1024 lds
M v[88:91],a[204:207],a[36:39],v[88:91]
W lgkmcnt(2)
T a[216:217],v187 offset:16392
M v[104:107],a[204:207],a[76:79],v[104:107]
B v220,s[44:47],s61 offen offset:2048 lds
M v[104:107],a[200:203],a[56:59],v[104:107]
T a[218:219],v187 offset:20488
B v221,s[44:47],s61 offen offset:3072 lds
s_add_u32 s61,s61,s75
s_and_b32 s61,s61,s74
s_add_u32 m0,s67,4096
s_add_u32 s68,s27,32768
M v[120:123],a[200:203],a[96:99],v[120:123]
B v212,s[48:51],s68 offen lds
M v[120:123],a[204:207],a[116:119],v[120:123]
T a[220:221],v187 offset:24584
M v[136:139],a[204:207],a[156:159],v[136:139]
B v213,s[48:51],s68 offen offset:1024 lds
M v[136:139],a[200:203],a[136:139],v[136:139]
W lgkmcnt(3)
T a[222:223],v187 offset:28680
M v[12:15],a[208:211],a[16:19],v[12:15]
W vmcnt(22)
s_barrier
s_mov_b32 m0,0x10000
M v[12:15],a[212:215],a[36:39],v[12:15]
W vmcnt(63)
T a[192:193],v184 offset:32768
B v218,s[44:47],s61 offen lds
M v[28:31],a[212:215],a[76:79],v[28:31]
R a[248:251],v210 offset:6144
M v[28:31],a[208:211],a[56:59],v[28:31]
T a[194:195],v184 offset:36864
M v[44:47],a[208:211],a[96:99],v[44:47]
R a[252:255],v211 offset:6144
B v219,s[44:47],s61 offen offset:1024 lds
M v[44:47],a[212:215],a[116:119],v[44:47]
T a[196:197],v184 offset:40960
M v[60:63],a[212:215],a[156:159],v[60:63]
M v[60:63],a[208:211],a[136:139],v[60:63]
W lgkmcnt(5)
T a[198:199],v184 offset:45056
M v[92:95],a[216:219],a[16:19],v[92:95]
B v220,s[44:47],s61 offen offset:2048 lds
M v[92:95],a[220:223],a[36:39],v[92:95]
W lgkmcnt(2)
T a[200:201],v184 offset:32776
M v[108:111],a[220:223],a[76:79],v[108:111]
B v221,s[44:47],s61 offen offset:3072 lds
s_add_u32 s61,s61,s62
M v[108:111],a[216:219],a[56:59],v[108:111]
s_add_u32 m0,s67,6144
s_add_u32 s68,s27,49152
T a[202:203],v184 offset:36872
M v[124:127],a[216:219],a[96:99],v[124:127]
B v212,s[48:51],s68 offen lds
s_mov_b32 m0,0x11000
M v[124:127],a[220:223],a[116:119],v[124:127]
T a[204:205],v184 offset:40968
B v218,s[44:47],s61 offen lds
s_add_u32 m0,s67,6144
M v[140:143],a[220:223],a[156:159],v[140:143]
B v213,s[48:51],s68 offen offset:1024 lds
M v[140:143],a[216:219],a[136:139],v[140:143]
W lgkmcnt(3)
s_mov_b32 m0,0x11000
T a[206:207],v184 offset:45064
M v[0:3],a[192:195],a[224:227],v[0:3]
B v219,s[44:47],s61 offen offset:1024 lds
M v[0:3],a[196:199],a[228:231],v[0:3]
W vmcnt(63)
T a[208:209],v185 offset:32768
M v[16:19],a[196:199],a[236:239],v[16:19]
R a[16:19],v68
M v[16:19],a[192:195],a[232:235],v[16:19]
T a[210:211],v185 offset:36864
B v220,s[44:47],s61 offen offset:2048 lds
M v[32:35],a[192:195],a[240:243],v[32:35]
R a[36:39],v69
M v[32:35],a[196:199],a[244:247],v[32:35]
T a[212:213],v185 offset:40960
M v[48:51],a[196:199],a[252:255],v[48:51]
B v221,s[44:47],s61 offen offset:3072 lds
s_add_u32 s61,s61,s63
M v[48:51],a[192:195],a[248:251],v[48:51]
W lgkmcnt(5)
T a[214:215],v185 offset:45056
M v[80:83],a[200:203],a[224:227],v[80:83]
M v[80:83],a[204:207],a[228:231],v[80:83]
W lgkmcnt(2)
s_mov_b32 m0,0x12000
T a[216:217],v185 offset:32776
M v[96:99],a[204:207],a[236:239],v[96:99]
B v218,s[44:47],s61 offen lds
M v[96:99],a[200:203],a[232:235],v[96:99]
s_mov_b32 m0,0xe000
s_add_u32 s27,s26,640
s_and_b32 s27,s27,0x380
s_add_u32 s27,s27,s66
s_add_u32 s68,s27,0
T a[218:219],v185 offset:36872
M v[112:115],a[200:203],a[240:243],v[112:115]
B v212,s[48:51],s68 offen lds
s_mov_b32 m0,0x12000
M v[112:115],a[204:207],a[244:247],v[112:115]
T a[220:221],v185 offset:40968
B v219,s[44:47],s61 offen offset:1024 lds
s_mov_b32 m0,0xe000
M v[128:131],a[204:207],a[252:255],v[128:131]
B v213,s[48:51],s68 offen offset:1024 lds
M v[128:131],a[200:203],a[248:251],v[128:131]
W lgkmcnt(3)
s_mov_b32 m0,0x12000
T a[222:223],v185 offset:45064
M v[4:7],a[208:211],a[224:227],v[4:7]
B v220,s[44:47],s61 offen offset:2048 lds
M v[4:7],a[212:215],a[228:231],v[4:7]
W vmcnt(63)
T a[192:193],v186 offset:32768
M v[20:23],a[212:215],a[236:239],v[20:23]
R a[56:59],v68 offset:2048
B v221,s[44:47],s61 offen offset:3072 lds
s_add_u32 s61,s61,s62
M v[20:23],a[208:211],a[232:235],v[20:23]
T a[194:195],v186 offset:36864
M v[36:39],a[208:211],a[240:243],v[36:39]
R a[76:79],v69 offset:2048
M v[36:39],a[212:215],a[244:247],v[36:39]
s_mov_b32 m0,0x13000
T a[196:197],v186 offset:40960
M v[52:55],a[212:215],a[252:255],v[52:55]
B v218,s[44:47],s61 offen lds
M v[52:55],a[208:211],a[248:251],v[52:55]
W lgkmcnt(5)
T a[198:199],v186 offset:45056
M v[84:87],a[216:219],a[224:227],v[84:87]
B v219,s[44:47],s61 offen offset:1024 lds
M v[84:87],a[220:223],a[228:231],v[84:87]
W lgkmcnt(2)
T a[200:201],v186 offset:32776
M v[100:103],a[220:223],a[236:239],v[100:103]
M v[100:103],a[216:219],a[232:235],v[100:103]
T a[202:203],v186 offset:36872
B v220,s[44:47],s61 offen offset:2048 lds
s_mov_b32 m0,0xe800
s_add_u32 s68,s27,16384
M v[116:119],a[216:219],a[240:243],v[116:119]
B v212,s[48:51],s68 offen lds
M v[116:119],a[220:223],a[244:247],v[116:119]
T a[204:205],v186 offset:40968
M v[132:135],a[220:223],a[252:255],v[132:135]
B v213,s[48:51],s68 offen offset:1024 lds
s_mov_b32 m0,0x13000
M v[132:135],a[216:219],a[248:251],v[132:135]
W lgkmcnt(3)
T a[206:207],v186 offset:45064
B v221,s[44:47],s61 offen offset:3072 lds
s_add_u32 s61,s61,s75
s_and_b32 s61,s61,s74
M v[8:11],a[192:195],a[224:227],v[8:11]
M v[8:11],a[196:199],a[228:231],v[8:11]
W vmcnt(63)
T a[208:209],v187 offset:32768
M v[24:27],a[196:199],a[236:239],v[24:27]
R a[96:99],v68 offset:4096
M v[24:27],a[192:195],a[232:235],v[24:27]
T a[210:211],v187 offset:36864
M v[40:43],a[192:195],a[240:243],v[40:43]
R a[116:119],v69 offset:4096
M v[40:43],a[196:199],a[244:247],v[40:43]
T a[212:213],v187 offset:40960
M v[56:59],a[196:199],a[252:255],v[56:59]
M v[56:59],a[192:195],a[248:251],v[56:59]
W lgkmcnt(5)
T a[214:215],v187 offset:45056
M v[88:91],a[200:203],a[224:227],v[88:91]
M v[88:91],a[204:207],a[228:231],v[88:91]
W lgkmcnt(2)
T a[216:217],v187 offset:32776
M v[104:107],a[204:207],a[236:239],v[104:107]
M v[104:107],a[200:203],a[232:235],v[104:107]
s_mov_b32 m0,0xf000
s_add_u32 s68,s27,32768
T a[218:219],v187 offset:36872
M v[120:123],a[200:203],a[240:243],v[120:123]
B v212,s[48:51],s68 offen lds
M v[120:123],a[204:207],a[244:247],v[120:123]
T a[220:221],v187 offset:40968
M v[136:139],a[204:207],a[252:255],v[136:139]
B v213,s[48:51],s68 offen offset:1024 lds
M v[136:139],a[200:203],a[248:251],v[136:139]
W lgkmcnt(3)
T a[222:223],v187 offset:45064
M v[12:15],a[208:211],a[224:227],v[12:15]
W vmcnt(26)
s_barrier
s_mov_b32 m0,0x14000
M v[12:15],a[212:215],a[228:231],v[12:15]
W vmcnt(63)
T a[192:193],v184 offset:49152
B v218,s[44:47],s61 offen lds
M v[28:31],a[212:215],a[236:239],v[28:31]
R a[136:139],v68 offset:6144
M v[28:31],a[208:211],a[232:235],v[28:31]
T a[194:195],v184 offset:53248
M v[44:47],a[208:211],a[240:243],v[44:47]
R a[156:159],v69 offset:6144
B v219,s[44:47],s61 offen offset:1024 lds
M v[44:47],a[212:215],a[244:247],v[44:47]
T a[196:197],v184 offset:57344
M v[60:63],a[212:215],a[252:255],v[60:63]
M v[60:63],a[208:211],a[248:251],v[60:63]
W lgkmcnt(5)
T a[198:199],v184 offset:61440
M v[92:95],a[216:219],a[224:227],v[92:95]
B v220,s[44:47],s61 offen offset:2048 lds
M v[92:95],a[220:223],a[228:231],v[92:95]
W lgkmcnt(2)
T a[200:201],v184 offset:49160
M v[108:111],a[220:223],a[236:239],v[108:111]
B v221,s[44:47],s61 offen offset:3072 lds
s_add_u32 s61,s61,s62
M v[108:111],a[216:219],a[232:235],v[108:111]
s_mov_b32 m0,0xf800
s_add_u32 s68,s27,49152
T a[202:203],v184 offset:53256
M v[124:127],a[216:219],a[240:243],v[124:127]
B v212,s[48:51],s68 offen lds
s_mov_b32 m0,0x15000
M v[124:127],a[220:223],a[244:247],v[124:127]
T a[204:205],v184 offset:57352
B v218,s[44:47],s61 offen lds
s_mov_b32 m0,0xf800
M v[140:143],a[220:223],a[252:255],v[140:143]
B v213,s[48:51],s68 offen offset:1024 lds
M v[140:143],a[216:219],a[248:251],v[140:143]
W lgkmcnt(3)
s_mov_b32 m0,0x15000
T a[206:207],v184 offset:61448
M v[0:3],a[192:195],a[16:19],v[0:3]
B v219,s[44:47],s61 offen offset:1024 lds
M v[0:3],a[196:199],a[36:39],v[0:3]
W vmcnt(63)
)ASM"
R"ASM(
T a[208:209],v185 offset:49152
M v[16:19],a[196:199],a[76:79],v[16:19]
R a[224:227],v210
M v[16:19],a[192:195],a[56:59],v[16:19]
T a[210:211],v185 offset:53248
B v220,s[44:47],s61 offen offset:2048 lds
M v[32:35],a[192:195],a[96:99],v[32:35]
R a[228:231],v211
M v[32:35],a[196:199],a[116:119],v[32:35]
T a[212:213],v185 offset:57344
M v[48:51],a[196:199],a[156:159],v[48:51]
B v221,s[44:47],s61 offen offset:3072 lds
s_add_u32 s61,s61,s63
M v[48:51],a[192:195],a[136:139],v[48:51]
W lgkmcnt(5)
T a[214:215],v185 offset:61440
M v[80:83],a[200:203],a[16:19],v[80:83]
M v[80:83],a[204:207],a[36:39],v[80:83]
W lgkmcnt(2)
s_mov_b32 m0,0x16000
T a[216:217],v185 offset:49160
M v[96:99],a[204:207],a[76:79],v[96:99]
B v218,s[44:47],s61 offen lds
M v[96:99],a[200:203],a[56:59],v[96:99]
s_add_u32 m0,s67,0
s_add_u32 s27,s26,768
s_and_b32 s27,s27,0x380
s_add_u32 s27,s27,s66
s_add_u32 s68,s27,0
T a[218:219],v185 offset:53256
M v[112:115],a[200:203],a[96:99],v[112:115]
B v212,s[48:51],s68 offen lds
s_mov_b32 m0,0x16000
M v[112:115],a[204:207],a[116:119],v[112:115]
T a[220:221],v185 offset:57352
B v219,s[44:47],s61 offen offset:1024 lds
s_add_u32 m0,s67,0
M v[128:131],a[204:207],a[156:159],v[128:131]
B v213,s[48:51],s68 offen offset:1024 lds
M v[128:131],a[200:203],a[136:139],v[128:131]
W lgkmcnt(3)
s_mov_b32 m0,0x16000
T a[222:223],v185 offset:61448
M v[4:7],a[208:211],a[16:19],v[4:7]
B v220,s[44:47],s61 offen offset:2048 lds
M v[4:7],a[212:215],a[36:39],v[4:7]
W vmcnt(63)
T a[192:193],v186 offset:49152
M v[20:23],a[212:215],a[76:79],v[20:23]
R a[232:235],v210 offset:2048
B v221,s[44:47],s61 offen offset:3072 lds
s_add_u32 s61,s61,s62
M v[20:23],a[208:211],a[56:59],v[20:23]
T a[194:195],v186 offset:53248
M v[36:39],a[208:211],a[96:99],v[36:39]
R a[236:239],v211 offset:2048
M v[36:39],a[212:215],a[116:119],v[36:39]
s_mov_b32 m0,0x17000
T a[196:197],v186 offset:57344
M v[52:55],a[212:215],a[156:159],v[52:55]
B v218,s[44:47],s61 offen lds
M v[52:55],a[208:211],a[136:139],v[52:55]
W lgkmcnt(5)
T a[198:199],v186 offset:61440
M v[84:87],a[216:219],a[16:19],v[84:87]
B v219,s[44:47],s61 offen offset:1024 lds
M v[84:87],a[220:223],a[36:39],v[84:87]
W lgkmcnt(2)
T a[200:201],v186 offset:49160
M v[100:103],a[220:223],a[76:79],v[100:103]
M v[100:103],a[216:219],a[56:59],v[100:103]
T a[202:203],v186 offset:53256
B v220,s[44:47],s61 offen offset:2048 lds
s_add_u32 m0,s67,2048
s_add_u32 s68,s27,16384
M v[116:119],a[216:219],a[96:99],v[116:119]
B v212,s[48:51],s68 offen lds
M v[116:119],a[220:223],a[116:119],v[116:119]
T a[204:205],v186 offset:57352
M v[132:135],a[220:223],a[156:159],v[132:135]
B v213,s[48:51],s68 offen offset:1024 lds
s_mov_b32 m0,0x17000
M v[132:135],a[216:219],a[136:139],v[132:135]
W lgkmcnt(3)
T a[206:207],v186 offset:61448
B v221,s[44:47],s61 offen offset:3072 lds
s_add_u32 s61,s61,s75
s_and_b32 s61,s61,s74
M v[8:11],a[192:195],a[16:19],v[8:11]
M v[8:11],a[196:199],a[36:39],v[8:11]
W vmcnt(46)
T a[208:209],v187 offset:49152
M v[24:27],a[196:199],a[76:79],v[24:27]
R a[240:243],v210 offset:4096
M v[24:27],a[192:195],a[56:59],v[24:27]
T a[210:211],v187 offset:53248
M v[40:43],a[192:195],a[96:99],v[40:43]
R a[244:247],v211 offset:4096
M v[40:43],a[196:199],a[116:119],v[40:43]
T a[212:213],v187 offset:57344
M v[56:59],a[196:199],a[156:159],v[56:59]
M v[56:59],a[192:195],a[136:139],v[56:59]
W lgkmcnt(5)
T a[214:215],v187 offset:61440
M v[88:91],a[200:203],a[16:19],v[88:91]
M v[88:91],a[204:207],a[36:39],v[88:91]
W lgkmcnt(2)
T a[216:217],v187 offset:49160
M v[104:107],a[204:207],a[76:79],v[104:107]
M v[104:107],a[200:203],a[56:59],v[104:107]
s_add_u32 m0,s67,4096
s_add_u32 s68,s27,32768
T a[218:219],v187 offset:53256
M v[120:123],a[200:203],a[96:99],v[120:123]
B v212,s[48:51],s68 offen lds
M v[120:123],a[204:207],a[116:119],v[120:123]
T a[220:221],v187 offset:57352
M v[136:139],a[204:207],a[156:159],v[136:139]
B v213,s[48:51],s68 offen offset:1024 lds
M v[136:139],a[200:203],a[136:139],v[136:139]
W lgkmcnt(3)
T a[222:223],v187 offset:61448
M v[12:15],a[208:211],a[16:19],v[12:15]
W vmcnt(26)
s_barrier
s_mov_b32 m0,0x18000
M v[12:15],a[212:215],a[36:39],v[12:15]
W vmcnt(63)
T a[192:193],v184
B v218,s[44:47],s61 offen lds
M v[28:31],a[212:215],a[76:79],v[28:31]
R a[248:251],v210 offset:6144
M v[28:31],a[208:211],a[56:59],v[28:31]
T a[194:195],v184 offset:4096
M v[44:47],a[208:211],a[96:99],v[44:47]
R a[252:255],v211 offset:6144
B v219,s[44:47],s61 offen offset:1024 lds
M v[44:47],a[212:215],a[116:119],v[44:47]
T a[196:197],v184 offset:8192
M v[60:63],a[212:215],a[156:159],v[60:63]
M v[60:63],a[208:211],a[136:139],v[60:63]
W lgkmcnt(5)
T a[198:199],v184 offset:12288
M v[92:95],a[216:219],a[16:19],v[92:95]
B v220,s[44:47],s61 offen offset:2048 lds
M v[92:95],a[220:223],a[36:39],v[92:95]
W lgkmcnt(2)
T a[200:201],v184 offset:8
M v[108:111],a[220:223],a[76:79],v[108:111]
B v221,s[44:47],s61 offen offset:3072 lds
s_add_u32 s61,s61,s62
M v[108:111],a[216:219],a[56:59],v[108:111]
s_add_u32 m0,s67,6144
s_add_u32 s68,s27,49152
T a[202:203],v184 offset:4104
M v[124:127],a[216:219],a[96:99],v[124:127]
B v212,s[48:51],s68 offen lds
s_mov_b32 m0,0x19000
M v[124:127],a[220:223],a[116:119],v[124:127]
T a[204:205],v184 offset:8200
B v218,s[44:47],s61 offen lds
s_add_u32 m0,s67,6144
M v[140:143],a[220:223],a[156:159],v[140:143]
B v213,s[48:51],s68 offen offset:1024 lds
M v[140:143],a[216:219],a[136:139],v[140:143]
W lgkmcnt(3)
s_mov_b32 m0,0x19000
T a[206:207],v184 offset:12296
M v[0:3],a[192:195],a[224:227],v[0:3]
B v219,s[44:47],s61 offen offset:1024 lds
M v[0:3],a[196:199],a[228:231],v[0:3]
W vmcnt(63)
T a[208:209],v185
M v[16:19],a[196:199],a[236:239],v[16:19]
R a[16:19],v68
M v[16:19],a[192:195],a[232:235],v[16:19]
T a[210:211],v185 offset:4096
B v220,s[44:47],s61 offen offset:2048 lds
M v[32:35],a[192:195],a[240:243],v[32:35]
R a[36:39],v69
M v[32:35],a[196:199],a[244:247],v[32:35]
T a[212:213],v185 offset:8192
M v[48:51],a[196:199],a[252:255],v[48:51]
B v221,s[44:47],s61 offen offset:3072 lds
s_add_u32 s61,s61,s63
M v[48:51],a[192:195],a[248:251],v[48:51]
W lgkmcnt(5)
T a[214:215],v185 offset:12288
M v[80:83],a[200:203],a[224:227],v[80:83]
M v[80:83],a[204:207],a[228:231],v[80:83]
W lgkmcnt(2)
s_mov_b32 m0,0x1a000
T a[216:217],v185 offset:8
M v[96:99],a[204:207],a[236:239],v[96:99]
B v218,s[44:47],s61 offen lds
M v[96:99],a[200:203],a[232:235],v[96:99]
s_mov_b32 m0,0xe000
s_add_u32 s27,s26,896
s_and_b32 s27,s27,0x380
s_add_u32 s27,s27,s66
s_add_u32 s68,s27,0
T a[218:219],v185 offset:4104
M v[112:115],a[200:203],a[240:243],v[112:115]
B v212,s[48:51],s68 offen lds
s_mov_b32 m0,0x1a000
M v[112:115],a[204:207],a[244:247],v[112:115]
T a[220:221],v185 offset:8200
B v219,s[44:47],s61 offen offset:1024 lds
s_mov_b32 m0,0xe000
M v[128:131],a[204:207],a[252:255],v[128:131]
B v213,s[48:51],s68 offen offset:1024 lds
M v[128:131],a[200:203],a[248:251],v[128:131]
W lgkmcnt(3)
s_mov_b32 m0,0x1a000
T a[222:223],v185 offset:12296
M v[4:7],a[208:211],a[224:227],v[4:7]
B v220,s[44:47],s61 offen offset:2048 lds
M v[4:7],a[212:215],a[228:231],v[4:7]
W vmcnt(63)
T a[192:193],v186
M v[20:23],a[212:215],a[236:239],v[20:23]
R a[56:59],v68 offset:2048
B v221,s[44:47],s61 offen offset:3072 lds
s_add_u32 s61,s61,s62
M v[20:23],a[208:211],a[232:235],v[20:23]
T a[194:195],v186 offset:4096
M v[36:39],a[208:211],a[240:243],v[36:39]
R a[76:79],v69 offset:2048
M v[36:39],a[212:215],a[244:247],v[36:39]
s_mov_b32 m0,0x1b000
T a[196:197],v186 offset:8192
M v[52:55],a[212:215],a[252:255],v[52:55]
B v218,s[44:47],s61 offen lds
M v[52:55],a[208:211],a[248:251],v[52:55]
W lgkmcnt(5)
T a[198:199],v186 offset:12288
M v[84:87],a[216:219],a[224:227],v[84:87]
B v219,s[44:47],s61 offen offset:1024 lds
M v[84:87],a[220:223],a[228:231],v[84:87]
W lgkmcnt(2)
T a[200:201],v186 offset:8
M v[100:103],a[220:223],a[236:239],v[100:103]
M v[100:103],a[216:219],a[232:235],v[100:103]
T a[202:203],v186 offset:4104
B v220,s[44:47],s61 offen offset:2048 lds
s_mov_b32 m0,0xe800
s_add_u32 s68,s27,16384
M v[116:119],a[216:219],a[240:243],v[116:119]
B v212,s[48:51],s68 offen lds
M v[116:119],a[220:223],a[244:247],v[116:119]
T a[204:205],v186 offset:8200
M v[132:135],a[220:223],a[252:255],v[132:135]
B v213,s[48:51],s68 offen offset:1024 lds
s_mov_b32 m0,0x1b000
M v[132:135],a[216:219],a[248:251],v[132:135]
W lgkmcnt(3)
T a[206:207],v186 offset:12296
B v221,s[44:47],s61 offen offset:3072 lds
s_add_u32 s61,s61,s75
s_and_b32 s61,s61,s74
M v[8:11],a[192:195],a[224:227],v[8:11]
M v[8:11],a[196:199],a[228:231],v[8:11]
W vmcnt(46)
T a[208:209],v187
M v[24:27],a[196:199],a[236:239],v[24:27]
R a[96:99],v68 offset:4096
M v[24:27],a[192:195],a[232:235],v[24:27]
T a[210:211],v187 offset:4096
M v[40:43],a[192:195],a[240:243],v[40:43]
R a[116:119],v69 offset:4096
M v[40:43],a[196:199],a[244:247],v[40:43]
T a[212:213],v187 offset:8192
M v[56:59],a[196:199],a[252:255],v[56:59]
M v[56:59],a[192:195],a[248:251],v[56:59]
W lgkmcnt(5)
T a[214:215],v187 offset:12288
M v[88:91],a[200:203],a[224:227],v[88:91]
M v[88:91],a[204:207],a[228:231],v[88:91]
W lgkmcnt(2)
T a[216:217],v187 offset:8
M v[104:107],a[204:207],a[236:239],v[104:107]
M v[104:107],a[200:203],a[232:235],v[104:107]
s_mov_b32 m0,0xf000
s_add_u32 s68,s27,32768
T a[218:219],v187 offset:4104
M v[120:123],a[200:203],a[240:243],v[120:123]
B v212,s[48:51],s68 offen lds
M v[120:123],a[204:207],a[244:247],v[120:123]
T a[220:221],v187 offset:8200
M v[136:139],a[204:207],a[252:255],v[136:139]
B v213,s[48:51],s68 offen offset:1024 lds
M v[136:139],a[200:203],a[248:251],v[136:139]
W lgkmcnt(3)
T a[222:223],v187 offset:12296
M v[12:15],a[208:211],a[224:227],v[12:15]
W vmcnt(26)
s_barrier
s_mov_b32 m0,0x1c000
M v[12:15],a[212:215],a[228:231],v[12:15]
W vmcnt(63)
)ASM"
R"ASM(
T a[192:193],v184 offset:16384
B v218,s[44:47],s61 offen lds
M v[28:31],a[212:215],a[236:239],v[28:31]
R a[136:139],v68 offset:6144
M v[28:31],a[208:211],a[232:235],v[28:31]
T a[194:195],v184 offset:20480
M v[44:47],a[208:211],a[240:243],v[44:47]
R a[156:159],v69 offset:6144
B v219,s[44:47],s61 offen offset:1024 lds
M v[44:47],a[212:215],a[244:247],v[44:47]
T a[196:197],v184 offset:24576
M v[60:63],a[212:215],a[252:255],v[60:63]
M v[60:63],a[208:211],a[248:251],v[60:63]
W lgkmcnt(5)
T a[198:199],v184 offset:28672
M v[92:95],a[216:219],a[224:227],v[92:95]
B v220,s[44:47],s61 offen offset:2048 lds
M v[92:95],a[220:223],a[228:231],v[92:95]
W lgkmcnt(2)
T a[200:201],v184 offset:16392
M v[108:111],a[220:223],a[236:239],v[108:111]
B v221,s[44:47],s61 offen offset:3072 lds
s_add_u32 s61,s61,s62
M v[108:111],a[216:219],a[232:235],v[108:111]
s_mov_b32 m0,0xf800
s_add_u32 s68,s27,49152
T a[202:203],v184 offset:20488
M v[124:127],a[216:219],a[240:243],v[124:127]
B v212,s[48:51],s68 offen lds
s_mov_b32 m0,0x1d000
M v[124:127],a[220:223],a[244:247],v[124:127]
T a[204:205],v184 offset:24584
B v218,s[44:47],s61 offen lds
s_mov_b32 m0,0xf800
M v[140:143],a[220:223],a[252:255],v[140:143]
B v213,s[48:51],s68 offen offset:1024 lds
M v[140:143],a[216:219],a[248:251],v[140:143]
W lgkmcnt(3)
s_mov_b32 m0,0x1d000
T a[206:207],v184 offset:28680
M v[0:3],a[192:195],a[16:19],v[0:3]
B v219,s[44:47],s61 offen offset:1024 lds
M v[0:3],a[196:199],a[36:39],v[0:3]
W vmcnt(63)
T a[208:209],v185 offset:16384
v_lshlrev_b32 v250,3,v245
s_mov_b32 exec_hi,0xff
buffer_load_dword v254,v250,s[36:39],s92 offen sc1
s_mov_b32 exec_hi,-1
M v[16:19],a[196:199],a[76:79],v[16:19]
R a[224:227],v210
M v[16:19],a[192:195],a[56:59],v[16:19]
T a[210:211],v185 offset:20480
B v220,s[44:47],s61 offen offset:2048 lds
M v[32:35],a[192:195],a[96:99],v[32:35]
R a[228:231],v211
M v[32:35],a[196:199],a[116:119],v[32:35]
T a[212:213],v185 offset:24576
M v[48:51],a[196:199],a[156:159],v[48:51]
B v221,s[44:47],s61 offen offset:3072 lds
s_add_u32 s61,s61,s63
M v[48:51],a[192:195],a[136:139],v[48:51]
W lgkmcnt(5)
T a[214:215],v185 offset:28672
M v[80:83],a[200:203],a[16:19],v[80:83]
M v[80:83],a[204:207],a[36:39],v[80:83]
W lgkmcnt(2)
s_mov_b32 m0,0x1e000
T a[216:217],v185 offset:16392
M v[96:99],a[204:207],a[76:79],v[96:99]
B v218,s[44:47],s61 offen lds
M v[96:99],a[200:203],a[56:59],v[96:99]
s_add_u32 m0,s67,0
s_add_u32 s27,s26,0
s_and_b32 s27,s27,0x380
s_add_u32 s27,s27,s66
s_add_u32 s68,s27,0
T a[218:219],v185 offset:20488
M v[112:115],a[200:203],a[96:99],v[112:115]
B v212,s[48:51],s68 offen lds
s_mov_b32 m0,0x1e000
M v[112:115],a[204:207],a[116:119],v[112:115]
T a[220:221],v185 offset:24584
B v219,s[44:47],s61 offen offset:1024 lds
s_add_u32 m0,s67,0
M v[128:131],a[204:207],a[156:159],v[128:131]
B v213,s[48:51],s68 offen offset:1024 lds
M v[128:131],a[200:203],a[136:139],v[128:131]
W lgkmcnt(3)
s_mov_b32 m0,0x1e000
T a[222:223],v185 offset:28680
M v[4:7],a[208:211],a[16:19],v[4:7]
B v220,s[44:47],s61 offen offset:2048 lds
M v[4:7],a[212:215],a[36:39],v[4:7]
W vmcnt(63)
T a[192:193],v186 offset:16384
M v[20:23],a[212:215],a[76:79],v[20:23]
R a[232:235],v210 offset:2048
B v221,s[44:47],s61 offen offset:3072 lds
s_add_u32 s61,s61,s62
M v[20:23],a[208:211],a[56:59],v[20:23]
T a[194:195],v186 offset:20480
M v[36:39],a[208:211],a[96:99],v[36:39]
R a[236:239],v211 offset:2048
M v[36:39],a[212:215],a[116:119],v[36:39]
s_mov_b32 m0,0x1f000
T a[196:197],v186 offset:24576
M v[52:55],a[212:215],a[156:159],v[52:55]
B v218,s[44:47],s61 offen lds
M v[52:55],a[208:211],a[136:139],v[52:55]
W lgkmcnt(5)
T a[198:199],v186 offset:28672
M v[84:87],a[216:219],a[16:19],v[84:87]
B v219,s[44:47],s61 offen offset:1024 lds
M v[84:87],a[220:223],a[36:39],v[84:87]
W lgkmcnt(2)
T a[200:201],v186 offset:16392
M v[100:103],a[220:223],a[76:79],v[100:103]
M v[100:103],a[216:219],a[56:59],v[100:103]
T a[202:203],v186 offset:20488
B v220,s[44:47],s61 offen offset:2048 lds
s_add_u32 m0,s67,2048
s_add_u32 s68,s27,16384
M v[116:119],a[216:219],a[96:99],v[116:119]
B v212,s[48:51],s68 offen lds
M v[116:119],a[220:223],a[116:119],v[116:119]
T a[204:205],v186 offset:24584
M v[132:135],a[220:223],a[156:159],v[132:135]
B v213,s[48:51],s68 offen offset:1024 lds
s_mov_b32 m0,0x1f000
M v[132:135],a[216:219],a[136:139],v[132:135]
W lgkmcnt(3)
T a[206:207],v186 offset:28680
B v221,s[44:47],s61 offen offset:3072 lds
s_add_u32 s61,s61,s75
s_and_b32 s61,s61,s74
M v[8:11],a[192:195],a[16:19],v[8:11]
M v[8:11],a[196:199],a[36:39],v[8:11]
W vmcnt(47)
T a[208:209],v187 offset:16384
M v[24:27],a[196:199],a[76:79],v[24:27]
R a[240:243],v210 offset:4096
M v[24:27],a[192:195],a[56:59],v[24:27]
T a[210:211],v187 offset:20480
M v[40:43],a[192:195],a[96:99],v[40:43]
R a[244:247],v211 offset:4096
M v[40:43],a[196:199],a[116:119],v[40:43]
T a[212:213],v187 offset:24576
M v[56:59],a[196:199],a[156:159],v[56:59]
M v[56:59],a[192:195],a[136:139],v[56:59]
W lgkmcnt(5)
T a[214:215],v187 offset:28672
M v[88:91],a[200:203],a[16:19],v[88:91]
M v[88:91],a[204:207],a[36:39],v[88:91]
W lgkmcnt(2)
T a[216:217],v187 offset:16392
M v[104:107],a[204:207],a[76:79],v[104:107]
M v[104:107],a[200:203],a[56:59],v[104:107]
s_add_u32 m0,s67,4096
s_add_u32 s68,s27,32768
T a[218:219],v187 offset:20488
M v[120:123],a[200:203],a[96:99],v[120:123]
B v212,s[48:51],s68 offen lds
M v[120:123],a[204:207],a[116:119],v[120:123]
T a[220:221],v187 offset:24584
M v[136:139],a[204:207],a[156:159],v[136:139]
B v213,s[48:51],s68 offen offset:1024 lds
M v[136:139],a[200:203],a[136:139],v[136:139]
W lgkmcnt(3)
T a[222:223],v187 offset:28680
M v[12:15],a[208:211],a[16:19],v[12:15]
W vmcnt(27)
s_barrier
s_mov_b32 m0,0x0
M v[12:15],a[212:215],a[36:39],v[12:15]
W vmcnt(63)
T a[192:193],v184 offset:32768
B v214,s[40:43],s60 offen nt lds
M v[28:31],a[212:215],a[76:79],v[28:31]
R a[248:251],v210 offset:6144
B v215,s[40:43],s60 offen offset:1024 nt lds
M v[28:31],a[208:211],a[56:59],v[28:31]
T a[194:195],v184 offset:36864
B v216,s[40:43],s60 offen offset:2048 nt lds
M v[44:47],a[208:211],a[96:99],v[44:47]
R a[252:255],v211 offset:6144
M v[44:47],a[212:215],a[116:119],v[44:47]
T a[196:197],v184 offset:40960
M v[60:63],a[212:215],a[156:159],v[60:63]
B v217,s[40:43],s60 offen offset:3072 nt lds
s_add_u32 s60,s60,s76
M v[60:63],a[208:211],a[136:139],v[60:63]
W lgkmcnt(5)
s_mov_b32 m0,0x1000
T a[198:199],v184 offset:45056
M v[92:95],a[216:219],a[16:19],v[92:95]
B v214,s[40:43],s60 offen nt lds
M v[92:95],a[220:223],a[36:39],v[92:95]
W lgkmcnt(2)
T a[200:201],v184 offset:32776
M v[108:111],a[220:223],a[76:79],v[108:111]
B v215,s[40:43],s60 offen offset:1024 nt lds
M v[108:111],a[216:219],a[56:59],v[108:111]
T a[202:203],v184 offset:36872
B v216,s[40:43],s60 offen offset:2048 nt lds
s_add_u32 m0,s67,6144
s_add_u32 s68,s27,49152
M v[124:127],a[216:219],a[96:99],v[124:127]
B v212,s[48:51],s68 offen lds
s_mov_b32 m0,0x1000
M v[124:127],a[220:223],a[116:119],v[124:127]
T a[204:205],v184 offset:40968
B v217,s[40:43],s60 offen offset:3072 nt lds
s_add_u32 s60,s60,s76
s_add_u32 m0,s67,6144
M v[140:143],a[220:223],a[156:159],v[140:143]
B v213,s[48:51],s68 offen offset:1024 lds
M v[140:143],a[216:219],a[136:139],v[140:143]
W lgkmcnt(3)
s_mov_b32 m0,0x2000
T a[206:207],v184 offset:45064
M v[0:3],a[192:195],a[224:227],v[0:3]
B v214,s[40:43],s60 offen nt lds
M v[0:3],a[196:199],a[228:231],v[0:3]
W vmcnt(63)
T a[208:209],v185 offset:32768
B v215,s[40:43],s60 offen offset:1024 nt lds
M v[16:19],a[196:199],a[236:239],v[16:19]
R a[16:19],v68
M v[16:19],a[192:195],a[232:235],v[16:19]
T a[210:211],v185 offset:36864
B v216,s[40:43],s60 offen offset:2048 nt lds
M v[32:35],a[192:195],a[240:243],v[32:35]
R a[36:39],v69
M v[32:35],a[196:199],a[244:247],v[32:35]
T a[212:213],v185 offset:40960
M v[48:51],a[196:199],a[252:255],v[48:51]
B v217,s[40:43],s60 offen offset:3072 nt lds
s_add_u32 s60,s60,s76
M v[48:51],a[192:195],a[248:251],v[48:51]
W lgkmcnt(5)
s_mov_b32 m0,0x3000
T a[214:215],v185 offset:45056
M v[80:83],a[200:203],a[224:227],v[80:83]
B v214,s[40:43],s60 offen nt lds
M v[80:83],a[204:207],a[228:231],v[80:83]
T a[216:217],v185 offset:32776
M v[96:99],a[204:207],a[236:239],v[96:99]
B v215,s[40:43],s60 offen offset:1024 nt lds
M v[96:99],a[200:203],a[232:235],v[96:99]
T a[218:219],v185 offset:36872
M v[112:115],a[200:203],a[240:243],v[112:115]
B v216,s[40:43],s60 offen offset:2048 nt lds
M v[112:115],a[204:207],a[244:247],v[112:115]
T a[220:221],v185 offset:40968
M v[128:131],a[204:207],a[252:255],v[128:131]
B v217,s[40:43],s60 offen offset:3072 nt lds
s_add_u32 s60,s60,s77
M v[128:131],a[200:203],a[248:251],v[128:131]
W lgkmcnt(3)
s_mov_b32 m0,0x4000
T a[222:223],v185 offset:45064
M v[4:7],a[208:211],a[224:227],v[4:7]
B v214,s[40:43],s60 offen nt lds
M v[4:7],a[212:215],a[228:231],v[4:7]
W vmcnt(63)
T a[192:193],v186 offset:32768
B v215,s[40:43],s60 offen offset:1024 nt lds
M v[20:23],a[212:215],a[236:239],v[20:23]
R a[56:59],v68 offset:2048
M v[20:23],a[208:211],a[232:235],v[20:23]
T a[194:195],v186 offset:36864
B v216,s[40:43],s60 offen offset:2048 nt lds
M v[36:39],a[208:211],a[240:243],v[36:39]
R a[76:79],v69 offset:2048
B v217,s[40:43],s60 offen offset:3072 nt lds
s_add_u32 s60,s60,s76
M v[36:39],a[212:215],a[244:247],v[36:39]
s_mov_b32 m0,0x5000
T a[196:197],v186 offset:40960
M v[52:55],a[212:215],a[252:255],v[52:55]
B v214,s[40:43],s60 offen nt lds
M v[52:55],a[208:211],a[248:251],v[52:55]
W lgkmcnt(5)
T a[198:199],v186 offset:45056
M v[84:87],a[216:219],a[224:227],v[84:87]
B v215,s[40:43],s60 offen offset:1024 nt lds
M v[84:87],a[220:223],a[228:231],v[84:87]
T a[200:201],v186 offset:32776
M v[100:103],a[220:223],a[236:239],v[100:103]
B v216,s[40:43],s60 offen offset:2048 nt lds
M v[100:103],a[216:219],a[232:235],v[100:103]
T a[202:203],v186 offset:36872
M v[116:119],a[216:219],a[240:243],v[116:119]
B v217,s[40:43],s60 offen offset:3072 nt lds
s_add_u32 s60,s60,s76
M v[116:119],a[220:223],a[244:247],v[116:119]
s_mov_b32 m0,0x6000
T a[204:205],v186 offset:40968
M v[132:135],a[220:223],a[252:255],v[132:135]
)ASM"
R"ASM(
B v214,s[40:43],s60 offen nt lds
M v[132:135],a[216:219],a[248:251],v[132:135]
W lgkmcnt(3)
T a[206:207],v186 offset:45064
M v[8:11],a[192:195],a[224:227],v[8:11]
B v215,s[40:43],s60 offen offset:1024 nt lds
M v[8:11],a[196:199],a[228:231],v[8:11]
W vmcnt(53)
T a[208:209],v187 offset:32768
M v[24:27],a[196:199],a[236:239],v[24:27]
R a[96:99],v68 offset:4096
B v216,s[40:43],s60 offen offset:2048 nt lds
M v[24:27],a[192:195],a[232:235],v[24:27]
T a[210:211],v187 offset:36864
M v[40:43],a[192:195],a[240:243],v[40:43]
R a[116:119],v69 offset:4096
B v217,s[40:43],s60 offen offset:3072 nt lds
s_add_u32 s60,s60,s76
M v[40:43],a[196:199],a[244:247],v[40:43]
s_mov_b32 m0,0x7000
T a[212:213],v187 offset:40960
M v[56:59],a[196:199],a[252:255],v[56:59]
B v214,s[40:43],s60 offen nt lds
M v[56:59],a[192:195],a[248:251],v[56:59]
W lgkmcnt(5)
T a[214:215],v187 offset:45056
M v[88:91],a[200:203],a[224:227],v[88:91]
B v215,s[40:43],s60 offen offset:1024 nt lds
M v[88:91],a[204:207],a[228:231],v[88:91]
T a[216:217],v187 offset:32776
M v[104:107],a[204:207],a[236:239],v[104:107]
B v216,s[40:43],s60 offen offset:2048 nt lds
M v[104:107],a[200:203],a[232:235],v[104:107]
T a[218:219],v187 offset:36872
M v[120:123],a[200:203],a[240:243],v[120:123]
B v217,s[40:43],s60 offen offset:3072 nt lds
s_add_u32 s60,s60,s77
M v[120:123],a[204:207],a[244:247],v[120:123]
T a[220:221],v187 offset:40968
s_mov_b32 s44,s12
s_and_b32 s45,s13,0xffff
s_mov_b32 s46,0x4000000
s_mov_b32 s47,0x20000
s_mov_b32 s95,256
v_mad_u32_u24 v218,v227,s95,v246
v_mad_u32_u24 v219,v227,s95,v247
v_mad_u32_u24 v220,v227,s95,v246
v_mad_u32_u24 v221,v227,s95,v247
s_mov_b32 s61,s82
s_mov_b32 s62,4096
s_mov_b32 s63,4096
s_mov_b32 s75,4096
s_mov_b32 s74,-1
s_mov_b32 m0,0x10000
M v[136:139],a[204:207],a[252:255],v[136:139]
B v218,s[44:47],s61 offen lds
M v[136:139],a[200:203],a[248:251],v[136:139]
W lgkmcnt(3)
T a[222:223],v187 offset:45064
M v[12:15],a[208:211],a[224:227],v[12:15]
W vmcnt(37)
s_barrier
M v[12:15],a[212:215],a[228:231],v[12:15]
W vmcnt(63)
T a[192:193],v184 offset:49152
B v219,s[44:47],s61 offen offset:1024 lds
M v[28:31],a[212:215],a[236:239],v[28:31]
R a[136:139],v68 offset:6144
B v220,s[44:47],s61 offen offset:2048 lds
M v[28:31],a[208:211],a[232:235],v[28:31]
T a[194:195],v184 offset:53248
M v[44:47],a[208:211],a[240:243],v[44:47]
R a[156:159],v69 offset:6144
B v221,s[44:47],s61 offen offset:3072 lds
s_add_u32 s61,s61,s62
M v[44:47],a[212:215],a[244:247],v[44:47]
s_mov_b32 m0,0x11000
T a[196:197],v184 offset:57344
M v[60:63],a[212:215],a[252:255],v[60:63]
B v218,s[44:47],s61 offen lds
M v[60:63],a[208:211],a[248:251],v[60:63]
W lgkmcnt(5)
T a[198:199],v184 offset:61440
M v[92:95],a[216:219],a[224:227],v[92:95]
B v219,s[44:47],s61 offen offset:1024 lds
M v[92:95],a[220:223],a[228:231],v[92:95]
T a[200:201],v184 offset:49160
M v[108:111],a[220:223],a[236:239],v[108:111]
B v220,s[44:47],s61 offen offset:2048 lds
M v[108:111],a[216:219],a[232:235],v[108:111]
T a[202:203],v184 offset:53256
M v[124:127],a[216:219],a[240:243],v[124:127]
B v221,s[44:47],s61 offen offset:3072 lds
s_add_u32 s61,s61,s63
M v[124:127],a[220:223],a[244:247],v[124:127]
s_mov_b32 m0,0x12000
T a[204:205],v184 offset:57352
M v[140:143],a[220:223],a[252:255],v[140:143]
B v218,s[44:47],s61 offen lds
M v[140:143],a[216:219],a[248:251],v[140:143]
W lgkmcnt(3)
T a[206:207],v184 offset:61448
M v[0:3],a[192:195],a[16:19],v[0:3]
B v219,s[44:47],s61 offen offset:1024 lds
M v[0:3],a[196:199],a[36:39],v[0:3]
W vmcnt(63)
T a[208:209],v185 offset:49152
B v220,s[44:47],s61 offen offset:2048 lds
M v[16:19],a[196:199],a[76:79],v[16:19]
R a[224:227],v210
M v[16:19],a[192:195],a[56:59],v[16:19]
T a[210:211],v185 offset:53248
B v221,s[44:47],s61 offen offset:3072 lds
s_add_u32 s61,s61,s62
M v[32:35],a[192:195],a[96:99],v[32:35]
R a[228:231],v211
M v[32:35],a[196:199],a[116:119],v[32:35]
s_mov_b32 m0,0x13000
T a[212:213],v185 offset:57344
M v[48:51],a[196:199],a[156:159],v[48:51]
B v218,s[44:47],s61 offen lds
M v[48:51],a[192:195],a[136:139],v[48:51]
W lgkmcnt(5)
T a[214:215],v185 offset:61440
M v[80:83],a[200:203],a[16:19],v[80:83]
B v219,s[44:47],s61 offen offset:1024 lds
M v[80:83],a[204:207],a[36:39],v[80:83]
W lgkmcnt(2)
T a[216:217],v185 offset:49160
M v[96:99],a[204:207],a[76:79],v[96:99]
B v220,s[44:47],s61 offen offset:2048 lds
M v[96:99],a[200:203],a[56:59],v[96:99]
T a[218:219],v185 offset:53256
B v221,s[44:47],s61 offen offset:3072 lds
s_add_u32 s61,s61,s75
s_and_b32 s61,s61,s74
s_add_u32 m0,s67,0
s_add_u32 s27,s26,128
s_and_b32 s27,s27,0x380
s_add_u32 s27,s27,s66
s_add_u32 s68,s27,0
M v[112:115],a[200:203],a[96:99],v[112:115]
B v212,s[48:51],s68 offen lds
s_mov_b32 m0,0x8000
M v[112:115],a[204:207],a[116:119],v[112:115]
T a[220:221],v185 offset:57352
B v214,s[40:43],s60 offen nt lds
s_add_u32 m0,s67,0
M v[128:131],a[204:207],a[156:159],v[128:131]
B v213,s[48:51],s68 offen offset:1024 lds
M v[128:131],a[200:203],a[136:139],v[128:131]
W lgkmcnt(3)
s_mov_b32 m0,0x8000
T a[222:223],v185 offset:61448
M v[4:7],a[208:211],a[16:19],v[4:7]
B v215,s[40:43],s60 offen offset:1024 nt lds
M v[4:7],a[212:215],a[36:39],v[4:7]
W vmcnt(63)
T a[192:193],v186 offset:49152
B v216,s[40:43],s60 offen offset:2048 nt lds
M v[20:23],a[212:215],a[76:79],v[20:23]
R a[232:235],v210 offset:2048
U v0,s64,v0
M v[20:23],a[208:211],a[56:59],v[20:23]
T a[194:195],v186 offset:53248
B v217,s[40:43],s60 offen offset:3072 nt lds
s_add_u32 s60,s60,s76
M v[36:39],a[208:211],a[96:99],v[36:39]
R a[236:239],v211 offset:2048
U v1,s64,v1
M v[36:39],a[212:215],a[116:119],v[36:39]
s_mov_b32 m0,0x9000
T a[196:197],v186 offset:57344
M v[52:55],a[212:215],a[156:159],v[52:55]
B v214,s[40:43],s60 offen nt lds
M v[52:55],a[208:211],a[136:139],v[52:55]
W lgkmcnt(5)
T a[198:199],v186 offset:61440
M v[84:87],a[216:219],a[16:19],v[84:87]
B v215,s[40:43],s60 offen offset:1024 nt lds
M v[84:87],a[220:223],a[36:39],v[84:87]
W lgkmcnt(2)
T a[200:201],v186 offset:49160
M v[100:103],a[220:223],a[76:79],v[100:103]
B v216,s[40:43],s60 offen offset:2048 nt lds
M v[100:103],a[216:219],a[56:59],v[100:103]
T a[202:203],v186 offset:53256
B v217,s[40:43],s60 offen offset:3072 nt lds
s_add_u32 s60,s60,s76
s_add_u32 m0,s67,2048
s_add_u32 s68,s27,16384
M v[116:119],a[216:219],a[96:99],v[116:119]
B v212,s[48:51],s68 offen lds
s_mov_b32 m0,0xa000
M v[116:119],a[220:223],a[116:119],v[116:119]
T a[204:205],v186 offset:57352
B v214,s[40:43],s60 offen nt lds
s_add_u32 m0,s67,2048
M v[132:135],a[220:223],a[156:159],v[132:135]
B v213,s[48:51],s68 offen offset:1024 lds
s_mov_b32 m0,0xa000
M v[132:135],a[216:219],a[136:139],v[132:135]
W lgkmcnt(3)
T a[206:207],v186 offset:61448
B v215,s[40:43],s60 offen offset:1024 nt lds
M v[8:11],a[192:195],a[16:19],v[8:11]
B v216,s[40:43],s60 offen offset:2048 nt lds
M v[8:11],a[196:199],a[36:39],v[8:11]
W vmcnt(63)
T a[208:209],v187 offset:49152
M v[24:27],a[196:199],a[76:79],v[24:27]
R a[240:243],v210 offset:4096
B v217,s[40:43],s60 offen offset:3072 nt lds
s_add_u32 s60,s60,s76
M v[24:27],a[192:195],a[56:59],v[24:27]
s_mov_b32 m0,0xb000
T a[210:211],v187 offset:53248
M v[40:43],a[192:195],a[96:99],v[40:43]
R a[244:247],v211 offset:4096
B v214,s[40:43],s60 offen nt lds
M v[40:43],a[196:199],a[116:119],v[40:43]
T a[212:213],v187 offset:57344
U v2,s64,v2
M v[56:59],a[196:199],a[156:159],v[56:59]
B v215,s[40:43],s60 offen offset:1024 nt lds
M v[56:59],a[192:195],a[136:139],v[56:59]
W lgkmcnt(5)
T a[214:215],v187 offset:61440
M v[88:91],a[200:203],a[16:19],v[88:91]
B v216,s[40:43],s60 offen offset:2048 nt lds
M v[88:91],a[204:207],a[36:39],v[88:91]
W lgkmcnt(2)
T a[216:217],v187 offset:49160
M v[104:107],a[204:207],a[76:79],v[104:107]
B v217,s[40:43],s60 offen offset:3072 nt lds
s_add_u32 s60,s60,s77
M v[104:107],a[200:203],a[56:59],v[104:107]
s_add_u32 m0,s67,4096
s_add_u32 s68,s27,32768
T a[218:219],v187 offset:53256
M v[120:123],a[200:203],a[96:99],v[120:123]
B v212,s[48:51],s68 offen lds
s_mov_b32 m0,0x14000
M v[120:123],a[204:207],a[116:119],v[120:123]
T a[220:221],v187 offset:57352
B v218,s[44:47],s61 offen lds
s_add_u32 m0,s67,4096
M v[136:139],a[204:207],a[156:159],v[136:139]
B v213,s[48:51],s68 offen offset:1024 lds
s_mov_b32 m0,0x14000
M v[136:139],a[200:203],a[136:139],v[136:139]
W lgkmcnt(3)
T a[222:223],v187 offset:61448
B v219,s[44:47],s61 offen offset:1024 lds
M v[12:15],a[208:211],a[16:19],v[12:15]
B v220,s[44:47],s61 offen offset:2048 lds
M v[12:15],a[212:215],a[36:39],v[12:15]
W vmcnt(63)
U v3,s64,v3
U v80,s64,v80
M v[28:31],a[212:215],a[76:79],v[28:31]
R a[248:251],v210 offset:6144
U v81,s64,v81
M v[28:31],a[208:211],a[56:59],v[28:31]
B v221,s[44:47],s61 offen offset:3072 lds
s_add_u32 s61,s61,s62
M v[44:47],a[208:211],a[96:99],v[44:47]
s_mov_b32 m0,0x15000
R a[252:255],v211 offset:6144
M v[44:47],a[212:215],a[116:119],v[44:47]
B v218,s[44:47],s61 offen lds
M v[60:63],a[212:215],a[156:159],v[60:63]
B v219,s[44:47],s61 offen offset:1024 lds
M v[60:63],a[208:211],a[136:139],v[60:63]
W lgkmcnt(2)
U v82,s64,v82
U v83,s64,v83
M v[92:95],a[216:219],a[16:19],v[92:95]
B v220,s[44:47],s61 offen offset:2048 lds
M v[92:95],a[220:223],a[36:39],v[92:95]
W lgkmcnt(0)
C v0,v0,v1
C v1,v2,v3
M v[108:111],a[220:223],a[76:79],v[108:111]
B v221,s[44:47],s61 offen offset:3072 lds
s_add_u32 s61,s61,s63
M v[108:111],a[216:219],a[56:59],v[108:111]
s_add_u32 m0,s67,6144
s_add_u32 s68,s27,49152
C v2,v80,v81
M v[124:127],a[216:219],a[96:99],v[124:127]
B v212,s[48:51],s68 offen lds
M v[124:127],a[220:223],a[116:119],v[124:127]
C v3,v82,v83
M v[140:143],a[220:223],a[156:159],v[140:143]
B v213,s[48:51],s68 offen offset:1024 lds
M v[140:143],a[216:219],a[136:139],v[140:143]
W vmcnt(63)
s_barrier
R a[160:163],v198
)ASM"
R"ASM(
R a[164:167],v199
R a[168:171],v200
R a[172:175],v201
s_nop 1
v_pk_mul_f32 v[16:17],v[16:17],s[64:65]
v_pk_mul_f32 v[18:19],v[18:19],s[64:65]
v_pk_mul_f32 v[96:97],v[96:97],s[64:65]
v_pk_mul_f32 v[98:99],v[98:99],s[64:65]
C v16,v16,v17
C v17,v18,v19
C v18,v96,v97
C v19,v98,v99
v_pk_mul_f32 v[32:33],v[32:33],s[64:65]
v_pk_mul_f32 v[34:35],v[34:35],s[64:65]
v_pk_mul_f32 v[112:113],v[112:113],s[64:65]
v_pk_mul_f32 v[114:115],v[114:115],s[64:65]
C v32,v32,v33
C v33,v34,v35
C v34,v112,v113
C v35,v114,v115
v_pk_mul_f32 v[48:49],v[48:49],s[64:65]
v_pk_mul_f32 v[50:51],v[50:51],s[64:65]
v_pk_mul_f32 v[128:129],v[128:129],s[64:65]
v_pk_mul_f32 v[130:131],v[130:131],s[64:65]
C v48,v48,v49
C v49,v50,v51
C v50,v128,v129
C v51,v130,v131
v_pk_mul_f32 v[4:5],v[4:5],s[64:65]
v_pk_mul_f32 v[6:7],v[6:7],s[64:65]
v_pk_mul_f32 v[84:85],v[84:85],s[64:65]
v_pk_mul_f32 v[86:87],v[86:87],s[64:65]
C v4,v4,v5
C v5,v6,v7
C v6,v84,v85
C v7,v86,v87
v_pk_mul_f32 v[20:21],v[20:21],s[64:65]
v_pk_mul_f32 v[22:23],v[22:23],s[64:65]
v_pk_mul_f32 v[100:101],v[100:101],s[64:65]
v_pk_mul_f32 v[102:103],v[102:103],s[64:65]
C v20,v20,v21
C v21,v22,v23
C v22,v100,v101
C v23,v102,v103
v_pk_mul_f32 v[36:37],v[36:37],s[64:65]
v_pk_mul_f32 v[38:39],v[38:39],s[64:65]
v_pk_mul_f32 v[116:117],v[116:117],s[64:65]
v_pk_mul_f32 v[118:119],v[118:119],s[64:65]
C v36,v36,v37
C v37,v38,v39
C v38,v116,v117
C v39,v118,v119
v_pk_mul_f32 v[52:53],v[52:53],s[64:65]
v_pk_mul_f32 v[54:55],v[54:55],s[64:65]
v_pk_mul_f32 v[132:133],v[132:133],s[64:65]
v_pk_mul_f32 v[134:135],v[134:135],s[64:65]
C v52,v52,v53
C v53,v54,v55
C v54,v132,v133
C v55,v134,v135
v_pk_mul_f32 v[8:9],v[8:9],s[64:65]
v_pk_mul_f32 v[10:11],v[10:11],s[64:65]
v_pk_mul_f32 v[88:89],v[88:89],s[64:65]
v_pk_mul_f32 v[90:91],v[90:91],s[64:65]
C v8,v8,v9
C v9,v10,v11
C v10,v88,v89
C v11,v90,v91
v_pk_mul_f32 v[24:25],v[24:25],s[64:65]
v_pk_mul_f32 v[26:27],v[26:27],s[64:65]
v_pk_mul_f32 v[104:105],v[104:105],s[64:65]
v_pk_mul_f32 v[106:107],v[106:107],s[64:65]
C v24,v24,v25
C v25,v26,v27
C v26,v104,v105
C v27,v106,v107
v_pk_mul_f32 v[40:41],v[40:41],s[64:65]
v_pk_mul_f32 v[42:43],v[42:43],s[64:65]
v_pk_mul_f32 v[120:121],v[120:121],s[64:65]
v_pk_mul_f32 v[122:123],v[122:123],s[64:65]
C v40,v40,v41
C v41,v42,v43
C v42,v120,v121
C v43,v122,v123
v_pk_mul_f32 v[56:57],v[56:57],s[64:65]
v_pk_mul_f32 v[58:59],v[58:59],s[64:65]
v_pk_mul_f32 v[136:137],v[136:137],s[64:65]
v_pk_mul_f32 v[138:139],v[138:139],s[64:65]
C v56,v56,v57
C v57,v58,v59
C v58,v136,v137
C v59,v138,v139
v_pk_mul_f32 v[12:13],v[12:13],s[64:65]
v_pk_mul_f32 v[14:15],v[14:15],s[64:65]
v_pk_mul_f32 v[92:93],v[92:93],s[64:65]
v_pk_mul_f32 v[94:95],v[94:95],s[64:65]
C v12,v12,v13
C v13,v14,v15
C v14,v92,v93
C v15,v94,v95
v_pk_mul_f32 v[28:29],v[28:29],s[64:65]
v_pk_mul_f32 v[30:31],v[30:31],s[64:65]
v_pk_mul_f32 v[108:109],v[108:109],s[64:65]
v_pk_mul_f32 v[110:111],v[110:111],s[64:65]
C v28,v28,v29
C v29,v30,v31
C v30,v108,v109
C v31,v110,v111
v_pk_mul_f32 v[44:45],v[44:45],s[64:65]
v_pk_mul_f32 v[46:47],v[46:47],s[64:65]
v_pk_mul_f32 v[124:125],v[124:125],s[64:65]
v_pk_mul_f32 v[126:127],v[126:127],s[64:65]
C v44,v44,v45
C v45,v46,v47
C v46,v124,v125
C v47,v126,v127
v_pk_mul_f32 v[60:61],v[60:61],s[64:65]
v_pk_mul_f32 v[62:63],v[62:63],s[64:65]
v_pk_mul_f32 v[140:141],v[140:141],s[64:65]
v_pk_mul_f32 v[142:143],v[142:143],s[64:65]
C v60,v60,v61
C v61,v62,v63
C v62,v140,v141
C v63,v142,v143
s_branch LPREb_%=
)ASM"
R"ASM(
LQ0b_%=:
W lgkmcnt(4)
s_mov_b32 m0,0xe000
s_add_u32 s27,s26,128
s_and_b32 s27,s27,0x380
s_add_u32 s27,s27,s66
s_add_u32 s68,s27,0
M v[0:3],a[192:195],a[224:227],0
B v212,s[48:51],s68 offen lds
M v[0:3],a[196:199],a[228:231],v[0:3]
T a[208:209],v185
M v[16:19],a[196:199],a[236:239],0
B v213,s[48:51],s68 offen offset:1024 lds
M v[16:19],a[192:195],a[232:235],v[16:19]
s_mov_b32 m0,0xe800
s_add_u32 s68,s27,16384
T a[210:211],v185 offset:4096
M v[32:35],a[192:195],a[240:243],0
B v212,s[48:51],s68 offen lds
M v[32:35],a[196:199],a[244:247],v[32:35]
T a[212:213],v185 offset:8192
M v[48:51],a[196:199],a[252:255],0
B v213,s[48:51],s68 offen offset:1024 lds
M v[48:51],a[192:195],a[248:251],v[48:51]
W lgkmcnt(3)
s_mov_b32 m0,0xf000
s_add_u32 s68,s27,32768
T a[214:215],v185 offset:12288
M v[80:83],a[200:203],a[224:227],0
B v212,s[48:51],s68 offen lds
M v[80:83],a[204:207],a[228:231],v[80:83]
T a[216:217],v185 offset:8
M v[96:99],a[204:207],a[236:239],0
B v213,s[48:51],s68 offen offset:1024 lds
M v[96:99],a[200:203],a[232:235],v[96:99]
T a[218:219],v185 offset:4104
s_cmp_lt_u32 s81,2300
s_cbranch_scc1 LTX0_%=
v_lshlrev_b32 v252,3,v245
s_mul_i32 s95,s28,19456
s_add_u32 s95,s95,s66
s_sub_u32 s95,s95,245760
s_mov_b64 exec,-1
buffer_load_dword v254,v252,s[48:51],s95 offen sc1
s_mov_b64 exec,-1
LTX0_%=:
s_mov_b32 m0,0xf800
s_add_u32 s68,s27,49152
M v[112:115],a[200:203],a[240:243],0
B v212,s[48:51],s68 offen lds
M v[112:115],a[204:207],a[244:247],v[112:115]
T a[220:221],v185 offset:8200
M v[128:131],a[204:207],a[252:255],0
B v213,s[48:51],s68 offen offset:1024 lds
M v[128:131],a[200:203],a[248:251],v[128:131]
W lgkmcnt(3)
s_add_u32 m0,s67,0
s_add_u32 s27,s26,256
s_and_b32 s27,s27,0x380
s_add_u32 s27,s27,s66
s_add_u32 s68,s27,0
T a[222:223],v185 offset:12296
M v[4:7],a[208:211],a[224:227],0
B v212,s[48:51],s68 offen lds
M v[4:7],a[212:215],a[228:231],v[4:7]
T a[192:193],v186
M v[20:23],a[212:215],a[236:239],0
B v213,s[48:51],s68 offen offset:1024 lds
M v[20:23],a[208:211],a[232:235],v[20:23]
s_add_u32 m0,s67,2048
s_add_u32 s68,s27,16384
T a[194:195],v186 offset:4096
M v[36:39],a[208:211],a[240:243],0
B v212,s[48:51],s68 offen lds
M v[36:39],a[212:215],a[244:247],v[36:39]
T a[196:197],v186 offset:8192
s_cmp_lt_u32 s81,2300
s_cbranch_scc1 LTX1_%=
v_lshlrev_b32 v252,3,v245
s_mul_i32 s95,s28,19456
s_add_u32 s95,s95,s66
s_sub_u32 s95,s95,237568
s_mov_b64 exec,-1
buffer_load_dword v254,v252,s[48:51],s95 offen sc1
s_mov_b64 exec,-1
LTX1_%=:
M v[52:55],a[212:215],a[252:255],0
B v213,s[48:51],s68 offen offset:1024 lds
M v[52:55],a[208:211],a[248:251],v[52:55]
W lgkmcnt(3)
s_add_u32 m0,s67,4096
s_add_u32 s68,s27,32768
T a[198:199],v186 offset:12288
M v[84:87],a[216:219],a[224:227],0
B v212,s[48:51],s68 offen lds
M v[84:87],a[220:223],a[228:231],v[84:87]
T a[200:201],v186 offset:8
M v[100:103],a[220:223],a[236:239],0
B v213,s[48:51],s68 offen offset:1024 lds
M v[100:103],a[216:219],a[232:235],v[100:103]
s_add_u32 m0,s67,6144
s_add_u32 s68,s27,49152
T a[202:203],v186 offset:4104
M v[116:119],a[216:219],a[240:243],0
B v212,s[48:51],s68 offen lds
M v[116:119],a[220:223],a[244:247],v[116:119]
T a[204:205],v186 offset:8200
M v[132:135],a[220:223],a[252:255],0
B v213,s[48:51],s68 offen offset:1024 lds
s_mov_b32 m0,0x18000
M v[132:135],a[216:219],a[248:251],v[132:135]
W lgkmcnt(3)
W vmcnt(14)
T a[206:207],v186 offset:12296
R a[16:19],v68
B v218,s[44:47],s61 offen lds
s_cmp_lt_u32 s81,2300
s_cbranch_scc1 LTX2_%=
v_lshlrev_b32 v252,3,v245
s_mul_i32 s95,s28,19456
s_add_u32 s95,s95,s66
s_sub_u32 s95,s95,229376
s_mov_b64 exec,0xffffff
buffer_load_dword v254,v252,s[48:51],s95 offen sc1
s_mov_b64 exec,-1
LTX2_%=:
M v[8:11],a[192:195],a[224:227],0
M v[8:11],a[196:199],a[228:231],v[8:11]
T a[208:209],v187
R a[36:39],v69
M v[24:27],a[196:199],a[236:239],0
B v219,s[44:47],s61 offen offset:1024 lds
M v[24:27],a[192:195],a[232:235],v[24:27]
T a[210:211],v187 offset:4096
M v[40:43],a[192:195],a[240:243],0
B v220,s[44:47],s61 offen offset:2048 lds
M v[40:43],a[196:199],a[244:247],v[40:43]
W lgkmcnt(1)
W vmcnt(15)
s_mov_b32 m0,0xe000
s_add_u32 s27,s26,384
s_and_b32 s27,s27,0x380
s_add_u32 s27,s27,s66
s_add_u32 s68,s27,0
T a[212:213],v187 offset:8192
R a[56:59],v68 offset:2048
M v[56:59],a[196:199],a[252:255],0
B v212,s[48:51],s68 offen lds
s_mov_b32 m0,0x18000
M v[56:59],a[192:195],a[248:251],v[56:59]
W lgkmcnt(15)
T a[214:215],v187 offset:12288
R a[76:79],v69 offset:2048
B v221,s[44:47],s61 offen offset:3072 lds
s_add_u32 s61,s61,s62
s_mov_b32 m0,0xe000
M v[88:91],a[200:203],a[224:227],0
B v213,s[48:51],s68 offen offset:1024 lds
M v[88:91],a[204:207],a[228:231],v[88:91]
s_mov_b32 m0,0x19000
T a[216:217],v187 offset:8
M v[104:107],a[204:207],a[236:239],0
B v218,s[44:47],s61 offen lds
M v[104:107],a[200:203],a[232:235],v[104:107]
W lgkmcnt(1)
W vmcnt(17)
s_mov_b32 m0,0xe800
s_add_u32 s68,s27,16384
T a[218:219],v187 offset:4104
R a[96:99],v68 offset:4096
M v[120:123],a[200:203],a[240:243],0
B v212,s[48:51],s68 offen lds
s_mov_b32 m0,0x19000
M v[120:123],a[204:207],a[244:247],v[120:123]
T a[220:221],v187 offset:8200
R a[116:119],v69 offset:4096
B v219,s[44:47],s61 offen offset:1024 lds
s_mov_b32 m0,0xe800
M v[136:139],a[204:207],a[252:255],0
B v213,s[48:51],s68 offen offset:1024 lds
M v[136:139],a[200:203],a[248:251],v[136:139]
W lgkmcnt(15)
T a[222:223],v187 offset:12296
M v[12:15],a[208:211],a[224:227],0
W vmcnt(63)
s_barrier
s_mov_b32 m0,0x19000
M v[12:15],a[212:215],a[228:231],v[12:15]
W lgkmcnt(1)
T a[192:193],v184 offset:16384
B v220,s[44:47],s61 offen offset:2048 lds
s_mov_b32 m0,0xf000
s_add_u32 s68,s27,32768
M v[28:31],a[212:215],a[236:239],0
B v212,s[48:51],s68 offen lds
s_mov_b32 m0,0x0
M v[28:31],a[208:211],a[232:235],v[28:31]
W vmcnt(20)
T a[194:195],v184 offset:20480
R a[136:139],v68 offset:6144
B v214,s[40:43],s60 offen nt lds
s_mov_b32 m0,0xf000
M v[44:47],a[208:211],a[240:243],0
B v213,s[48:51],s68 offen offset:1024 lds
s_mov_b32 m0,0x19000
M v[44:47],a[212:215],a[244:247],v[44:47]
T a[196:197],v184 offset:24576
R a[156:159],v69 offset:6144
B v221,s[44:47],s61 offen offset:3072 lds
s_add_u32 s61,s61,s63
s_mov_b32 m0,0x0
M v[60:63],a[212:215],a[252:255],0
B v215,s[40:43],s60 offen offset:1024 nt lds
s_mov_b32 m0,0x1a000
M v[60:63],a[208:211],a[248:251],v[60:63]
W lgkmcnt(5)
T a[198:199],v184 offset:28672
B v218,s[44:47],s61 offen lds
s_mov_b32 m0,0x0
M v[92:95],a[216:219],a[224:227],0
B v216,s[40:43],s60 offen offset:2048 nt lds
s_mov_b32 m0,0x1a000
M v[92:95],a[220:223],a[228:231],v[92:95]
W lgkmcnt(1)
T a[200:201],v184 offset:16392
B v219,s[44:47],s61 offen offset:1024 lds
s_mov_b32 m0,0xf800
s_add_u32 s68,s27,49152
M v[108:111],a[220:223],a[236:239],0
B v212,s[48:51],s68 offen lds
s_mov_b32 m0,0x0
M v[108:111],a[216:219],a[232:235],v[108:111]
T a[202:203],v184 offset:20488
B v217,s[40:43],s60 offen offset:3072 nt lds
s_add_u32 s60,s60,s76
s_mov_b32 m0,0xf800
M v[124:127],a[216:219],a[240:243],0
B v213,s[48:51],s68 offen offset:1024 lds
s_mov_b32 m0,0x1a000
M v[124:127],a[220:223],a[244:247],v[124:127]
T a[204:205],v184 offset:24584
B v220,s[44:47],s61 offen offset:2048 lds
s_mov_b32 m0,0x1000
M v[140:143],a[220:223],a[252:255],0
B v214,s[40:43],s60 offen nt lds
M v[140:143],a[216:219],a[248:251],v[140:143]
W lgkmcnt(3)
s_mov_b32 m0,0x1a000
T a[206:207],v184 offset:28680
M v[0:3],a[192:195],a[16:19],v[0:3]
B v221,s[44:47],s61 offen offset:3072 lds
s_add_u32 s61,s61,s62
s_mov_b32 m0,0x1000
M v[0:3],a[196:199],a[36:39],v[0:3]
W vmcnt(31)
T a[208:209],v185 offset:16384
B v215,s[40:43],s60 offen offset:1024 nt lds
s_mov_b32 m0,0x1b000
M v[16:19],a[196:199],a[76:79],v[16:19]
R a[224:227],v210
B v218,s[44:47],s61 offen lds
M v[16:19],a[192:195],a[56:59],v[16:19]
T a[210:211],v185 offset:20480
M v[32:35],a[192:195],a[96:99],v[32:35]
R a[228:231],v211
M v[32:35],a[196:199],a[116:119],v[32:35]
T a[212:213],v185 offset:24576
M v[48:51],a[196:199],a[156:159],v[48:51]
B v219,s[44:47],s61 offen offset:1024 lds
s_mov_b32 m0,0x1000
M v[48:51],a[192:195],a[136:139],v[48:51]
W lgkmcnt(5)
T a[214:215],v185 offset:28672
B v216,s[40:43],s60 offen offset:2048 nt lds
s_mov_b32 m0,0x1b000
M v[80:83],a[200:203],a[16:19],v[80:83]
B v220,s[44:47],s61 offen offset:2048 lds
s_mov_b32 m0,0x1000
M v[80:83],a[204:207],a[36:39],v[80:83]
W lgkmcnt(2)
T a[216:217],v185 offset:16392
B v217,s[40:43],s60 offen offset:3072 nt lds
s_add_u32 s60,s60,s76
M v[96:99],a[204:207],a[76:79],v[96:99]
s_mov_b32 m0,0x1b000
M v[96:99],a[200:203],a[56:59],v[96:99]
T a[218:219],v185 offset:20488
B v221,s[44:47],s61 offen offset:3072 lds
s_add_u32 s61,s61,s75
s_and_b32 s61,s61,s74
s_add_u32 m0,s67,0
s_add_u32 s27,s26,512
s_and_b32 s27,s27,0x380
s_add_u32 s27,s27,s66
s_add_u32 s68,s27,0
M v[112:115],a[200:203],a[96:99],v[112:115]
B v212,s[48:51],s68 offen lds
s_mov_b32 m0,0x2000
M v[112:115],a[204:207],a[116:119],v[112:115]
T a[220:221],v185 offset:24584
)ASM"
R"ASM(
B v214,s[40:43],s60 offen nt lds
s_add_u32 m0,s67,0
M v[128:131],a[204:207],a[156:159],v[128:131]
B v213,s[48:51],s68 offen offset:1024 lds
s_mov_b32 m0,0x1c000
M v[128:131],a[200:203],a[136:139],v[128:131]
W lgkmcnt(3)
T a[222:223],v185 offset:28680
B v218,s[44:47],s61 offen lds
M v[4:7],a[208:211],a[16:19],v[4:7]
B v219,s[44:47],s61 offen offset:1024 lds
s_mov_b32 m0,0x2000
M v[4:7],a[212:215],a[36:39],v[4:7]
W vmcnt(41)
T a[192:193],v186 offset:16384
B v215,s[40:43],s60 offen offset:1024 nt lds
s_mov_b32 m0,0x1c000
M v[20:23],a[212:215],a[76:79],v[20:23]
R a[232:235],v210 offset:2048
B v220,s[44:47],s61 offen offset:2048 lds
M v[20:23],a[208:211],a[56:59],v[20:23]
T a[194:195],v186 offset:20480
B v221,s[44:47],s61 offen offset:3072 lds
s_add_u32 s61,s61,s62
s_mov_b32 m0,0x2000
M v[36:39],a[208:211],a[96:99],v[36:39]
R a[236:239],v211 offset:2048
B v216,s[40:43],s60 offen offset:2048 nt lds
M v[36:39],a[212:215],a[116:119],v[36:39]
s_mov_b32 m0,0x1d000
T a[196:197],v186 offset:24576
M v[52:55],a[212:215],a[156:159],v[52:55]
B v218,s[44:47],s61 offen lds
s_mov_b32 m0,0x2000
M v[52:55],a[208:211],a[136:139],v[52:55]
W lgkmcnt(5)
T a[198:199],v186 offset:28672
B v217,s[40:43],s60 offen offset:3072 nt lds
s_add_u32 s60,s60,s76
s_mov_b32 m0,0x1d000
M v[84:87],a[216:219],a[16:19],v[84:87]
B v219,s[44:47],s61 offen offset:1024 lds
M v[84:87],a[220:223],a[36:39],v[84:87]
W lgkmcnt(2)
T a[200:201],v186 offset:16392
M v[100:103],a[220:223],a[76:79],v[100:103]
B v220,s[44:47],s61 offen offset:2048 lds
s_mov_b32 m0,0x3000
M v[100:103],a[216:219],a[56:59],v[100:103]
T a[202:203],v186 offset:20488
B v214,s[40:43],s60 offen nt lds
s_add_u32 m0,s67,2048
s_add_u32 s68,s27,16384
M v[116:119],a[216:219],a[96:99],v[116:119]
B v212,s[48:51],s68 offen lds
s_mov_b32 m0,0x1d000
M v[116:119],a[220:223],a[116:119],v[116:119]
T a[204:205],v186 offset:24584
B v221,s[44:47],s61 offen offset:3072 lds
s_add_u32 s61,s61,s63
s_add_u32 m0,s67,2048
M v[132:135],a[220:223],a[156:159],v[132:135]
B v213,s[48:51],s68 offen offset:1024 lds
s_mov_b32 m0,0x1e000
M v[132:135],a[216:219],a[136:139],v[132:135]
W lgkmcnt(3)
T a[206:207],v186 offset:28680
B v218,s[44:47],s61 offen lds
s_mov_b32 m0,0x3000
M v[8:11],a[192:195],a[16:19],v[8:11]
B v215,s[40:43],s60 offen offset:1024 nt lds
s_mov_b32 m0,0x1e000
M v[8:11],a[196:199],a[36:39],v[8:11]
W vmcnt(53)
T a[208:209],v187 offset:16384
B v219,s[44:47],s61 offen offset:1024 lds
M v[24:27],a[196:199],a[76:79],v[24:27]
R a[240:243],v210 offset:4096
B v220,s[44:47],s61 offen offset:2048 lds
s_mov_b32 m0,0x3000
M v[24:27],a[192:195],a[56:59],v[24:27]
T a[210:211],v187 offset:20480
B v216,s[40:43],s60 offen offset:2048 nt lds
s_mov_b32 m0,0x1e000
M v[40:43],a[192:195],a[96:99],v[40:43]
R a[244:247],v211 offset:4096
B v221,s[44:47],s61 offen offset:3072 lds
s_add_u32 s61,s61,s62
s_mov_b32 m0,0x3000
M v[40:43],a[196:199],a[116:119],v[40:43]
T a[212:213],v187 offset:24576
B v217,s[40:43],s60 offen offset:3072 nt lds
s_add_u32 s60,s60,s77
s_mov_b32 m0,0x1f000
M v[56:59],a[196:199],a[156:159],v[56:59]
B v218,s[44:47],s61 offen lds
M v[56:59],a[192:195],a[136:139],v[56:59]
W lgkmcnt(5)
T a[214:215],v187 offset:28672
M v[88:91],a[200:203],a[16:19],v[88:91]
B v219,s[44:47],s61 offen offset:1024 lds
s_mov_b32 m0,0x4000
M v[88:91],a[204:207],a[36:39],v[88:91]
W lgkmcnt(2)
T a[216:217],v187 offset:16392
B v214,s[40:43],s60 offen nt lds
s_mov_b32 m0,0x1f000
M v[104:107],a[204:207],a[76:79],v[104:107]
B v220,s[44:47],s61 offen offset:2048 lds
M v[104:107],a[200:203],a[56:59],v[104:107]
T a[218:219],v187 offset:20488
B v221,s[44:47],s61 offen offset:3072 lds
s_add_u32 s61,s61,s75
s_and_b32 s61,s61,s74
s_add_u32 m0,s67,4096
s_add_u32 s68,s27,32768
M v[120:123],a[200:203],a[96:99],v[120:123]
B v212,s[48:51],s68 offen lds
s_mov_b32 m0,0x4000
M v[120:123],a[204:207],a[116:119],v[120:123]
T a[220:221],v187 offset:24584
B v215,s[40:43],s60 offen offset:1024 nt lds
s_add_u32 m0,s67,4096
M v[136:139],a[204:207],a[156:159],v[136:139]
B v213,s[48:51],s68 offen offset:1024 lds
s_mov_b32 m0,0x4000
M v[136:139],a[200:203],a[136:139],v[136:139]
W lgkmcnt(3)
T a[222:223],v187 offset:28680
B v216,s[40:43],s60 offen offset:2048 nt lds
M v[12:15],a[208:211],a[16:19],v[12:15]
W vmcnt(33)
s_barrier
s_mov_b32 m0,0x10000
M v[12:15],a[212:215],a[36:39],v[12:15]
W vmcnt(63)
T a[192:193],v184 offset:32768
B v218,s[44:47],s61 offen lds
s_mov_b32 m0,0x4000
M v[28:31],a[212:215],a[76:79],v[28:31]
R a[248:251],v210 offset:6144
B v217,s[40:43],s60 offen offset:3072 nt lds
s_add_u32 s60,s60,s76
M v[28:31],a[208:211],a[56:59],v[28:31]
s_mov_b32 m0,0x10000
T a[194:195],v184 offset:36864
M v[44:47],a[208:211],a[96:99],v[44:47]
R a[252:255],v211 offset:6144
B v219,s[44:47],s61 offen offset:1024 lds
M v[44:47],a[212:215],a[116:119],v[44:47]
s_mov_b32 m0,0x5000
T a[196:197],v184 offset:40960
M v[60:63],a[212:215],a[156:159],v[60:63]
B v214,s[40:43],s60 offen nt lds
M v[60:63],a[208:211],a[136:139],v[60:63]
W lgkmcnt(5)
s_mov_b32 m0,0x10000
T a[198:199],v184 offset:45056
M v[92:95],a[216:219],a[16:19],v[92:95]
B v220,s[44:47],s61 offen offset:2048 lds
M v[92:95],a[220:223],a[36:39],v[92:95]
W lgkmcnt(2)
s_mov_b32 m0,0x5000
T a[200:201],v184 offset:32776
M v[108:111],a[220:223],a[76:79],v[108:111]
B v215,s[40:43],s60 offen offset:1024 nt lds
s_mov_b32 m0,0x10000
M v[108:111],a[216:219],a[56:59],v[108:111]
T a[202:203],v184 offset:36872
B v221,s[44:47],s61 offen offset:3072 lds
s_add_u32 s61,s61,s62
s_add_u32 m0,s67,6144
s_add_u32 s68,s27,49152
M v[124:127],a[216:219],a[96:99],v[124:127]
B v212,s[48:51],s68 offen lds
s_mov_b32 m0,0x11000
M v[124:127],a[220:223],a[116:119],v[124:127]
T a[204:205],v184 offset:40968
B v218,s[44:47],s61 offen lds
s_add_u32 m0,s67,6144
M v[140:143],a[220:223],a[156:159],v[140:143]
B v213,s[48:51],s68 offen offset:1024 lds
s_mov_b32 m0,0x5000
M v[140:143],a[216:219],a[136:139],v[140:143]
W lgkmcnt(3)
T a[206:207],v184 offset:45064
B v216,s[40:43],s60 offen offset:2048 nt lds
s_mov_b32 m0,0x11000
M v[0:3],a[192:195],a[224:227],v[0:3]
B v219,s[44:47],s61 offen offset:1024 lds
s_mov_b32 m0,0x5000
M v[0:3],a[196:199],a[228:231],v[0:3]
W vmcnt(63)
T a[208:209],v185 offset:32768
B v217,s[40:43],s60 offen offset:3072 nt lds
s_add_u32 s60,s60,s76
M v[16:19],a[196:199],a[236:239],v[16:19]
s_mov_b32 m0,0x11000
R a[16:19],v68
M v[16:19],a[192:195],a[232:235],v[16:19]
T a[210:211],v185 offset:36864
B v220,s[44:47],s61 offen offset:2048 lds
s_mov_b32 m0,0x6000
M v[32:35],a[192:195],a[240:243],v[32:35]
R a[36:39],v69
B v214,s[40:43],s60 offen nt lds
M v[32:35],a[196:199],a[244:247],v[32:35]
s_mov_b32 m0,0x11000
T a[212:213],v185 offset:40960
M v[48:51],a[196:199],a[252:255],v[48:51]
B v221,s[44:47],s61 offen offset:3072 lds
s_add_u32 s61,s61,s63
M v[48:51],a[192:195],a[248:251],v[48:51]
W lgkmcnt(5)
s_mov_b32 m0,0x6000
T a[214:215],v185 offset:45056
M v[80:83],a[200:203],a[224:227],v[80:83]
B v215,s[40:43],s60 offen offset:1024 nt lds
M v[80:83],a[204:207],a[228:231],v[80:83]
W lgkmcnt(2)
s_mov_b32 m0,0x12000
T a[216:217],v185 offset:32776
M v[96:99],a[204:207],a[236:239],v[96:99]
B v218,s[44:47],s61 offen lds
s_mov_b32 m0,0x6000
M v[96:99],a[200:203],a[232:235],v[96:99]
T a[218:219],v185 offset:36872
B v216,s[40:43],s60 offen offset:2048 nt lds
s_mov_b32 m0,0xe000
s_add_u32 s27,s26,640
s_and_b32 s27,s27,0x380
s_add_u32 s27,s27,s66
s_add_u32 s68,s27,0
M v[112:115],a[200:203],a[240:243],v[112:115]
B v212,s[48:51],s68 offen lds
s_mov_b32 m0,0x12000
M v[112:115],a[204:207],a[244:247],v[112:115]
T a[220:221],v185 offset:40968
B v219,s[44:47],s61 offen offset:1024 lds
s_mov_b32 m0,0xe000
M v[128:131],a[204:207],a[252:255],v[128:131]
B v213,s[48:51],s68 offen offset:1024 lds
s_mov_b32 m0,0x6000
M v[128:131],a[200:203],a[248:251],v[128:131]
W lgkmcnt(3)
T a[222:223],v185 offset:45064
B v217,s[40:43],s60 offen offset:3072 nt lds
s_add_u32 s60,s60,s76
s_mov_b32 m0,0x12000
M v[4:7],a[208:211],a[224:227],v[4:7]
B v220,s[44:47],s61 offen offset:2048 lds
M v[4:7],a[212:215],a[228:231],v[4:7]
W vmcnt(63)
T a[192:193],v186 offset:32768
M v[20:23],a[212:215],a[236:239],v[20:23]
R a[56:59],v68 offset:2048
B v221,s[44:47],s61 offen offset:3072 lds
s_add_u32 s61,s61,s62
s_mov_b32 m0,0x7000
M v[20:23],a[208:211],a[232:235],v[20:23]
T a[194:195],v186 offset:36864
B v214,s[40:43],s60 offen nt lds
M v[36:39],a[208:211],a[240:243],v[36:39]
R a[76:79],v69 offset:2048
M v[36:39],a[212:215],a[244:247],v[36:39]
s_mov_b32 m0,0x13000
T a[196:197],v186 offset:40960
M v[52:55],a[212:215],a[252:255],v[52:55]
B v218,s[44:47],s61 offen lds
s_mov_b32 m0,0x7000
M v[52:55],a[208:211],a[248:251],v[52:55]
W lgkmcnt(5)
T a[198:199],v186 offset:45056
B v215,s[40:43],s60 offen offset:1024 nt lds
s_mov_b32 m0,0x13000
M v[84:87],a[216:219],a[224:227],v[84:87]
B v219,s[44:47],s61 offen offset:1024 lds
M v[84:87],a[220:223],a[228:231],v[84:87]
W lgkmcnt(2)
s_mov_b32 m0,0x7000
T a[200:201],v186 offset:32776
M v[100:103],a[220:223],a[236:239],v[100:103]
B v216,s[40:43],s60 offen offset:2048 nt lds
s_mov_b32 m0,0x13000
M v[100:103],a[216:219],a[232:235],v[100:103]
T a[202:203],v186 offset:36872
B v220,s[44:47],s61 offen offset:2048 lds
s_mov_b32 m0,0xe800
s_add_u32 s68,s27,16384
M v[116:119],a[216:219],a[240:243],v[116:119]
B v212,s[48:51],s68 offen lds
s_mov_b32 m0,0x7000
M v[116:119],a[220:223],a[244:247],v[116:119]
T a[204:205],v186 offset:40968
B v217,s[40:43],s60 offen offset:3072 nt lds
s_add_u32 s60,s60,s77
s_mov_b32 m0,0xe800
M v[132:135],a[220:223],a[252:255],v[132:135]
B v213,s[48:51],s68 offen offset:1024 lds
s_mov_b32 m0,0x13000
)ASM"
R"ASM(
M v[132:135],a[216:219],a[248:251],v[132:135]
W lgkmcnt(3)
T a[206:207],v186 offset:45064
B v221,s[44:47],s61 offen offset:3072 lds
s_add_u32 s61,s61,s75
s_and_b32 s61,s61,s74
M v[8:11],a[192:195],a[224:227],v[8:11]
M v[8:11],a[196:199],a[228:231],v[8:11]
W vmcnt(63)
T a[208:209],v187 offset:32768
M v[24:27],a[196:199],a[236:239],v[24:27]
R a[96:99],v68 offset:4096
M v[24:27],a[192:195],a[232:235],v[24:27]
T a[210:211],v187 offset:36864
M v[40:43],a[192:195],a[240:243],v[40:43]
R a[116:119],v69 offset:4096
M v[40:43],a[196:199],a[244:247],v[40:43]
T a[212:213],v187 offset:40960
M v[56:59],a[196:199],a[252:255],v[56:59]
M v[56:59],a[192:195],a[248:251],v[56:59]
W lgkmcnt(5)
T a[214:215],v187 offset:45056
M v[88:91],a[200:203],a[224:227],v[88:91]
M v[88:91],a[204:207],a[228:231],v[88:91]
W lgkmcnt(2)
T a[216:217],v187 offset:32776
M v[104:107],a[204:207],a[236:239],v[104:107]
M v[104:107],a[200:203],a[232:235],v[104:107]
s_mov_b32 m0,0xf000
s_add_u32 s68,s27,32768
T a[218:219],v187 offset:36872
M v[120:123],a[200:203],a[240:243],v[120:123]
B v212,s[48:51],s68 offen lds
M v[120:123],a[204:207],a[244:247],v[120:123]
T a[220:221],v187 offset:40968
M v[136:139],a[204:207],a[252:255],v[136:139]
B v213,s[48:51],s68 offen offset:1024 lds
M v[136:139],a[200:203],a[248:251],v[136:139]
W lgkmcnt(3)
T a[222:223],v187 offset:45064
M v[12:15],a[208:211],a[224:227],v[12:15]
W vmcnt(41)
s_barrier
s_mov_b32 m0,0x14000
M v[12:15],a[212:215],a[228:231],v[12:15]
W vmcnt(63)
T a[192:193],v184 offset:49152
B v218,s[44:47],s61 offen lds
M v[28:31],a[212:215],a[236:239],v[28:31]
R a[136:139],v68 offset:6144
M v[28:31],a[208:211],a[232:235],v[28:31]
T a[194:195],v184 offset:53248
M v[44:47],a[208:211],a[240:243],v[44:47]
R a[156:159],v69 offset:6144
B v219,s[44:47],s61 offen offset:1024 lds
M v[44:47],a[212:215],a[244:247],v[44:47]
T a[196:197],v184 offset:57344
M v[60:63],a[212:215],a[252:255],v[60:63]
M v[60:63],a[208:211],a[248:251],v[60:63]
W lgkmcnt(5)
T a[198:199],v184 offset:61440
M v[92:95],a[216:219],a[224:227],v[92:95]
B v220,s[44:47],s61 offen offset:2048 lds
M v[92:95],a[220:223],a[228:231],v[92:95]
W lgkmcnt(2)
T a[200:201],v184 offset:49160
M v[108:111],a[220:223],a[236:239],v[108:111]
B v221,s[44:47],s61 offen offset:3072 lds
s_add_u32 s61,s61,s62
M v[108:111],a[216:219],a[232:235],v[108:111]
s_mov_b32 m0,0xf800
s_add_u32 s68,s27,49152
T a[202:203],v184 offset:53256
M v[124:127],a[216:219],a[240:243],v[124:127]
B v212,s[48:51],s68 offen lds
s_mov_b32 m0,0x15000
M v[124:127],a[220:223],a[244:247],v[124:127]
T a[204:205],v184 offset:57352
B v218,s[44:47],s61 offen lds
s_mov_b32 m0,0xf800
M v[140:143],a[220:223],a[252:255],v[140:143]
B v213,s[48:51],s68 offen offset:1024 lds
M v[140:143],a[216:219],a[248:251],v[140:143]
W lgkmcnt(3)
s_mov_b32 m0,0x15000
T a[206:207],v184 offset:61448
M v[0:3],a[192:195],a[16:19],v[0:3]
B v219,s[44:47],s61 offen offset:1024 lds
M v[0:3],a[196:199],a[36:39],v[0:3]
W vmcnt(63)
T a[208:209],v185 offset:49152
M v[16:19],a[196:199],a[76:79],v[16:19]
R a[224:227],v210
M v[16:19],a[192:195],a[56:59],v[16:19]
T a[210:211],v185 offset:53248
B v220,s[44:47],s61 offen offset:2048 lds
M v[32:35],a[192:195],a[96:99],v[32:35]
R a[228:231],v211
M v[32:35],a[196:199],a[116:119],v[32:35]
T a[212:213],v185 offset:57344
M v[48:51],a[196:199],a[156:159],v[48:51]
B v221,s[44:47],s61 offen offset:3072 lds
s_add_u32 s61,s61,s63
M v[48:51],a[192:195],a[136:139],v[48:51]
W lgkmcnt(5)
T a[214:215],v185 offset:61440
M v[80:83],a[200:203],a[16:19],v[80:83]
M v[80:83],a[204:207],a[36:39],v[80:83]
W lgkmcnt(2)
s_mov_b32 m0,0x16000
T a[216:217],v185 offset:49160
M v[96:99],a[204:207],a[76:79],v[96:99]
B v218,s[44:47],s61 offen lds
M v[96:99],a[200:203],a[56:59],v[96:99]
s_add_u32 m0,s67,0
s_add_u32 s27,s26,768
s_and_b32 s27,s27,0x380
s_add_u32 s27,s27,s66
s_add_u32 s68,s27,0
T a[218:219],v185 offset:53256
M v[112:115],a[200:203],a[96:99],v[112:115]
B v212,s[48:51],s68 offen lds
s_mov_b32 m0,0x16000
M v[112:115],a[204:207],a[116:119],v[112:115]
T a[220:221],v185 offset:57352
B v219,s[44:47],s61 offen offset:1024 lds
s_add_u32 m0,s67,0
M v[128:131],a[204:207],a[156:159],v[128:131]
B v213,s[48:51],s68 offen offset:1024 lds
M v[128:131],a[200:203],a[136:139],v[128:131]
W lgkmcnt(3)
s_mov_b32 m0,0x16000
T a[222:223],v185 offset:61448
M v[4:7],a[208:211],a[16:19],v[4:7]
B v220,s[44:47],s61 offen offset:2048 lds
M v[4:7],a[212:215],a[36:39],v[4:7]
W vmcnt(63)
T a[192:193],v186 offset:49152
M v[20:23],a[212:215],a[76:79],v[20:23]
R a[232:235],v210 offset:2048
B v221,s[44:47],s61 offen offset:3072 lds
s_add_u32 s61,s61,s62
M v[20:23],a[208:211],a[56:59],v[20:23]
T a[194:195],v186 offset:53248
M v[36:39],a[208:211],a[96:99],v[36:39]
R a[236:239],v211 offset:2048
M v[36:39],a[212:215],a[116:119],v[36:39]
s_mov_b32 m0,0x17000
T a[196:197],v186 offset:57344
M v[52:55],a[212:215],a[156:159],v[52:55]
B v218,s[44:47],s61 offen lds
M v[52:55],a[208:211],a[136:139],v[52:55]
W lgkmcnt(5)
T a[198:199],v186 offset:61440
M v[84:87],a[216:219],a[16:19],v[84:87]
B v219,s[44:47],s61 offen offset:1024 lds
M v[84:87],a[220:223],a[36:39],v[84:87]
W lgkmcnt(2)
T a[200:201],v186 offset:49160
M v[100:103],a[220:223],a[76:79],v[100:103]
M v[100:103],a[216:219],a[56:59],v[100:103]
T a[202:203],v186 offset:53256
B v220,s[44:47],s61 offen offset:2048 lds
s_add_u32 m0,s67,2048
s_add_u32 s68,s27,16384
M v[116:119],a[216:219],a[96:99],v[116:119]
B v212,s[48:51],s68 offen lds
M v[116:119],a[220:223],a[116:119],v[116:119]
T a[204:205],v186 offset:57352
M v[132:135],a[220:223],a[156:159],v[132:135]
B v213,s[48:51],s68 offen offset:1024 lds
s_mov_b32 m0,0x17000
M v[132:135],a[216:219],a[136:139],v[132:135]
W lgkmcnt(3)
T a[206:207],v186 offset:61448
B v221,s[44:47],s61 offen offset:3072 lds
s_add_u32 s61,s61,s75
s_and_b32 s61,s61,s74
M v[8:11],a[192:195],a[16:19],v[8:11]
M v[8:11],a[196:199],a[36:39],v[8:11]
W vmcnt(60)
T a[208:209],v187 offset:49152
M v[24:27],a[196:199],a[76:79],v[24:27]
R a[240:243],v210 offset:4096
M v[24:27],a[192:195],a[56:59],v[24:27]
T a[210:211],v187 offset:53248
M v[40:43],a[192:195],a[96:99],v[40:43]
R a[244:247],v211 offset:4096
M v[40:43],a[196:199],a[116:119],v[40:43]
T a[212:213],v187 offset:57344
M v[56:59],a[196:199],a[156:159],v[56:59]
M v[56:59],a[192:195],a[136:139],v[56:59]
W lgkmcnt(5)
T a[214:215],v187 offset:61440
M v[88:91],a[200:203],a[16:19],v[88:91]
M v[88:91],a[204:207],a[36:39],v[88:91]
W lgkmcnt(2)
T a[216:217],v187 offset:49160
M v[104:107],a[204:207],a[76:79],v[104:107]
M v[104:107],a[200:203],a[56:59],v[104:107]
s_add_u32 m0,s67,4096
s_add_u32 s68,s27,32768
T a[218:219],v187 offset:53256
M v[120:123],a[200:203],a[96:99],v[120:123]
B v212,s[48:51],s68 offen lds
M v[120:123],a[204:207],a[116:119],v[120:123]
T a[220:221],v187 offset:57352
M v[136:139],a[204:207],a[156:159],v[136:139]
B v213,s[48:51],s68 offen offset:1024 lds
M v[136:139],a[200:203],a[136:139],v[136:139]
W lgkmcnt(3)
T a[222:223],v187 offset:61448
M v[12:15],a[208:211],a[16:19],v[12:15]
W vmcnt(26)
s_barrier
s_mov_b32 m0,0x18000
M v[12:15],a[212:215],a[36:39],v[12:15]
W vmcnt(63)
T a[192:193],v184
B v218,s[44:47],s61 offen lds
M v[28:31],a[212:215],a[76:79],v[28:31]
R a[248:251],v210 offset:6144
M v[28:31],a[208:211],a[56:59],v[28:31]
T a[194:195],v184 offset:4096
M v[44:47],a[208:211],a[96:99],v[44:47]
R a[252:255],v211 offset:6144
B v219,s[44:47],s61 offen offset:1024 lds
M v[44:47],a[212:215],a[116:119],v[44:47]
T a[196:197],v184 offset:8192
M v[60:63],a[212:215],a[156:159],v[60:63]
M v[60:63],a[208:211],a[136:139],v[60:63]
W lgkmcnt(5)
T a[198:199],v184 offset:12288
M v[92:95],a[216:219],a[16:19],v[92:95]
B v220,s[44:47],s61 offen offset:2048 lds
M v[92:95],a[220:223],a[36:39],v[92:95]
W lgkmcnt(2)
T a[200:201],v184 offset:8
M v[108:111],a[220:223],a[76:79],v[108:111]
B v221,s[44:47],s61 offen offset:3072 lds
s_add_u32 s61,s61,s62
M v[108:111],a[216:219],a[56:59],v[108:111]
s_add_u32 m0,s67,6144
s_add_u32 s68,s27,49152
T a[202:203],v184 offset:4104
M v[124:127],a[216:219],a[96:99],v[124:127]
B v212,s[48:51],s68 offen lds
s_mov_b32 m0,0x19000
M v[124:127],a[220:223],a[116:119],v[124:127]
T a[204:205],v184 offset:8200
B v218,s[44:47],s61 offen lds
s_add_u32 m0,s67,6144
M v[140:143],a[220:223],a[156:159],v[140:143]
B v213,s[48:51],s68 offen offset:1024 lds
M v[140:143],a[216:219],a[136:139],v[140:143]
W lgkmcnt(3)
s_mov_b32 m0,0x19000
T a[206:207],v184 offset:12296
M v[0:3],a[192:195],a[224:227],v[0:3]
B v219,s[44:47],s61 offen offset:1024 lds
M v[0:3],a[196:199],a[228:231],v[0:3]
W vmcnt(63)
T a[208:209],v185
M v[16:19],a[196:199],a[236:239],v[16:19]
R a[16:19],v68
M v[16:19],a[192:195],a[232:235],v[16:19]
T a[210:211],v185 offset:4096
B v220,s[44:47],s61 offen offset:2048 lds
M v[32:35],a[192:195],a[240:243],v[32:35]
R a[36:39],v69
M v[32:35],a[196:199],a[244:247],v[32:35]
T a[212:213],v185 offset:8192
M v[48:51],a[196:199],a[252:255],v[48:51]
B v221,s[44:47],s61 offen offset:3072 lds
s_add_u32 s61,s61,s63
M v[48:51],a[192:195],a[248:251],v[48:51]
W lgkmcnt(5)
T a[214:215],v185 offset:12288
M v[80:83],a[200:203],a[224:227],v[80:83]
M v[80:83],a[204:207],a[228:231],v[80:83]
W lgkmcnt(2)
s_mov_b32 m0,0x1a000
T a[216:217],v185 offset:8
M v[96:99],a[204:207],a[236:239],v[96:99]
B v218,s[44:47],s61 offen lds
M v[96:99],a[200:203],a[232:235],v[96:99]
s_mov_b32 m0,0xe000
s_add_u32 s27,s26,896
s_and_b32 s27,s27,0x380
s_add_u32 s27,s27,s66
s_add_u32 s68,s27,0
T a[218:219],v185 offset:4104
M v[112:115],a[200:203],a[240:243],v[112:115]
B v212,s[48:51],s68 offen lds
s_mov_b32 m0,0x1a000
M v[112:115],a[204:207],a[244:247],v[112:115]
T a[220:221],v185 offset:8200
B v219,s[44:47],s61 offen offset:1024 lds
s_mov_b32 m0,0xe000
M v[128:131],a[204:207],a[252:255],v[128:131]
)ASM"
R"ASM(
B v213,s[48:51],s68 offen offset:1024 lds
M v[128:131],a[200:203],a[248:251],v[128:131]
W lgkmcnt(3)
s_mov_b32 m0,0x1a000
T a[222:223],v185 offset:12296
M v[4:7],a[208:211],a[224:227],v[4:7]
B v220,s[44:47],s61 offen offset:2048 lds
M v[4:7],a[212:215],a[228:231],v[4:7]
W vmcnt(63)
T a[192:193],v186
M v[20:23],a[212:215],a[236:239],v[20:23]
R a[56:59],v68 offset:2048
B v221,s[44:47],s61 offen offset:3072 lds
s_add_u32 s61,s61,s62
M v[20:23],a[208:211],a[232:235],v[20:23]
T a[194:195],v186 offset:4096
M v[36:39],a[208:211],a[240:243],v[36:39]
R a[76:79],v69 offset:2048
M v[36:39],a[212:215],a[244:247],v[36:39]
s_mov_b32 m0,0x1b000
T a[196:197],v186 offset:8192
M v[52:55],a[212:215],a[252:255],v[52:55]
B v218,s[44:47],s61 offen lds
M v[52:55],a[208:211],a[248:251],v[52:55]
W lgkmcnt(5)
T a[198:199],v186 offset:12288
M v[84:87],a[216:219],a[224:227],v[84:87]
B v219,s[44:47],s61 offen offset:1024 lds
M v[84:87],a[220:223],a[228:231],v[84:87]
W lgkmcnt(2)
T a[200:201],v186 offset:8
M v[100:103],a[220:223],a[236:239],v[100:103]
M v[100:103],a[216:219],a[232:235],v[100:103]
T a[202:203],v186 offset:4104
B v220,s[44:47],s61 offen offset:2048 lds
s_mov_b32 m0,0xe800
s_add_u32 s68,s27,16384
M v[116:119],a[216:219],a[240:243],v[116:119]
B v212,s[48:51],s68 offen lds
M v[116:119],a[220:223],a[244:247],v[116:119]
T a[204:205],v186 offset:8200
M v[132:135],a[220:223],a[252:255],v[132:135]
B v213,s[48:51],s68 offen offset:1024 lds
s_mov_b32 m0,0x1b000
M v[132:135],a[216:219],a[248:251],v[132:135]
W lgkmcnt(3)
T a[206:207],v186 offset:12296
B v221,s[44:47],s61 offen offset:3072 lds
s_add_u32 s61,s61,s75
s_and_b32 s61,s61,s74
M v[8:11],a[192:195],a[224:227],v[8:11]
M v[8:11],a[196:199],a[228:231],v[8:11]
W vmcnt(46)
T a[208:209],v187
M v[24:27],a[196:199],a[236:239],v[24:27]
R a[96:99],v68 offset:4096
M v[24:27],a[192:195],a[232:235],v[24:27]
T a[210:211],v187 offset:4096
M v[40:43],a[192:195],a[240:243],v[40:43]
R a[116:119],v69 offset:4096
M v[40:43],a[196:199],a[244:247],v[40:43]
T a[212:213],v187 offset:8192
M v[56:59],a[196:199],a[252:255],v[56:59]
M v[56:59],a[192:195],a[248:251],v[56:59]
W lgkmcnt(5)
T a[214:215],v187 offset:12288
M v[88:91],a[200:203],a[224:227],v[88:91]
M v[88:91],a[204:207],a[228:231],v[88:91]
W lgkmcnt(2)
T a[216:217],v187 offset:8
M v[104:107],a[204:207],a[236:239],v[104:107]
M v[104:107],a[200:203],a[232:235],v[104:107]
s_mov_b32 m0,0xf000
s_add_u32 s68,s27,32768
T a[218:219],v187 offset:4104
M v[120:123],a[200:203],a[240:243],v[120:123]
B v212,s[48:51],s68 offen lds
M v[120:123],a[204:207],a[244:247],v[120:123]
T a[220:221],v187 offset:8200
M v[136:139],a[204:207],a[252:255],v[136:139]
B v213,s[48:51],s68 offen offset:1024 lds
M v[136:139],a[200:203],a[248:251],v[136:139]
W lgkmcnt(3)
T a[222:223],v187 offset:12296
M v[12:15],a[208:211],a[224:227],v[12:15]
W vmcnt(26)
s_barrier
s_mov_b32 m0,0x1c000
M v[12:15],a[212:215],a[228:231],v[12:15]
W vmcnt(63)
T a[192:193],v184 offset:16384
B v218,s[44:47],s61 offen lds
M v[28:31],a[212:215],a[236:239],v[28:31]
R a[136:139],v68 offset:6144
M v[28:31],a[208:211],a[232:235],v[28:31]
T a[194:195],v184 offset:20480
M v[44:47],a[208:211],a[240:243],v[44:47]
R a[156:159],v69 offset:6144
B v219,s[44:47],s61 offen offset:1024 lds
M v[44:47],a[212:215],a[244:247],v[44:47]
T a[196:197],v184 offset:24576
M v[60:63],a[212:215],a[252:255],v[60:63]
M v[60:63],a[208:211],a[248:251],v[60:63]
W lgkmcnt(5)
T a[198:199],v184 offset:28672
M v[92:95],a[216:219],a[224:227],v[92:95]
B v220,s[44:47],s61 offen offset:2048 lds
M v[92:95],a[220:223],a[228:231],v[92:95]
W lgkmcnt(2)
T a[200:201],v184 offset:16392
M v[108:111],a[220:223],a[236:239],v[108:111]
B v221,s[44:47],s61 offen offset:3072 lds
s_add_u32 s61,s61,s62
M v[108:111],a[216:219],a[232:235],v[108:111]
s_mov_b32 m0,0xf800
s_add_u32 s68,s27,49152
T a[202:203],v184 offset:20488
M v[124:127],a[216:219],a[240:243],v[124:127]
B v212,s[48:51],s68 offen lds
s_mov_b32 m0,0x1d000
M v[124:127],a[220:223],a[244:247],v[124:127]
T a[204:205],v184 offset:24584
B v218,s[44:47],s61 offen lds
s_mov_b32 m0,0xf800
M v[140:143],a[220:223],a[252:255],v[140:143]
B v213,s[48:51],s68 offen offset:1024 lds
M v[140:143],a[216:219],a[248:251],v[140:143]
W lgkmcnt(3)
s_mov_b32 m0,0x1d000
T a[206:207],v184 offset:28680
M v[0:3],a[192:195],a[16:19],v[0:3]
B v219,s[44:47],s61 offen offset:1024 lds
M v[0:3],a[196:199],a[36:39],v[0:3]
W vmcnt(63)
T a[208:209],v185 offset:16384
v_lshlrev_b32 v250,3,v245
s_mov_b32 exec_hi,0xff
buffer_load_dword v254,v250,s[36:39],s92 offen sc1
s_mov_b32 exec_hi,-1
M v[16:19],a[196:199],a[76:79],v[16:19]
R a[224:227],v210
M v[16:19],a[192:195],a[56:59],v[16:19]
T a[210:211],v185 offset:20480
B v220,s[44:47],s61 offen offset:2048 lds
M v[32:35],a[192:195],a[96:99],v[32:35]
R a[228:231],v211
M v[32:35],a[196:199],a[116:119],v[32:35]
T a[212:213],v185 offset:24576
M v[48:51],a[196:199],a[156:159],v[48:51]
B v221,s[44:47],s61 offen offset:3072 lds
s_add_u32 s61,s61,s63
M v[48:51],a[192:195],a[136:139],v[48:51]
W lgkmcnt(5)
T a[214:215],v185 offset:28672
M v[80:83],a[200:203],a[16:19],v[80:83]
M v[80:83],a[204:207],a[36:39],v[80:83]
W lgkmcnt(2)
s_mov_b32 m0,0x1e000
T a[216:217],v185 offset:16392
M v[96:99],a[204:207],a[76:79],v[96:99]
B v218,s[44:47],s61 offen lds
M v[96:99],a[200:203],a[56:59],v[96:99]
s_add_u32 m0,s67,0
s_add_u32 s27,s26,0
s_and_b32 s27,s27,0x380
s_add_u32 s27,s27,s66
s_add_u32 s68,s27,0
T a[218:219],v185 offset:20488
M v[112:115],a[200:203],a[96:99],v[112:115]
B v212,s[48:51],s68 offen lds
s_mov_b32 m0,0x1e000
M v[112:115],a[204:207],a[116:119],v[112:115]
T a[220:221],v185 offset:24584
B v219,s[44:47],s61 offen offset:1024 lds
s_add_u32 m0,s67,0
M v[128:131],a[204:207],a[156:159],v[128:131]
B v213,s[48:51],s68 offen offset:1024 lds
M v[128:131],a[200:203],a[136:139],v[128:131]
W lgkmcnt(3)
s_mov_b32 m0,0x1e000
T a[222:223],v185 offset:28680
M v[4:7],a[208:211],a[16:19],v[4:7]
B v220,s[44:47],s61 offen offset:2048 lds
M v[4:7],a[212:215],a[36:39],v[4:7]
W vmcnt(63)
T a[192:193],v186 offset:16384
M v[20:23],a[212:215],a[76:79],v[20:23]
R a[232:235],v210 offset:2048
B v221,s[44:47],s61 offen offset:3072 lds
s_add_u32 s61,s61,s62
M v[20:23],a[208:211],a[56:59],v[20:23]
T a[194:195],v186 offset:20480
M v[36:39],a[208:211],a[96:99],v[36:39]
R a[236:239],v211 offset:2048
M v[36:39],a[212:215],a[116:119],v[36:39]
s_mov_b32 m0,0x1f000
T a[196:197],v186 offset:24576
M v[52:55],a[212:215],a[156:159],v[52:55]
B v218,s[44:47],s61 offen lds
M v[52:55],a[208:211],a[136:139],v[52:55]
W lgkmcnt(5)
T a[198:199],v186 offset:28672
M v[84:87],a[216:219],a[16:19],v[84:87]
B v219,s[44:47],s61 offen offset:1024 lds
M v[84:87],a[220:223],a[36:39],v[84:87]
W lgkmcnt(2)
T a[200:201],v186 offset:16392
M v[100:103],a[220:223],a[76:79],v[100:103]
M v[100:103],a[216:219],a[56:59],v[100:103]
T a[202:203],v186 offset:20488
B v220,s[44:47],s61 offen offset:2048 lds
s_add_u32 m0,s67,2048
s_add_u32 s68,s27,16384
M v[116:119],a[216:219],a[96:99],v[116:119]
B v212,s[48:51],s68 offen lds
M v[116:119],a[220:223],a[116:119],v[116:119]
T a[204:205],v186 offset:24584
M v[132:135],a[220:223],a[156:159],v[132:135]
B v213,s[48:51],s68 offen offset:1024 lds
s_mov_b32 m0,0x1f000
M v[132:135],a[216:219],a[136:139],v[132:135]
W lgkmcnt(3)
T a[206:207],v186 offset:28680
B v221,s[44:47],s61 offen offset:3072 lds
s_add_u32 s61,s61,s75
s_and_b32 s61,s61,s74
M v[8:11],a[192:195],a[16:19],v[8:11]
M v[8:11],a[196:199],a[36:39],v[8:11]
W vmcnt(47)
T a[208:209],v187 offset:16384
M v[24:27],a[196:199],a[76:79],v[24:27]
R a[240:243],v210 offset:4096
M v[24:27],a[192:195],a[56:59],v[24:27]
T a[210:211],v187 offset:20480
M v[40:43],a[192:195],a[96:99],v[40:43]
R a[244:247],v211 offset:4096
M v[40:43],a[196:199],a[116:119],v[40:43]
T a[212:213],v187 offset:24576
M v[56:59],a[196:199],a[156:159],v[56:59]
M v[56:59],a[192:195],a[136:139],v[56:59]
W lgkmcnt(5)
T a[214:215],v187 offset:28672
M v[88:91],a[200:203],a[16:19],v[88:91]
M v[88:91],a[204:207],a[36:39],v[88:91]
W lgkmcnt(2)
T a[216:217],v187 offset:16392
M v[104:107],a[204:207],a[76:79],v[104:107]
M v[104:107],a[200:203],a[56:59],v[104:107]
s_add_u32 m0,s67,4096
s_add_u32 s68,s27,32768
T a[218:219],v187 offset:20488
M v[120:123],a[200:203],a[96:99],v[120:123]
B v212,s[48:51],s68 offen lds
M v[120:123],a[204:207],a[116:119],v[120:123]
T a[220:221],v187 offset:24584
M v[136:139],a[204:207],a[156:159],v[136:139]
B v213,s[48:51],s68 offen offset:1024 lds
M v[136:139],a[200:203],a[136:139],v[136:139]
W lgkmcnt(3)
T a[222:223],v187 offset:28680
M v[12:15],a[208:211],a[16:19],v[12:15]
W vmcnt(27)
s_barrier
M v[12:15],a[212:215],a[36:39],v[12:15]
W vmcnt(63)
T a[192:193],v184 offset:32768
M v[28:31],a[212:215],a[76:79],v[28:31]
R a[248:251],v210 offset:6144
M v[28:31],a[208:211],a[56:59],v[28:31]
T a[194:195],v184 offset:36864
M v[44:47],a[208:211],a[96:99],v[44:47]
R a[252:255],v211 offset:6144
M v[44:47],a[212:215],a[116:119],v[44:47]
T a[196:197],v184 offset:40960
M v[60:63],a[212:215],a[156:159],v[60:63]
M v[60:63],a[208:211],a[136:139],v[60:63]
W lgkmcnt(5)
T a[198:199],v184 offset:45056
M v[92:95],a[216:219],a[16:19],v[92:95]
M v[92:95],a[220:223],a[36:39],v[92:95]
W lgkmcnt(2)
T a[200:201],v184 offset:32776
M v[108:111],a[220:223],a[76:79],v[108:111]
M v[108:111],a[216:219],a[56:59],v[108:111]
s_add_u32 m0,s67,6144
s_add_u32 s68,s27,49152
T a[202:203],v184 offset:36872
M v[124:127],a[216:219],a[96:99],v[124:127]
B v212,s[48:51],s68 offen lds
M v[124:127],a[220:223],a[116:119],v[124:127]
T a[204:205],v184 offset:40968
M v[140:143],a[220:223],a[156:159],v[140:143]
B v213,s[48:51],s68 offen offset:1024 lds
M v[140:143],a[216:219],a[136:139],v[140:143]
W lgkmcnt(3)
T a[206:207],v184 offset:45064
M v[0:3],a[192:195],a[224:227],v[0:3]
M v[0:3],a[196:199],a[228:231],v[0:3]
W vmcnt(63)
T a[208:209],v185 offset:32768
)ASM"
R"ASM(
s_mov_b32 s44,s12
s_and_b32 s45,s13,0xffff
s_mov_b32 s46,0x4000000
s_mov_b32 s47,0x20000
s_mov_b32 s95,256
v_mad_u32_u24 v218,v227,s95,v246
v_mad_u32_u24 v219,v227,s95,v247
v_mad_u32_u24 v220,v227,s95,v246
v_mad_u32_u24 v221,v227,s95,v247
s_mov_b32 s61,s82
s_mov_b32 s62,4096
s_mov_b32 s63,4096
s_mov_b32 s75,4096
s_mov_b32 s74,-1
s_mov_b32 m0,0x10000
M v[16:19],a[196:199],a[236:239],v[16:19]
R a[16:19],v68
B v218,s[44:47],s61 offen lds
M v[16:19],a[192:195],a[232:235],v[16:19]
T a[210:211],v185 offset:36864
M v[32:35],a[192:195],a[240:243],v[32:35]
R a[36:39],v69
M v[32:35],a[196:199],a[244:247],v[32:35]
T a[212:213],v185 offset:40960
M v[48:51],a[196:199],a[252:255],v[48:51]
B v219,s[44:47],s61 offen offset:1024 lds
M v[48:51],a[192:195],a[248:251],v[48:51]
W lgkmcnt(5)
T a[214:215],v185 offset:45056
M v[80:83],a[200:203],a[224:227],v[80:83]
B v220,s[44:47],s61 offen offset:2048 lds
M v[80:83],a[204:207],a[228:231],v[80:83]
T a[216:217],v185 offset:32776
M v[96:99],a[204:207],a[236:239],v[96:99]
M v[96:99],a[200:203],a[232:235],v[96:99]
T a[218:219],v185 offset:36872
M v[112:115],a[200:203],a[240:243],v[112:115]
B v221,s[44:47],s61 offen offset:3072 lds
s_add_u32 s61,s61,s62
M v[112:115],a[204:207],a[244:247],v[112:115]
s_mov_b32 m0,0x11000
T a[220:221],v185 offset:40968
M v[128:131],a[204:207],a[252:255],v[128:131]
B v218,s[44:47],s61 offen lds
M v[128:131],a[200:203],a[248:251],v[128:131]
W lgkmcnt(3)
T a[222:223],v185 offset:45064
M v[4:7],a[208:211],a[224:227],v[4:7]
M v[4:7],a[212:215],a[228:231],v[4:7]
W vmcnt(63)
T a[192:193],v186 offset:32768
B v219,s[44:47],s61 offen offset:1024 lds
M v[20:23],a[212:215],a[236:239],v[20:23]
R a[56:59],v68 offset:2048
M v[20:23],a[208:211],a[232:235],v[20:23]
T a[194:195],v186 offset:36864
M v[36:39],a[208:211],a[240:243],v[36:39]
R a[76:79],v69 offset:2048
B v220,s[44:47],s61 offen offset:2048 lds
M v[36:39],a[212:215],a[244:247],v[36:39]
T a[196:197],v186 offset:40960
M v[52:55],a[212:215],a[252:255],v[52:55]
M v[52:55],a[208:211],a[248:251],v[52:55]
W lgkmcnt(5)
T a[198:199],v186 offset:45056
M v[84:87],a[216:219],a[224:227],v[84:87]
B v221,s[44:47],s61 offen offset:3072 lds
s_add_u32 s61,s61,s63
M v[84:87],a[220:223],a[228:231],v[84:87]
s_mov_b32 m0,0x12000
T a[200:201],v186 offset:32776
M v[100:103],a[220:223],a[236:239],v[100:103]
B v218,s[44:47],s61 offen lds
M v[100:103],a[216:219],a[232:235],v[100:103]
T a[202:203],v186 offset:36872
M v[116:119],a[216:219],a[240:243],v[116:119]
M v[116:119],a[220:223],a[244:247],v[116:119]
T a[204:205],v186 offset:40968
M v[132:135],a[220:223],a[252:255],v[132:135]
B v219,s[44:47],s61 offen offset:1024 lds
M v[132:135],a[216:219],a[248:251],v[132:135]
W lgkmcnt(3)
T a[206:207],v186 offset:45064
M v[8:11],a[192:195],a[224:227],v[8:11]
B v220,s[44:47],s61 offen offset:2048 lds
M v[8:11],a[196:199],a[228:231],v[8:11]
W vmcnt(38)
T a[208:209],v187 offset:32768
M v[24:27],a[196:199],a[236:239],v[24:27]
R a[96:99],v68 offset:4096
M v[24:27],a[192:195],a[232:235],v[24:27]
T a[210:211],v187 offset:36864
B v221,s[44:47],s61 offen offset:3072 lds
s_add_u32 s61,s61,s62
M v[40:43],a[192:195],a[240:243],v[40:43]
R a[116:119],v69 offset:4096
M v[40:43],a[196:199],a[244:247],v[40:43]
s_mov_b32 m0,0x13000
T a[212:213],v187 offset:40960
M v[56:59],a[196:199],a[252:255],v[56:59]
B v218,s[44:47],s61 offen lds
M v[56:59],a[192:195],a[248:251],v[56:59]
W lgkmcnt(5)
T a[214:215],v187 offset:45056
M v[88:91],a[200:203],a[224:227],v[88:91]
M v[88:91],a[204:207],a[228:231],v[88:91]
T a[216:217],v187 offset:32776
M v[104:107],a[204:207],a[236:239],v[104:107]
B v219,s[44:47],s61 offen offset:1024 lds
M v[104:107],a[200:203],a[232:235],v[104:107]
T a[218:219],v187 offset:36872
M v[120:123],a[200:203],a[240:243],v[120:123]
B v220,s[44:47],s61 offen offset:2048 lds
M v[120:123],a[204:207],a[244:247],v[120:123]
T a[220:221],v187 offset:40968
M v[136:139],a[204:207],a[252:255],v[136:139]
M v[136:139],a[200:203],a[248:251],v[136:139]
W lgkmcnt(3)
T a[222:223],v187 offset:45064
B v221,s[44:47],s61 offen offset:3072 lds
s_add_u32 s61,s61,s75
s_and_b32 s61,s61,s74
M v[12:15],a[208:211],a[224:227],v[12:15]
W vmcnt(20)
s_barrier
M v[12:15],a[212:215],a[228:231],v[12:15]
W vmcnt(63)
s_mov_b32 m0,0x8000
T a[192:193],v184 offset:49152
M v[28:31],a[212:215],a[236:239],v[28:31]
R a[136:139],v68 offset:6144
B v214,s[40:43],s60 offen nt lds
M v[28:31],a[208:211],a[232:235],v[28:31]
T a[194:195],v184 offset:53248
M v[44:47],a[208:211],a[240:243],v[44:47]
R a[156:159],v69 offset:6144
M v[44:47],a[212:215],a[244:247],v[44:47]
T a[196:197],v184 offset:57344
M v[60:63],a[212:215],a[252:255],v[60:63]
B v215,s[40:43],s60 offen offset:1024 nt lds
M v[60:63],a[208:211],a[248:251],v[60:63]
W lgkmcnt(5)
T a[198:199],v184 offset:61440
M v[92:95],a[216:219],a[224:227],v[92:95]
B v216,s[40:43],s60 offen offset:2048 nt lds
M v[92:95],a[220:223],a[228:231],v[92:95]
T a[200:201],v184 offset:49160
M v[108:111],a[220:223],a[236:239],v[108:111]
M v[108:111],a[216:219],a[232:235],v[108:111]
T a[202:203],v184 offset:53256
M v[124:127],a[216:219],a[240:243],v[124:127]
B v217,s[40:43],s60 offen offset:3072 nt lds
s_add_u32 s60,s60,s76
M v[124:127],a[220:223],a[244:247],v[124:127]
T a[204:205],v184 offset:57352
M v[140:143],a[220:223],a[252:255],v[140:143]
M v[140:143],a[216:219],a[248:251],v[140:143]
W lgkmcnt(3)
s_mov_b32 m0,0x9000
T a[206:207],v184 offset:61448
M v[0:3],a[192:195],a[16:19],v[0:3]
B v214,s[40:43],s60 offen nt lds
M v[0:3],a[196:199],a[36:39],v[0:3]
W vmcnt(63)
T a[208:209],v185 offset:49152
M v[16:19],a[196:199],a[76:79],v[16:19]
R a[224:227],v210
B v215,s[40:43],s60 offen offset:1024 nt lds
M v[16:19],a[192:195],a[56:59],v[16:19]
T a[210:211],v185 offset:53248
M v[32:35],a[192:195],a[96:99],v[32:35]
R a[228:231],v211
M v[32:35],a[196:199],a[116:119],v[32:35]
T a[212:213],v185 offset:57344
M v[48:51],a[196:199],a[156:159],v[48:51]
B v216,s[40:43],s60 offen offset:2048 nt lds
M v[48:51],a[192:195],a[136:139],v[48:51]
W lgkmcnt(5)
T a[214:215],v185 offset:61440
M v[80:83],a[200:203],a[16:19],v[80:83]
B v217,s[40:43],s60 offen offset:3072 nt lds
s_add_u32 s60,s60,s76
M v[80:83],a[204:207],a[36:39],v[80:83]
W lgkmcnt(2)
T a[216:217],v185 offset:49160
M v[96:99],a[204:207],a[76:79],v[96:99]
s_mov_b32 m0,0xa000
M v[96:99],a[200:203],a[56:59],v[96:99]
T a[218:219],v185 offset:53256
B v214,s[40:43],s60 offen nt lds
s_add_u32 m0,s67,0
s_add_u32 s27,s26,128
s_and_b32 s27,s27,0x380
s_add_u32 s27,s27,s66
s_add_u32 s68,s27,0
M v[112:115],a[200:203],a[96:99],v[112:115]
B v212,s[48:51],s68 offen lds
M v[112:115],a[204:207],a[116:119],v[112:115]
T a[220:221],v185 offset:57352
M v[128:131],a[204:207],a[156:159],v[128:131]
B v213,s[48:51],s68 offen offset:1024 lds
s_mov_b32 m0,0xa000
M v[128:131],a[200:203],a[136:139],v[128:131]
W lgkmcnt(3)
T a[222:223],v185 offset:61448
B v215,s[40:43],s60 offen offset:1024 nt lds
M v[4:7],a[208:211],a[16:19],v[4:7]
U v0,s64,v0
U v1,s64,v1
M v[4:7],a[212:215],a[36:39],v[4:7]
W vmcnt(63)
T a[192:193],v186 offset:49152
B v216,s[40:43],s60 offen offset:2048 nt lds
M v[20:23],a[212:215],a[76:79],v[20:23]
R a[232:235],v210 offset:2048
U v2,s64,v2
M v[20:23],a[208:211],a[56:59],v[20:23]
T a[194:195],v186 offset:53248
U v3,s64,v3
M v[36:39],a[208:211],a[96:99],v[36:39]
R a[236:239],v211 offset:2048
B v217,s[40:43],s60 offen offset:3072 nt lds
s_add_u32 s60,s60,s76
M v[36:39],a[212:215],a[116:119],v[36:39]
T a[196:197],v186 offset:57344
U v80,s64,v80
M v[52:55],a[212:215],a[156:159],v[52:55]
U v81,s64,v81
U v82,s64,v82
M v[52:55],a[208:211],a[136:139],v[52:55]
W lgkmcnt(5)
s_mov_b32 m0,0xb000
T a[198:199],v186 offset:61440
M v[84:87],a[216:219],a[16:19],v[84:87]
B v214,s[40:43],s60 offen nt lds
M v[84:87],a[220:223],a[36:39],v[84:87]
W lgkmcnt(2)
T a[200:201],v186 offset:49160
M v[100:103],a[220:223],a[76:79],v[100:103]
B v215,s[40:43],s60 offen offset:1024 nt lds
M v[100:103],a[216:219],a[56:59],v[100:103]
s_add_u32 m0,s67,2048
s_add_u32 s68,s27,16384
T a[202:203],v186 offset:53256
M v[116:119],a[216:219],a[96:99],v[116:119]
B v212,s[48:51],s68 offen lds
s_mov_b32 m0,0xb000
M v[116:119],a[220:223],a[116:119],v[116:119]
T a[204:205],v186 offset:57352
B v216,s[40:43],s60 offen offset:2048 nt lds
s_add_u32 m0,s67,2048
M v[132:135],a[220:223],a[156:159],v[132:135]
B v213,s[48:51],s68 offen offset:1024 lds
M v[132:135],a[216:219],a[136:139],v[132:135]
W lgkmcnt(3)
s_mov_b32 m0,0xb000
T a[206:207],v186 offset:61448
M v[8:11],a[192:195],a[16:19],v[8:11]
B v217,s[40:43],s60 offen offset:3072 nt lds
s_add_u32 s60,s60,s77
M v[8:11],a[196:199],a[36:39],v[8:11]
W vmcnt(38)
T a[208:209],v187 offset:49152
M v[24:27],a[196:199],a[76:79],v[24:27]
s_mov_b32 m0,0x14000
R a[240:243],v210 offset:4096
M v[24:27],a[192:195],a[56:59],v[24:27]
T a[210:211],v187 offset:53248
B v218,s[44:47],s61 offen lds
M v[40:43],a[192:195],a[96:99],v[40:43]
R a[244:247],v211 offset:4096
U v83,s64,v83
M v[40:43],a[196:199],a[116:119],v[40:43]
T a[212:213],v187 offset:57344
C v0,v0,v1
M v[56:59],a[196:199],a[156:159],v[56:59]
B v219,s[44:47],s61 offen offset:1024 lds
M v[56:59],a[192:195],a[136:139],v[56:59]
W lgkmcnt(5)
T a[214:215],v187 offset:61440
M v[88:91],a[200:203],a[16:19],v[88:91]
C v1,v2,v3
C v2,v80,v81
M v[88:91],a[204:207],a[36:39],v[88:91]
W lgkmcnt(2)
T a[216:217],v187 offset:49160
M v[104:107],a[204:207],a[76:79],v[104:107]
B v220,s[44:47],s61 offen offset:2048 lds
M v[104:107],a[200:203],a[56:59],v[104:107]
s_add_u32 m0,s67,4096
s_add_u32 s68,s27,32768
T a[218:219],v187 offset:53256
M v[120:123],a[200:203],a[96:99],v[120:123]
B v212,s[48:51],s68 offen lds
s_mov_b32 m0,0x14000
M v[120:123],a[204:207],a[116:119],v[120:123]
T a[220:221],v187 offset:57352
B v221,s[44:47],s61 offen offset:3072 lds
s_add_u32 s61,s61,s62
s_add_u32 m0,s67,4096
)ASM"
R"ASM(
M v[136:139],a[204:207],a[156:159],v[136:139]
B v213,s[48:51],s68 offen offset:1024 lds
M v[136:139],a[200:203],a[136:139],v[136:139]
W lgkmcnt(3)
s_mov_b32 m0,0x15000
T a[222:223],v187 offset:61448
M v[12:15],a[208:211],a[16:19],v[12:15]
B v218,s[44:47],s61 offen lds
M v[12:15],a[212:215],a[36:39],v[12:15]
W vmcnt(43)
C v3,v82,v83
U v16,s64,v16
M v[28:31],a[212:215],a[76:79],v[28:31]
R a[248:251],v210 offset:6144
U v17,s64,v17
M v[28:31],a[208:211],a[56:59],v[28:31]
B v219,s[44:47],s61 offen offset:1024 lds
M v[44:47],a[208:211],a[96:99],v[44:47]
R a[252:255],v211 offset:6144
U v18,s64,v18
M v[44:47],a[212:215],a[116:119],v[44:47]
B v220,s[44:47],s61 offen offset:2048 lds
M v[60:63],a[212:215],a[156:159],v[60:63]
U v19,s64,v19
U v96,s64,v96
M v[60:63],a[208:211],a[136:139],v[60:63]
W lgkmcnt(2)
U v97,s64,v97
U v98,s64,v98
M v[92:95],a[216:219],a[16:19],v[92:95]
B v221,s[44:47],s61 offen offset:3072 lds
s_add_u32 s61,s61,s63
M v[92:95],a[220:223],a[36:39],v[92:95]
W lgkmcnt(0)
U v99,s64,v99
C v16,v16,v17
M v[108:111],a[220:223],a[76:79],v[108:111]
C v17,v18,v19
C v18,v96,v97
M v[108:111],a[216:219],a[56:59],v[108:111]
s_add_u32 m0,s67,6144
s_add_u32 s68,s27,49152
C v19,v98,v99
M v[124:127],a[216:219],a[96:99],v[124:127]
B v212,s[48:51],s68 offen lds
M v[124:127],a[220:223],a[116:119],v[124:127]
M v[140:143],a[220:223],a[156:159],v[140:143]
B v213,s[48:51],s68 offen offset:1024 lds
M v[140:143],a[216:219],a[136:139],v[140:143]
W vmcnt(63)
s_barrier
R a[160:163],v198
R a[164:167],v199
R a[168:171],v200
R a[172:175],v201
s_nop 1
v_pk_mul_f32 v[32:33],v[32:33],s[64:65]
v_pk_mul_f32 v[34:35],v[34:35],s[64:65]
v_pk_mul_f32 v[112:113],v[112:113],s[64:65]
v_pk_mul_f32 v[114:115],v[114:115],s[64:65]
C v32,v32,v33
C v33,v34,v35
C v34,v112,v113
C v35,v114,v115
v_pk_mul_f32 v[48:49],v[48:49],s[64:65]
v_pk_mul_f32 v[50:51],v[50:51],s[64:65]
v_pk_mul_f32 v[128:129],v[128:129],s[64:65]
v_pk_mul_f32 v[130:131],v[130:131],s[64:65]
C v48,v48,v49
C v49,v50,v51
C v50,v128,v129
C v51,v130,v131
v_pk_mul_f32 v[4:5],v[4:5],s[64:65]
v_pk_mul_f32 v[6:7],v[6:7],s[64:65]
v_pk_mul_f32 v[84:85],v[84:85],s[64:65]
v_pk_mul_f32 v[86:87],v[86:87],s[64:65]
C v4,v4,v5
C v5,v6,v7
C v6,v84,v85
C v7,v86,v87
v_pk_mul_f32 v[20:21],v[20:21],s[64:65]
v_pk_mul_f32 v[22:23],v[22:23],s[64:65]
v_pk_mul_f32 v[100:101],v[100:101],s[64:65]
v_pk_mul_f32 v[102:103],v[102:103],s[64:65]
C v20,v20,v21
C v21,v22,v23
C v22,v100,v101
C v23,v102,v103
v_pk_mul_f32 v[36:37],v[36:37],s[64:65]
v_pk_mul_f32 v[38:39],v[38:39],s[64:65]
v_pk_mul_f32 v[116:117],v[116:117],s[64:65]
v_pk_mul_f32 v[118:119],v[118:119],s[64:65]
C v36,v36,v37
C v37,v38,v39
C v38,v116,v117
C v39,v118,v119
v_pk_mul_f32 v[52:53],v[52:53],s[64:65]
v_pk_mul_f32 v[54:55],v[54:55],s[64:65]
v_pk_mul_f32 v[132:133],v[132:133],s[64:65]
v_pk_mul_f32 v[134:135],v[134:135],s[64:65]
C v52,v52,v53
C v53,v54,v55
C v54,v132,v133
C v55,v134,v135
v_pk_mul_f32 v[8:9],v[8:9],s[64:65]
v_pk_mul_f32 v[10:11],v[10:11],s[64:65]
v_pk_mul_f32 v[88:89],v[88:89],s[64:65]
v_pk_mul_f32 v[90:91],v[90:91],s[64:65]
C v8,v8,v9
C v9,v10,v11
C v10,v88,v89
C v11,v90,v91
v_pk_mul_f32 v[24:25],v[24:25],s[64:65]
v_pk_mul_f32 v[26:27],v[26:27],s[64:65]
v_pk_mul_f32 v[104:105],v[104:105],s[64:65]
v_pk_mul_f32 v[106:107],v[106:107],s[64:65]
C v24,v24,v25
C v25,v26,v27
C v26,v104,v105
C v27,v106,v107
v_pk_mul_f32 v[40:41],v[40:41],s[64:65]
v_pk_mul_f32 v[42:43],v[42:43],s[64:65]
v_pk_mul_f32 v[120:121],v[120:121],s[64:65]
v_pk_mul_f32 v[122:123],v[122:123],s[64:65]
C v40,v40,v41
C v41,v42,v43
C v42,v120,v121
C v43,v122,v123
v_pk_mul_f32 v[56:57],v[56:57],s[64:65]
v_pk_mul_f32 v[58:59],v[58:59],s[64:65]
v_pk_mul_f32 v[136:137],v[136:137],s[64:65]
v_pk_mul_f32 v[138:139],v[138:139],s[64:65]
C v56,v56,v57
C v57,v58,v59
C v58,v136,v137
C v59,v138,v139
v_pk_mul_f32 v[12:13],v[12:13],s[64:65]
v_pk_mul_f32 v[14:15],v[14:15],s[64:65]
v_pk_mul_f32 v[92:93],v[92:93],s[64:65]
v_pk_mul_f32 v[94:95],v[94:95],s[64:65]
C v12,v12,v13
C v13,v14,v15
C v14,v92,v93
C v15,v94,v95
v_pk_mul_f32 v[28:29],v[28:29],s[64:65]
v_pk_mul_f32 v[30:31],v[30:31],s[64:65]
v_pk_mul_f32 v[108:109],v[108:109],s[64:65]
v_pk_mul_f32 v[110:111],v[110:111],s[64:65]
C v28,v28,v29
C v29,v30,v31
C v30,v108,v109
C v31,v110,v111
v_pk_mul_f32 v[44:45],v[44:45],s[64:65]
v_pk_mul_f32 v[46:47],v[46:47],s[64:65]
v_pk_mul_f32 v[124:125],v[124:125],s[64:65]
v_pk_mul_f32 v[126:127],v[126:127],s[64:65]
C v44,v44,v45
C v45,v46,v47
C v46,v124,v125
C v47,v126,v127
v_pk_mul_f32 v[60:61],v[60:61],s[64:65]
v_pk_mul_f32 v[62:63],v[62:63],s[64:65]
v_pk_mul_f32 v[140:141],v[140:141],s[64:65]
v_pk_mul_f32 v[142:143],v[142:143],s[64:65]
C v60,v60,v61
C v61,v62,v63
C v62,v140,v141
C v63,v142,v143
)ASM"
R"ASM(
LPREb_%=:
W lgkmcnt(0)
s_mov_b32 s69,0
s_mov_b32 m0,0x16000
M v[120:123],a[160:163],v[0:3],0
B v218,s[44:47],s61 offen lds
v_cmp_eq_u32 vcc,1,v244
v_mov_b32 v250,0x44444444
M v[120:123],a[164:167],v[4:7],v[120:123]
R a[176:179],v198 offset:4096
v_mov_b32 v251,0xeeeeeeee
v_cndmask_b32 v253,v250,v251,vcc
M v[120:123],a[168:171],v[8:11],v[120:123]
R a[180:183],v199 offset:4096
B v219,s[44:47],s61 offen offset:1024 lds
v_cmp_gt_u32 vcc,2,v244
v_mov_b32 v251,0x3f803f80
M v[120:123],a[172:175],v[12:15],v[120:123]
R a[184:187],v200 offset:4096
AW a16,0
v_cndmask_b32 v250,0,v251,vcc
M v[124:127],a[160:163],v[16:19],0
R a[188:191],v201 offset:4096
B v220,s[44:47],s61 offen offset:2048 lds
AW a96,v250
AW a97,v250
M v[124:127],a[164:167],v[20:23],v[124:127]
AW a98,v250
AW a99,v250
M v[124:127],a[168:171],v[24:27],v[124:127]
B v221,s[44:47],s61 offen offset:3072 lds
s_add_u32 s61,s61,s62
AW a17,0
AW a36,0
M v[124:127],a[172:175],v[28:31],v[124:127]
s_mov_b32 m0,0x17000
AW a37,0
AW a56,0
M v[128:131],a[160:163],v[32:35],0
B v218,s[44:47],s61 offen lds
AW a57,0
AW a76,0
M v[128:131],a[164:167],v[36:39],v[128:131]
AW a77,0
M v[128:131],a[168:171],v[40:43],v[128:131]
B v219,s[44:47],s61 offen offset:1024 lds
M v[128:131],a[172:175],v[44:47],v[128:131]
v_max3_f32 v160,v120,v121,v222
v_max3_f32 v160,v122,v123,v160
M v[132:135],a[160:163],v[48:51],0
B v220,s[44:47],s61 offen offset:2048 lds
M v[132:135],a[164:167],v[52:55],v[132:135]
v_mov_b32 v161,v160
M v[132:135],a[168:171],v[56:59],v[132:135]
B v221,s[44:47],s61 offen offset:3072 lds
s_add_u32 s61,s61,s75
s_and_b32 s61,s61,s74
M v[132:135],a[172:175],v[60:63],v[132:135]
W lgkmcnt(0)
s_mov_b32 m0,0xc000
v_max3_f32 v164,v124,v125,v223
v_permlane32_swap_b32 v160,v161
v_max3_f32 v164,v126,v127,v164
v_max3_f32 v168,v128,v129,v224
v_mov_b32 v165,v164
v_max3_f32 v168,v130,v131,v168
v_max_f32 v160,v160,v161
v_mov_b32 v169,v168
v_permlane32_swap_b32 v164,v165
v_max3_f32 v172,v132,v133,v225
v_permlane32_swap_b32 v168,v169
v_max3_f32 v172,v134,v135,v172
v_max_f32 v164,v164,v165
v_mov_b32 v173,v172
v_max_f32 v168,v168,v169
v_mov_b32 v161,v160
v_permlane32_swap_b32 v172,v173
v_mov_b32 v165,v164
v_max_f32 v172,v172,v173
v_mov_b32 v169,v168
v_mov_b32 v173,v172
v_permlane16_swap_b32 v160,v161
v_permlane16_swap_b32 v164,v165
v_permlane16_swap_b32 v168,v169
v_permlane16_swap_b32 v172,v173
v_max_f32 v160,v160,v161
v_max_f32 v164,v164,v165
v_max_f32 v168,v168,v169
v_max_f32 v172,v172,v173
v_sub_f32 v161,0xc1400000,v160
v_sub_f32 v165,0xc1400000,v164
v_sub_f32 v169,0xc1400000,v168
v_sub_f32 v173,0xc1400000,v172
v_sub_f32 v162,0xc1400000,v160
v_sub_f32 v166,0xc1400000,v164
v_sub_f32 v170,0xc1400000,v168
v_sub_f32 v174,0xc1400000,v172
v_sub_f32 v163,0xc1400000,v160
v_sub_f32 v167,0xc1400000,v164
v_sub_f32 v171,0xc1400000,v168
v_sub_f32 v175,0xc1400000,v172
v_sub_f32 v160,0xc1400000,v160
v_sub_f32 v164,0xc1400000,v164
v_sub_f32 v168,0xc1400000,v168
v_sub_f32 v172,0xc1400000,v172
F v120,v120,v160
F v124,v124,v164
F v128,v128,v168
F v132,v132,v172
F v121,v121,v160
F v125,v125,v164
F v129,v129,v168
F v133,v133,v172
F v122,v122,v160
F v126,v126,v164
F v130,v130,v168
F v134,v134,v172
F v123,v123,v160
F v127,v127,v164
F v131,v131,v168
F v135,v135,v172
M v[136:139],a[176:179],v[0:3],v[160:163]
B v214,s[40:43],s60 offen nt lds
E v120,v120
E v121,v121
M v[136:139],a[180:183],v[4:7],v[136:139]
R a[160:163],v198 offset:8192
E v122,v122
E v123,v123
M v[136:139],a[184:187],v[8:11],v[136:139]
R a[164:167],v199 offset:8192
B v215,s[40:43],s60 offen offset:1024 nt lds
C v80,v120,v121
C v81,v122,v123
M v[136:139],a[188:191],v[12:15],v[136:139]
R a[168:171],v200 offset:8192
E v124,v124
E v125,v125
M v[140:143],a[176:179],v[16:19],v[164:167]
R a[172:175],v201 offset:8192
B v216,s[40:43],s60 offen offset:2048 nt lds
E v126,v126
E v127,v127
M v[140:143],a[180:183],v[20:23],v[140:143]
C v88,v124,v125
C v89,v126,v127
M v[140:143],a[184:187],v[24:27],v[140:143]
B v217,s[40:43],s60 offen offset:3072 nt lds
s_add_u32 s60,s60,s76
E v128,v128
E v129,v129
M v[140:143],a[188:191],v[28:31],v[140:143]
s_mov_b32 m0,0xd000
E v130,v130
M v[144:147],a[176:179],v[32:35],v[168:171]
B v214,s[40:43],s60 offen nt lds
E v131,v131
C v96,v128,v129
M v[144:147],a[180:183],v[36:39],v[144:147]
C v97,v130,v131
E v132,v132
M v[144:147],a[184:187],v[40:43],v[144:147]
B v215,s[40:43],s60 offen offset:1024 nt lds
E v133,v133
E v134,v134
M v[144:147],a[188:191],v[44:47],v[144:147]
E v135,v135
C v104,v132,v133
M v[148:151],a[176:179],v[48:51],v[172:175]
B v216,s[40:43],s60 offen offset:2048 nt lds
C v105,v134,v135
E v136,v136
M v[148:151],a[180:183],v[52:55],v[148:151]
E v137,v137
E v138,v138
M v[148:151],a[184:187],v[56:59],v[148:151]
B v217,s[40:43],s60 offen offset:3072 nt lds
s_add_u32 s60,s60,s76
E v139,v139
C v82,v136,v137
M v[148:151],a[188:191],v[60:63],v[148:151]
W lgkmcnt(0)
s_mov_b32 m0,0xe000
C v83,v138,v139
M v[152:155],a[160:163],v[0:3],v[160:163]
B v214,s[40:43],s60 offen nt lds
E v140,v140
E v141,v141
M v[152:155],a[164:167],v[4:7],v[152:155]
R a[176:179],v198 offset:12288
E v142,v142
E v143,v143
M v[152:155],a[168:171],v[8:11],v[152:155]
R a[180:183],v199 offset:12288
B v215,s[40:43],s60 offen offset:1024 nt lds
C v90,v140,v141
C v91,v142,v143
M v[152:155],a[172:175],v[12:15],v[152:155]
R a[184:187],v200 offset:12288
E v144,v144
E v145,v145
M v[156:159],a[160:163],v[16:19],v[164:167]
R a[188:191],v201 offset:12288
B v216,s[40:43],s60 offen offset:2048 nt lds
E v146,v146
E v147,v147
M v[156:159],a[164:167],v[20:23],v[156:159]
C v98,v144,v145
C v99,v146,v147
M v[156:159],a[168:171],v[24:27],v[156:159]
B v217,s[40:43],s60 offen offset:3072 nt lds
s_add_u32 s60,s60,s76
E v148,v148
E v149,v149
M v[156:159],a[172:175],v[28:31],v[156:159]
s_mov_b32 m0,0xf000
E v150,v150
M v[120:123],a[160:163],v[32:35],v[168:171]
B v214,s[40:43],s60 offen nt lds
E v151,v151
C v106,v148,v149
M v[120:123],a[164:167],v[36:39],v[120:123]
C v107,v150,v151
E v152,v152
M v[120:123],a[168:171],v[40:43],v[120:123]
B v215,s[40:43],s60 offen offset:1024 nt lds
E v153,v153
E v154,v154
M v[120:123],a[172:175],v[44:47],v[120:123]
E v155,v155
C v84,v152,v153
M v[124:127],a[160:163],v[48:51],v[172:175]
B v216,s[40:43],s60 offen offset:2048 nt lds
C v85,v154,v155
E v156,v156
M v[124:127],a[164:167],v[52:55],v[124:127]
E v157,v157
E v158,v158
M v[124:127],a[168:171],v[56:59],v[124:127]
B v217,s[40:43],s60 offen offset:3072 nt lds
s_add_u32 s60,s60,s77
E v159,v159
C v92,v156,v157
M v[124:127],a[172:175],v[60:63],v[124:127]
W lgkmcnt(0)
C v93,v158,v159
M v[128:131],a[176:179],v[0:3],v[160:163]
W vmcnt(33)
s_barrier
s_mov_b32 m0,0x18000
E v120,v120
E v121,v121
M v[128:131],a[180:183],v[4:7],v[128:131]
T a[192:193],v202
B v218,s[44:47],s61 offen lds
E v122,v122
E v123,v123
M v[128:131],a[184:187],v[8:11],v[128:131]
T a[194:195],v202 offset:4096
C v100,v120,v121
C v101,v122,v123
M v[128:131],a[188:191],v[12:15],v[128:131]
T a[196:197],v202 offset:8192
B v219,s[44:47],s61 offen offset:1024 lds
E v124,v124
E v125,v125
M v[132:135],a[176:179],v[16:19],v[164:167]
T a[198:199],v202 offset:12288
E v126,v126
E v127,v127
M v[132:135],a[180:183],v[20:23],v[132:135]
R a[160:163],v198 offset:16384
B v220,s[44:47],s61 offen offset:2048 lds
C v108,v124,v125
C v109,v126,v127
M v[132:135],a[184:187],v[24:27],v[132:135]
R a[164:167],v199 offset:16384
M v[132:135],a[188:191],v[28:31],v[132:135]
R a[168:171],v200 offset:16384
B v221,s[44:47],s61 offen offset:3072 lds
s_add_u32 s61,s61,s62
E v128,v128
E v129,v129
E v130,v130
M v[136:139],a[176:179],v[32:35],v[168:171]
s_mov_b32 m0,0x19000
R a[172:175],v201 offset:16384
E v131,v131
C v86,v128,v129
M v[136:139],a[180:183],v[36:39],v[136:139]
T a[200:201],v203
B v218,s[44:47],s61 offen lds
C v87,v130,v131
M v[136:139],a[184:187],v[40:43],v[136:139]
T a[202:203],v203 offset:4096
M v[136:139],a[188:191],v[44:47],v[136:139]
T a[204:205],v203 offset:8192
B v219,s[44:47],s61 offen offset:1024 lds
E v132,v132
E v133,v133
)ASM"
R"ASM(
E v134,v134
E v135,v135
C v94,v132,v133
M v[184:187],a[176:179],v[48:51],v[172:175]
T a[206:207],v203 offset:12288
C v95,v134,v135
M v[184:187],a[180:183],v[52:55],v[184:187]
B v220,s[44:47],s61 offen offset:2048 lds
M v[184:187],a[184:187],v[56:59],v[184:187]
M v[184:187],a[188:191],v[60:63],v[184:187]
B v221,s[44:47],s61 offen offset:3072 lds
s_add_u32 s61,s61,s63
E v136,v136
E v137,v137
E v138,v138
E v139,v139
C v102,v136,v137
C v103,v138,v139
s_cmp_eq_u32 s70,0
s_cbranch_scc1 LNOPUBb_%=
W vmcnt(39)
v_mov_b32 v248,s72
v_mov_b32 v249,s73
s_mov_b64 exec,1
buffer_store_dwordx2 v[248:249],off,s[52:55],s85 sc1
s_mov_b64 exec,-1
LNOPUBb_%=:
s_nop 0
W lgkmcnt(4)
s_cmp_ge_u32 s81,2300
s_cbranch_scc1 LPEELc_%=
)ASM"
R"ASM(
M v[180:183],a[160:163],v[0:3],v[160:163]
E v184,v184
M v[180:183],a[164:167],v[4:7],v[180:183]
s_mov_b32 m0,0x1a000
E v185,v185
M v[180:183],a[168:171],v[8:11],v[180:183]
T a[208:209],v204
B v218,s[44:47],s61 offen lds
M v[180:183],a[172:175],v[12:15],v[180:183]
E v186,v186
M a[0:3],a[192:195],v[80:83],0
T a[210:211],v204 offset:4096
C v110,v184,v185
M a[0:3],a[196:199],v[84:87],a[0:3]
E v187,v187
M a[4:7],a[196:199],v[92:95],0
T a[212:213],v204 offset:8192
C v111,v186,v187
M a[4:7],a[192:195],v[88:91],a[4:7]
B v219,s[44:47],s61 offen offset:1024 lds
M v[184:187],a[160:163],v[16:19],v[164:167]
E v180,v180
M v[184:187],a[164:167],v[20:23],v[184:187]
E v181,v181
M v[184:187],a[168:171],v[24:27],v[184:187]
T a[214:215],v204 offset:12288
B v220,s[44:47],s61 offen offset:2048 lds
M v[184:187],a[172:175],v[28:31],v[184:187]
W lgkmcnt(4)
E v182,v182
M a[8:11],a[192:195],v[96:99],0
R a[176:179],v198 offset:20480
C v120,v180,v181
M a[8:11],a[196:199],v[100:103],a[8:11]
E v183,v183
M a[12:15],a[196:199],v[108:111],0
R a[180:183],v199 offset:20480
C v121,v182,v183
M a[12:15],a[192:195],v[104:107],a[12:15]
B v221,s[44:47],s61 offen offset:3072 lds
s_add_u32 s61,s61,s62
M v[180:183],a[160:163],v[32:35],v[168:171]
E v184,v184
M v[180:183],a[164:167],v[36:39],v[180:183]
s_mov_b32 m0,0x1b000
E v185,v185
M v[180:183],a[168:171],v[40:43],v[180:183]
R a[184:187],v200 offset:20480
B v218,s[44:47],s61 offen lds
M v[180:183],a[172:175],v[44:47],v[180:183]
E v186,v186
M a[20:23],a[200:203],v[80:83],0
R a[188:191],v201 offset:20480
C v128,v184,v185
M a[20:23],a[204:207],v[84:87],a[20:23]
E v187,v187
M a[24:27],a[204:207],v[92:95],0
T a[216:217],v205
C v129,v186,v187
M a[24:27],a[200:203],v[88:91],a[24:27]
B v219,s[44:47],s61 offen offset:1024 lds
v_smfmac_f32_16x16x64_bf16 a[16:19],a[96:99],v[80:87],v253
M v[184:187],a[160:163],v[48:51],v[172:175]
E v180,v180
M v[184:187],a[164:167],v[52:55],v[184:187]
E v181,v181
M v[184:187],a[168:171],v[56:59],v[184:187]
T a[218:219],v205 offset:4096
B v220,s[44:47],s61 offen offset:2048 lds
M v[184:187],a[172:175],v[60:63],v[184:187]
E v182,v182
M a[28:31],a[200:203],v[96:99],0
T a[220:221],v205 offset:8192
C v136,v180,v181
M a[28:31],a[204:207],v[100:103],a[28:31]
W lgkmcnt(3)
E v183,v183
M a[32:35],a[204:207],v[108:111],0
T a[222:223],v205 offset:12288
C v137,v182,v183
M a[32:35],a[200:203],v[104:107],a[32:35]
B v221,s[44:47],s61 offen offset:3072 lds
s_add_u32 s61,s61,s75
s_and_b32 s61,s61,s74
M v[180:183],a[176:179],v[0:3],v[160:163]
E v184,v184
M v[180:183],a[180:183],v[4:7],v[180:183]
s_mov_b32 m0,0x0
E v185,v185
M v[180:183],a[184:187],v[8:11],v[180:183]
T a[192:193],v206
B v214,s[40:43],s60 offen lds
M v[180:183],a[188:191],v[12:15],v[180:183]
E v186,v186
M a[40:43],a[208:211],v[80:83],0
T a[194:195],v206 offset:4096
C v144,v184,v185
M a[40:43],a[212:215],v[84:87],a[40:43]
E v187,v187
M a[44:47],a[212:215],v[92:95],0
T a[196:197],v206 offset:8192
C v145,v186,v187
M a[44:47],a[208:211],v[88:91],a[44:47]
B v215,s[40:43],s60 offen offset:1024 lds
M v[184:187],a[176:179],v[16:19],v[164:167]
E v180,v180
M v[184:187],a[180:183],v[20:23],v[184:187]
E v181,v181
M v[184:187],a[184:187],v[24:27],v[184:187]
T a[198:199],v206 offset:12288
B v216,s[40:43],s60 offen offset:2048 lds
M v[184:187],a[188:191],v[28:31],v[184:187]
W lgkmcnt(4)
E v182,v182
M a[48:51],a[208:211],v[96:99],0
R a[160:163],v198 offset:24576
C v122,v180,v181
M a[48:51],a[212:215],v[100:103],a[48:51]
E v183,v183
M a[52:55],a[212:215],v[108:111],0
R a[164:167],v199 offset:24576
C v123,v182,v183
M a[52:55],a[208:211],v[104:107],a[52:55]
B v217,s[40:43],s60 offen offset:3072 lds
s_add_u32 s60,s60,s76
M v[180:183],a[176:179],v[32:35],v[168:171]
E v184,v184
M v[180:183],a[180:183],v[36:39],v[180:183]
s_mov_b32 m0,0x1000
E v185,v185
M v[180:183],a[184:187],v[40:43],v[180:183]
R a[168:171],v200 offset:24576
B v214,s[40:43],s60 offen lds
M v[180:183],a[188:191],v[44:47],v[180:183]
E v186,v186
M a[60:63],a[216:219],v[80:83],0
R a[172:175],v201 offset:24576
C v130,v184,v185
M a[60:63],a[220:223],v[84:87],a[60:63]
E v187,v187
M a[64:67],a[220:223],v[92:95],0
T a[200:201],v207
C v131,v186,v187
M a[64:67],a[216:219],v[88:91],a[64:67]
B v215,s[40:43],s60 offen offset:1024 lds
v_smfmac_f32_16x16x64_bf16 a[36:39],a[96:99],v[88:95],v253
M v[184:187],a[176:179],v[48:51],v[172:175]
E v180,v180
M v[184:187],a[180:183],v[52:55],v[184:187]
E v181,v181
M v[184:187],a[184:187],v[56:59],v[184:187]
T a[202:203],v207 offset:4096
B v216,s[40:43],s60 offen offset:2048 lds
M v[184:187],a[188:191],v[60:63],v[184:187]
E v182,v182
M a[68:71],a[216:219],v[96:99],0
T a[204:205],v207 offset:8192
C v138,v180,v181
M a[68:71],a[220:223],v[100:103],a[68:71]
W lgkmcnt(3)
E v183,v183
M a[72:75],a[220:223],v[108:111],0
T a[206:207],v207 offset:12288
C v139,v182,v183
M a[72:75],a[216:219],v[104:107],a[72:75]
B v217,s[40:43],s60 offen offset:3072 lds
s_add_u32 s60,s60,s76
M v[180:183],a[160:163],v[0:3],v[160:163]
E v184,v184
M v[180:183],a[164:167],v[4:7],v[180:183]
s_mov_b32 m0,0x2000
E v185,v185
M v[180:183],a[168:171],v[8:11],v[180:183]
T a[208:209],v208
B v214,s[40:43],s60 offen lds
M v[180:183],a[172:175],v[12:15],v[180:183]
E v186,v186
M a[80:83],a[192:195],v[80:83],0
T a[210:211],v208 offset:4096
C v146,v184,v185
M a[80:83],a[196:199],v[84:87],a[80:83]
E v187,v187
M a[84:87],a[196:199],v[92:95],0
T a[212:213],v208 offset:8192
C v147,v186,v187
M a[84:87],a[192:195],v[88:91],a[84:87]
B v215,s[40:43],s60 offen offset:1024 lds
M v[184:187],a[160:163],v[16:19],v[164:167]
E v180,v180
M v[184:187],a[164:167],v[20:23],v[184:187]
E v181,v181
M v[184:187],a[168:171],v[24:27],v[184:187]
T a[214:215],v208 offset:12288
B v216,s[40:43],s60 offen offset:2048 lds
M v[184:187],a[172:175],v[28:31],v[184:187]
W lgkmcnt(4)
E v182,v182
M a[88:91],a[192:195],v[96:99],0
R a[176:179],v198 offset:28672
C v124,v180,v181
M a[88:91],a[196:199],v[100:103],a[88:91]
E v183,v183
M a[92:95],a[196:199],v[108:111],0
R a[180:183],v199 offset:28672
C v125,v182,v183
M a[92:95],a[192:195],v[104:107],a[92:95]
B v217,s[40:43],s60 offen offset:3072 lds
s_add_u32 s60,s60,s76
M v[180:183],a[160:163],v[32:35],v[168:171]
E v184,v184
M v[180:183],a[164:167],v[36:39],v[180:183]
s_mov_b32 m0,0x3000
E v185,v185
M v[180:183],a[168:171],v[40:43],v[180:183]
R a[184:187],v200 offset:28672
B v214,s[40:43],s60 offen lds
M v[180:183],a[172:175],v[44:47],v[180:183]
E v186,v186
M a[100:103],a[200:203],v[80:83],0
R a[188:191],v201 offset:28672
C v132,v184,v185
M a[100:103],a[204:207],v[84:87],a[100:103]
E v187,v187
M a[104:107],a[204:207],v[92:95],0
T a[216:217],v209
C v133,v186,v187
M a[104:107],a[200:203],v[88:91],a[104:107]
B v215,s[40:43],s60 offen offset:1024 lds
v_smfmac_f32_16x16x64_bf16 a[56:59],a[96:99],v[96:103],v253
M v[184:187],a[160:163],v[48:51],v[172:175]
E v180,v180
M v[184:187],a[164:167],v[52:55],v[184:187]
E v181,v181
M v[184:187],a[168:171],v[56:59],v[184:187]
T a[218:219],v209 offset:4096
B v216,s[40:43],s60 offen offset:2048 lds
M v[184:187],a[172:175],v[60:63],v[184:187]
E v182,v182
M a[108:111],a[200:203],v[96:99],0
T a[220:221],v209 offset:8192
C v140,v180,v181
M a[108:111],a[204:207],v[100:103],a[108:111]
W lgkmcnt(3)
E v183,v183
M a[112:115],a[204:207],v[108:111],0
T a[222:223],v209 offset:12288
C v141,v182,v183
M a[112:115],a[200:203],v[104:107],a[112:115]
B v217,s[40:43],s60 offen offset:3072 lds
s_add_u32 s60,s60,s77
M v[180:183],a[176:179],v[0:3],v[160:163]
W vmcnt(48)
s_barrier
E v184,v184
M v[180:183],a[180:183],v[4:7],v[180:183]
s_mov_b32 m0,0x1c000
E v185,v185
M v[180:183],a[184:187],v[8:11],v[180:183]
T a[192:193],v202 offset:16384
B v218,s[44:47],s61 offen lds
M v[180:183],a[188:191],v[12:15],v[180:183]
E v186,v186
M a[120:123],a[208:211],v[80:83],0
T a[194:195],v202 offset:20480
C v148,v184,v185
M a[120:123],a[212:215],v[84:87],a[120:123]
E v187,v187
M a[124:127],a[212:215],v[92:95],0
T a[196:197],v202 offset:24576
C v149,v186,v187
M a[124:127],a[208:211],v[88:91],a[124:127]
B v219,s[44:47],s61 offen offset:1024 lds
M v[184:187],a[176:179],v[16:19],v[164:167]
E v180,v180
M v[184:187],a[180:183],v[20:23],v[184:187]
E v181,v181
M v[184:187],a[184:187],v[24:27],v[184:187]
T a[198:199],v202 offset:28672
B v220,s[44:47],s61 offen offset:2048 lds
M v[184:187],a[188:191],v[28:31],v[184:187]
W lgkmcnt(4)
E v182,v182
M a[128:131],a[208:211],v[96:99],0
R a[160:163],v198 offset:32768
C v126,v180,v181
M a[128:131],a[212:215],v[100:103],a[128:131]
E v183,v183
M a[132:135],a[212:215],v[108:111],0
R a[164:167],v199 offset:32768
C v127,v182,v183
M a[132:135],a[208:211],v[104:107],a[132:135]
B v221,s[44:47],s61 offen offset:3072 lds
s_add_u32 s61,s61,s62
M v[180:183],a[176:179],v[32:35],v[168:171]
E v184,v184
M v[180:183],a[180:183],v[36:39],v[180:183]
s_mov_b32 m0,0x1d000
E v185,v185
M v[180:183],a[184:187],v[40:43],v[180:183]
R a[168:171],v200 offset:32768
)ASM"
R"ASM(
B v218,s[44:47],s61 offen lds
M v[180:183],a[188:191],v[44:47],v[180:183]
E v186,v186
M a[140:143],a[216:219],v[80:83],0
R a[172:175],v201 offset:32768
C v134,v184,v185
M a[140:143],a[220:223],v[84:87],a[140:143]
E v187,v187
M a[144:147],a[220:223],v[92:95],0
T a[200:201],v203 offset:16384
C v135,v186,v187
M a[144:147],a[216:219],v[88:91],a[144:147]
B v219,s[44:47],s61 offen offset:1024 lds
v_smfmac_f32_16x16x64_bf16 a[76:79],a[96:99],v[104:111],v253
M v[184:187],a[176:179],v[48:51],v[172:175]
E v180,v180
M v[184:187],a[180:183],v[52:55],v[184:187]
E v181,v181
M v[184:187],a[184:187],v[56:59],v[184:187]
T a[202:203],v203 offset:20480
B v220,s[44:47],s61 offen offset:2048 lds
M v[184:187],a[188:191],v[60:63],v[184:187]
E v182,v182
M a[148:151],a[216:219],v[96:99],0
T a[204:205],v203 offset:24576
C v142,v180,v181
M a[148:151],a[220:223],v[100:103],a[148:151]
W lgkmcnt(3)
E v183,v183
M a[152:155],a[220:223],v[108:111],0
T a[206:207],v203 offset:28672
C v143,v182,v183
M a[152:155],a[216:219],v[104:107],a[152:155]
B v221,s[44:47],s61 offen offset:3072 lds
s_add_u32 s61,s61,s63
s_branch LIT1b_%=
)ASM"
R"ASM(
.p2align 4
LLOOPb_%=:
s_cmp_ge_u32 s69,1
s_cbranch_scc1 LSWK_%=
LSWKR_%=:
)ASM"
R"ASM(
M v[180:183],a[160:163],v[0:3],v[160:163]
E v184,v184
M v[180:183],a[164:167],v[4:7],v[180:183]
s_mov_b32 m0,0x1a000
E v185,v185
M v[180:183],a[168:171],v[8:11],v[180:183]
T a[208:209],v204
B v218,s[44:47],s61 offen lds
M v[180:183],a[172:175],v[12:15],v[180:183]
E v186,v186
M a[0:3],a[192:195],v[80:83],a[0:3]
T a[210:211],v204 offset:4096
C v110,v184,v185
M a[0:3],a[196:199],v[84:87],a[0:3]
E v187,v187
M a[4:7],a[196:199],v[92:95],a[4:7]
T a[212:213],v204 offset:8192
C v111,v186,v187
M a[4:7],a[192:195],v[88:91],a[4:7]
B v219,s[44:47],s61 offen offset:1024 lds
M v[184:187],a[160:163],v[16:19],v[164:167]
E v180,v180
M v[184:187],a[164:167],v[20:23],v[184:187]
E v181,v181
M v[184:187],a[168:171],v[24:27],v[184:187]
T a[214:215],v204 offset:12288
B v220,s[44:47],s61 offen offset:2048 lds
M v[184:187],a[172:175],v[28:31],v[184:187]
W lgkmcnt(4)
E v182,v182
M a[8:11],a[192:195],v[96:99],a[8:11]
R a[176:179],v198 offset:20480
C v120,v180,v181
M a[8:11],a[196:199],v[100:103],a[8:11]
E v183,v183
M a[12:15],a[196:199],v[108:111],a[12:15]
R a[180:183],v199 offset:20480
C v121,v182,v183
M a[12:15],a[192:195],v[104:107],a[12:15]
B v221,s[44:47],s61 offen offset:3072 lds
s_add_u32 s61,s61,s62
M v[180:183],a[160:163],v[32:35],v[168:171]
E v184,v184
M v[180:183],a[164:167],v[36:39],v[180:183]
s_mov_b32 m0,0x1b000
E v185,v185
M v[180:183],a[168:171],v[40:43],v[180:183]
R a[184:187],v200 offset:20480
B v218,s[44:47],s61 offen lds
M v[180:183],a[172:175],v[44:47],v[180:183]
E v186,v186
M a[20:23],a[200:203],v[80:83],a[20:23]
R a[188:191],v201 offset:20480
C v128,v184,v185
M a[20:23],a[204:207],v[84:87],a[20:23]
E v187,v187
M a[24:27],a[204:207],v[92:95],a[24:27]
T a[216:217],v205
C v129,v186,v187
M a[24:27],a[200:203],v[88:91],a[24:27]
B v219,s[44:47],s61 offen offset:1024 lds
v_smfmac_f32_16x16x64_bf16 a[16:19],a[96:99],v[80:87],v253
M v[184:187],a[160:163],v[48:51],v[172:175]
E v180,v180
M v[184:187],a[164:167],v[52:55],v[184:187]
E v181,v181
M v[184:187],a[168:171],v[56:59],v[184:187]
T a[218:219],v205 offset:4096
B v220,s[44:47],s61 offen offset:2048 lds
M v[184:187],a[172:175],v[60:63],v[184:187]
E v182,v182
M a[28:31],a[200:203],v[96:99],a[28:31]
T a[220:221],v205 offset:8192
C v136,v180,v181
M a[28:31],a[204:207],v[100:103],a[28:31]
W lgkmcnt(3)
E v183,v183
M a[32:35],a[204:207],v[108:111],a[32:35]
T a[222:223],v205 offset:12288
C v137,v182,v183
M a[32:35],a[200:203],v[104:107],a[32:35]
B v221,s[44:47],s61 offen offset:3072 lds
s_add_u32 s61,s61,s75
s_and_b32 s61,s61,s74
M v[180:183],a[176:179],v[0:3],v[160:163]
E v184,v184
M v[180:183],a[180:183],v[4:7],v[180:183]
s_mov_b32 m0,0x0
E v185,v185
M v[180:183],a[184:187],v[8:11],v[180:183]
T a[192:193],v206
B v214,s[40:43],s60 offen lds
M v[180:183],a[188:191],v[12:15],v[180:183]
E v186,v186
M a[40:43],a[208:211],v[80:83],a[40:43]
T a[194:195],v206 offset:4096
C v144,v184,v185
M a[40:43],a[212:215],v[84:87],a[40:43]
E v187,v187
M a[44:47],a[212:215],v[92:95],a[44:47]
T a[196:197],v206 offset:8192
C v145,v186,v187
M a[44:47],a[208:211],v[88:91],a[44:47]
B v215,s[40:43],s60 offen offset:1024 lds
M v[184:187],a[176:179],v[16:19],v[164:167]
E v180,v180
M v[184:187],a[180:183],v[20:23],v[184:187]
E v181,v181
M v[184:187],a[184:187],v[24:27],v[184:187]
T a[198:199],v206 offset:12288
B v216,s[40:43],s60 offen offset:2048 lds
M v[184:187],a[188:191],v[28:31],v[184:187]
W lgkmcnt(4)
E v182,v182
M a[48:51],a[208:211],v[96:99],a[48:51]
R a[160:163],v198 offset:24576
C v122,v180,v181
M a[48:51],a[212:215],v[100:103],a[48:51]
E v183,v183
M a[52:55],a[212:215],v[108:111],a[52:55]
R a[164:167],v199 offset:24576
C v123,v182,v183
M a[52:55],a[208:211],v[104:107],a[52:55]
B v217,s[40:43],s60 offen offset:3072 lds
s_add_u32 s60,s60,s76
M v[180:183],a[176:179],v[32:35],v[168:171]
E v184,v184
M v[180:183],a[180:183],v[36:39],v[180:183]
s_mov_b32 m0,0x1000
E v185,v185
M v[180:183],a[184:187],v[40:43],v[180:183]
R a[168:171],v200 offset:24576
B v214,s[40:43],s60 offen lds
M v[180:183],a[188:191],v[44:47],v[180:183]
E v186,v186
M a[60:63],a[216:219],v[80:83],a[60:63]
R a[172:175],v201 offset:24576
C v130,v184,v185
M a[60:63],a[220:223],v[84:87],a[60:63]
E v187,v187
M a[64:67],a[220:223],v[92:95],a[64:67]
T a[200:201],v207
C v131,v186,v187
M a[64:67],a[216:219],v[88:91],a[64:67]
B v215,s[40:43],s60 offen offset:1024 lds
v_smfmac_f32_16x16x64_bf16 a[36:39],a[96:99],v[88:95],v253
M v[184:187],a[176:179],v[48:51],v[172:175]
E v180,v180
M v[184:187],a[180:183],v[52:55],v[184:187]
E v181,v181
M v[184:187],a[184:187],v[56:59],v[184:187]
T a[202:203],v207 offset:4096
B v216,s[40:43],s60 offen offset:2048 lds
M v[184:187],a[188:191],v[60:63],v[184:187]
E v182,v182
M a[68:71],a[216:219],v[96:99],a[68:71]
T a[204:205],v207 offset:8192
C v138,v180,v181
M a[68:71],a[220:223],v[100:103],a[68:71]
W lgkmcnt(3)
E v183,v183
M a[72:75],a[220:223],v[108:111],a[72:75]
T a[206:207],v207 offset:12288
C v139,v182,v183
M a[72:75],a[216:219],v[104:107],a[72:75]
B v217,s[40:43],s60 offen offset:3072 lds
s_add_u32 s60,s60,s76
M v[180:183],a[160:163],v[0:3],v[160:163]
E v184,v184
M v[180:183],a[164:167],v[4:7],v[180:183]
s_mov_b32 m0,0x2000
E v185,v185
M v[180:183],a[168:171],v[8:11],v[180:183]
T a[208:209],v208
B v214,s[40:43],s60 offen lds
M v[180:183],a[172:175],v[12:15],v[180:183]
E v186,v186
M a[80:83],a[192:195],v[80:83],a[80:83]
T a[210:211],v208 offset:4096
C v146,v184,v185
M a[80:83],a[196:199],v[84:87],a[80:83]
E v187,v187
M a[84:87],a[196:199],v[92:95],a[84:87]
T a[212:213],v208 offset:8192
C v147,v186,v187
M a[84:87],a[192:195],v[88:91],a[84:87]
B v215,s[40:43],s60 offen offset:1024 lds
M v[184:187],a[160:163],v[16:19],v[164:167]
E v180,v180
M v[184:187],a[164:167],v[20:23],v[184:187]
E v181,v181
M v[184:187],a[168:171],v[24:27],v[184:187]
T a[214:215],v208 offset:12288
B v216,s[40:43],s60 offen offset:2048 lds
M v[184:187],a[172:175],v[28:31],v[184:187]
W lgkmcnt(4)
E v182,v182
M a[88:91],a[192:195],v[96:99],a[88:91]
R a[176:179],v198 offset:28672
C v124,v180,v181
M a[88:91],a[196:199],v[100:103],a[88:91]
E v183,v183
M a[92:95],a[196:199],v[108:111],a[92:95]
R a[180:183],v199 offset:28672
C v125,v182,v183
M a[92:95],a[192:195],v[104:107],a[92:95]
B v217,s[40:43],s60 offen offset:3072 lds
s_add_u32 s60,s60,s76
M v[180:183],a[160:163],v[32:35],v[168:171]
E v184,v184
M v[180:183],a[164:167],v[36:39],v[180:183]
s_mov_b32 m0,0x3000
E v185,v185
M v[180:183],a[168:171],v[40:43],v[180:183]
R a[184:187],v200 offset:28672
B v214,s[40:43],s60 offen lds
M v[180:183],a[172:175],v[44:47],v[180:183]
E v186,v186
M a[100:103],a[200:203],v[80:83],a[100:103]
R a[188:191],v201 offset:28672
C v132,v184,v185
M a[100:103],a[204:207],v[84:87],a[100:103]
E v187,v187
M a[104:107],a[204:207],v[92:95],a[104:107]
T a[216:217],v209
C v133,v186,v187
M a[104:107],a[200:203],v[88:91],a[104:107]
B v215,s[40:43],s60 offen offset:1024 lds
v_smfmac_f32_16x16x64_bf16 a[56:59],a[96:99],v[96:103],v253
M v[184:187],a[160:163],v[48:51],v[172:175]
E v180,v180
M v[184:187],a[164:167],v[52:55],v[184:187]
E v181,v181
M v[184:187],a[168:171],v[56:59],v[184:187]
T a[218:219],v209 offset:4096
B v216,s[40:43],s60 offen offset:2048 lds
M v[184:187],a[172:175],v[60:63],v[184:187]
E v182,v182
M a[108:111],a[200:203],v[96:99],a[108:111]
T a[220:221],v209 offset:8192
C v140,v180,v181
M a[108:111],a[204:207],v[100:103],a[108:111]
W lgkmcnt(3)
E v183,v183
M a[112:115],a[204:207],v[108:111],a[112:115]
T a[222:223],v209 offset:12288
C v141,v182,v183
M a[112:115],a[200:203],v[104:107],a[112:115]
B v217,s[40:43],s60 offen offset:3072 lds
s_add_u32 s60,s60,s77
M v[180:183],a[176:179],v[0:3],v[160:163]
W vmcnt(48)
s_barrier
E v184,v184
M v[180:183],a[180:183],v[4:7],v[180:183]
s_mov_b32 m0,0x1c000
E v185,v185
M v[180:183],a[184:187],v[8:11],v[180:183]
T a[192:193],v202 offset:16384
B v218,s[44:47],s61 offen lds
M v[180:183],a[188:191],v[12:15],v[180:183]
E v186,v186
M a[120:123],a[208:211],v[80:83],a[120:123]
T a[194:195],v202 offset:20480
C v148,v184,v185
M a[120:123],a[212:215],v[84:87],a[120:123]
E v187,v187
M a[124:127],a[212:215],v[92:95],a[124:127]
T a[196:197],v202 offset:24576
C v149,v186,v187
M a[124:127],a[208:211],v[88:91],a[124:127]
B v219,s[44:47],s61 offen offset:1024 lds
M v[184:187],a[176:179],v[16:19],v[164:167]
E v180,v180
M v[184:187],a[180:183],v[20:23],v[184:187]
E v181,v181
M v[184:187],a[184:187],v[24:27],v[184:187]
T a[198:199],v202 offset:28672
B v220,s[44:47],s61 offen offset:2048 lds
M v[184:187],a[188:191],v[28:31],v[184:187]
W lgkmcnt(4)
E v182,v182
M a[128:131],a[208:211],v[96:99],a[128:131]
R a[160:163],v198 offset:32768
C v126,v180,v181
M a[128:131],a[212:215],v[100:103],a[128:131]
E v183,v183
M a[132:135],a[212:215],v[108:111],a[132:135]
R a[164:167],v199 offset:32768
C v127,v182,v183
M a[132:135],a[208:211],v[104:107],a[132:135]
B v221,s[44:47],s61 offen offset:3072 lds
s_add_u32 s61,s61,s62
M v[180:183],a[176:179],v[32:35],v[168:171]
E v184,v184
M v[180:183],a[180:183],v[36:39],v[180:183]
s_mov_b32 m0,0x1d000
E v185,v185
M v[180:183],a[184:187],v[40:43],v[180:183]
R a[168:171],v200 offset:32768
)ASM"
R"ASM(
B v218,s[44:47],s61 offen lds
M v[180:183],a[188:191],v[44:47],v[180:183]
E v186,v186
M a[140:143],a[216:219],v[80:83],a[140:143]
R a[172:175],v201 offset:32768
C v134,v184,v185
M a[140:143],a[220:223],v[84:87],a[140:143]
E v187,v187
M a[144:147],a[220:223],v[92:95],a[144:147]
T a[200:201],v203 offset:16384
C v135,v186,v187
M a[144:147],a[216:219],v[88:91],a[144:147]
B v219,s[44:47],s61 offen offset:1024 lds
v_smfmac_f32_16x16x64_bf16 a[76:79],a[96:99],v[104:111],v253
M v[184:187],a[176:179],v[48:51],v[172:175]
E v180,v180
M v[184:187],a[180:183],v[52:55],v[184:187]
E v181,v181
M v[184:187],a[184:187],v[56:59],v[184:187]
T a[202:203],v203 offset:20480
B v220,s[44:47],s61 offen offset:2048 lds
M v[184:187],a[188:191],v[60:63],v[184:187]
E v182,v182
M a[148:151],a[216:219],v[96:99],a[148:151]
T a[204:205],v203 offset:24576
C v142,v180,v181
M a[148:151],a[220:223],v[100:103],a[148:151]
W lgkmcnt(3)
E v183,v183
M a[152:155],a[220:223],v[108:111],a[152:155]
T a[206:207],v203 offset:28672
C v143,v182,v183
M a[152:155],a[216:219],v[104:107],a[152:155]
B v221,s[44:47],s61 offen offset:3072 lds
s_add_u32 s61,s61,s63
)ASM"
R"ASM(
LIT1b_%=:
M v[180:183],a[160:163],v[0:3],v[160:163]
E v184,v184
M v[180:183],a[164:167],v[4:7],v[180:183]
s_mov_b32 m0,0x1e000
E v185,v185
M v[180:183],a[168:171],v[8:11],v[180:183]
T a[208:209],v204 offset:16384
B v218,s[44:47],s61 offen lds
M v[180:183],a[172:175],v[12:15],v[180:183]
E v186,v186
M a[0:3],a[192:195],v[120:123],a[0:3]
T a[210:211],v204 offset:20480
C v150,v184,v185
M a[0:3],a[196:199],v[124:127],a[0:3]
E v187,v187
M a[4:7],a[196:199],v[132:135],a[4:7]
T a[212:213],v204 offset:24576
C v151,v186,v187
M a[4:7],a[192:195],v[128:131],a[4:7]
B v219,s[44:47],s61 offen offset:1024 lds
M v[184:187],a[160:163],v[16:19],v[164:167]
E v180,v180
M v[184:187],a[164:167],v[20:23],v[184:187]
E v181,v181
M v[184:187],a[168:171],v[24:27],v[184:187]
T a[214:215],v204 offset:28672
B v220,s[44:47],s61 offen offset:2048 lds
M v[184:187],a[172:175],v[28:31],v[184:187]
W lgkmcnt(4)
E v182,v182
M a[8:11],a[192:195],v[136:139],a[8:11]
R a[176:179],v198 offset:36864
C v80,v180,v181
M a[8:11],a[196:199],v[140:143],a[8:11]
E v183,v183
M a[12:15],a[196:199],v[148:151],a[12:15]
R a[180:183],v199 offset:36864
C v81,v182,v183
M a[12:15],a[192:195],v[144:147],a[12:15]
B v221,s[44:47],s61 offen offset:3072 lds
s_add_u32 s61,s61,s62
M v[180:183],a[160:163],v[32:35],v[168:171]
E v184,v184
M v[180:183],a[164:167],v[36:39],v[180:183]
s_mov_b32 m0,0x1f000
E v185,v185
M v[180:183],a[168:171],v[40:43],v[180:183]
R a[184:187],v200 offset:36864
B v218,s[44:47],s61 offen lds
M v[180:183],a[172:175],v[44:47],v[180:183]
E v186,v186
M a[20:23],a[200:203],v[120:123],a[20:23]
R a[188:191],v201 offset:36864
C v88,v184,v185
M a[20:23],a[204:207],v[124:127],a[20:23]
E v187,v187
M a[24:27],a[204:207],v[132:135],a[24:27]
T a[216:217],v205 offset:16384
C v89,v186,v187
M a[24:27],a[200:203],v[128:131],a[24:27]
B v219,s[44:47],s61 offen offset:1024 lds
v_smfmac_f32_16x16x64_bf16 a[16:19],a[96:99],v[120:127],v253
M v[184:187],a[160:163],v[48:51],v[172:175]
E v180,v180
M v[184:187],a[164:167],v[52:55],v[184:187]
E v181,v181
M v[184:187],a[168:171],v[56:59],v[184:187]
T a[218:219],v205 offset:20480
B v220,s[44:47],s61 offen offset:2048 lds
M v[184:187],a[172:175],v[60:63],v[184:187]
E v182,v182
M a[28:31],a[200:203],v[136:139],a[28:31]
T a[220:221],v205 offset:24576
C v96,v180,v181
M a[28:31],a[204:207],v[140:143],a[28:31]
W lgkmcnt(3)
E v183,v183
M a[32:35],a[204:207],v[148:151],a[32:35]
T a[222:223],v205 offset:28672
C v97,v182,v183
M a[32:35],a[200:203],v[144:147],a[32:35]
B v221,s[44:47],s61 offen offset:3072 lds
s_add_u32 s61,s61,s75
s_and_b32 s61,s61,s74
M v[180:183],a[176:179],v[0:3],v[160:163]
E v184,v184
M v[180:183],a[180:183],v[4:7],v[180:183]
s_mov_b32 m0,0x4000
E v185,v185
M v[180:183],a[184:187],v[8:11],v[180:183]
T a[192:193],v206 offset:16384
B v214,s[40:43],s60 offen nt lds
M v[180:183],a[188:191],v[12:15],v[180:183]
E v186,v186
M a[40:43],a[208:211],v[120:123],a[40:43]
T a[194:195],v206 offset:20480
C v104,v184,v185
M a[40:43],a[212:215],v[124:127],a[40:43]
E v187,v187
M a[44:47],a[212:215],v[132:135],a[44:47]
T a[196:197],v206 offset:24576
C v105,v186,v187
M a[44:47],a[208:211],v[128:131],a[44:47]
B v215,s[40:43],s60 offen offset:1024 nt lds
M v[184:187],a[176:179],v[16:19],v[164:167]
E v180,v180
M v[184:187],a[180:183],v[20:23],v[184:187]
E v181,v181
M v[184:187],a[184:187],v[24:27],v[184:187]
T a[198:199],v206 offset:28672
B v216,s[40:43],s60 offen offset:2048 nt lds
M v[184:187],a[188:191],v[28:31],v[184:187]
W lgkmcnt(4)
E v182,v182
M a[48:51],a[208:211],v[136:139],a[48:51]
R a[160:163],v198 offset:40960
C v82,v180,v181
M a[48:51],a[212:215],v[140:143],a[48:51]
E v183,v183
M a[52:55],a[212:215],v[148:151],a[52:55]
R a[164:167],v199 offset:40960
C v83,v182,v183
M a[52:55],a[208:211],v[144:147],a[52:55]
B v217,s[40:43],s60 offen offset:3072 nt lds
s_add_u32 s60,s60,s76
M v[180:183],a[176:179],v[32:35],v[168:171]
E v184,v184
M v[180:183],a[180:183],v[36:39],v[180:183]
s_mov_b32 m0,0x5000
E v185,v185
M v[180:183],a[184:187],v[40:43],v[180:183]
R a[168:171],v200 offset:40960
B v214,s[40:43],s60 offen nt lds
M v[180:183],a[188:191],v[44:47],v[180:183]
E v186,v186
M a[60:63],a[216:219],v[120:123],a[60:63]
R a[172:175],v201 offset:40960
C v90,v184,v185
M a[60:63],a[220:223],v[124:127],a[60:63]
E v187,v187
M a[64:67],a[220:223],v[132:135],a[64:67]
T a[200:201],v207 offset:16384
C v91,v186,v187
M a[64:67],a[216:219],v[128:131],a[64:67]
B v215,s[40:43],s60 offen offset:1024 nt lds
v_smfmac_f32_16x16x64_bf16 a[36:39],a[96:99],v[128:135],v253
M v[184:187],a[176:179],v[48:51],v[172:175]
E v180,v180
M v[184:187],a[180:183],v[52:55],v[184:187]
E v181,v181
M v[184:187],a[184:187],v[56:59],v[184:187]
T a[202:203],v207 offset:20480
B v216,s[40:43],s60 offen offset:2048 nt lds
M v[184:187],a[188:191],v[60:63],v[184:187]
E v182,v182
M a[68:71],a[216:219],v[136:139],a[68:71]
T a[204:205],v207 offset:24576
C v98,v180,v181
M a[68:71],a[220:223],v[140:143],a[68:71]
W lgkmcnt(3)
E v183,v183
M a[72:75],a[220:223],v[148:151],a[72:75]
T a[206:207],v207 offset:28672
C v99,v182,v183
M a[72:75],a[216:219],v[144:147],a[72:75]
B v217,s[40:43],s60 offen offset:3072 nt lds
s_add_u32 s60,s60,s76
s_branch LSWV_%=
LSWVR_%=:
)ASM"
R"ASM(
M v[180:183],a[160:163],v[0:3],v[160:163]
E v184,v184
M v[180:183],a[164:167],v[4:7],v[180:183]
s_mov_b32 m0,0x6000
E v185,v185
M v[180:183],a[168:171],v[8:11],v[180:183]
T a[208:209],v208 offset:16384
B v214,s[40:43],s60 offen nt lds
M v[180:183],a[172:175],v[12:15],v[180:183]
E v186,v186
M a[80:83],a[192:195],v[120:123],a[80:83]
T a[210:211],v208 offset:20480
C v106,v184,v185
M a[80:83],a[196:199],v[124:127],a[80:83]
E v187,v187
M a[84:87],a[196:199],v[132:135],a[84:87]
T a[212:213],v208 offset:24576
C v107,v186,v187
M a[84:87],a[192:195],v[128:131],a[84:87]
B v215,s[40:43],s60 offen offset:1024 nt lds
M v[184:187],a[160:163],v[16:19],v[164:167]
E v180,v180
M v[184:187],a[164:167],v[20:23],v[184:187]
E v181,v181
M v[184:187],a[168:171],v[24:27],v[184:187]
T a[214:215],v208 offset:28672
B v216,s[40:43],s60 offen offset:2048 nt lds
M v[184:187],a[172:175],v[28:31],v[184:187]
W lgkmcnt(4)
E v182,v182
M a[88:91],a[192:195],v[136:139],a[88:91]
R a[176:179],v198 offset:45056
C v84,v180,v181
M a[88:91],a[196:199],v[140:143],a[88:91]
E v183,v183
M a[92:95],a[196:199],v[148:151],a[92:95]
R a[180:183],v199 offset:45056
C v85,v182,v183
M a[92:95],a[192:195],v[144:147],a[92:95]
B v217,s[40:43],s60 offen offset:3072 nt lds
s_add_u32 s60,s60,s76
M v[180:183],a[160:163],v[32:35],v[168:171]
E v184,v184
M v[180:183],a[164:167],v[36:39],v[180:183]
s_mov_b32 m0,0x7000
E v185,v185
M v[180:183],a[168:171],v[40:43],v[180:183]
R a[184:187],v200 offset:45056
B v214,s[40:43],s60 offen nt lds
M v[180:183],a[172:175],v[44:47],v[180:183]
E v186,v186
M a[100:103],a[200:203],v[120:123],a[100:103]
R a[188:191],v201 offset:45056
C v92,v184,v185
M a[100:103],a[204:207],v[124:127],a[100:103]
E v187,v187
M a[104:107],a[204:207],v[132:135],a[104:107]
T a[216:217],v209 offset:16384
C v93,v186,v187
M a[104:107],a[200:203],v[128:131],a[104:107]
B v215,s[40:43],s60 offen offset:1024 nt lds
v_smfmac_f32_16x16x64_bf16 a[56:59],a[96:99],v[136:143],v253
M v[184:187],a[160:163],v[48:51],v[172:175]
E v180,v180
M v[184:187],a[164:167],v[52:55],v[184:187]
E v181,v181
M v[184:187],a[168:171],v[56:59],v[184:187]
T a[218:219],v209 offset:20480
B v216,s[40:43],s60 offen offset:2048 nt lds
M v[184:187],a[172:175],v[60:63],v[184:187]
E v182,v182
M a[108:111],a[200:203],v[136:139],a[108:111]
T a[220:221],v209 offset:24576
C v100,v180,v181
M a[108:111],a[204:207],v[140:143],a[108:111]
W lgkmcnt(3)
E v183,v183
M a[112:115],a[204:207],v[148:151],a[112:115]
T a[222:223],v209 offset:28672
C v101,v182,v183
M a[112:115],a[200:203],v[144:147],a[112:115]
B v217,s[40:43],s60 offen offset:3072 nt lds
s_add_u32 s60,s60,s77
M v[180:183],a[176:179],v[0:3],v[160:163]
W vmcnt(48)
s_barrier
E v184,v184
M v[180:183],a[180:183],v[4:7],v[180:183]
s_mov_b32 m0,0x10000
E v185,v185
M v[180:183],a[184:187],v[8:11],v[180:183]
T a[192:193],v202 offset:32768
B v218,s[44:47],s61 offen lds
M v[180:183],a[188:191],v[12:15],v[180:183]
E v186,v186
M a[120:123],a[208:211],v[120:123],a[120:123]
T a[194:195],v202 offset:36864
C v108,v184,v185
M a[120:123],a[212:215],v[124:127],a[120:123]
E v187,v187
M a[124:127],a[212:215],v[132:135],a[124:127]
T a[196:197],v202 offset:40960
C v109,v186,v187
M a[124:127],a[208:211],v[128:131],a[124:127]
B v219,s[44:47],s61 offen offset:1024 lds
M v[184:187],a[176:179],v[16:19],v[164:167]
E v180,v180
M v[184:187],a[180:183],v[20:23],v[184:187]
E v181,v181
M v[184:187],a[184:187],v[24:27],v[184:187]
T a[198:199],v202 offset:45056
B v220,s[44:47],s61 offen offset:2048 lds
M v[184:187],a[188:191],v[28:31],v[184:187]
W lgkmcnt(4)
E v182,v182
M a[128:131],a[208:211],v[136:139],a[128:131]
R a[160:163],v198 offset:49152
C v86,v180,v181
M a[128:131],a[212:215],v[140:143],a[128:131]
E v183,v183
M a[132:135],a[212:215],v[148:151],a[132:135]
R a[164:167],v199 offset:49152
C v87,v182,v183
M a[132:135],a[208:211],v[144:147],a[132:135]
B v221,s[44:47],s61 offen offset:3072 lds
s_add_u32 s61,s61,s62
M v[180:183],a[176:179],v[32:35],v[168:171]
E v184,v184
M v[180:183],a[180:183],v[36:39],v[180:183]
s_mov_b32 m0,0x11000
E v185,v185
M v[180:183],a[184:187],v[40:43],v[180:183]
R a[168:171],v200 offset:49152
B v218,s[44:47],s61 offen lds
M v[180:183],a[188:191],v[44:47],v[180:183]
E v186,v186
M a[140:143],a[216:219],v[120:123],a[140:143]
R a[172:175],v201 offset:49152
C v94,v184,v185
M a[140:143],a[220:223],v[124:127],a[140:143]
E v187,v187
M a[144:147],a[220:223],v[132:135],a[144:147]
T a[200:201],v203 offset:32768
C v95,v186,v187
M a[144:147],a[216:219],v[128:131],a[144:147]
B v219,s[44:47],s61 offen offset:1024 lds
v_smfmac_f32_16x16x64_bf16 a[76:79],a[96:99],v[144:151],v253
M v[184:187],a[176:179],v[48:51],v[172:175]
E v180,v180
M v[184:187],a[180:183],v[52:55],v[184:187]
E v181,v181
M v[184:187],a[184:187],v[56:59],v[184:187]
T a[202:203],v203 offset:36864
B v220,s[44:47],s61 offen offset:2048 lds
M v[184:187],a[188:191],v[60:63],v[184:187]
E v182,v182
M a[148:151],a[216:219],v[136:139],a[148:151]
T a[204:205],v203 offset:40960
C v102,v180,v181
M a[148:151],a[220:223],v[140:143],a[148:151]
W lgkmcnt(3)
E v183,v183
M a[152:155],a[220:223],v[148:151],a[152:155]
T a[206:207],v203 offset:45056
C v103,v182,v183
M a[152:155],a[216:219],v[144:147],a[152:155]
B v221,s[44:47],s61 offen offset:3072 lds
s_add_u32 s61,s61,s63
)ASM"
R"ASM(
M v[180:183],a[160:163],v[0:3],v[160:163]
E v184,v184
M v[180:183],a[164:167],v[4:7],v[180:183]
s_mov_b32 m0,0x12000
E v185,v185
M v[180:183],a[168:171],v[8:11],v[180:183]
T a[208:209],v204 offset:32768
B v218,s[44:47],s61 offen lds
M v[180:183],a[172:175],v[12:15],v[180:183]
E v186,v186
M a[0:3],a[192:195],v[80:83],a[0:3]
T a[210:211],v204 offset:36864
C v110,v184,v185
M a[0:3],a[196:199],v[84:87],a[0:3]
E v187,v187
M a[4:7],a[196:199],v[92:95],a[4:7]
T a[212:213],v204 offset:40960
C v111,v186,v187
M a[4:7],a[192:195],v[88:91],a[4:7]
B v219,s[44:47],s61 offen offset:1024 lds
M v[184:187],a[160:163],v[16:19],v[164:167]
E v180,v180
M v[184:187],a[164:167],v[20:23],v[184:187]
E v181,v181
M v[184:187],a[168:171],v[24:27],v[184:187]
T a[214:215],v204 offset:45056
B v220,s[44:47],s61 offen offset:2048 lds
M v[184:187],a[172:175],v[28:31],v[184:187]
W lgkmcnt(4)
E v182,v182
M a[8:11],a[192:195],v[96:99],a[8:11]
R a[176:179],v198 offset:53248
C v120,v180,v181
M a[8:11],a[196:199],v[100:103],a[8:11]
E v183,v183
M a[12:15],a[196:199],v[108:111],a[12:15]
R a[180:183],v199 offset:53248
C v121,v182,v183
M a[12:15],a[192:195],v[104:107],a[12:15]
B v221,s[44:47],s61 offen offset:3072 lds
s_add_u32 s61,s61,s62
M v[180:183],a[160:163],v[32:35],v[168:171]
E v184,v184
M v[180:183],a[164:167],v[36:39],v[180:183]
s_mov_b32 m0,0x13000
E v185,v185
M v[180:183],a[168:171],v[40:43],v[180:183]
R a[184:187],v200 offset:53248
B v218,s[44:47],s61 offen lds
M v[180:183],a[172:175],v[44:47],v[180:183]
E v186,v186
M a[20:23],a[200:203],v[80:83],a[20:23]
R a[188:191],v201 offset:53248
C v128,v184,v185
M a[20:23],a[204:207],v[84:87],a[20:23]
E v187,v187
M a[24:27],a[204:207],v[92:95],a[24:27]
T a[216:217],v205 offset:32768
C v129,v186,v187
M a[24:27],a[200:203],v[88:91],a[24:27]
B v219,s[44:47],s61 offen offset:1024 lds
v_smfmac_f32_16x16x64_bf16 a[16:19],a[96:99],v[80:87],v253
M v[184:187],a[160:163],v[48:51],v[172:175]
E v180,v180
M v[184:187],a[164:167],v[52:55],v[184:187]
E v181,v181
M v[184:187],a[168:171],v[56:59],v[184:187]
T a[218:219],v205 offset:36864
B v220,s[44:47],s61 offen offset:2048 lds
M v[184:187],a[172:175],v[60:63],v[184:187]
E v182,v182
M a[28:31],a[200:203],v[96:99],a[28:31]
T a[220:221],v205 offset:40960
C v136,v180,v181
M a[28:31],a[204:207],v[100:103],a[28:31]
W lgkmcnt(3)
E v183,v183
M a[32:35],a[204:207],v[108:111],a[32:35]
T a[222:223],v205 offset:45056
C v137,v182,v183
M a[32:35],a[200:203],v[104:107],a[32:35]
B v221,s[44:47],s61 offen offset:3072 lds
s_add_u32 s61,s61,s75
s_and_b32 s61,s61,s74
M v[180:183],a[176:179],v[0:3],v[160:163]
E v184,v184
M v[180:183],a[180:183],v[4:7],v[180:183]
s_mov_b32 m0,0x8000
E v185,v185
M v[180:183],a[184:187],v[8:11],v[180:183]
T a[192:193],v206 offset:32768
B v214,s[40:43],s60 offen nt lds
M v[180:183],a[188:191],v[12:15],v[180:183]
E v186,v186
M a[40:43],a[208:211],v[80:83],a[40:43]
T a[194:195],v206 offset:36864
C v144,v184,v185
M a[40:43],a[212:215],v[84:87],a[40:43]
E v187,v187
M a[44:47],a[212:215],v[92:95],a[44:47]
T a[196:197],v206 offset:40960
C v145,v186,v187
M a[44:47],a[208:211],v[88:91],a[44:47]
B v215,s[40:43],s60 offen offset:1024 nt lds
M v[184:187],a[176:179],v[16:19],v[164:167]
E v180,v180
M v[184:187],a[180:183],v[20:23],v[184:187]
E v181,v181
M v[184:187],a[184:187],v[24:27],v[184:187]
T a[198:199],v206 offset:45056
B v216,s[40:43],s60 offen offset:2048 nt lds
M v[184:187],a[188:191],v[28:31],v[184:187]
W lgkmcnt(4)
E v182,v182
M a[48:51],a[208:211],v[96:99],a[48:51]
R a[160:163],v198 offset:57344
C v122,v180,v181
M a[48:51],a[212:215],v[100:103],a[48:51]
E v183,v183
M a[52:55],a[212:215],v[108:111],a[52:55]
R a[164:167],v199 offset:57344
C v123,v182,v183
M a[52:55],a[208:211],v[104:107],a[52:55]
B v217,s[40:43],s60 offen offset:3072 nt lds
s_add_u32 s60,s60,s76
M v[180:183],a[176:179],v[32:35],v[168:171]
E v184,v184
M v[180:183],a[180:183],v[36:39],v[180:183]
s_mov_b32 m0,0x9000
E v185,v185
M v[180:183],a[184:187],v[40:43],v[180:183]
R a[168:171],v200 offset:57344
B v214,s[40:43],s60 offen nt lds
M v[180:183],a[188:191],v[44:47],v[180:183]
E v186,v186
M a[60:63],a[216:219],v[80:83],a[60:63]
R a[172:175],v201 offset:57344
C v130,v184,v185
M a[60:63],a[220:223],v[84:87],a[60:63]
E v187,v187
M a[64:67],a[220:223],v[92:95],a[64:67]
T a[200:201],v207 offset:32768
C v131,v186,v187
M a[64:67],a[216:219],v[88:91],a[64:67]
B v215,s[40:43],s60 offen offset:1024 nt lds
v_smfmac_f32_16x16x64_bf16 a[36:39],a[96:99],v[88:95],v253
M v[184:187],a[176:179],v[48:51],v[172:175]
E v180,v180
M v[184:187],a[180:183],v[52:55],v[184:187]
E v181,v181
M v[184:187],a[184:187],v[56:59],v[184:187]
T a[202:203],v207 offset:36864
B v216,s[40:43],s60 offen offset:2048 nt lds
M v[184:187],a[188:191],v[60:63],v[184:187]
E v182,v182
M a[68:71],a[216:219],v[96:99],a[68:71]
T a[204:205],v207 offset:40960
C v138,v180,v181
M a[68:71],a[220:223],v[100:103],a[68:71]
W lgkmcnt(3)
E v183,v183
M a[72:75],a[220:223],v[108:111],a[72:75]
T a[206:207],v207 offset:45056
C v139,v182,v183
M a[72:75],a[216:219],v[104:107],a[72:75]
B v217,s[40:43],s60 offen offset:3072 nt lds
s_add_u32 s60,s60,s76
M v[180:183],a[160:163],v[0:3],v[160:163]
E v184,v184
M v[180:183],a[164:167],v[4:7],v[180:183]
s_mov_b32 m0,0xa000
E v185,v185
M v[180:183],a[168:171],v[8:11],v[180:183]
T a[208:209],v208 offset:32768
B v214,s[40:43],s60 offen nt lds
M v[180:183],a[172:175],v[12:15],v[180:183]
E v186,v186
M a[80:83],a[192:195],v[80:83],a[80:83]
T a[210:211],v208 offset:36864
C v146,v184,v185
M a[80:83],a[196:199],v[84:87],a[80:83]
E v187,v187
M a[84:87],a[196:199],v[92:95],a[84:87]
T a[212:213],v208 offset:40960
C v147,v186,v187
M a[84:87],a[192:195],v[88:91],a[84:87]
B v215,s[40:43],s60 offen offset:1024 nt lds
M v[184:187],a[160:163],v[16:19],v[164:167]
E v180,v180
M v[184:187],a[164:167],v[20:23],v[184:187]
E v181,v181
M v[184:187],a[168:171],v[24:27],v[184:187]
T a[214:215],v208 offset:45056
B v216,s[40:43],s60 offen offset:2048 nt lds
M v[184:187],a[172:175],v[28:31],v[184:187]
W lgkmcnt(4)
E v182,v182
M a[88:91],a[192:195],v[96:99],a[88:91]
R a[176:179],v198 offset:61440
C v124,v180,v181
M a[88:91],a[196:199],v[100:103],a[88:91]
E v183,v183
M a[92:95],a[196:199],v[108:111],a[92:95]
R a[180:183],v199 offset:61440
C v125,v182,v183
M a[92:95],a[192:195],v[104:107],a[92:95]
B v217,s[40:43],s60 offen offset:3072 nt lds
s_add_u32 s60,s60,s76
M v[180:183],a[160:163],v[32:35],v[168:171]
E v184,v184
M v[180:183],a[164:167],v[36:39],v[180:183]
s_mov_b32 m0,0xb000
E v185,v185
M v[180:183],a[168:171],v[40:43],v[180:183]
R a[184:187],v200 offset:61440
B v214,s[40:43],s60 offen nt lds
M v[180:183],a[172:175],v[44:47],v[180:183]
E v186,v186
M a[100:103],a[200:203],v[80:83],a[100:103]
R a[188:191],v201 offset:61440
C v132,v184,v185
M a[100:103],a[204:207],v[84:87],a[100:103]
E v187,v187
M a[104:107],a[204:207],v[92:95],a[104:107]
T a[216:217],v209 offset:32768
C v133,v186,v187
M a[104:107],a[200:203],v[88:91],a[104:107]
B v215,s[40:43],s60 offen offset:1024 nt lds
v_smfmac_f32_16x16x64_bf16 a[56:59],a[96:99],v[96:103],v253
M v[184:187],a[160:163],v[48:51],v[172:175]
E v180,v180
M v[184:187],a[164:167],v[52:55],v[184:187]
E v181,v181
M v[184:187],a[168:171],v[56:59],v[184:187]
T a[218:219],v209 offset:36864
B v216,s[40:43],s60 offen offset:2048 nt lds
M v[184:187],a[172:175],v[60:63],v[184:187]
E v182,v182
M a[108:111],a[200:203],v[96:99],a[108:111]
T a[220:221],v209 offset:40960
C v140,v180,v181
M a[108:111],a[204:207],v[100:103],a[108:111]
W lgkmcnt(3)
E v183,v183
M a[112:115],a[204:207],v[108:111],a[112:115]
T a[222:223],v209 offset:45056
C v141,v182,v183
M a[112:115],a[200:203],v[104:107],a[112:115]
B v217,s[40:43],s60 offen offset:3072 nt lds
s_add_u32 s60,s60,s77
M v[180:183],a[176:179],v[0:3],v[160:163]
W vmcnt(48)
s_barrier
E v184,v184
M v[180:183],a[180:183],v[4:7],v[180:183]
s_mov_b32 m0,0x14000
E v185,v185
M v[180:183],a[184:187],v[8:11],v[180:183]
T a[192:193],v202 offset:49152
B v218,s[44:47],s61 offen lds
M v[180:183],a[188:191],v[12:15],v[180:183]
E v186,v186
M a[120:123],a[208:211],v[80:83],a[120:123]
T a[194:195],v202 offset:53248
C v148,v184,v185
M a[120:123],a[212:215],v[84:87],a[120:123]
E v187,v187
M a[124:127],a[212:215],v[92:95],a[124:127]
T a[196:197],v202 offset:57344
C v149,v186,v187
M a[124:127],a[208:211],v[88:91],a[124:127]
B v219,s[44:47],s61 offen offset:1024 lds
M v[184:187],a[176:179],v[16:19],v[164:167]
E v180,v180
M v[184:187],a[180:183],v[20:23],v[184:187]
E v181,v181
M v[184:187],a[184:187],v[24:27],v[184:187]
T a[198:199],v202 offset:61440
B v220,s[44:47],s61 offen offset:2048 lds
M v[184:187],a[188:191],v[28:31],v[184:187]
W lgkmcnt(4)
E v182,v182
M a[128:131],a[208:211],v[96:99],a[128:131]
R a[160:163],v198
C v126,v180,v181
M a[128:131],a[212:215],v[100:103],a[128:131]
E v183,v183
M a[132:135],a[212:215],v[108:111],a[132:135]
R a[164:167],v199
C v127,v182,v183
M a[132:135],a[208:211],v[104:107],a[132:135]
B v221,s[44:47],s61 offen offset:3072 lds
s_add_u32 s61,s61,s62
M v[180:183],a[176:179],v[32:35],v[168:171]
E v184,v184
M v[180:183],a[180:183],v[36:39],v[180:183]
s_mov_b32 m0,0x15000
E v185,v185
M v[180:183],a[184:187],v[40:43],v[180:183]
R a[168:171],v200
)ASM"
R"ASM(
B v218,s[44:47],s61 offen lds
M v[180:183],a[188:191],v[44:47],v[180:183]
E v186,v186
M a[140:143],a[216:219],v[80:83],a[140:143]
R a[172:175],v201
C v134,v184,v185
M a[140:143],a[220:223],v[84:87],a[140:143]
E v187,v187
M a[144:147],a[220:223],v[92:95],a[144:147]
T a[200:201],v203 offset:49152
C v135,v186,v187
M a[144:147],a[216:219],v[88:91],a[144:147]
B v219,s[44:47],s61 offen offset:1024 lds
v_smfmac_f32_16x16x64_bf16 a[76:79],a[96:99],v[104:111],v253
M v[184:187],a[176:179],v[48:51],v[172:175]
E v180,v180
M v[184:187],a[180:183],v[52:55],v[184:187]
E v181,v181
M v[184:187],a[184:187],v[56:59],v[184:187]
T a[202:203],v203 offset:53248
B v220,s[44:47],s61 offen offset:2048 lds
M v[184:187],a[188:191],v[60:63],v[184:187]
E v182,v182
M a[148:151],a[216:219],v[96:99],a[148:151]
T a[204:205],v203 offset:57344
C v142,v180,v181
M a[148:151],a[220:223],v[100:103],a[148:151]
W lgkmcnt(3)
E v183,v183
M a[152:155],a[220:223],v[108:111],a[152:155]
T a[206:207],v203 offset:61440
C v143,v182,v183
M a[152:155],a[216:219],v[104:107],a[152:155]
B v221,s[44:47],s61 offen offset:3072 lds
s_add_u32 s61,s61,s63
s_cmp_eq_u32 s69,3
s_cbranch_scc1 LEXITb_%=
)ASM"
R"ASM(
M v[180:183],a[160:163],v[0:3],v[160:163]
E v184,v184
M v[180:183],a[164:167],v[4:7],v[180:183]
s_mov_b32 m0,0x16000
E v185,v185
M v[180:183],a[168:171],v[8:11],v[180:183]
T a[208:209],v204 offset:49152
B v218,s[44:47],s61 offen lds
M v[180:183],a[172:175],v[12:15],v[180:183]
E v186,v186
M a[0:3],a[192:195],v[120:123],a[0:3]
T a[210:211],v204 offset:53248
C v150,v184,v185
M a[0:3],a[196:199],v[124:127],a[0:3]
E v187,v187
M a[4:7],a[196:199],v[132:135],a[4:7]
T a[212:213],v204 offset:57344
C v151,v186,v187
M a[4:7],a[192:195],v[128:131],a[4:7]
B v219,s[44:47],s61 offen offset:1024 lds
M v[184:187],a[160:163],v[16:19],v[164:167]
E v180,v180
M v[184:187],a[164:167],v[20:23],v[184:187]
E v181,v181
M v[184:187],a[168:171],v[24:27],v[184:187]
T a[214:215],v204 offset:61440
B v220,s[44:47],s61 offen offset:2048 lds
M v[184:187],a[172:175],v[28:31],v[184:187]
W lgkmcnt(4)
E v182,v182
M a[8:11],a[192:195],v[136:139],a[8:11]
R a[176:179],v198 offset:4096
C v80,v180,v181
M a[8:11],a[196:199],v[140:143],a[8:11]
E v183,v183
M a[12:15],a[196:199],v[148:151],a[12:15]
R a[180:183],v199 offset:4096
C v81,v182,v183
M a[12:15],a[192:195],v[144:147],a[12:15]
B v221,s[44:47],s61 offen offset:3072 lds
s_add_u32 s61,s61,s62
M v[180:183],a[160:163],v[32:35],v[168:171]
E v184,v184
M v[180:183],a[164:167],v[36:39],v[180:183]
s_mov_b32 m0,0x17000
E v185,v185
M v[180:183],a[168:171],v[40:43],v[180:183]
R a[184:187],v200 offset:4096
B v218,s[44:47],s61 offen lds
M v[180:183],a[172:175],v[44:47],v[180:183]
E v186,v186
M a[20:23],a[200:203],v[120:123],a[20:23]
R a[188:191],v201 offset:4096
C v88,v184,v185
M a[20:23],a[204:207],v[124:127],a[20:23]
E v187,v187
M a[24:27],a[204:207],v[132:135],a[24:27]
T a[216:217],v205 offset:49152
C v89,v186,v187
M a[24:27],a[200:203],v[128:131],a[24:27]
B v219,s[44:47],s61 offen offset:1024 lds
v_smfmac_f32_16x16x64_bf16 a[16:19],a[96:99],v[120:127],v253
M v[184:187],a[160:163],v[48:51],v[172:175]
E v180,v180
M v[184:187],a[164:167],v[52:55],v[184:187]
E v181,v181
M v[184:187],a[168:171],v[56:59],v[184:187]
T a[218:219],v205 offset:53248
B v220,s[44:47],s61 offen offset:2048 lds
M v[184:187],a[172:175],v[60:63],v[184:187]
E v182,v182
M a[28:31],a[200:203],v[136:139],a[28:31]
T a[220:221],v205 offset:57344
C v96,v180,v181
M a[28:31],a[204:207],v[140:143],a[28:31]
W lgkmcnt(3)
E v183,v183
M a[32:35],a[204:207],v[148:151],a[32:35]
T a[222:223],v205 offset:61440
C v97,v182,v183
M a[32:35],a[200:203],v[144:147],a[32:35]
B v221,s[44:47],s61 offen offset:3072 lds
s_add_u32 s61,s61,s75
s_and_b32 s61,s61,s74
M v[180:183],a[176:179],v[0:3],v[160:163]
E v184,v184
M v[180:183],a[180:183],v[4:7],v[180:183]
s_mov_b32 m0,0xc000
E v185,v185
M v[180:183],a[184:187],v[8:11],v[180:183]
T a[192:193],v206 offset:49152
B v214,s[40:43],s60 offen nt lds
M v[180:183],a[188:191],v[12:15],v[180:183]
E v186,v186
M a[40:43],a[208:211],v[120:123],a[40:43]
T a[194:195],v206 offset:53248
C v104,v184,v185
M a[40:43],a[212:215],v[124:127],a[40:43]
E v187,v187
M a[44:47],a[212:215],v[132:135],a[44:47]
T a[196:197],v206 offset:57344
C v105,v186,v187
M a[44:47],a[208:211],v[128:131],a[44:47]
B v215,s[40:43],s60 offen offset:1024 nt lds
M v[184:187],a[176:179],v[16:19],v[164:167]
E v180,v180
M v[184:187],a[180:183],v[20:23],v[184:187]
E v181,v181
M v[184:187],a[184:187],v[24:27],v[184:187]
T a[198:199],v206 offset:61440
B v216,s[40:43],s60 offen offset:2048 nt lds
M v[184:187],a[188:191],v[28:31],v[184:187]
W lgkmcnt(4)
E v182,v182
M a[48:51],a[208:211],v[136:139],a[48:51]
R a[160:163],v198 offset:8192
C v82,v180,v181
M a[48:51],a[212:215],v[140:143],a[48:51]
E v183,v183
M a[52:55],a[212:215],v[148:151],a[52:55]
R a[164:167],v199 offset:8192
C v83,v182,v183
M a[52:55],a[208:211],v[144:147],a[52:55]
B v217,s[40:43],s60 offen offset:3072 nt lds
s_add_u32 s60,s60,s76
M v[180:183],a[176:179],v[32:35],v[168:171]
E v184,v184
M v[180:183],a[180:183],v[36:39],v[180:183]
s_mov_b32 m0,0xd000
E v185,v185
M v[180:183],a[184:187],v[40:43],v[180:183]
R a[168:171],v200 offset:8192
B v214,s[40:43],s60 offen nt lds
M v[180:183],a[188:191],v[44:47],v[180:183]
E v186,v186
M a[60:63],a[216:219],v[120:123],a[60:63]
R a[172:175],v201 offset:8192
C v90,v184,v185
M a[60:63],a[220:223],v[124:127],a[60:63]
E v187,v187
M a[64:67],a[220:223],v[132:135],a[64:67]
T a[200:201],v207 offset:49152
C v91,v186,v187
M a[64:67],a[216:219],v[128:131],a[64:67]
B v215,s[40:43],s60 offen offset:1024 nt lds
v_smfmac_f32_16x16x64_bf16 a[36:39],a[96:99],v[128:135],v253
M v[184:187],a[176:179],v[48:51],v[172:175]
E v180,v180
M v[184:187],a[180:183],v[52:55],v[184:187]
E v181,v181
M v[184:187],a[184:187],v[56:59],v[184:187]
T a[202:203],v207 offset:53248
B v216,s[40:43],s60 offen offset:2048 nt lds
M v[184:187],a[188:191],v[60:63],v[184:187]
E v182,v182
M a[68:71],a[216:219],v[136:139],a[68:71]
T a[204:205],v207 offset:57344
C v98,v180,v181
M a[68:71],a[220:223],v[140:143],a[68:71]
W lgkmcnt(3)
E v183,v183
M a[72:75],a[220:223],v[148:151],a[72:75]
T a[206:207],v207 offset:61440
C v99,v182,v183
M a[72:75],a[216:219],v[144:147],a[72:75]
B v217,s[40:43],s60 offen offset:3072 nt lds
s_add_u32 s60,s60,s76
M v[180:183],a[160:163],v[0:3],v[160:163]
E v184,v184
M v[180:183],a[164:167],v[4:7],v[180:183]
s_mov_b32 m0,0xe000
E v185,v185
M v[180:183],a[168:171],v[8:11],v[180:183]
T a[208:209],v208 offset:49152
B v214,s[40:43],s60 offen nt lds
M v[180:183],a[172:175],v[12:15],v[180:183]
E v186,v186
M a[80:83],a[192:195],v[120:123],a[80:83]
T a[210:211],v208 offset:53248
C v106,v184,v185
M a[80:83],a[196:199],v[124:127],a[80:83]
E v187,v187
M a[84:87],a[196:199],v[132:135],a[84:87]
T a[212:213],v208 offset:57344
C v107,v186,v187
M a[84:87],a[192:195],v[128:131],a[84:87]
B v215,s[40:43],s60 offen offset:1024 nt lds
M v[184:187],a[160:163],v[16:19],v[164:167]
E v180,v180
M v[184:187],a[164:167],v[20:23],v[184:187]
E v181,v181
M v[184:187],a[168:171],v[24:27],v[184:187]
T a[214:215],v208 offset:61440
B v216,s[40:43],s60 offen offset:2048 nt lds
M v[184:187],a[172:175],v[28:31],v[184:187]
W lgkmcnt(4)
E v182,v182
M a[88:91],a[192:195],v[136:139],a[88:91]
R a[176:179],v198 offset:12288
C v84,v180,v181
M a[88:91],a[196:199],v[140:143],a[88:91]
E v183,v183
M a[92:95],a[196:199],v[148:151],a[92:95]
R a[180:183],v199 offset:12288
C v85,v182,v183
M a[92:95],a[192:195],v[144:147],a[92:95]
B v217,s[40:43],s60 offen offset:3072 nt lds
s_add_u32 s60,s60,s76
M v[180:183],a[160:163],v[32:35],v[168:171]
E v184,v184
M v[180:183],a[164:167],v[36:39],v[180:183]
s_mov_b32 m0,0xf000
E v185,v185
M v[180:183],a[168:171],v[40:43],v[180:183]
R a[184:187],v200 offset:12288
B v214,s[40:43],s60 offen nt lds
M v[180:183],a[172:175],v[44:47],v[180:183]
E v186,v186
M a[100:103],a[200:203],v[120:123],a[100:103]
R a[188:191],v201 offset:12288
C v92,v184,v185
M a[100:103],a[204:207],v[124:127],a[100:103]
E v187,v187
M a[104:107],a[204:207],v[132:135],a[104:107]
T a[216:217],v209 offset:49152
C v93,v186,v187
M a[104:107],a[200:203],v[128:131],a[104:107]
B v215,s[40:43],s60 offen offset:1024 nt lds
v_smfmac_f32_16x16x64_bf16 a[56:59],a[96:99],v[136:143],v253
M v[184:187],a[160:163],v[48:51],v[172:175]
E v180,v180
M v[184:187],a[164:167],v[52:55],v[184:187]
E v181,v181
M v[184:187],a[168:171],v[56:59],v[184:187]
T a[218:219],v209 offset:53248
B v216,s[40:43],s60 offen offset:2048 nt lds
M v[184:187],a[172:175],v[60:63],v[184:187]
E v182,v182
M a[108:111],a[200:203],v[136:139],a[108:111]
T a[220:221],v209 offset:57344
C v100,v180,v181
M a[108:111],a[204:207],v[140:143],a[108:111]
W lgkmcnt(3)
E v183,v183
M a[112:115],a[204:207],v[148:151],a[112:115]
T a[222:223],v209 offset:61440
C v101,v182,v183
M a[112:115],a[200:203],v[144:147],a[112:115]
B v217,s[40:43],s60 offen offset:3072 nt lds
s_add_u32 s60,s60,s77
M v[180:183],a[176:179],v[0:3],v[160:163]
W vmcnt(48)
s_barrier
E v184,v184
M v[180:183],a[180:183],v[4:7],v[180:183]
s_mov_b32 m0,0x18000
E v185,v185
M v[180:183],a[184:187],v[8:11],v[180:183]
T a[192:193],v202
B v218,s[44:47],s61 offen lds
M v[180:183],a[188:191],v[12:15],v[180:183]
E v186,v186
M a[120:123],a[208:211],v[120:123],a[120:123]
T a[194:195],v202 offset:4096
C v108,v184,v185
M a[120:123],a[212:215],v[124:127],a[120:123]
E v187,v187
M a[124:127],a[212:215],v[132:135],a[124:127]
T a[196:197],v202 offset:8192
C v109,v186,v187
M a[124:127],a[208:211],v[128:131],a[124:127]
B v219,s[44:47],s61 offen offset:1024 lds
M v[184:187],a[176:179],v[16:19],v[164:167]
E v180,v180
M v[184:187],a[180:183],v[20:23],v[184:187]
E v181,v181
M v[184:187],a[184:187],v[24:27],v[184:187]
T a[198:199],v202 offset:12288
B v220,s[44:47],s61 offen offset:2048 lds
M v[184:187],a[188:191],v[28:31],v[184:187]
W lgkmcnt(4)
E v182,v182
M a[128:131],a[208:211],v[136:139],a[128:131]
R a[160:163],v198 offset:16384
C v86,v180,v181
M a[128:131],a[212:215],v[140:143],a[128:131]
E v183,v183
M a[132:135],a[212:215],v[148:151],a[132:135]
R a[164:167],v199 offset:16384
C v87,v182,v183
M a[132:135],a[208:211],v[144:147],a[132:135]
B v221,s[44:47],s61 offen offset:3072 lds
s_add_u32 s61,s61,s62
M v[180:183],a[176:179],v[32:35],v[168:171]
E v184,v184
M v[180:183],a[180:183],v[36:39],v[180:183]
s_mov_b32 m0,0x19000
E v185,v185
M v[180:183],a[184:187],v[40:43],v[180:183]
R a[168:171],v200 offset:16384
)ASM"
R"ASM(
B v218,s[44:47],s61 offen lds
M v[180:183],a[188:191],v[44:47],v[180:183]
E v186,v186
M a[140:143],a[216:219],v[120:123],a[140:143]
R a[172:175],v201 offset:16384
C v94,v184,v185
M a[140:143],a[220:223],v[124:127],a[140:143]
E v187,v187
M a[144:147],a[220:223],v[132:135],a[144:147]
T a[200:201],v203
C v95,v186,v187
M a[144:147],a[216:219],v[128:131],a[144:147]
B v219,s[44:47],s61 offen offset:1024 lds
v_smfmac_f32_16x16x64_bf16 a[76:79],a[96:99],v[144:151],v253
M v[184:187],a[176:179],v[48:51],v[172:175]
E v180,v180
M v[184:187],a[180:183],v[52:55],v[184:187]
E v181,v181
M v[184:187],a[184:187],v[56:59],v[184:187]
T a[202:203],v203 offset:4096
B v220,s[44:47],s61 offen offset:2048 lds
M v[184:187],a[188:191],v[60:63],v[184:187]
E v182,v182
M a[148:151],a[216:219],v[136:139],a[148:151]
T a[204:205],v203 offset:8192
C v102,v180,v181
M a[148:151],a[220:223],v[140:143],a[148:151]
W lgkmcnt(3)
E v183,v183
M a[152:155],a[220:223],v[148:151],a[152:155]
T a[206:207],v203 offset:12288
C v103,v182,v183
M a[152:155],a[216:219],v[144:147],a[152:155]
s_add_u32 s69,s69,1
B v221,s[44:47],s61 offen offset:3072 lds
s_add_u32 s61,s61,s63
s_branch LLOOPb_%=
)ASM"
R"ASM(
LEXITb_%=:
s_cmp_eq_u32 s70,1
s_cbranch_scc1 LDRAIN1b_%=
)ASM"
R"ASM(
LDRAIN0b_%=:
v_smfmac_f32_16x16x64_bf16 a[16:19],a[96:99],v[120:127],v253
E v184,v184
v_smfmac_f32_16x16x64_bf16 a[36:39],a[96:99],v[128:135],v253
E v185,v185
v_smfmac_f32_16x16x64_bf16 a[56:59],a[96:99],v[136:143],v253
E v186,v186
E v187,v187
M a[0:3],a[192:195],v[120:123],a[0:3]
C v150,v184,v185
C v151,v186,v187
M a[0:3],a[196:199],v[124:127],a[0:3]
T a[208:209],v204 offset:49152
M a[4:7],a[196:199],v[132:135],a[4:7]
AR v188,a16
AR v189,a17
v_smfmac_f32_16x16x64_bf16 a[76:79],a[96:99],v[144:151],v253
M a[4:7],a[192:195],v[128:131],a[4:7]
T a[210:211],v204 offset:53248
AR v190,a36
AR v191,a37
M a[8:11],a[192:195],v[136:139],a[8:11]
AR v192,a56
AR v193,a57
M a[8:11],a[196:199],v[140:143],a[8:11]
s_mov_b32 m0,0x16000
T a[212:213],v204 offset:57344
M a[12:15],a[196:199],v[148:151],a[12:15]
B v218,s[44:47],s61 offen lds
M a[12:15],a[192:195],v[144:147],a[12:15]
W lgkmcnt(3)
T a[214:215],v204 offset:61440
AR v194,a76
AR v195,a77
M a[20:23],a[200:203],v[120:123],a[20:23]
B v219,s[44:47],s61 offen offset:1024 lds
F v188,v188,v189
M a[20:23],a[204:207],v[124:127],a[20:23]
T a[216:217],v205 offset:49152
F v190,v190,v191
M a[24:27],a[204:207],v[132:135],a[24:27]
B v220,s[44:47],s61 offen offset:2048 lds
F v192,v192,v193
M a[24:27],a[200:203],v[128:131],a[24:27]
T a[218:219],v205 offset:53248
F v194,v194,v195
M a[28:31],a[200:203],v[136:139],a[28:31]
B v221,s[44:47],s61 offen offset:3072 lds
s_add_u32 s61,s61,s62
v_mov_b32 v189,v188
M a[28:31],a[204:207],v[140:143],a[28:31]
s_mov_b32 m0,0x17000
T a[220:221],v205 offset:57344
v_mov_b32 v191,v190
M a[32:35],a[204:207],v[148:151],a[32:35]
B v218,s[44:47],s61 offen lds
v_mov_b32 v193,v192
M a[32:35],a[200:203],v[144:147],a[32:35]
W lgkmcnt(3)
T a[222:223],v205 offset:61440
v_mov_b32 v195,v194
M a[40:43],a[208:211],v[120:123],a[40:43]
B v219,s[44:47],s61 offen offset:1024 lds
v_permlane32_swap_b32 v188,v189
M a[40:43],a[212:215],v[124:127],a[40:43]
T a[192:193],v206 offset:49152
v_permlane32_swap_b32 v190,v191
M a[44:47],a[212:215],v[132:135],a[44:47]
B v220,s[44:47],s61 offen offset:2048 lds
v_permlane32_swap_b32 v192,v193
M a[44:47],a[208:211],v[128:131],a[44:47]
T a[194:195],v206 offset:53248
v_permlane32_swap_b32 v194,v195
M a[48:51],a[208:211],v[136:139],a[48:51]
B v221,s[44:47],s61 offen offset:3072 lds
s_add_u32 s61,s61,s75
s_and_b32 s61,s61,s74
F v188,v188,v189
M a[48:51],a[212:215],v[140:143],a[48:51]
T a[196:197],v206 offset:57344
F v190,v190,v191
M a[52:55],a[212:215],v[148:151],a[52:55]
F v192,v192,v193
F v194,v194,v195
M a[52:55],a[208:211],v[144:147],a[52:55]
W lgkmcnt(3)
T a[198:199],v206 offset:61440
v_mov_b32 v189,v188
M a[60:63],a[216:219],v[120:123],a[60:63]
v_mov_b32 v191,v190
v_mov_b32 v193,v192
M a[60:63],a[220:223],v[124:127],a[60:63]
T a[200:201],v207 offset:49152
v_mov_b32 v195,v194
M a[64:67],a[220:223],v[132:135],a[64:67]
v_permlane16_swap_b32 v188,v189
v_permlane16_swap_b32 v190,v191
M a[64:67],a[216:219],v[128:131],a[64:67]
T a[202:203],v207 offset:53248
v_permlane16_swap_b32 v192,v193
M a[68:71],a[216:219],v[136:139],a[68:71]
v_permlane16_swap_b32 v194,v195
F v188,v188,v189
M a[68:71],a[220:223],v[140:143],a[68:71]
T a[204:205],v207 offset:57344
F v190,v190,v191
M a[72:75],a[220:223],v[148:151],a[72:75]
F v192,v192,v193
F v194,v194,v195
M a[72:75],a[216:219],v[144:147],a[72:75]
W lgkmcnt(3)
T a[206:207],v207 offset:61440
v_lshrrev_b32 v251,1,v245
M a[80:83],a[192:195],v[120:123],a[80:83]
v_xor_b32 v251,v251,v245
v_and_b32 v251,24,v251
M a[80:83],a[196:199],v[124:127],a[80:83]
T a[208:209],v208 offset:49152
v_and_b32 v252,32,v245
M a[84:87],a[196:199],v[132:135],a[84:87]
v_or_b32 v251,v251,v252
v_xor_b32 v184,v251,v202
M a[84:87],a[192:195],v[128:131],a[84:87]
T a[210:211],v208 offset:53248
v_xor_b32 v185,v251,v204
M a[88:91],a[192:195],v[136:139],a[88:91]
v_xor_b32 v186,v251,v206
v_xor_b32 v187,v251,v208
M a[88:91],a[196:199],v[140:143],a[88:91]
T a[212:213],v208 offset:57344
F v250,v188,v190
M a[92:95],a[196:199],v[148:151],a[92:95]
F v251,v192,v194
F v250,v250,v251
M a[92:95],a[192:195],v[144:147],a[92:95]
W lgkmcnt(3)
T a[214:215],v208 offset:61440
v_mov_b32 v252,s87
M a[100:103],a[200:203],v[120:123],a[100:103]
v_cmp_ngt_f32 vcc,0x5f800000,v250
v_lshlrev_b32 v252,2,v252
M a[100:103],a[204:207],v[124:127],a[100:103]
T a[216:217],v209 offset:49152
s_or_b32 s95,vcc_lo,vcc_hi
M a[104:107],a[204:207],v[132:135],a[104:107]
v_add_u32 v252,0x18000,v252
v_mov_b32 v251,s95
M a[104:107],a[200:203],v[128:131],a[104:107]
T a[218:219],v209 offset:53248
ds_write_b32 v252,v251
v_mov_b32 v253,0x18000
M a[108:111],a[200:203],v[136:139],a[108:111]
v_rcp_f32 v188,v188
M a[108:111],a[204:207],v[140:143],a[108:111]
T a[220:221],v209 offset:57344
v_rcp_f32 v190,v190
M a[112:115],a[204:207],v[148:151],a[112:115]
v_rcp_f32 v192,v192
M a[112:115],a[200:203],v[144:147],a[112:115]
W lgkmcnt(4)
T a[222:223],v209 offset:61440
v_rcp_f32 v194,v194
M a[120:123],a[208:211],v[120:123],a[120:123]
W vmcnt(32)
W lgkmcnt(0)
s_barrier
R v[180:183],v253
M a[120:123],a[212:215],v[124:127],a[120:123]
T a[192:193],v184
M a[124:127],a[212:215],v[132:135],a[124:127]
M a[124:127],a[208:211],v[128:131],a[124:127]
T a[194:195],v184 offset:4096
M a[128:131],a[208:211],v[136:139],a[128:131]
M a[128:131],a[212:215],v[140:143],a[128:131]
T a[196:197],v184 offset:8192
M a[132:135],a[212:215],v[148:151],a[132:135]
W lgkmcnt(3)
s_barrier
v_or3_b32 v250,v180,v181,v182
M a[132:135],a[208:211],v[144:147],a[132:135]
W lgkmcnt(15)
T a[198:199],v184 offset:12288
v_or_b32 v250,v250,v183
M a[140:143],a[216:219],v[120:123],a[140:143]
v_readfirstlane_b32 s95,v250
M a[140:143],a[220:223],v[124:127],a[140:143]
T a[200:201],v184 offset:8
M a[144:147],a[220:223],v[132:135],a[144:147]
s_cmp_eq_u32 s71,0
s_cselect_b32 s95,s95,0
M a[144:147],a[216:219],v[128:131],a[144:147]
T a[202:203],v184 offset:4104
M a[148:151],a[216:219],v[136:139],a[148:151]
M a[148:151],a[220:223],v[140:143],a[148:151]
T a[204:205],v184 offset:8200
M a[152:155],a[220:223],v[148:151],a[152:155]
M a[152:155],a[216:219],v[144:147],a[152:155]
s_cmp_lg_u32 s95,0
T a[206:207],v184 offset:12296
s_cbranch_scc1 LFBb_%=
)ASM"
R"ASM(
s_mov_b32 s71,0
s_mov_b32 s70,1
s_add_u32 s82,s82,4194304
)ASM"
R"ASM(
LQ1b_%=:
W lgkmcnt(4)
s_mov_b32 m0,0x18000
M v[0:3],a[192:195],a[224:227],0
B v218,s[44:47],s61 offen lds
M v[0:3],a[196:199],a[228:231],v[0:3]
W vmcnt(63)
T a[208:209],v185
M v[16:19],a[196:199],a[236:239],0
R a[16:19],v210
M v[16:19],a[192:195],a[232:235],v[16:19]
T a[210:211],v185 offset:4096
B v219,s[44:47],s61 offen offset:1024 lds
M v[32:35],a[192:195],a[240:243],0
R a[36:39],v211
AR v160,a0
AR v161,a1
AR v162,a2
M v[32:35],a[196:199],a[244:247],v[32:35]
T a[212:213],v185 offset:8192
AR v163,a3
M v[48:51],a[196:199],a[252:255],0
B v220,s[44:47],s61 offen offset:2048 lds
M v[48:51],a[192:195],a[248:251],v[48:51]
W lgkmcnt(5)
T a[214:215],v185 offset:12288
AR v164,a20
AR v165,a21
M v[80:83],a[200:203],a[224:227],0
AR v166,a22
AR v167,a23
M v[80:83],a[204:207],a[228:231],v[80:83]
W lgkmcnt(2)
T a[216:217],v185 offset:8
M v[96:99],a[204:207],a[236:239],0
B v221,s[44:47],s61 offen offset:3072 lds
s_add_u32 s61,s61,s62
U v160,v160,v188
U v161,v161,v188
M v[96:99],a[200:203],a[232:235],v[96:99]
s_add_u32 m0,s67,0
s_add_u32 s27,s26,256
s_and_b32 s27,s27,0x380
s_add_u32 s27,s27,s66
s_add_u32 s68,s27,0
T a[218:219],v185 offset:4104
M v[112:115],a[200:203],a[240:243],0
B v212,s[48:51],s68 offen lds
s_mov_b32 m0,0x19000
M v[112:115],a[204:207],a[244:247],v[112:115]
T a[220:221],v185 offset:8200
B v218,s[44:47],s61 offen lds
U v162,v162,v188
U v163,v163,v188
s_add_u32 m0,s67,0
M v[128:131],a[204:207],a[252:255],0
B v213,s[48:51],s68 offen offset:1024 lds
M v[128:131],a[200:203],a[248:251],v[128:131]
W lgkmcnt(3)
s_mov_b32 m0,0x19000
T a[222:223],v185 offset:12296
M v[4:7],a[208:211],a[224:227],0
B v219,s[44:47],s61 offen offset:1024 lds
U v164,v164,v188
U v165,v165,v188
M v[4:7],a[212:215],a[228:231],v[4:7]
W vmcnt(63)
T a[192:193],v186
M v[20:23],a[212:215],a[236:239],0
R a[56:59],v210 offset:2048
B v220,s[44:47],s61 offen offset:2048 lds
M v[20:23],a[208:211],a[232:235],v[20:23]
T a[194:195],v186 offset:4096
U v166,v166,v188
U v167,v167,v188
C v168,v160,v161
M v[36:39],a[208:211],a[240:243],0
R a[76:79],v211 offset:2048
C v169,v162,v163
M v[36:39],a[212:215],a[244:247],v[36:39]
T a[196:197],v186 offset:8192
C v170,v164,v165
M v[52:55],a[212:215],a[252:255],0
B v221,s[44:47],s61 offen offset:3072 lds
s_add_u32 s61,s61,s63
C v171,v166,v167
M v[52:55],a[208:211],a[248:251],v[52:55]
W lgkmcnt(5)
s_mov_b32 m0,0x1a000
T a[198:199],v186 offset:12288
M v[84:87],a[216:219],a[224:227],0
B v218,s[44:47],s61 offen lds
s_add_u32 s94,s84,0
M v[84:87],a[220:223],a[228:231],v[84:87]
W lgkmcnt(2)
T a[200:201],v186 offset:8
buffer_store_dwordx4 v[168:171],v245,s[52:55],s94 offen sc1
AR v160,a4
M v[100:103],a[220:223],a[236:239],0
AR v161,a5
AR v162,a6
M v[100:103],a[216:219],a[232:235],v[100:103]
T a[202:203],v186 offset:4104
B v219,s[44:47],s61 offen offset:1024 lds
s_add_u32 m0,s67,2048
s_add_u32 s68,s27,16384
M v[116:119],a[216:219],a[240:243],0
B v212,s[48:51],s68 offen lds
AR v163,a7
M v[116:119],a[220:223],a[244:247],v[116:119]
T a[204:205],v186 offset:8200
AR v164,a24
M v[132:135],a[220:223],a[252:255],0
B v213,s[48:51],s68 offen offset:1024 lds
s_mov_b32 m0,0x1a000
M v[132:135],a[216:219],a[248:251],v[132:135]
W lgkmcnt(3)
T a[206:207],v186 offset:12296
B v220,s[44:47],s61 offen offset:2048 lds
AR v165,a25
M v[8:11],a[192:195],a[224:227],0
AR v166,a26
AR v167,a27
M v[8:11],a[196:199],a[228:231],v[8:11]
W vmcnt(63)
T a[208:209],v187
B v221,s[44:47],s61 offen offset:3072 lds
s_add_u32 s61,s61,s62
M v[24:27],a[196:199],a[236:239],0
R a[96:99],v210 offset:4096
U v160,v160,v190
U v161,v161,v190
M v[24:27],a[192:195],a[232:235],v[24:27]
s_mov_b32 m0,0x1b000
T a[210:211],v187 offset:4096
M v[40:43],a[192:195],a[240:243],0
R a[116:119],v211 offset:4096
B v218,s[44:47],s61 offen lds
M v[40:43],a[196:199],a[244:247],v[40:43]
T a[212:213],v187 offset:8192
U v162,v162,v190
U v163,v163,v190
M v[56:59],a[196:199],a[252:255],0
U v164,v164,v190
U v165,v165,v190
M v[56:59],a[192:195],a[248:251],v[56:59]
W lgkmcnt(5)
T a[214:215],v187 offset:12288
M v[88:91],a[200:203],a[224:227],0
B v219,s[44:47],s61 offen offset:1024 lds
U v166,v166,v190
M v[88:91],a[204:207],a[228:231],v[88:91]
W lgkmcnt(2)
T a[216:217],v187 offset:8
M v[104:107],a[204:207],a[236:239],0
B v220,s[44:47],s61 offen offset:2048 lds
M v[104:107],a[200:203],a[232:235],v[104:107]
s_add_u32 m0,s67,4096
s_add_u32 s68,s27,32768
T a[218:219],v187 offset:4104
M v[120:123],a[200:203],a[240:243],0
B v212,s[48:51],s68 offen lds
s_mov_b32 m0,0x1b000
M v[120:123],a[204:207],a[244:247],v[120:123]
T a[220:221],v187 offset:8200
B v221,s[44:47],s61 offen offset:3072 lds
s_add_u32 s61,s61,s75
s_and_b32 s61,s61,s74
s_add_u32 m0,s67,4096
M v[136:139],a[204:207],a[252:255],0
B v213,s[48:51],s68 offen offset:1024 lds
M v[136:139],a[200:203],a[248:251],v[136:139]
W lgkmcnt(3)
T a[222:223],v187 offset:12296
M v[12:15],a[208:211],a[224:227],0
W vmcnt(23)
s_barrier
s_mov_b32 m0,0x1c000
M v[12:15],a[212:215],a[228:231],v[12:15]
W vmcnt(63)
T a[192:193],v184 offset:16384
B v218,s[44:47],s61 offen lds
M v[28:31],a[212:215],a[236:239],0
R a[136:139],v210 offset:6144
U v167,v167,v190
M v[28:31],a[208:211],a[232:235],v[28:31]
T a[194:195],v184 offset:20480
C v172,v160,v161
M v[44:47],a[208:211],a[240:243],0
R a[156:159],v211 offset:6144
B v219,s[44:47],s61 offen offset:1024 lds
M v[44:47],a[212:215],a[244:247],v[44:47]
T a[196:197],v184 offset:24576
C v173,v162,v163
M v[60:63],a[212:215],a[252:255],0
C v174,v164,v165
C v175,v166,v167
M v[60:63],a[208:211],a[248:251],v[60:63]
W lgkmcnt(5)
T a[198:199],v184 offset:28672
AR v160,a8
AR v161,a9
M v[92:95],a[216:219],a[224:227],0
B v220,s[44:47],s61 offen offset:2048 lds
M v[92:95],a[220:223],a[228:231],v[92:95]
W lgkmcnt(2)
T a[200:201],v184 offset:16392
M v[108:111],a[220:223],a[236:239],0
B v221,s[44:47],s61 offen offset:3072 lds
s_add_u32 s61,s61,s62
AR v162,a10
AR v163,a11
M v[108:111],a[216:219],a[232:235],v[108:111]
s_add_u32 m0,s67,6144
s_add_u32 s68,s27,49152
T a[202:203],v184 offset:20488
M v[124:127],a[216:219],a[240:243],0
B v212,s[48:51],s68 offen lds
s_mov_b32 m0,0x1d000
M v[124:127],a[220:223],a[244:247],v[124:127]
T a[204:205],v184 offset:24584
B v218,s[44:47],s61 offen lds
AR v164,a28
AR v165,a29
s_add_u32 m0,s67,6144
M v[140:143],a[220:223],a[252:255],0
B v213,s[48:51],s68 offen offset:1024 lds
M v[140:143],a[216:219],a[248:251],v[140:143]
W lgkmcnt(3)
s_mov_b32 m0,0x1d000
T a[206:207],v184 offset:28680
M v[0:3],a[192:195],a[16:19],v[0:3]
B v219,s[44:47],s61 offen offset:1024 lds
AR v166,a30
AR v167,a31
M v[0:3],a[196:199],a[36:39],v[0:3]
W vmcnt(24)
T a[208:209],v185 offset:16384
M v[16:19],a[196:199],a[76:79],v[16:19]
R a[224:227],v210
U v160,v160,v192
M v[16:19],a[192:195],a[56:59],v[16:19]
T a[210:211],v185 offset:20480
B v220,s[44:47],s61 offen offset:2048 lds
U v161,v161,v192
U v162,v162,v192
M v[32:35],a[192:195],a[96:99],v[32:35]
s_add_u32 s94,s84,1024
R a[228:231],v211
M v[32:35],a[196:199],a[116:119],v[32:35]
T a[212:213],v185 offset:24576
buffer_store_dwordx4 v[172:175],v245,s[52:55],s94 offen sc1
M v[48:51],a[196:199],a[156:159],v[48:51]
B v221,s[44:47],s61 offen offset:3072 lds
s_add_u32 s61,s61,s63
U v163,v163,v192
M v[48:51],a[192:195],a[136:139],v[48:51]
W lgkmcnt(5)
T a[214:215],v185 offset:28672
M v[80:83],a[200:203],a[16:19],v[80:83]
U v164,v164,v192
U v165,v165,v192
M v[80:83],a[204:207],a[36:39],v[80:83]
W lgkmcnt(2)
s_mov_b32 m0,0x1e000
T a[216:217],v185 offset:16392
U v166,v166,v192
M v[96:99],a[204:207],a[76:79],v[96:99]
B v218,s[44:47],s61 offen lds
M v[96:99],a[200:203],a[56:59],v[96:99]
s_add_u32 m0,s67,0
s_add_u32 s27,s26,384
s_and_b32 s27,s27,0x380
s_add_u32 s27,s27,s66
s_add_u32 s68,s27,0
T a[218:219],v185 offset:20488
M v[112:115],a[200:203],a[96:99],v[112:115]
B v212,s[48:51],s68 offen lds
U v167,v167,v192
s_mov_b32 m0,0x1e000
M v[112:115],a[204:207],a[116:119],v[112:115]
T a[220:221],v185 offset:24584
B v219,s[44:47],s61 offen offset:1024 lds
s_add_u32 m0,s67,0
M v[128:131],a[204:207],a[156:159],v[128:131]
B v213,s[48:51],s68 offen offset:1024 lds
M v[128:131],a[200:203],a[136:139],v[128:131]
W lgkmcnt(3)
s_mov_b32 m0,0x1e000
T a[222:223],v185 offset:28680
C v176,v160,v161
M v[4:7],a[208:211],a[16:19],v[4:7]
B v220,s[44:47],s61 offen offset:2048 lds
M v[4:7],a[212:215],a[36:39],v[4:7]
W vmcnt(24)
T a[192:193],v186 offset:16384
M v[20:23],a[212:215],a[76:79],v[20:23]
R a[232:235],v210 offset:2048
B v221,s[44:47],s61 offen offset:3072 lds
s_add_u32 s61,s61,s62
)ASM"
R"ASM(
C v177,v162,v163
M v[20:23],a[208:211],a[56:59],v[20:23]
T a[194:195],v186 offset:20480
C v178,v164,v165
M v[36:39],a[208:211],a[96:99],v[36:39]
R a[236:239],v211 offset:2048
C v179,v166,v167
M v[36:39],a[212:215],a[116:119],v[36:39]
s_mov_b32 m0,0x1f000
T a[196:197],v186 offset:24576
AR v160,a12
M v[52:55],a[212:215],a[156:159],v[52:55]
B v218,s[44:47],s61 offen lds
M v[52:55],a[208:211],a[136:139],v[52:55]
W lgkmcnt(5)
T a[198:199],v186 offset:28672
M v[84:87],a[216:219],a[16:19],v[84:87]
B v219,s[44:47],s61 offen offset:1024 lds
AR v161,a13
M v[84:87],a[220:223],a[36:39],v[84:87]
W lgkmcnt(2)
T a[200:201],v186 offset:16392
M v[100:103],a[220:223],a[76:79],v[100:103]
AR v162,a14
AR v163,a15
M v[100:103],a[216:219],a[56:59],v[100:103]
T a[202:203],v186 offset:20488
B v220,s[44:47],s61 offen offset:2048 lds
AR v164,a32
s_add_u32 m0,s67,2048
s_add_u32 s68,s27,16384
M v[116:119],a[216:219],a[96:99],v[116:119]
B v212,s[48:51],s68 offen lds
M v[116:119],a[220:223],a[116:119],v[116:119]
T a[204:205],v186 offset:24584
AR v165,a33
M v[132:135],a[220:223],a[156:159],v[132:135]
B v213,s[48:51],s68 offen offset:1024 lds
AR v166,a34
s_mov_b32 m0,0x1f000
M v[132:135],a[216:219],a[136:139],v[132:135]
W lgkmcnt(3)
T a[206:207],v186 offset:28680
B v221,s[44:47],s61 offen offset:3072 lds
s_add_u32 s61,s61,s75
s_and_b32 s61,s61,s74
M v[8:11],a[192:195],a[16:19],v[8:11]
AR v167,a35
U v160,v160,v194
M v[8:11],a[196:199],a[36:39],v[8:11]
W vmcnt(23)
T a[208:209],v187 offset:16384
M v[24:27],a[196:199],a[76:79],v[24:27]
R a[240:243],v210 offset:4096
U v161,v161,v194
M v[24:27],a[192:195],a[56:59],v[24:27]
T a[210:211],v187 offset:20480
U v162,v162,v194
M v[40:43],a[192:195],a[96:99],v[40:43]
R a[244:247],v211 offset:4096
U v163,v163,v194
M v[40:43],a[196:199],a[116:119],v[40:43]
T a[212:213],v187 offset:24576
U v164,v164,v194
M v[56:59],a[196:199],a[156:159],v[56:59]
U v165,v165,v194
U v166,v166,v194
M v[56:59],a[192:195],a[136:139],v[56:59]
W lgkmcnt(5)
T a[214:215],v187 offset:28672
M v[88:91],a[200:203],a[16:19],v[88:91]
U v167,v167,v194
C v180,v160,v161
M v[88:91],a[204:207],a[36:39],v[88:91]
W lgkmcnt(2)
T a[216:217],v187 offset:16392
M v[104:107],a[204:207],a[76:79],v[104:107]
C v181,v162,v163
C v182,v164,v165
M v[104:107],a[200:203],a[56:59],v[104:107]
s_add_u32 m0,s67,4096
s_add_u32 s68,s27,32768
T a[218:219],v187 offset:20488
M v[120:123],a[200:203],a[96:99],v[120:123]
B v212,s[48:51],s68 offen lds
M v[120:123],a[204:207],a[116:119],v[120:123]
T a[220:221],v187 offset:24584
C v183,v166,v167
M v[136:139],a[204:207],a[156:159],v[136:139]
B v213,s[48:51],s68 offen offset:1024 lds
M v[136:139],a[200:203],a[136:139],v[136:139]
W lgkmcnt(3)
T a[222:223],v187 offset:28680
M v[12:15],a[208:211],a[16:19],v[12:15]
W vmcnt(63)
s_barrier
s_mov_b32 m0,0x10000
M v[12:15],a[212:215],a[36:39],v[12:15]
W vmcnt(18)
T a[192:193],v184 offset:32768
B v218,s[44:47],s61 offen lds
s_add_u32 s94,s84,2048
M v[28:31],a[212:215],a[76:79],v[28:31]
R a[248:251],v210 offset:6144
buffer_store_dwordx4 v[176:179],v245,s[52:55],s94 offen sc1
M v[28:31],a[208:211],a[56:59],v[28:31]
T a[194:195],v184 offset:36864
AR v160,a40
AR v161,a41
AR v162,a42
M v[44:47],a[208:211],a[96:99],v[44:47]
R a[252:255],v211 offset:6144
B v219,s[44:47],s61 offen offset:1024 lds
M v[44:47],a[212:215],a[116:119],v[44:47]
T a[196:197],v184 offset:40960
AR v163,a43
M v[60:63],a[212:215],a[156:159],v[60:63]
AR v164,a60
AR v165,a61
AR v166,a62
M v[60:63],a[208:211],a[136:139],v[60:63]
W lgkmcnt(5)
T a[198:199],v184 offset:45056
M v[92:95],a[216:219],a[16:19],v[92:95]
B v220,s[44:47],s61 offen offset:2048 lds
M v[92:95],a[220:223],a[36:39],v[92:95]
W lgkmcnt(2)
T a[200:201],v184 offset:32776
AR v167,a63
M v[108:111],a[220:223],a[76:79],v[108:111]
B v221,s[44:47],s61 offen offset:3072 lds
s_add_u32 s61,s61,s62
M v[108:111],a[216:219],a[56:59],v[108:111]
s_add_u32 m0,s67,6144
s_add_u32 s68,s27,49152
T a[202:203],v184 offset:36872
M v[124:127],a[216:219],a[96:99],v[124:127]
B v212,s[48:51],s68 offen lds
U v160,v160,v188
s_mov_b32 m0,0x11000
M v[124:127],a[220:223],a[116:119],v[124:127]
T a[204:205],v184 offset:40968
B v218,s[44:47],s61 offen lds
s_add_u32 m0,s67,6144
M v[140:143],a[220:223],a[156:159],v[140:143]
B v213,s[48:51],s68 offen offset:1024 lds
M v[140:143],a[216:219],a[136:139],v[140:143]
W lgkmcnt(3)
s_mov_b32 m0,0x11000
T a[206:207],v184 offset:45064
U v161,v161,v188
M v[0:3],a[192:195],a[224:227],v[0:3]
B v219,s[44:47],s61 offen offset:1024 lds
M v[0:3],a[196:199],a[228:231],v[0:3]
W vmcnt(19)
T a[208:209],v185 offset:32768
M v[16:19],a[196:199],a[236:239],v[16:19]
R a[16:19],v210
U v162,v162,v188
U v163,v163,v188
M v[16:19],a[192:195],a[232:235],v[16:19]
T a[210:211],v185 offset:36864
B v220,s[44:47],s61 offen offset:2048 lds
M v[32:35],a[192:195],a[240:243],v[32:35]
R a[36:39],v211
U v164,v164,v188
M v[32:35],a[196:199],a[244:247],v[32:35]
T a[212:213],v185 offset:40960
U v165,v165,v188
U v166,v166,v188
M v[48:51],a[196:199],a[252:255],v[48:51]
B v221,s[44:47],s61 offen offset:3072 lds
s_add_u32 s61,s61,s63
M v[48:51],a[192:195],a[248:251],v[48:51]
W lgkmcnt(5)
T a[214:215],v185 offset:45056
M v[80:83],a[200:203],a[224:227],v[80:83]
U v167,v167,v188
C v72,v160,v161
C v73,v162,v163
M v[80:83],a[204:207],a[228:231],v[80:83]
W lgkmcnt(2)
s_mov_b32 m0,0x12000
T a[216:217],v185 offset:32776
M v[96:99],a[204:207],a[236:239],v[96:99]
B v218,s[44:47],s61 offen lds
M v[96:99],a[200:203],a[232:235],v[96:99]
s_add_u32 m0,s67,0
s_add_u32 s27,s26,512
s_and_b32 s27,s27,0x380
s_add_u32 s27,s27,s66
s_add_u32 s68,s27,0
T a[218:219],v185 offset:36872
C v74,v164,v165
M v[112:115],a[200:203],a[240:243],v[112:115]
B v212,s[48:51],s68 offen lds
s_mov_b32 m0,0x12000
M v[112:115],a[204:207],a[244:247],v[112:115]
T a[220:221],v185 offset:40968
B v219,s[44:47],s61 offen offset:1024 lds
s_add_u32 m0,s67,0
M v[128:131],a[204:207],a[252:255],v[128:131]
B v213,s[48:51],s68 offen offset:1024 lds
C v75,v166,v167
M v[128:131],a[200:203],a[248:251],v[128:131]
W lgkmcnt(3)
s_mov_b32 m0,0x12000
T a[222:223],v185 offset:45064
M v[4:7],a[208:211],a[224:227],v[4:7]
B v220,s[44:47],s61 offen offset:2048 lds
M v[4:7],a[212:215],a[228:231],v[4:7]
W vmcnt(19)
T a[192:193],v186 offset:32768
AR v160,a44
M v[20:23],a[212:215],a[236:239],v[20:23]
R a[56:59],v210 offset:2048
B v221,s[44:47],s61 offen offset:3072 lds
s_add_u32 s61,s61,s62
M v[20:23],a[208:211],a[232:235],v[20:23]
T a[194:195],v186 offset:36864
AR v161,a45
M v[36:39],a[208:211],a[240:243],v[36:39]
R a[76:79],v211 offset:2048
AR v162,a46
AR v163,a47
M v[36:39],a[212:215],a[244:247],v[36:39]
s_mov_b32 m0,0x13000
T a[196:197],v186 offset:40960
M v[52:55],a[212:215],a[252:255],v[52:55]
B v218,s[44:47],s61 offen lds
M v[52:55],a[208:211],a[248:251],v[52:55]
W lgkmcnt(5)
T a[198:199],v186 offset:45056
AR v164,a64
M v[84:87],a[216:219],a[224:227],v[84:87]
B v219,s[44:47],s61 offen offset:1024 lds
M v[84:87],a[220:223],a[228:231],v[84:87]
W lgkmcnt(2)
T a[200:201],v186 offset:32776
M v[100:103],a[220:223],a[236:239],v[100:103]
AR v165,a65
AR v166,a66
AR v167,a67
M v[100:103],a[216:219],a[232:235],v[100:103]
T a[202:203],v186 offset:36872
B v220,s[44:47],s61 offen offset:2048 lds
s_add_u32 m0,s67,2048
s_add_u32 s68,s27,16384
M v[116:119],a[216:219],a[240:243],v[116:119]
B v212,s[48:51],s68 offen lds
M v[116:119],a[220:223],a[244:247],v[116:119]
T a[204:205],v186 offset:40968
U v160,v160,v190
M v[132:135],a[220:223],a[252:255],v[132:135]
B v213,s[48:51],s68 offen offset:1024 lds
s_mov_b32 m0,0x13000
M v[132:135],a[216:219],a[248:251],v[132:135]
W lgkmcnt(3)
T a[206:207],v186 offset:45064
B v221,s[44:47],s61 offen offset:3072 lds
s_add_u32 s61,s61,s75
s_and_b32 s61,s61,s74
s_add_u32 s94,s84,3072
M v[8:11],a[192:195],a[224:227],v[8:11]
buffer_store_dwordx4 v[180:183],v245,s[52:55],s94 offen sc1
M v[8:11],a[196:199],a[228:231],v[8:11]
W vmcnt(24)
T a[208:209],v187 offset:32768
M v[24:27],a[196:199],a[236:239],v[24:27]
R a[96:99],v210 offset:4096
U v161,v161,v190
M v[24:27],a[192:195],a[232:235],v[24:27]
T a[210:211],v187 offset:36864
U v162,v162,v190
M v[40:43],a[192:195],a[240:243],v[40:43]
R a[116:119],v211 offset:4096
U v163,v163,v190
M v[40:43],a[196:199],a[244:247],v[40:43]
T a[212:213],v187 offset:40960
U v164,v164,v190
M v[56:59],a[196:199],a[252:255],v[56:59]
U v165,v165,v190
U v166,v166,v190
M v[56:59],a[192:195],a[248:251],v[56:59]
W lgkmcnt(5)
T a[214:215],v187 offset:45056
M v[88:91],a[200:203],a[224:227],v[88:91]
U v167,v167,v190
C v168,v160,v161
M v[88:91],a[204:207],a[228:231],v[88:91]
W lgkmcnt(2)
T a[216:217],v187 offset:32776
M v[104:107],a[204:207],a[236:239],v[104:107]
C v169,v162,v163
C v170,v164,v165
M v[104:107],a[200:203],a[232:235],v[104:107]
s_add_u32 m0,s67,4096
s_add_u32 s68,s27,32768
T a[218:219],v187 offset:36872
M v[120:123],a[200:203],a[240:243],v[120:123]
)ASM"
R"ASM(
B v212,s[48:51],s68 offen lds
M v[120:123],a[204:207],a[244:247],v[120:123]
T a[220:221],v187 offset:40968
C v171,v166,v167
M v[136:139],a[204:207],a[252:255],v[136:139]
B v213,s[48:51],s68 offen offset:1024 lds
M v[136:139],a[200:203],a[248:251],v[136:139]
W lgkmcnt(3)
T a[222:223],v187 offset:45064
AR v160,a48
M v[12:15],a[208:211],a[224:227],v[12:15]
W vmcnt(63)
s_barrier
s_mov_b32 m0,0x14000
M v[12:15],a[212:215],a[228:231],v[12:15]
W vmcnt(18)
T a[192:193],v184 offset:49152
B v218,s[44:47],s61 offen lds
M v[28:31],a[212:215],a[236:239],v[28:31]
R a[136:139],v210 offset:6144
AR v161,a49
AR v162,a50
M v[28:31],a[208:211],a[232:235],v[28:31]
T a[194:195],v184 offset:53248
AR v163,a51
M v[44:47],a[208:211],a[240:243],v[44:47]
R a[156:159],v211 offset:6144
B v219,s[44:47],s61 offen offset:1024 lds
M v[44:47],a[212:215],a[244:247],v[44:47]
T a[196:197],v184 offset:57344
AR v164,a68
AR v165,a69
M v[60:63],a[212:215],a[252:255],v[60:63]
AR v166,a70
AR v167,a71
M v[60:63],a[208:211],a[248:251],v[60:63]
W lgkmcnt(5)
T a[198:199],v184 offset:61440
M v[92:95],a[216:219],a[224:227],v[92:95]
B v220,s[44:47],s61 offen offset:2048 lds
U v160,v160,v192
M v[92:95],a[220:223],a[228:231],v[92:95]
W lgkmcnt(2)
T a[200:201],v184 offset:49160
M v[108:111],a[220:223],a[236:239],v[108:111]
B v221,s[44:47],s61 offen offset:3072 lds
s_add_u32 s61,s61,s62
M v[108:111],a[216:219],a[232:235],v[108:111]
s_add_u32 m0,s67,6144
s_add_u32 s68,s27,49152
T a[202:203],v184 offset:53256
U v161,v161,v192
M v[124:127],a[216:219],a[240:243],v[124:127]
B v212,s[48:51],s68 offen lds
s_mov_b32 m0,0x15000
M v[124:127],a[220:223],a[244:247],v[124:127]
T a[204:205],v184 offset:57352
B v218,s[44:47],s61 offen lds
s_add_u32 m0,s67,6144
M v[140:143],a[220:223],a[252:255],v[140:143]
B v213,s[48:51],s68 offen offset:1024 lds
U v162,v162,v192
M v[140:143],a[216:219],a[248:251],v[140:143]
W lgkmcnt(3)
s_mov_b32 m0,0x15000
T a[206:207],v184 offset:61448
M v[0:3],a[192:195],a[16:19],v[0:3]
B v219,s[44:47],s61 offen offset:1024 lds
M v[0:3],a[196:199],a[36:39],v[0:3]
W vmcnt(19)
T a[208:209],v185 offset:49152
U v163,v163,v192
M v[16:19],a[196:199],a[76:79],v[16:19]
R a[224:227],v210
U v164,v164,v192
M v[16:19],a[192:195],a[56:59],v[16:19]
T a[210:211],v185 offset:53248
B v220,s[44:47],s61 offen offset:2048 lds
M v[32:35],a[192:195],a[96:99],v[32:35]
R a[228:231],v211
U v165,v165,v192
U v166,v166,v192
M v[32:35],a[196:199],a[116:119],v[32:35]
T a[212:213],v185 offset:57344
U v167,v167,v192
M v[48:51],a[196:199],a[156:159],v[48:51]
B v221,s[44:47],s61 offen offset:3072 lds
s_add_u32 s61,s61,s63
M v[48:51],a[192:195],a[136:139],v[48:51]
W lgkmcnt(5)
T a[214:215],v185 offset:61440
C v172,v160,v161
M v[80:83],a[200:203],a[16:19],v[80:83]
C v173,v162,v163
C v174,v164,v165
M v[80:83],a[204:207],a[36:39],v[80:83]
W lgkmcnt(2)
s_mov_b32 m0,0x16000
T a[216:217],v185 offset:49160
M v[96:99],a[204:207],a[76:79],v[96:99]
B v218,s[44:47],s61 offen lds
C v175,v166,v167
M v[96:99],a[200:203],a[56:59],v[96:99]
s_add_u32 m0,s67,0
s_add_u32 s27,s26,640
s_and_b32 s27,s27,0x380
s_add_u32 s27,s27,s66
s_add_u32 s68,s27,0
T a[218:219],v185 offset:53256
M v[112:115],a[200:203],a[96:99],v[112:115]
B v212,s[48:51],s68 offen lds
s_mov_b32 m0,0x16000
M v[112:115],a[204:207],a[116:119],v[112:115]
T a[220:221],v185 offset:57352
B v219,s[44:47],s61 offen offset:1024 lds
AR v160,a52
s_add_u32 m0,s67,0
M v[128:131],a[204:207],a[156:159],v[128:131]
B v213,s[48:51],s68 offen offset:1024 lds
s_add_u32 s94,s84,5120
M v[128:131],a[200:203],a[136:139],v[128:131]
W lgkmcnt(3)
T a[222:223],v185 offset:61448
buffer_store_dwordx4 v[72:75],v245,s[52:55],s94 offen sc1
s_mov_b32 m0,0x16000
M v[4:7],a[208:211],a[16:19],v[4:7]
B v220,s[44:47],s61 offen offset:2048 lds
AR v161,a53
M v[4:7],a[212:215],a[36:39],v[4:7]
W vmcnt(20)
T a[192:193],v186 offset:49152
M v[20:23],a[212:215],a[76:79],v[20:23]
R a[232:235],v210 offset:2048
B v221,s[44:47],s61 offen offset:3072 lds
s_add_u32 s61,s61,s62
M v[20:23],a[208:211],a[56:59],v[20:23]
T a[194:195],v186 offset:53248
AR v162,a54
AR v163,a55
M v[36:39],a[208:211],a[96:99],v[36:39]
R a[236:239],v211 offset:2048
AR v164,a72
M v[36:39],a[212:215],a[116:119],v[36:39]
s_mov_b32 m0,0x17000
T a[196:197],v186 offset:57344
M v[52:55],a[212:215],a[156:159],v[52:55]
B v218,s[44:47],s61 offen lds
M v[52:55],a[208:211],a[136:139],v[52:55]
W lgkmcnt(5)
T a[198:199],v186 offset:61440
M v[84:87],a[216:219],a[16:19],v[84:87]
B v219,s[44:47],s61 offen offset:1024 lds
M v[84:87],a[220:223],a[36:39],v[84:87]
W lgkmcnt(2)
T a[200:201],v186 offset:49160
M v[100:103],a[220:223],a[76:79],v[100:103]
AR v165,a73
AR v166,a74
M v[100:103],a[216:219],a[56:59],v[100:103]
T a[202:203],v186 offset:53256
B v220,s[44:47],s61 offen offset:2048 lds
s_add_u32 m0,s67,2048
s_add_u32 s68,s27,16384
M v[116:119],a[216:219],a[96:99],v[116:119]
B v212,s[48:51],s68 offen lds
M v[116:119],a[220:223],a[116:119],v[116:119]
T a[204:205],v186 offset:57352
AR v167,a75
M v[132:135],a[220:223],a[156:159],v[132:135]
B v213,s[48:51],s68 offen offset:1024 lds
s_mov_b32 m0,0x17000
M v[132:135],a[216:219],a[136:139],v[132:135]
W lgkmcnt(3)
T a[206:207],v186 offset:61448
B v221,s[44:47],s61 offen offset:3072 lds
s_add_u32 s61,s61,s75
s_and_b32 s61,s61,s74
M v[8:11],a[192:195],a[16:19],v[8:11]
U v160,v160,v194
U v161,v161,v194
M v[8:11],a[196:199],a[36:39],v[8:11]
W vmcnt(23)
T a[208:209],v187 offset:49152
M v[24:27],a[196:199],a[76:79],v[24:27]
R a[240:243],v210 offset:4096
U v162,v162,v194
M v[24:27],a[192:195],a[56:59],v[24:27]
T a[210:211],v187 offset:53248
U v163,v163,v194
M v[40:43],a[192:195],a[96:99],v[40:43]
R a[244:247],v211 offset:4096
U v164,v164,v194
M v[40:43],a[196:199],a[116:119],v[40:43]
T a[212:213],v187 offset:57344
U v165,v165,v194
M v[56:59],a[196:199],a[156:159],v[56:59]
U v166,v166,v194
U v167,v167,v194
M v[56:59],a[192:195],a[136:139],v[56:59]
W lgkmcnt(5)
T a[214:215],v187 offset:61440
M v[88:91],a[200:203],a[16:19],v[88:91]
C v176,v160,v161
C v177,v162,v163
M v[88:91],a[204:207],a[36:39],v[88:91]
W lgkmcnt(2)
T a[216:217],v187 offset:49160
M v[104:107],a[204:207],a[76:79],v[104:107]
C v178,v164,v165
C v179,v166,v167
M v[104:107],a[200:203],a[56:59],v[104:107]
s_add_u32 m0,s67,4096
s_add_u32 s68,s27,32768
T a[218:219],v187 offset:53256
AR v160,a80
M v[120:123],a[200:203],a[96:99],v[120:123]
B v212,s[48:51],s68 offen lds
M v[120:123],a[204:207],a[116:119],v[120:123]
T a[220:221],v187 offset:57352
AR v161,a81
M v[136:139],a[204:207],a[156:159],v[136:139]
B v213,s[48:51],s68 offen offset:1024 lds
AR v162,a82
M v[136:139],a[200:203],a[136:139],v[136:139]
W lgkmcnt(3)
T a[222:223],v187 offset:61448
M v[12:15],a[208:211],a[16:19],v[12:15]
W vmcnt(63)
s_barrier
s_mov_b32 m0,0x18000
M v[12:15],a[212:215],a[36:39],v[12:15]
W vmcnt(18)
T a[192:193],v184
B v218,s[44:47],s61 offen lds
AR v163,a83
M v[28:31],a[212:215],a[76:79],v[28:31]
R a[248:251],v210 offset:6144
AR v164,a100
M v[28:31],a[208:211],a[56:59],v[28:31]
T a[194:195],v184 offset:4096
AR v165,a101
M v[44:47],a[208:211],a[96:99],v[44:47]
R a[252:255],v211 offset:6144
B v219,s[44:47],s61 offen offset:1024 lds
AR v166,a102
M v[44:47],a[212:215],a[116:119],v[44:47]
T a[196:197],v184 offset:8192
AR v167,a103
M v[60:63],a[212:215],a[156:159],v[60:63]
U v160,v160,v188
U v161,v161,v188
M v[60:63],a[208:211],a[136:139],v[60:63]
W lgkmcnt(5)
T a[198:199],v184 offset:12288
U v162,v162,v188
M v[92:95],a[216:219],a[16:19],v[92:95]
B v220,s[44:47],s61 offen offset:2048 lds
s_add_u32 s94,s84,6144
M v[92:95],a[220:223],a[36:39],v[92:95]
W lgkmcnt(2)
T a[200:201],v184 offset:8
buffer_store_dwordx4 v[168:171],v245,s[52:55],s94 offen sc1
M v[108:111],a[220:223],a[76:79],v[108:111]
B v221,s[44:47],s61 offen offset:3072 lds
s_add_u32 s61,s61,s62
U v163,v163,v188
M v[108:111],a[216:219],a[56:59],v[108:111]
s_add_u32 m0,s67,6144
s_add_u32 s68,s27,49152
T a[202:203],v184 offset:4104
M v[124:127],a[216:219],a[96:99],v[124:127]
B v212,s[48:51],s68 offen lds
s_mov_b32 m0,0x19000
M v[124:127],a[220:223],a[116:119],v[124:127]
T a[204:205],v184 offset:8200
B v218,s[44:47],s61 offen lds
U v164,v164,v188
s_add_u32 m0,s67,6144
M v[140:143],a[220:223],a[156:159],v[140:143]
B v213,s[48:51],s68 offen offset:1024 lds
M v[140:143],a[216:219],a[136:139],v[140:143]
W lgkmcnt(3)
s_mov_b32 m0,0x19000
T a[206:207],v184 offset:12296
M v[0:3],a[192:195],a[224:227],v[0:3]
B v219,s[44:47],s61 offen offset:1024 lds
U v165,v165,v188
M v[0:3],a[196:199],a[228:231],v[0:3]
W vmcnt(20)
T a[208:209],v185
M v[16:19],a[196:199],a[236:239],v[16:19]
R a[16:19],v210
U v166,v166,v188
M v[16:19],a[192:195],a[232:235],v[16:19]
T a[210:211],v185 offset:4096
B v220,s[44:47],s61 offen offset:2048 lds
U v167,v167,v188
M v[32:35],a[192:195],a[240:243],v[32:35]
R a[36:39],v211
C v180,v160,v161
)ASM"
R"ASM(
M v[32:35],a[196:199],a[244:247],v[32:35]
T a[212:213],v185 offset:8192
C v181,v162,v163
M v[48:51],a[196:199],a[252:255],v[48:51]
B v221,s[44:47],s61 offen offset:3072 lds
s_add_u32 s61,s61,s63
M v[48:51],a[192:195],a[248:251],v[48:51]
W lgkmcnt(5)
T a[214:215],v185 offset:12288
M v[80:83],a[200:203],a[224:227],v[80:83]
C v182,v164,v165
C v183,v166,v167
M v[80:83],a[204:207],a[228:231],v[80:83]
W lgkmcnt(2)
s_mov_b32 m0,0x1a000
T a[216:217],v185 offset:8
AR v160,a84
M v[96:99],a[204:207],a[236:239],v[96:99]
B v218,s[44:47],s61 offen lds
M v[96:99],a[200:203],a[232:235],v[96:99]
s_add_u32 m0,s67,0
s_add_u32 s27,s26,768
s_and_b32 s27,s27,0x380
s_add_u32 s27,s27,s66
s_add_u32 s68,s27,0
T a[218:219],v185 offset:4104
M v[112:115],a[200:203],a[240:243],v[112:115]
B v212,s[48:51],s68 offen lds
AR v161,a85
s_mov_b32 m0,0x1a000
M v[112:115],a[204:207],a[244:247],v[112:115]
T a[220:221],v185 offset:8200
B v219,s[44:47],s61 offen offset:1024 lds
s_add_u32 m0,s67,0
M v[128:131],a[204:207],a[252:255],v[128:131]
B v213,s[48:51],s68 offen offset:1024 lds
M v[128:131],a[200:203],a[248:251],v[128:131]
W lgkmcnt(3)
s_mov_b32 m0,0x1a000
T a[222:223],v185 offset:12296
AR v162,a86
M v[4:7],a[208:211],a[224:227],v[4:7]
B v220,s[44:47],s61 offen offset:2048 lds
M v[4:7],a[212:215],a[228:231],v[4:7]
W vmcnt(19)
T a[192:193],v186
M v[20:23],a[212:215],a[236:239],v[20:23]
R a[56:59],v210 offset:2048
B v221,s[44:47],s61 offen offset:3072 lds
s_add_u32 s61,s61,s62
AR v163,a87
M v[20:23],a[208:211],a[232:235],v[20:23]
T a[194:195],v186 offset:4096
AR v164,a104
M v[36:39],a[208:211],a[240:243],v[36:39]
R a[76:79],v211 offset:2048
AR v165,a105
M v[36:39],a[212:215],a[244:247],v[36:39]
s_mov_b32 m0,0x1b000
T a[196:197],v186 offset:8192
AR v166,a106
M v[52:55],a[212:215],a[252:255],v[52:55]
B v218,s[44:47],s61 offen lds
M v[52:55],a[208:211],a[248:251],v[52:55]
W lgkmcnt(5)
T a[198:199],v186 offset:12288
M v[84:87],a[216:219],a[224:227],v[84:87]
B v219,s[44:47],s61 offen offset:1024 lds
AR v167,a107
M v[84:87],a[220:223],a[228:231],v[84:87]
W lgkmcnt(2)
T a[200:201],v186 offset:8
M v[100:103],a[220:223],a[236:239],v[100:103]
U v160,v160,v190
U v161,v161,v190
M v[100:103],a[216:219],a[232:235],v[100:103]
T a[202:203],v186 offset:4104
B v220,s[44:47],s61 offen offset:2048 lds
U v162,v162,v190
s_add_u32 m0,s67,2048
s_add_u32 s68,s27,16384
M v[116:119],a[216:219],a[240:243],v[116:119]
B v212,s[48:51],s68 offen lds
M v[116:119],a[220:223],a[244:247],v[116:119]
T a[204:205],v186 offset:8200
U v163,v163,v190
M v[132:135],a[220:223],a[252:255],v[132:135]
B v213,s[48:51],s68 offen offset:1024 lds
U v164,v164,v190
s_mov_b32 m0,0x1b000
M v[132:135],a[216:219],a[248:251],v[132:135]
W lgkmcnt(3)
T a[206:207],v186 offset:12296
B v221,s[44:47],s61 offen offset:3072 lds
s_add_u32 s61,s61,s75
s_and_b32 s61,s61,s74
M v[8:11],a[192:195],a[224:227],v[8:11]
U v165,v165,v190
U v166,v166,v190
M v[8:11],a[196:199],a[228:231],v[8:11]
W vmcnt(23)
T a[208:209],v187
U v167,v167,v190
M v[24:27],a[196:199],a[236:239],v[24:27]
R a[96:99],v210 offset:4096
C v72,v160,v161
M v[24:27],a[192:195],a[232:235],v[24:27]
T a[210:211],v187 offset:4096
C v73,v162,v163
M v[40:43],a[192:195],a[240:243],v[40:43]
R a[116:119],v211 offset:4096
C v74,v164,v165
C v75,v166,v167
M v[40:43],a[196:199],a[244:247],v[40:43]
s_add_u32 s94,s84,7168
T a[212:213],v187 offset:8192
M v[56:59],a[196:199],a[252:255],v[56:59]
buffer_store_dwordx4 v[172:175],v245,s[52:55],s94 offen sc1
M v[56:59],a[192:195],a[248:251],v[56:59]
W lgkmcnt(5)
T a[214:215],v187 offset:12288
AR v160,a88
M v[88:91],a[200:203],a[224:227],v[88:91]
AR v161,a89
AR v162,a90
M v[88:91],a[204:207],a[228:231],v[88:91]
W lgkmcnt(2)
T a[216:217],v187 offset:8
M v[104:107],a[204:207],a[236:239],v[104:107]
AR v163,a91
AR v164,a108
AR v165,a109
M v[104:107],a[200:203],a[232:235],v[104:107]
s_add_u32 m0,s67,4096
s_add_u32 s68,s27,32768
T a[218:219],v187 offset:4104
M v[120:123],a[200:203],a[240:243],v[120:123]
B v212,s[48:51],s68 offen lds
M v[120:123],a[204:207],a[244:247],v[120:123]
T a[220:221],v187 offset:8200
AR v166,a110
AR v167,a111
M v[136:139],a[204:207],a[252:255],v[136:139]
B v213,s[48:51],s68 offen offset:1024 lds
M v[136:139],a[200:203],a[248:251],v[136:139]
W lgkmcnt(3)
T a[222:223],v187 offset:12296
M v[12:15],a[208:211],a[224:227],v[12:15]
W vmcnt(63)
s_barrier
s_mov_b32 m0,0x1c000
U v160,v160,v192
M v[12:15],a[212:215],a[228:231],v[12:15]
W vmcnt(18)
T a[192:193],v184 offset:16384
B v218,s[44:47],s61 offen lds
M v[28:31],a[212:215],a[236:239],v[28:31]
R a[136:139],v210 offset:6144
U v161,v161,v192
M v[28:31],a[208:211],a[232:235],v[28:31]
T a[194:195],v184 offset:20480
U v162,v162,v192
U v163,v163,v192
M v[44:47],a[208:211],a[240:243],v[44:47]
R a[156:159],v211 offset:6144
B v219,s[44:47],s61 offen offset:1024 lds
M v[44:47],a[212:215],a[244:247],v[44:47]
T a[196:197],v184 offset:24576
U v164,v164,v192
M v[60:63],a[212:215],a[252:255],v[60:63]
U v165,v165,v192
U v166,v166,v192
U v167,v167,v192
M v[60:63],a[208:211],a[248:251],v[60:63]
W lgkmcnt(5)
T a[198:199],v184 offset:28672
M v[92:95],a[216:219],a[224:227],v[92:95]
B v220,s[44:47],s61 offen offset:2048 lds
M v[92:95],a[220:223],a[228:231],v[92:95]
W lgkmcnt(2)
T a[200:201],v184 offset:16392
C v168,v160,v161
M v[108:111],a[220:223],a[236:239],v[108:111]
B v221,s[44:47],s61 offen offset:3072 lds
s_add_u32 s61,s61,s62
M v[108:111],a[216:219],a[232:235],v[108:111]
s_add_u32 m0,s67,6144
s_add_u32 s68,s27,49152
T a[202:203],v184 offset:20488
M v[124:127],a[216:219],a[240:243],v[124:127]
B v212,s[48:51],s68 offen lds
C v169,v162,v163
s_mov_b32 m0,0x1d000
M v[124:127],a[220:223],a[244:247],v[124:127]
T a[204:205],v184 offset:24584
B v218,s[44:47],s61 offen lds
s_add_u32 m0,s67,6144
M v[140:143],a[220:223],a[252:255],v[140:143]
B v213,s[48:51],s68 offen offset:1024 lds
M v[140:143],a[216:219],a[248:251],v[140:143]
W lgkmcnt(3)
s_mov_b32 m0,0x1d000
T a[206:207],v184 offset:28680
M v[0:3],a[192:195],a[16:19],v[0:3]
B v219,s[44:47],s61 offen offset:1024 lds
M v[0:3],a[196:199],a[36:39],v[0:3]
W vmcnt(19)
T a[208:209],v185 offset:16384
M v[16:19],a[196:199],a[76:79],v[16:19]
R a[224:227],v210
C v170,v164,v165
M v[16:19],a[192:195],a[56:59],v[16:19]
T a[210:211],v185 offset:20480
B v220,s[44:47],s61 offen offset:2048 lds
M v[32:35],a[192:195],a[96:99],v[32:35]
R a[228:231],v211
C v171,v166,v167
M v[32:35],a[196:199],a[116:119],v[32:35]
T a[212:213],v185 offset:24576
AR v160,a92
AR v161,a93
M v[48:51],a[196:199],a[156:159],v[48:51]
B v221,s[44:47],s61 offen offset:3072 lds
s_add_u32 s61,s61,s63
M v[48:51],a[192:195],a[136:139],v[48:51]
W lgkmcnt(5)
T a[214:215],v185 offset:28672
M v[80:83],a[200:203],a[16:19],v[80:83]
AR v162,a94
AR v163,a95
AR v164,a112
M v[80:83],a[204:207],a[36:39],v[80:83]
W lgkmcnt(2)
s_mov_b32 m0,0x1e000
T a[216:217],v185 offset:16392
M v[96:99],a[204:207],a[76:79],v[96:99]
B v218,s[44:47],s61 offen lds
M v[96:99],a[200:203],a[56:59],v[96:99]
s_add_u32 m0,s67,0
s_add_u32 s27,s26,896
s_and_b32 s27,s27,0x380
s_add_u32 s27,s27,s66
s_add_u32 s68,s27,0
T a[218:219],v185 offset:20488
AR v165,a113
M v[112:115],a[200:203],a[96:99],v[112:115]
B v212,s[48:51],s68 offen lds
s_mov_b32 m0,0x1e000
M v[112:115],a[204:207],a[116:119],v[112:115]
T a[220:221],v185 offset:24584
B v219,s[44:47],s61 offen offset:1024 lds
s_add_u32 m0,s67,0
M v[128:131],a[204:207],a[156:159],v[128:131]
B v213,s[48:51],s68 offen offset:1024 lds
AR v166,a114
M v[128:131],a[200:203],a[136:139],v[128:131]
W lgkmcnt(3)
s_mov_b32 m0,0x1e000
T a[222:223],v185 offset:28680
M v[4:7],a[208:211],a[16:19],v[4:7]
B v220,s[44:47],s61 offen offset:2048 lds
M v[4:7],a[212:215],a[36:39],v[4:7]
W vmcnt(19)
T a[192:193],v186 offset:16384
AR v167,a115
M v[20:23],a[212:215],a[76:79],v[20:23]
R a[232:235],v210 offset:2048
B v221,s[44:47],s61 offen offset:3072 lds
s_add_u32 s61,s61,s62
s_add_u32 s94,s84,8192
M v[20:23],a[208:211],a[56:59],v[20:23]
T a[194:195],v186 offset:20480
buffer_store_dwordx4 v[176:179],v245,s[52:55],s94 offen sc1
M v[36:39],a[208:211],a[96:99],v[36:39]
R a[236:239],v211 offset:2048
U v160,v160,v194
U v161,v161,v194
M v[36:39],a[212:215],a[116:119],v[36:39]
s_mov_b32 m0,0x1f000
T a[196:197],v186 offset:24576
M v[52:55],a[212:215],a[156:159],v[52:55]
B v218,s[44:47],s61 offen lds
M v[52:55],a[208:211],a[136:139],v[52:55]
W lgkmcnt(5)
T a[198:199],v186 offset:28672
U v162,v162,v194
M v[84:87],a[216:219],a[16:19],v[84:87]
B v219,s[44:47],s61 offen offset:1024 lds
M v[84:87],a[220:223],a[36:39],v[84:87]
W lgkmcnt(2)
T a[200:201],v186 offset:16392
M v[100:103],a[220:223],a[76:79],v[100:103]
U v163,v163,v194
U v164,v164,v194
U v165,v165,v194
M v[100:103],a[216:219],a[56:59],v[100:103]
T a[202:203],v186 offset:20488
B v220,s[44:47],s61 offen offset:2048 lds
s_add_u32 m0,s67,2048
s_add_u32 s68,s27,16384
)ASM"
R"ASM(
M v[116:119],a[216:219],a[96:99],v[116:119]
B v212,s[48:51],s68 offen lds
M v[116:119],a[220:223],a[116:119],v[116:119]
T a[204:205],v186 offset:24584
U v166,v166,v194
U v167,v167,v194
M v[132:135],a[220:223],a[156:159],v[132:135]
B v213,s[48:51],s68 offen offset:1024 lds
s_mov_b32 m0,0x1f000
M v[132:135],a[216:219],a[136:139],v[132:135]
W lgkmcnt(3)
T a[206:207],v186 offset:28680
B v221,s[44:47],s61 offen offset:3072 lds
s_add_u32 s61,s61,s75
s_and_b32 s61,s61,s74
M v[8:11],a[192:195],a[16:19],v[8:11]
C v172,v160,v161
C v173,v162,v163
M v[8:11],a[196:199],a[36:39],v[8:11]
W vmcnt(23)
T a[208:209],v187 offset:16384
M v[24:27],a[196:199],a[76:79],v[24:27]
R a[240:243],v210 offset:4096
C v174,v164,v165
M v[24:27],a[192:195],a[56:59],v[24:27]
T a[210:211],v187 offset:20480
C v175,v166,v167
M v[40:43],a[192:195],a[96:99],v[40:43]
R a[244:247],v211 offset:4096
AR v160,a120
M v[40:43],a[196:199],a[116:119],v[40:43]
T a[212:213],v187 offset:24576
AR v161,a121
M v[56:59],a[196:199],a[156:159],v[56:59]
AR v162,a122
AR v163,a123
M v[56:59],a[192:195],a[136:139],v[56:59]
W lgkmcnt(5)
T a[214:215],v187 offset:28672
M v[88:91],a[200:203],a[16:19],v[88:91]
AR v164,a140
AR v165,a141
M v[88:91],a[204:207],a[36:39],v[88:91]
W lgkmcnt(2)
T a[216:217],v187 offset:16392
M v[104:107],a[204:207],a[76:79],v[104:107]
AR v166,a142
AR v167,a143
M v[104:107],a[200:203],a[56:59],v[104:107]
s_add_u32 m0,s67,4096
s_add_u32 s68,s27,32768
T a[218:219],v187 offset:20488
M v[120:123],a[200:203],a[96:99],v[120:123]
B v212,s[48:51],s68 offen lds
M v[120:123],a[204:207],a[116:119],v[120:123]
T a[220:221],v187 offset:24584
U v160,v160,v188
M v[136:139],a[204:207],a[156:159],v[136:139]
B v213,s[48:51],s68 offen offset:1024 lds
M v[136:139],a[200:203],a[136:139],v[136:139]
W lgkmcnt(3)
T a[222:223],v187 offset:28680
M v[12:15],a[208:211],a[16:19],v[12:15]
W vmcnt(63)
s_barrier
U v161,v161,v188
M v[12:15],a[212:215],a[36:39],v[12:15]
W vmcnt(18)
T a[192:193],v184 offset:32768
M v[28:31],a[212:215],a[76:79],v[28:31]
R a[248:251],v210 offset:6144
U v162,v162,v188
M v[28:31],a[208:211],a[56:59],v[28:31]
T a[194:195],v184 offset:36864
U v163,v163,v188
M v[44:47],a[208:211],a[96:99],v[44:47]
R a[252:255],v211 offset:6144
U v164,v164,v188
M v[44:47],a[212:215],a[116:119],v[44:47]
T a[196:197],v184 offset:40960
U v165,v165,v188
M v[60:63],a[212:215],a[156:159],v[60:63]
U v166,v166,v188
U v167,v167,v188
M v[60:63],a[208:211],a[136:139],v[60:63]
W lgkmcnt(5)
T a[198:199],v184 offset:45056
M v[92:95],a[216:219],a[16:19],v[92:95]
C v176,v160,v161
C v177,v162,v163
M v[92:95],a[220:223],a[36:39],v[92:95]
W lgkmcnt(2)
T a[200:201],v184 offset:32776
M v[108:111],a[220:223],a[76:79],v[108:111]
C v178,v164,v165
C v179,v166,v167
M v[108:111],a[216:219],a[56:59],v[108:111]
s_add_u32 m0,s67,6144
s_add_u32 s68,s27,49152
T a[202:203],v184 offset:36872
M v[124:127],a[216:219],a[96:99],v[124:127]
B v212,s[48:51],s68 offen lds
M v[124:127],a[220:223],a[116:119],v[124:127]
T a[204:205],v184 offset:40968
M v[140:143],a[220:223],a[156:159],v[140:143]
B v213,s[48:51],s68 offen offset:1024 lds
M v[140:143],a[216:219],a[136:139],v[140:143]
W lgkmcnt(3)
s_add_u32 s94,s84,10240
T a[206:207],v184 offset:45064
M v[0:3],a[192:195],a[224:227],v[0:3]
buffer_store_dwordx4 v[180:183],v245,s[52:55],s94 offen sc1
M v[0:3],a[196:199],a[228:231],v[0:3]
W vmcnt(14)
T a[208:209],v185 offset:32768
s_mov_b32 s44,s12
s_and_b32 s45,s13,0xffff
s_mov_b32 s46,0x4000000
s_mov_b32 s47,0x20000
s_mov_b32 s95,256
v_mad_u32_u24 v218,v227,s95,v246
v_mad_u32_u24 v219,v227,s95,v247
v_mad_u32_u24 v220,v227,s95,v246
v_mad_u32_u24 v221,v227,s95,v247
s_mov_b32 s61,s82
s_mov_b32 s62,4096
s_mov_b32 s63,4096
s_mov_b32 s75,4096
s_mov_b32 s74,-1
s_mov_b32 m0,0x10000
M v[16:19],a[196:199],a[236:239],v[16:19]
R a[16:19],v210
B v218,s[44:47],s61 offen lds
M v[16:19],a[192:195],a[232:235],v[16:19]
T a[210:211],v185 offset:36864
AR v160,a124
M v[32:35],a[192:195],a[240:243],v[32:35]
R a[36:39],v211
AR v161,a125
M v[32:35],a[196:199],a[244:247],v[32:35]
T a[212:213],v185 offset:40960
AR v162,a126
M v[48:51],a[196:199],a[252:255],v[48:51]
AR v163,a127
AR v164,a144
M v[48:51],a[192:195],a[248:251],v[48:51]
W lgkmcnt(5)
T a[214:215],v185 offset:45056
M v[80:83],a[200:203],a[224:227],v[80:83]
B v219,s[44:47],s61 offen offset:1024 lds
M v[80:83],a[204:207],a[228:231],v[80:83]
T a[216:217],v185 offset:32776
AR v165,a145
M v[96:99],a[204:207],a[236:239],v[96:99]
AR v166,a146
AR v167,a147
M v[96:99],a[200:203],a[232:235],v[96:99]
T a[218:219],v185 offset:36872
U v160,v160,v190
M v[112:115],a[200:203],a[240:243],v[112:115]
B v220,s[44:47],s61 offen offset:2048 lds
s_add_u32 s94,s84,11264
M v[112:115],a[204:207],a[244:247],v[112:115]
T a[220:221],v185 offset:40968
buffer_store_dwordx4 v[72:75],v245,s[52:55],s94 offen sc1
M v[128:131],a[204:207],a[252:255],v[128:131]
U v161,v161,v190
U v162,v162,v190
M v[128:131],a[200:203],a[248:251],v[128:131]
W lgkmcnt(3)
T a[222:223],v185 offset:45064
M v[4:7],a[208:211],a[224:227],v[4:7]
U v163,v163,v190
U v164,v164,v190
M v[4:7],a[212:215],a[228:231],v[4:7]
W vmcnt(10)
T a[192:193],v186 offset:32768
B v221,s[44:47],s61 offen offset:3072 lds
s_add_u32 s61,s61,s62
M v[20:23],a[212:215],a[236:239],v[20:23]
R a[56:59],v210 offset:2048
U v165,v165,v190
M v[20:23],a[208:211],a[232:235],v[20:23]
T a[194:195],v186 offset:36864
U v166,v166,v190
M v[36:39],a[208:211],a[240:243],v[36:39]
R a[76:79],v211 offset:2048
U v167,v167,v190
M v[36:39],a[212:215],a[244:247],v[36:39]
s_mov_b32 m0,0x11000
T a[196:197],v186 offset:40960
M v[52:55],a[212:215],a[252:255],v[52:55]
B v218,s[44:47],s61 offen lds
M v[52:55],a[208:211],a[248:251],v[52:55]
W lgkmcnt(5)
s_add_u32 s94,s84,12288
T a[198:199],v186 offset:45056
M v[84:87],a[216:219],a[224:227],v[84:87]
buffer_store_dwordx4 v[168:171],v245,s[52:55],s94 offen sc1
M v[84:87],a[220:223],a[228:231],v[84:87]
T a[200:201],v186 offset:32776
C v180,v160,v161
M v[100:103],a[220:223],a[236:239],v[100:103]
C v181,v162,v163
C v182,v164,v165
M v[100:103],a[216:219],a[232:235],v[100:103]
T a[202:203],v186 offset:36872
C v183,v166,v167
M v[116:119],a[216:219],a[240:243],v[116:119]
B v219,s[44:47],s61 offen offset:1024 lds
M v[116:119],a[220:223],a[244:247],v[116:119]
T a[204:205],v186 offset:40968
M v[132:135],a[220:223],a[252:255],v[132:135]
M v[132:135],a[216:219],a[248:251],v[132:135]
W lgkmcnt(3)
T a[206:207],v186 offset:45064
M v[8:11],a[192:195],a[224:227],v[8:11]
B v220,s[44:47],s61 offen offset:2048 lds
M v[8:11],a[196:199],a[228:231],v[8:11]
W vmcnt(12)
T a[208:209],v187 offset:32768
M v[24:27],a[196:199],a[236:239],v[24:27]
R a[96:99],v210 offset:4096
AR v160,a128
M v[24:27],a[192:195],a[232:235],v[24:27]
s_add_u32 s94,s84,13312
T a[210:211],v187 offset:36864
M v[40:43],a[192:195],a[240:243],v[40:43]
R a[116:119],v211 offset:4096
buffer_store_dwordx4 v[172:175],v245,s[52:55],s94 offen sc1
M v[40:43],a[196:199],a[244:247],v[40:43]
T a[212:213],v187 offset:40960
AR v161,a129
M v[56:59],a[196:199],a[252:255],v[56:59]
B v221,s[44:47],s61 offen offset:3072 lds
s_add_u32 s61,s61,s63
M v[56:59],a[192:195],a[248:251],v[56:59]
W lgkmcnt(5)
T a[214:215],v187 offset:45056
M v[88:91],a[200:203],a[224:227],v[88:91]
AR v162,a130
AR v163,a131
M v[88:91],a[204:207],a[228:231],v[88:91]
s_mov_b32 m0,0x12000
T a[216:217],v187 offset:32776
M v[104:107],a[204:207],a[236:239],v[104:107]
B v218,s[44:47],s61 offen lds
M v[104:107],a[200:203],a[232:235],v[104:107]
T a[218:219],v187 offset:36872
AR v164,a148
M v[120:123],a[200:203],a[240:243],v[120:123]
AR v165,a149
AR v166,a150
M v[120:123],a[204:207],a[244:247],v[120:123]
T a[220:221],v187 offset:40968
AR v167,a151
M v[136:139],a[204:207],a[252:255],v[136:139]
U v160,v160,v192
U v161,v161,v192
M v[136:139],a[200:203],a[248:251],v[136:139]
W lgkmcnt(3)
T a[222:223],v187 offset:45064
B v219,s[44:47],s61 offen offset:1024 lds
M v[12:15],a[208:211],a[224:227],v[12:15]
W vmcnt(63)
s_barrier
s_add_u32 s94,s84,15360
M v[12:15],a[212:215],a[228:231],v[12:15]
W vmcnt(14)
T a[192:193],v184 offset:49152
buffer_store_dwordx4 v[176:179],v245,s[52:55],s94 offen sc1
M v[28:31],a[212:215],a[236:239],v[28:31]
R a[136:139],v210 offset:6144
U v162,v162,v192
M v[28:31],a[208:211],a[232:235],v[28:31]
T a[194:195],v184 offset:53248
U v163,v163,v192
M v[44:47],a[208:211],a[240:243],v[44:47]
R a[156:159],v211 offset:6144
B v220,s[44:47],s61 offen offset:2048 lds
M v[44:47],a[212:215],a[244:247],v[44:47]
T a[196:197],v184 offset:57344
U v164,v164,v192
M v[60:63],a[212:215],a[252:255],v[60:63]
U v165,v165,v192
U v166,v166,v192
M v[60:63],a[208:211],a[248:251],v[60:63]
W lgkmcnt(5)
T a[198:199],v184 offset:61440
M v[92:95],a[216:219],a[224:227],v[92:95]
U v167,v167,v192
C v72,v160,v161
M v[92:95],a[220:223],a[228:231],v[92:95]
T a[200:201],v184 offset:49160
C v73,v162,v163
M v[108:111],a[220:223],a[236:239],v[108:111]
B v221,s[44:47],s61 offen offset:3072 lds
s_add_u32 s61,s61,s62
M v[108:111],a[216:219],a[232:235],v[108:111]
T a[202:203],v184 offset:53256
)ASM"
R"ASM(
C v74,v164,v165
M v[124:127],a[216:219],a[240:243],v[124:127]
s_add_u32 s94,s84,16384
C v75,v166,v167
M v[124:127],a[220:223],a[244:247],v[124:127]
T a[204:205],v184 offset:57352
buffer_store_dwordx4 v[180:183],v245,s[52:55],s94 offen sc1
M v[140:143],a[220:223],a[252:255],v[140:143]
M v[140:143],a[216:219],a[248:251],v[140:143]
W lgkmcnt(3)
s_mov_b32 m0,0x13000
T a[206:207],v184 offset:61448
M v[0:3],a[192:195],a[16:19],v[0:3]
B v218,s[44:47],s61 offen lds
M v[0:3],a[196:199],a[36:39],v[0:3]
T a[208:209],v185 offset:49152
AR v160,a132
AR v161,a133
M v[16:19],a[196:199],a[76:79],v[16:19]
AR v162,a134
AR v163,a135
M v[16:19],a[192:195],a[56:59],v[16:19]
T a[210:211],v185 offset:53248
AR v164,a152
M v[32:35],a[192:195],a[96:99],v[32:35]
B v219,s[44:47],s61 offen offset:1024 lds
AR v165,a153
M v[32:35],a[196:199],a[116:119],v[32:35]
T a[212:213],v185 offset:57344
AR v166,a154
M v[48:51],a[196:199],a[156:159],v[48:51]
AR v167,a155
U v160,v160,v194
M v[48:51],a[192:195],a[136:139],v[48:51]
W lgkmcnt(3)
T a[214:215],v185 offset:61440
M v[80:83],a[200:203],a[16:19],v[80:83]
s_add_u32 s94,s84,17408
U v161,v161,v194
U v162,v162,v194
M v[80:83],a[204:207],a[36:39],v[80:83]
T a[216:217],v185 offset:49160
buffer_store_dwordx4 v[72:75],v245,s[52:55],s94 offen sc1
M v[96:99],a[204:207],a[76:79],v[96:99]
B v220,s[44:47],s61 offen offset:2048 lds
M v[96:99],a[200:203],a[56:59],v[96:99]
T a[218:219],v185 offset:53256
U v163,v163,v194
M v[112:115],a[200:203],a[96:99],v[112:115]
U v164,v164,v194
U v165,v165,v194
M v[112:115],a[204:207],a[116:119],v[112:115]
T a[220:221],v185 offset:57352
U v166,v166,v194
M v[128:131],a[204:207],a[156:159],v[128:131]
B v221,s[44:47],s61 offen offset:3072 lds
s_add_u32 s61,s61,s75
s_and_b32 s61,s61,s74
M v[128:131],a[200:203],a[136:139],v[128:131]
W lgkmcnt(3)
T a[222:223],v185 offset:61448
M v[4:7],a[208:211],a[16:19],v[4:7]
U v167,v167,v194
C v168,v160,v161
M v[4:7],a[212:215],a[36:39],v[4:7]
T a[192:193],v186 offset:49152
C v169,v162,v163
M v[20:23],a[212:215],a[76:79],v[20:23]
C v170,v164,v165
C v171,v166,v167
M v[20:23],a[208:211],a[56:59],v[20:23]
s_mov_b32 m0,0x14000
T a[194:195],v186 offset:53248
M v[36:39],a[208:211],a[96:99],v[36:39]
B v218,s[44:47],s61 offen lds
s_add_u32 s94,s84,18432
M v[36:39],a[212:215],a[116:119],v[36:39]
T a[196:197],v186 offset:57344
buffer_store_dwordx4 v[168:171],v245,s[52:55],s94 offen sc1
M v[52:55],a[212:215],a[156:159],v[52:55]
v_mov_b32 v222,0xff800000
v_mov_b32 v223,0xff800000
M v[52:55],a[208:211],a[136:139],v[52:55]
W lgkmcnt(3)
T a[198:199],v186 offset:61440
M v[84:87],a[216:219],a[16:19],v[84:87]
B v219,s[44:47],s61 offen offset:1024 lds
M v[84:87],a[220:223],a[36:39],v[84:87]
T a[200:201],v186 offset:49160
v_mov_b32 v224,0xff800000
M v[100:103],a[220:223],a[76:79],v[100:103]
v_mov_b32 v225,0xff800000
U v0,s64,v0
M v[100:103],a[216:219],a[56:59],v[100:103]
T a[202:203],v186 offset:53256
U v1,s64,v1
M v[116:119],a[216:219],a[96:99],v[116:119]
U v2,s64,v2
U v3,s64,v3
M v[116:119],a[220:223],a[116:119],v[116:119]
T a[204:205],v186 offset:57352
U v80,s64,v80
M v[132:135],a[220:223],a[156:159],v[132:135]
B v220,s[44:47],s61 offen offset:2048 lds
M v[132:135],a[216:219],a[136:139],v[132:135]
W lgkmcnt(3)
T a[206:207],v186 offset:61448
M v[8:11],a[192:195],a[16:19],v[8:11]
U v81,s64,v81
U v82,s64,v82
M v[8:11],a[196:199],a[36:39],v[8:11]
T a[208:209],v187 offset:49152
U v83,s64,v83
M v[24:27],a[196:199],a[76:79],v[24:27]
B v221,s[44:47],s61 offen offset:3072 lds
s_add_u32 s61,s61,s62
M v[24:27],a[192:195],a[56:59],v[24:27]
T a[210:211],v187 offset:53248
C v0,v0,v1
M v[40:43],a[192:195],a[96:99],v[40:43]
C v1,v2,v3
C v2,v80,v81
M v[40:43],a[196:199],a[116:119],v[40:43]
T a[212:213],v187 offset:57344
C v3,v82,v83
M v[56:59],a[196:199],a[156:159],v[56:59]
U v16,s64,v16
U v17,s64,v17
M v[56:59],a[192:195],a[136:139],v[56:59]
W lgkmcnt(3)
s_mov_b32 m0,0x15000
T a[214:215],v187 offset:61440
M v[88:91],a[200:203],a[16:19],v[88:91]
B v218,s[44:47],s61 offen lds
M v[88:91],a[204:207],a[36:39],v[88:91]
T a[216:217],v187 offset:49160
U v18,s64,v18
M v[104:107],a[204:207],a[76:79],v[104:107]
U v19,s64,v19
U v96,s64,v96
M v[104:107],a[200:203],a[56:59],v[104:107]
T a[218:219],v187 offset:53256
U v97,s64,v97
M v[120:123],a[200:203],a[96:99],v[120:123]
B v219,s[44:47],s61 offen offset:1024 lds
M v[120:123],a[204:207],a[116:119],v[120:123]
T a[220:221],v187 offset:57352
U v98,s64,v98
M v[136:139],a[204:207],a[156:159],v[136:139]
U v99,s64,v99
C v16,v16,v17
M v[136:139],a[200:203],a[136:139],v[136:139]
W lgkmcnt(3)
T a[222:223],v187 offset:61448
M v[12:15],a[208:211],a[16:19],v[12:15]
C v17,v18,v19
C v18,v96,v97
M v[12:15],a[212:215],a[36:39],v[12:15]
B v220,s[44:47],s61 offen offset:2048 lds
M v[28:31],a[212:215],a[76:79],v[28:31]
C v19,v98,v99
U v32,s64,v32
M v[28:31],a[208:211],a[56:59],v[28:31]
U v33,s64,v33
U v34,s64,v34
M v[44:47],a[208:211],a[96:99],v[44:47]
U v35,s64,v35
U v112,s64,v112
M v[44:47],a[212:215],a[116:119],v[44:47]
U v113,s64,v113
U v114,s64,v114
M v[60:63],a[212:215],a[156:159],v[60:63]
B v221,s[44:47],s61 offen offset:3072 lds
s_add_u32 s61,s61,s63
M v[60:63],a[208:211],a[136:139],v[60:63]
W lgkmcnt(0)
U v115,s64,v115
C v32,v32,v33
M v[92:95],a[216:219],a[16:19],v[92:95]
C v33,v34,v35
C v34,v112,v113
M v[92:95],a[220:223],a[36:39],v[92:95]
C v35,v114,v115
M v[108:111],a[220:223],a[76:79],v[108:111]
M v[108:111],a[216:219],a[56:59],v[108:111]
M v[124:127],a[216:219],a[96:99],v[124:127]
M v[124:127],a[220:223],a[116:119],v[124:127]
M v[140:143],a[220:223],a[156:159],v[140:143]
M v[140:143],a[216:219],a[136:139],v[140:143]
s_add_u32 s84,s84,327680
W vmcnt(63)
s_barrier
R a[160:163],v198
R a[164:167],v199
R a[168:171],v200
R a[172:175],v201
s_nop 1
v_pk_mul_f32 v[48:49],v[48:49],s[64:65]
v_pk_mul_f32 v[50:51],v[50:51],s[64:65]
v_pk_mul_f32 v[128:129],v[128:129],s[64:65]
v_pk_mul_f32 v[130:131],v[130:131],s[64:65]
C v48,v48,v49
C v49,v50,v51
C v50,v128,v129
C v51,v130,v131
v_pk_mul_f32 v[4:5],v[4:5],s[64:65]
v_pk_mul_f32 v[6:7],v[6:7],s[64:65]
v_pk_mul_f32 v[84:85],v[84:85],s[64:65]
v_pk_mul_f32 v[86:87],v[86:87],s[64:65]
C v4,v4,v5
C v5,v6,v7
C v6,v84,v85
C v7,v86,v87
v_pk_mul_f32 v[20:21],v[20:21],s[64:65]
v_pk_mul_f32 v[22:23],v[22:23],s[64:65]
v_pk_mul_f32 v[100:101],v[100:101],s[64:65]
v_pk_mul_f32 v[102:103],v[102:103],s[64:65]
C v20,v20,v21
C v21,v22,v23
C v22,v100,v101
C v23,v102,v103
v_pk_mul_f32 v[36:37],v[36:37],s[64:65]
v_pk_mul_f32 v[38:39],v[38:39],s[64:65]
v_pk_mul_f32 v[116:117],v[116:117],s[64:65]
v_pk_mul_f32 v[118:119],v[118:119],s[64:65]
C v36,v36,v37
C v37,v38,v39
C v38,v116,v117
C v39,v118,v119
v_pk_mul_f32 v[52:53],v[52:53],s[64:65]
v_pk_mul_f32 v[54:55],v[54:55],s[64:65]
v_pk_mul_f32 v[132:133],v[132:133],s[64:65]
v_pk_mul_f32 v[134:135],v[134:135],s[64:65]
C v52,v52,v53
C v53,v54,v55
C v54,v132,v133
C v55,v134,v135
v_pk_mul_f32 v[8:9],v[8:9],s[64:65]
v_pk_mul_f32 v[10:11],v[10:11],s[64:65]
v_pk_mul_f32 v[88:89],v[88:89],s[64:65]
v_pk_mul_f32 v[90:91],v[90:91],s[64:65]
C v8,v8,v9
C v9,v10,v11
C v10,v88,v89
C v11,v90,v91
v_pk_mul_f32 v[24:25],v[24:25],s[64:65]
v_pk_mul_f32 v[26:27],v[26:27],s[64:65]
v_pk_mul_f32 v[104:105],v[104:105],s[64:65]
v_pk_mul_f32 v[106:107],v[106:107],s[64:65]
C v24,v24,v25
C v25,v26,v27
C v26,v104,v105
C v27,v106,v107
v_pk_mul_f32 v[40:41],v[40:41],s[64:65]
v_pk_mul_f32 v[42:43],v[42:43],s[64:65]
v_pk_mul_f32 v[120:121],v[120:121],s[64:65]
v_pk_mul_f32 v[122:123],v[122:123],s[64:65]
C v40,v40,v41
C v41,v42,v43
C v42,v120,v121
C v43,v122,v123
v_pk_mul_f32 v[56:57],v[56:57],s[64:65]
v_pk_mul_f32 v[58:59],v[58:59],s[64:65]
v_pk_mul_f32 v[136:137],v[136:137],s[64:65]
v_pk_mul_f32 v[138:139],v[138:139],s[64:65]
C v56,v56,v57
C v57,v58,v59
C v58,v136,v137
C v59,v138,v139
v_pk_mul_f32 v[12:13],v[12:13],s[64:65]
v_pk_mul_f32 v[14:15],v[14:15],s[64:65]
v_pk_mul_f32 v[92:93],v[92:93],s[64:65]
v_pk_mul_f32 v[94:95],v[94:95],s[64:65]
C v12,v12,v13
C v13,v14,v15
C v14,v92,v93
C v15,v94,v95
v_pk_mul_f32 v[28:29],v[28:29],s[64:65]
v_pk_mul_f32 v[30:31],v[30:31],s[64:65]
v_pk_mul_f32 v[108:109],v[108:109],s[64:65]
v_pk_mul_f32 v[110:111],v[110:111],s[64:65]
C v28,v28,v29
C v29,v30,v31
C v30,v108,v109
C v31,v110,v111
v_pk_mul_f32 v[44:45],v[44:45],s[64:65]
v_pk_mul_f32 v[46:47],v[46:47],s[64:65]
v_pk_mul_f32 v[124:125],v[124:125],s[64:65]
v_pk_mul_f32 v[126:127],v[126:127],s[64:65]
C v44,v44,v45
C v45,v46,v47
C v46,v124,v125
C v47,v126,v127
v_pk_mul_f32 v[60:61],v[60:61],s[64:65]
v_pk_mul_f32 v[62:63],v[62:63],s[64:65]
v_pk_mul_f32 v[140:141],v[140:141],s[64:65]
v_pk_mul_f32 v[142:143],v[142:143],s[64:65]
C v60,v60,v61
C v61,v62,v63
C v62,v140,v141
)ASM"
R"ASM(
C v63,v142,v143
s_branch LPREb_%=
)ASM"
R"ASM(
LDRAIN1b_%=:
v_smfmac_f32_16x16x64_bf16 a[16:19],a[96:99],v[120:127],v253
E v184,v184
v_smfmac_f32_16x16x64_bf16 a[36:39],a[96:99],v[128:135],v253
E v185,v185
v_smfmac_f32_16x16x64_bf16 a[56:59],a[96:99],v[136:143],v253
E v186,v186
E v187,v187
M a[0:3],a[192:195],v[120:123],a[0:3]
s_mul_i32 s79,s4,10240
C v150,v184,v185
C v151,v186,v187
M a[0:3],a[196:199],v[124:127],a[0:3]
s_mov_b32 s33,0x7ff00000
T a[208:209],v204 offset:49152
M a[4:7],a[196:199],v[132:135],a[4:7]
AR v188,a16
AR v189,a17
v_add_u32 v240,4096,v245
v_smfmac_f32_16x16x64_bf16 a[76:79],a[96:99],v[144:151],v253
M a[4:7],a[192:195],v[128:131],a[4:7]
T a[210:211],v204 offset:53248
AR v190,a36
AR v191,a37
M a[8:11],a[192:195],v[136:139],a[8:11]
AR v192,a56
AR v193,a57
v_add_u32 v241,8192,v245
M a[8:11],a[196:199],v[140:143],a[8:11]
s_mov_b32 m0,0x16000
T a[212:213],v204 offset:57344
M a[12:15],a[196:199],v[148:151],a[12:15]
B v218,s[44:47],s61 offen lds
M a[12:15],a[192:195],v[144:147],a[12:15]
W lgkmcnt(3)
T a[214:215],v204 offset:61440
AR v194,a76
AR v195,a77
M a[20:23],a[200:203],v[120:123],a[20:23]
B v219,s[44:47],s61 offen offset:1024 lds
F v188,v188,v189
M a[20:23],a[204:207],v[124:127],a[20:23]
T a[216:217],v205 offset:49152
F v190,v190,v191
M a[24:27],a[204:207],v[132:135],a[24:27]
B v220,s[44:47],s61 offen offset:2048 lds
F v192,v192,v193
M a[24:27],a[200:203],v[128:131],a[24:27]
T a[218:219],v205 offset:53248
F v194,v194,v195
M a[28:31],a[200:203],v[136:139],a[28:31]
B v221,s[44:47],s61 offen offset:3072 lds
s_add_u32 s61,s61,s62
v_mov_b32 v189,v188
M a[28:31],a[204:207],v[140:143],a[28:31]
s_mov_b32 m0,0x17000
T a[220:221],v205 offset:57344
v_mov_b32 v191,v190
M a[32:35],a[204:207],v[148:151],a[32:35]
B v218,s[44:47],s61 offen lds
v_mov_b32 v193,v192
M a[32:35],a[200:203],v[144:147],a[32:35]
W lgkmcnt(3)
T a[222:223],v205 offset:61440
v_mov_b32 v195,v194
M a[40:43],a[208:211],v[120:123],a[40:43]
B v219,s[44:47],s61 offen offset:1024 lds
v_permlane32_swap_b32 v188,v189
M a[40:43],a[212:215],v[124:127],a[40:43]
T a[192:193],v206 offset:49152
v_permlane32_swap_b32 v190,v191
M a[44:47],a[212:215],v[132:135],a[44:47]
B v220,s[44:47],s61 offen offset:2048 lds
v_permlane32_swap_b32 v192,v193
M a[44:47],a[208:211],v[128:131],a[44:47]
T a[194:195],v206 offset:53248
v_permlane32_swap_b32 v194,v195
M a[48:51],a[208:211],v[136:139],a[48:51]
B v221,s[44:47],s61 offen offset:3072 lds
s_add_u32 s61,s61,s75
s_and_b32 s61,s61,s74
F v188,v188,v189
M a[48:51],a[212:215],v[140:143],a[48:51]
T a[196:197],v206 offset:57344
B a[224:227],v245,s[96:99],s79 offen sc1
F v190,v190,v191
M a[52:55],a[212:215],v[148:151],a[52:55]
B a[228:231],v240,s[96:99],s79 offen offset:1024 sc1
F v192,v192,v193
M a[52:55],a[208:211],v[144:147],a[52:55]
W lgkmcnt(3)
T a[198:199],v206 offset:61440
B a[232:235],v245,s[96:99],s79 offen offset:1024 sc1
F v194,v194,v195
M a[60:63],a[216:219],v[120:123],a[60:63]
B a[236:239],v240,s[96:99],s79 offen offset:2048 sc1
v_mov_b32 v189,v188
M a[60:63],a[220:223],v[124:127],a[60:63]
T a[200:201],v207 offset:49152
v_mov_b32 v191,v190
M a[64:67],a[220:223],v[132:135],a[64:67]
B a[240:243],v245,s[96:99],s79 offen offset:2048 sc1
v_mov_b32 v193,v192
M a[64:67],a[216:219],v[128:131],a[64:67]
T a[202:203],v207 offset:53248
B a[244:247],v240,s[96:99],s79 offen offset:3072 sc1
v_mov_b32 v195,v194
M a[68:71],a[216:219],v[136:139],a[68:71]
B a[248:251],v245,s[96:99],s79 offen offset:3072 sc1
v_permlane16_swap_b32 v188,v189
M a[68:71],a[220:223],v[140:143],a[68:71]
T a[204:205],v207 offset:57344
v_permlane16_swap_b32 v190,v191
M a[72:75],a[220:223],v[148:151],a[72:75]
B a[252:255],v241,s[96:99],s79 offen sc1
v_permlane16_swap_b32 v192,v193
s_or_b32 s34,s79,s35
M a[72:75],a[216:219],v[144:147],a[72:75]
W lgkmcnt(3)
T a[206:207],v207 offset:61440
B v[228:231],v240,s[96:99],s34 offen sc1
v_permlane16_swap_b32 v194,v195
M a[80:83],a[192:195],v[120:123],a[80:83]
B v[232:235],v241,s[96:99],s34 offen offset:1024 sc1
s_add_u32 s79,s79,40960
F v188,v188,v189
M a[80:83],a[196:199],v[124:127],a[80:83]
T a[208:209],v208 offset:49152
F v190,v190,v191
M a[84:87],a[196:199],v[132:135],a[84:87]
B v[80:83],v245,s[96:99],s79 offen sc1
F v192,v192,v193
M a[84:87],a[192:195],v[128:131],a[84:87]
T a[210:211],v208 offset:53248
B v[84:87],v240,s[96:99],s79 offen offset:1024 sc1
F v194,v194,v195
M a[88:91],a[192:195],v[136:139],a[88:91]
B v[88:91],v245,s[96:99],s79 offen offset:1024 sc1
F v250,v188,v190
M a[88:91],a[196:199],v[140:143],a[88:91]
T a[212:213],v208 offset:57344
B v[92:95],v240,s[96:99],s79 offen offset:2048 sc1
F v251,v192,v194
M a[92:95],a[196:199],v[148:151],a[92:95]
F v250,v250,v251
v_mov_b32 v252,s87
M a[92:95],a[192:195],v[144:147],a[92:95]
W lgkmcnt(3)
T a[214:215],v208 offset:61440
B v[96:99],v245,s[96:99],s79 offen offset:2048 sc1
v_cmp_ngt_f32 vcc,0x5f800000,v250
M a[100:103],a[200:203],v[120:123],a[100:103]
B v[100:103],v240,s[96:99],s79 offen offset:3072 sc1
v_lshlrev_b32 v252,2,v252
M a[100:103],a[204:207],v[124:127],a[100:103]
T a[216:217],v209 offset:49152
B v[104:107],v245,s[96:99],s79 offen offset:3072 sc1
s_or_b32 s95,vcc_lo,vcc_hi
M a[104:107],a[204:207],v[132:135],a[104:107]
v_add_u32 v252,0x27000,v252
v_mov_b32 v251,s95
M a[104:107],a[200:203],v[128:131],a[104:107]
T a[218:219],v209 offset:53248
B v[108:111],v241,s[96:99],s79 offen sc1
ds_write_b32 v252,v251
v_mov_b32 v253,0x27000
s_or_b32 s34,s79,s35
M a[108:111],a[200:203],v[136:139],a[108:111]
B v[112:115],v240,s[96:99],s34 offen sc1
v_rcp_f32 v188,v188
M a[108:111],a[204:207],v[140:143],a[108:111]
T a[220:221],v209 offset:57344
B v[116:119],v241,s[96:99],s34 offen offset:1024 sc1
s_add_u32 s79,s79,40960
v_rcp_f32 v190,v190
M a[112:115],a[204:207],v[148:151],a[112:115]
v_rcp_f32 v192,v192
M a[112:115],a[200:203],v[144:147],a[112:115]
W lgkmcnt(4)
T a[222:223],v209 offset:61440
v_rcp_f32 v194,v194
M a[120:123],a[208:211],v[120:123],a[120:123]
W vmcnt(52)
W lgkmcnt(0)
s_barrier
s_mov_b32 m0,0x18000
R v[180:183],v253
M a[120:123],a[212:215],v[124:127],a[120:123]
s_and_b32 s94,s4,1
s_mul_i32 s94,s94,65664
B v218,s[44:47],s61 offen lds
B v219,s[44:47],s61 offen offset:1024 lds
M a[124:127],a[212:215],v[132:135],a[124:127]
s_and_b32 s81,s4,2
s_lshl_b32 s81,s81,5
B v220,s[44:47],s61 offen offset:2048 lds
M a[124:127],a[208:211],v[128:131],a[124:127]
s_xor_b32 s81,s81,s94
s_xor_b32 s81,s81,0x10000
B v221,s[44:47],s61 offen offset:3072 lds
s_add_u32 s61,s61,s62
s_mov_b32 m0,0x19000
M a[128:131],a[208:211],v[136:139],a[128:131]
B v218,s[44:47],s61 offen lds
v_xor_b32 v160,s81,v202
M a[128:131],a[212:215],v[140:143],a[128:131]
B v219,s[44:47],s61 offen offset:1024 lds
v_xor_b32 v161,s81,v203
M a[132:135],a[212:215],v[148:151],a[132:135]
W lgkmcnt(0)
B v220,s[44:47],s61 offen offset:2048 lds
v_xor_b32 v162,s81,v204
v_or3_b32 v250,v180,v181,v182
M a[132:135],a[208:211],v[144:147],a[132:135]
W lgkmcnt(15)
B v221,s[44:47],s61 offen offset:3072 lds
s_add_u32 s61,s61,s63
v_xor_b32 v163,s81,v205
v_or_b32 v250,v250,v183
s_mov_b32 m0,0x1a000
M a[140:143],a[216:219],v[120:123],a[140:143]
B v218,s[44:47],s61 offen lds
B v219,s[44:47],s61 offen offset:1024 lds
v_xor_b32 v164,s81,v206
v_readfirstlane_b32 s95,v250
M a[140:143],a[220:223],v[124:127],a[140:143]
B v220,s[44:47],s61 offen offset:2048 lds
v_xor_b32 v165,s81,v207
M a[144:147],a[220:223],v[132:135],a[144:147]
s_cmp_eq_u32 s71,0
s_cselect_b32 s95,s95,0
B v221,s[44:47],s61 offen offset:3072 lds
s_add_u32 s61,s61,s62
v_xor_b32 v166,s81,v208
s_mov_b32 m0,0x1b000
M a[144:147],a[216:219],v[128:131],a[144:147]
B v218,s[44:47],s61 offen lds
v_xor_b32 v167,s81,v209
M a[148:151],a[216:219],v[136:139],a[148:151]
B v219,s[44:47],s61 offen offset:1024 lds
M a[148:151],a[220:223],v[140:143],a[148:151]
B v220,s[44:47],s61 offen offset:2048 lds
M a[152:155],a[220:223],v[148:151],a[152:155]
B v221,s[44:47],s61 offen offset:3072 lds
s_add_u32 s61,s61,s75
s_and_b32 s61,s61,s74
M a[152:155],a[216:219],v[144:147],a[152:155]
s_cmp_lg_u32 s95,0
s_cbranch_scc1 LFBb_%=
)ASM"
R"ASM(
s_cmp_eq_u32 s91,3
s_cbranch_scc1 LOP4_%=
s_branch LOP5_%=
)ASM"
R"ASM(
LSWK_%=:
s_lshl_b32 s95,s70,2
s_add_u32 s95,s95,s69
s_cmp_eq_u32 s95,3
s_cbranch_scc1 LSK03_%=
s_cmp_eq_u32 s95,5
s_cbranch_scc1 LSK11_%=
s_cmp_eq_u32 s95,6
s_cbranch_scc1 LSK12_%=
s_cmp_eq_u32 s95,7
s_cbranch_scc1 LSK13_%=
s_branch LSWKR_%=
LSK03_%=:
s_add_u32 s60,s82,4194304
s_branch LSWKR_%=
LSK11_%=:
v_lshrrev_b32 v250,1,v245
v_and_b32 v250,0xf8,v250
s_add_u32 s95,s86,0
buffer_load_dwordx2 v[248:249],v250,s[52:55],s95 offen sc1
s_branch LSWKR_%=
LSK12_%=:
W vmcnt(63)
s_mov_b32 s94,0
LPAa_%=:
v_cmp_ne_u64 vcc,s[72:73],v[248:249]
s_nop 3
s_cmp_eq_u64 vcc,0
s_cbranch_scc1 LPAb_%=
s_add_u32 s94,s94,1
s_cmp_gt_u32 s94,262144
s_cbranch_scc1 LPAb_%=
s_sleep 4
v_lshrrev_b32 v250,1,v245
v_and_b32 v250,0xf8,v250
s_add_u32 s95,s86,0
buffer_load_dwordx2 v[248:249],v250,s[52:55],s95 offen sc1
W vmcnt(0)
s_branch LPAa_%=
LPAb_%=:
v_lshlrev_b32 v250,10,v227
v_lshl_add_u32 v250,v244,3,v250
s_and_b32 s95,s85,0xfffff000
buffer_load_dwordx2 v[248:249],v250,s[52:55],s95 offen sc1
s_branch LSWKR_%=
LSK13_%=:
s_cmp_eq_u32 s90,0x7fffff00
s_cbranch_scc1 LNOE_%=
W vmcnt(63)
s_mov_b32 s94,0
LPEa_%=:
v_cmp_ne_u64 vcc,s[72:73],v[248:249]
s_nop 3
s_cmp_eq_u64 vcc,0
s_cbranch_scc1 LPEb_%=
s_add_u32 s94,s94,1
s_cmp_gt_u32 s94,262144
s_cbranch_scc1 LPEb_%=
s_sleep 4
v_lshlrev_b32 v250,10,v227
v_lshl_add_u32 v250,v244,3,v250
s_and_b32 s95,s85,0xfffff000
buffer_load_dwordx2 v[248:249],v250,s[52:55],s95 offen sc1
W vmcnt(0)
s_branch LPEa_%=
LPEb_%=:
v_mov_b32 v248,s72
v_mov_b32 v249,s73
s_mov_b64 exec,1
buffer_store_dwordx2 v[248:249],off,s[52:55],s90 sc1
s_mov_b64 exec,-1
LNOE_%=:
s_mov_b32 s40,s16
s_and_b32 s41,s17,0xffff
s_mov_b32 s42,0x400000
s_mov_b32 s43,0x20000
s_mov_b32 s95,1024
v_mad_u32_u24 v214,v227,s95,v246
v_mad_u32_u24 v215,v227,s95,v247
v_add_u32 v215,3072,v215
v_mad_u32_u24 v216,v227,s95,v246
v_add_u32 v216,6144,v216
v_mad_u32_u24 v217,v227,s95,v247
v_add_u32 v217,9216,v217
s_mov_b32 s60,s88
s_mov_b32 s76,16384
s_mov_b32 s77,212992
s_branch LSWKR_%=
)ASM"
R"ASM(
LSWV_%=:
s_lshl_b32 s95,s70,2
s_add_u32 s95,s95,s69
s_cmp_eq_u32 s95,0
s_cbranch_scc1 LSV00_%=
s_cmp_eq_u32 s95,3
s_cbranch_scc1 LSV03_%=
s_cmp_eq_u32 s95,5
s_cbranch_scc1 LSV11_%=
s_cmp_eq_u32 s95,6
s_cbranch_scc1 LSV12_%=
s_cmp_eq_u32 s95,7
s_cbranch_scc1 LSV13_%=
s_branch LSWVR_%=
LSV00_%=:
v_lshlrev_b32 v250,3,v245
s_mov_b32 exec_hi,0xff
buffer_load_dword v254,v250,s[36:39],s92 offen sc1
s_mov_b32 exec_hi,-1
s_branch LSWVR_%=
LSV03_%=:
s_mov_b32 s44,s14
s_and_b32 s45,s15,0xffff
s_mov_b32 s46,0x400000
s_mov_b32 s47,0x20000
s_mov_b32 s95,8192
v_mad_u32_u24 v218,v227,s95,v246
v_mad_u32_u24 v219,v227,s95,v247
v_add_u32 v219,64512,v219
v_mad_u32_u24 v220,v227,s95,v246
v_add_u32 v220,129024,v220
v_mad_u32_u24 v221,v227,s95,v247
v_add_u32 v221,193536,v221
s_add_u32 s61,s83,4096
s_mov_b32 s62,32768
s_mov_b32 s63,229376
s_mov_b32 s75,229376
s_mov_b32 s74,0x3fffff
v_lshlrev_b32 v250,3,v245
s_mov_b32 exec_hi,0xff
buffer_load_dword v254,v250,s[36:39],s92 offen sc1
s_mov_b32 exec_hi,-1
s_sub_u32 s94,384,s14
s_and_b32 s94,s94,0x300
v_lshlrev_b32 v250,13,v244
v_lshl_add_u32 v250,v227,10,v250
s_lshl_b32 s95,s29,17
s_add_u32 s95,s95,s94
s_add_u32 s95,s95,4096
buffer_load_dword v254,v250,s[44:47],s95 offen sc1
buffer_load_dword v254,v250,s[44:47],s95 offen offset:128 sc1
s_branch LSWVR_%=
LSV11_%=:
s_branch LSWVR_%=
LSV12_%=:
s_branch LSWVR_%=
LSV13_%=:
s_mov_b32 s44,s16
s_and_b32 s45,s17,0xffff
s_mov_b32 s46,0x400000
s_mov_b32 s47,0x20000
s_mov_b32 s95,1024
v_mad_u32_u24 v218,v227,s95,v246
v_mad_u32_u24 v219,v227,s95,v247
v_add_u32 v219,3072,v219
v_mad_u32_u24 v220,v227,s95,v246
v_add_u32 v220,6144,v220
v_mad_u32_u24 v221,v227,s95,v247
v_add_u32 v221,9216,v221
s_add_u32 s61,s88,65536
s_mov_b32 s62,16384
s_mov_b32 s63,16384
s_mov_b32 s75,212992
s_mov_b32 s74,-1
v_lshlrev_b32 v250,3,v245
s_mov_b32 exec_hi,0xff
buffer_load_dword v254,v250,s[36:39],s92 offen sc1
s_mov_b32 exec_hi,-1
s_branch LSWVR_%=
)ASM"
R"ASM(
LPEELc_%=:
M v[180:183],a[160:163],v[0:3],v[160:163]
E v184,v184
M v[180:183],a[164:167],v[4:7],v[180:183]
s_mov_b32 m0,0x1a000
E v185,v185
M v[180:183],a[168:171],v[8:11],v[180:183]
T a[208:209],v204
B v218,s[44:47],s61 offen nt lds
M v[180:183],a[172:175],v[12:15],v[180:183]
E v186,v186
M a[0:3],a[192:195],v[80:83],0
T a[210:211],v204 offset:4096
C v110,v184,v185
M a[0:3],a[196:199],v[84:87],a[0:3]
E v187,v187
M a[4:7],a[196:199],v[92:95],0
T a[212:213],v204 offset:8192
C v111,v186,v187
M a[4:7],a[192:195],v[88:91],a[4:7]
B v219,s[44:47],s61 offen offset:1024 nt lds
M v[184:187],a[160:163],v[16:19],v[164:167]
E v180,v180
M v[184:187],a[164:167],v[20:23],v[184:187]
E v181,v181
M v[184:187],a[168:171],v[24:27],v[184:187]
T a[214:215],v204 offset:12288
B v220,s[44:47],s61 offen offset:2048 nt lds
M v[184:187],a[172:175],v[28:31],v[184:187]
W lgkmcnt(4)
E v182,v182
M a[8:11],a[192:195],v[96:99],0
R a[176:179],v198 offset:20480
C v120,v180,v181
M a[8:11],a[196:199],v[100:103],a[8:11]
E v183,v183
M a[12:15],a[196:199],v[108:111],0
R a[180:183],v199 offset:20480
C v121,v182,v183
M a[12:15],a[192:195],v[104:107],a[12:15]
B v221,s[44:47],s61 offen offset:3072 nt lds
s_add_u32 s61,s61,s62
M v[180:183],a[160:163],v[32:35],v[168:171]
E v184,v184
M v[180:183],a[164:167],v[36:39],v[180:183]
s_mov_b32 m0,0x1b000
E v185,v185
M v[180:183],a[168:171],v[40:43],v[180:183]
R a[184:187],v200 offset:20480
B v218,s[44:47],s61 offen nt lds
M v[180:183],a[172:175],v[44:47],v[180:183]
E v186,v186
M a[20:23],a[200:203],v[80:83],0
R a[188:191],v201 offset:20480
C v128,v184,v185
M a[20:23],a[204:207],v[84:87],a[20:23]
E v187,v187
M a[24:27],a[204:207],v[92:95],0
T a[216:217],v205
C v129,v186,v187
M a[24:27],a[200:203],v[88:91],a[24:27]
B v219,s[44:47],s61 offen offset:1024 nt lds
v_smfmac_f32_16x16x64_bf16 a[16:19],a[96:99],v[80:87],v253
M v[184:187],a[160:163],v[48:51],v[172:175]
E v180,v180
M v[184:187],a[164:167],v[52:55],v[184:187]
E v181,v181
M v[184:187],a[168:171],v[56:59],v[184:187]
T a[218:219],v205 offset:4096
B v220,s[44:47],s61 offen offset:2048 nt lds
M v[184:187],a[172:175],v[60:63],v[184:187]
E v182,v182
M a[28:31],a[200:203],v[96:99],0
T a[220:221],v205 offset:8192
C v136,v180,v181
M a[28:31],a[204:207],v[100:103],a[28:31]
W lgkmcnt(3)
E v183,v183
M a[32:35],a[204:207],v[108:111],0
T a[222:223],v205 offset:12288
C v137,v182,v183
M a[32:35],a[200:203],v[104:107],a[32:35]
B v221,s[44:47],s61 offen offset:3072 nt lds
s_add_u32 s61,s61,s75
s_and_b32 s61,s61,s74
M v[180:183],a[176:179],v[0:3],v[160:163]
E v184,v184
M v[180:183],a[180:183],v[4:7],v[180:183]
s_mov_b32 m0,0x0
E v185,v185
M v[180:183],a[184:187],v[8:11],v[180:183]
T a[192:193],v206
B v214,s[40:43],s60 offen nt lds
M v[180:183],a[188:191],v[12:15],v[180:183]
E v186,v186
M a[40:43],a[208:211],v[80:83],0
T a[194:195],v206 offset:4096
C v144,v184,v185
M a[40:43],a[212:215],v[84:87],a[40:43]
E v187,v187
M a[44:47],a[212:215],v[92:95],0
T a[196:197],v206 offset:8192
C v145,v186,v187
M a[44:47],a[208:211],v[88:91],a[44:47]
B v215,s[40:43],s60 offen offset:1024 nt lds
M v[184:187],a[176:179],v[16:19],v[164:167]
E v180,v180
M v[184:187],a[180:183],v[20:23],v[184:187]
E v181,v181
M v[184:187],a[184:187],v[24:27],v[184:187]
T a[198:199],v206 offset:12288
B v216,s[40:43],s60 offen offset:2048 nt lds
M v[184:187],a[188:191],v[28:31],v[184:187]
W lgkmcnt(4)
E v182,v182
M a[48:51],a[208:211],v[96:99],0
R a[160:163],v198 offset:24576
C v122,v180,v181
M a[48:51],a[212:215],v[100:103],a[48:51]
E v183,v183
M a[52:55],a[212:215],v[108:111],0
R a[164:167],v199 offset:24576
C v123,v182,v183
M a[52:55],a[208:211],v[104:107],a[52:55]
B v217,s[40:43],s60 offen offset:3072 nt lds
s_add_u32 s60,s60,s76
M v[180:183],a[176:179],v[32:35],v[168:171]
E v184,v184
M v[180:183],a[180:183],v[36:39],v[180:183]
s_mov_b32 m0,0x1000
E v185,v185
M v[180:183],a[184:187],v[40:43],v[180:183]
R a[168:171],v200 offset:24576
B v214,s[40:43],s60 offen nt lds
M v[180:183],a[188:191],v[44:47],v[180:183]
E v186,v186
M a[60:63],a[216:219],v[80:83],0
R a[172:175],v201 offset:24576
C v130,v184,v185
M a[60:63],a[220:223],v[84:87],a[60:63]
E v187,v187
M a[64:67],a[220:223],v[92:95],0
T a[200:201],v207
C v131,v186,v187
M a[64:67],a[216:219],v[88:91],a[64:67]
B v215,s[40:43],s60 offen offset:1024 nt lds
v_smfmac_f32_16x16x64_bf16 a[36:39],a[96:99],v[88:95],v253
M v[184:187],a[176:179],v[48:51],v[172:175]
E v180,v180
M v[184:187],a[180:183],v[52:55],v[184:187]
E v181,v181
M v[184:187],a[184:187],v[56:59],v[184:187]
T a[202:203],v207 offset:4096
B v216,s[40:43],s60 offen offset:2048 nt lds
M v[184:187],a[188:191],v[60:63],v[184:187]
E v182,v182
M a[68:71],a[216:219],v[96:99],0
T a[204:205],v207 offset:8192
C v138,v180,v181
M a[68:71],a[220:223],v[100:103],a[68:71]
W lgkmcnt(3)
E v183,v183
M a[72:75],a[220:223],v[108:111],0
T a[206:207],v207 offset:12288
C v139,v182,v183
M a[72:75],a[216:219],v[104:107],a[72:75]
B v217,s[40:43],s60 offen offset:3072 nt lds
s_add_u32 s60,s60,s76
M v[180:183],a[160:163],v[0:3],v[160:163]
E v184,v184
M v[180:183],a[164:167],v[4:7],v[180:183]
s_mov_b32 m0,0x2000
E v185,v185
M v[180:183],a[168:171],v[8:11],v[180:183]
T a[208:209],v208
B v214,s[40:43],s60 offen nt lds
M v[180:183],a[172:175],v[12:15],v[180:183]
E v186,v186
M a[80:83],a[192:195],v[80:83],0
T a[210:211],v208 offset:4096
C v146,v184,v185
M a[80:83],a[196:199],v[84:87],a[80:83]
E v187,v187
M a[84:87],a[196:199],v[92:95],0
T a[212:213],v208 offset:8192
C v147,v186,v187
M a[84:87],a[192:195],v[88:91],a[84:87]
B v215,s[40:43],s60 offen offset:1024 nt lds
M v[184:187],a[160:163],v[16:19],v[164:167]
E v180,v180
M v[184:187],a[164:167],v[20:23],v[184:187]
E v181,v181
M v[184:187],a[168:171],v[24:27],v[184:187]
T a[214:215],v208 offset:12288
B v216,s[40:43],s60 offen offset:2048 nt lds
M v[184:187],a[172:175],v[28:31],v[184:187]
W lgkmcnt(4)
E v182,v182
M a[88:91],a[192:195],v[96:99],0
R a[176:179],v198 offset:28672
C v124,v180,v181
M a[88:91],a[196:199],v[100:103],a[88:91]
E v183,v183
M a[92:95],a[196:199],v[108:111],0
R a[180:183],v199 offset:28672
C v125,v182,v183
M a[92:95],a[192:195],v[104:107],a[92:95]
B v217,s[40:43],s60 offen offset:3072 nt lds
s_add_u32 s60,s60,s76
M v[180:183],a[160:163],v[32:35],v[168:171]
E v184,v184
M v[180:183],a[164:167],v[36:39],v[180:183]
s_mov_b32 m0,0x3000
E v185,v185
M v[180:183],a[168:171],v[40:43],v[180:183]
R a[184:187],v200 offset:28672
B v214,s[40:43],s60 offen nt lds
M v[180:183],a[172:175],v[44:47],v[180:183]
E v186,v186
M a[100:103],a[200:203],v[80:83],0
R a[188:191],v201 offset:28672
C v132,v184,v185
M a[100:103],a[204:207],v[84:87],a[100:103]
E v187,v187
M a[104:107],a[204:207],v[92:95],0
T a[216:217],v209
C v133,v186,v187
M a[104:107],a[200:203],v[88:91],a[104:107]
B v215,s[40:43],s60 offen offset:1024 nt lds
v_smfmac_f32_16x16x64_bf16 a[56:59],a[96:99],v[96:103],v253
M v[184:187],a[160:163],v[48:51],v[172:175]
E v180,v180
M v[184:187],a[164:167],v[52:55],v[184:187]
E v181,v181
M v[184:187],a[168:171],v[56:59],v[184:187]
T a[218:219],v209 offset:4096
B v216,s[40:43],s60 offen offset:2048 nt lds
M v[184:187],a[172:175],v[60:63],v[184:187]
E v182,v182
M a[108:111],a[200:203],v[96:99],0
T a[220:221],v209 offset:8192
C v140,v180,v181
M a[108:111],a[204:207],v[100:103],a[108:111]
W lgkmcnt(3)
E v183,v183
M a[112:115],a[204:207],v[108:111],0
T a[222:223],v209 offset:12288
C v141,v182,v183
M a[112:115],a[200:203],v[104:107],a[112:115]
B v217,s[40:43],s60 offen offset:3072 nt lds
s_add_u32 s60,s60,s77
M v[180:183],a[176:179],v[0:3],v[160:163]
W vmcnt(48)
s_barrier
E v184,v184
M v[180:183],a[180:183],v[4:7],v[180:183]
s_mov_b32 m0,0x1c000
E v185,v185
M v[180:183],a[184:187],v[8:11],v[180:183]
T a[192:193],v202 offset:16384
B v218,s[44:47],s61 offen nt lds
M v[180:183],a[188:191],v[12:15],v[180:183]
E v186,v186
M a[120:123],a[208:211],v[80:83],0
T a[194:195],v202 offset:20480
C v148,v184,v185
M a[120:123],a[212:215],v[84:87],a[120:123]
E v187,v187
M a[124:127],a[212:215],v[92:95],0
T a[196:197],v202 offset:24576
C v149,v186,v187
M a[124:127],a[208:211],v[88:91],a[124:127]
B v219,s[44:47],s61 offen offset:1024 nt lds
M v[184:187],a[176:179],v[16:19],v[164:167]
E v180,v180
M v[184:187],a[180:183],v[20:23],v[184:187]
E v181,v181
M v[184:187],a[184:187],v[24:27],v[184:187]
T a[198:199],v202 offset:28672
B v220,s[44:47],s61 offen offset:2048 nt lds
M v[184:187],a[188:191],v[28:31],v[184:187]
W lgkmcnt(4)
E v182,v182
M a[128:131],a[208:211],v[96:99],0
R a[160:163],v198 offset:32768
C v126,v180,v181
M a[128:131],a[212:215],v[100:103],a[128:131]
E v183,v183
M a[132:135],a[212:215],v[108:111],0
R a[164:167],v199 offset:32768
C v127,v182,v183
M a[132:135],a[208:211],v[104:107],a[132:135]
B v221,s[44:47],s61 offen offset:3072 nt lds
s_add_u32 s61,s61,s62
M v[180:183],a[176:179],v[32:35],v[168:171]
E v184,v184
M v[180:183],a[180:183],v[36:39],v[180:183]
s_mov_b32 m0,0x1d000
E v185,v185
M v[180:183],a[184:187],v[40:43],v[180:183]
)ASM"
R"ASM(
R a[168:171],v200 offset:32768
B v218,s[44:47],s61 offen nt lds
M v[180:183],a[188:191],v[44:47],v[180:183]
E v186,v186
M a[140:143],a[216:219],v[80:83],0
R a[172:175],v201 offset:32768
C v134,v184,v185
M a[140:143],a[220:223],v[84:87],a[140:143]
E v187,v187
M a[144:147],a[220:223],v[92:95],0
T a[200:201],v203 offset:16384
C v135,v186,v187
M a[144:147],a[216:219],v[88:91],a[144:147]
B v219,s[44:47],s61 offen offset:1024 nt lds
v_smfmac_f32_16x16x64_bf16 a[76:79],a[96:99],v[104:111],v253
M v[184:187],a[176:179],v[48:51],v[172:175]
E v180,v180
M v[184:187],a[180:183],v[52:55],v[184:187]
E v181,v181
M v[184:187],a[184:187],v[56:59],v[184:187]
T a[202:203],v203 offset:20480
B v220,s[44:47],s61 offen offset:2048 nt lds
M v[184:187],a[188:191],v[60:63],v[184:187]
E v182,v182
M a[148:151],a[216:219],v[96:99],0
T a[204:205],v203 offset:24576
C v142,v180,v181
M a[148:151],a[220:223],v[100:103],a[148:151]
W lgkmcnt(3)
E v183,v183
M a[152:155],a[220:223],v[108:111],0
T a[206:207],v203 offset:28672
C v143,v182,v183
M a[152:155],a[216:219],v[104:107],a[152:155]
B v221,s[44:47],s61 offen offset:3072 nt lds
s_add_u32 s61,s61,s63
s_branch LCIT1b_%=
.p2align 4
LCLOOPb_%=:
s_cmp_ge_u32 s69,1
s_cbranch_scc1 LCSWK_%=
LCSWKR_%=:
M v[180:183],a[160:163],v[0:3],v[160:163]
E v184,v184
M v[180:183],a[164:167],v[4:7],v[180:183]
s_mov_b32 m0,0x1a000
E v185,v185
M v[180:183],a[168:171],v[8:11],v[180:183]
T a[208:209],v204
B v218,s[44:47],s61 offen nt lds
M v[180:183],a[172:175],v[12:15],v[180:183]
E v186,v186
M a[0:3],a[192:195],v[80:83],a[0:3]
T a[210:211],v204 offset:4096
C v110,v184,v185
M a[0:3],a[196:199],v[84:87],a[0:3]
E v187,v187
M a[4:7],a[196:199],v[92:95],a[4:7]
T a[212:213],v204 offset:8192
C v111,v186,v187
M a[4:7],a[192:195],v[88:91],a[4:7]
B v219,s[44:47],s61 offen offset:1024 nt lds
M v[184:187],a[160:163],v[16:19],v[164:167]
E v180,v180
M v[184:187],a[164:167],v[20:23],v[184:187]
E v181,v181
M v[184:187],a[168:171],v[24:27],v[184:187]
T a[214:215],v204 offset:12288
B v220,s[44:47],s61 offen offset:2048 nt lds
M v[184:187],a[172:175],v[28:31],v[184:187]
W lgkmcnt(4)
E v182,v182
M a[8:11],a[192:195],v[96:99],a[8:11]
R a[176:179],v198 offset:20480
C v120,v180,v181
M a[8:11],a[196:199],v[100:103],a[8:11]
E v183,v183
M a[12:15],a[196:199],v[108:111],a[12:15]
R a[180:183],v199 offset:20480
C v121,v182,v183
M a[12:15],a[192:195],v[104:107],a[12:15]
B v221,s[44:47],s61 offen offset:3072 nt lds
s_add_u32 s61,s61,s62
M v[180:183],a[160:163],v[32:35],v[168:171]
E v184,v184
M v[180:183],a[164:167],v[36:39],v[180:183]
s_mov_b32 m0,0x1b000
E v185,v185
M v[180:183],a[168:171],v[40:43],v[180:183]
R a[184:187],v200 offset:20480
B v218,s[44:47],s61 offen nt lds
M v[180:183],a[172:175],v[44:47],v[180:183]
E v186,v186
M a[20:23],a[200:203],v[80:83],a[20:23]
R a[188:191],v201 offset:20480
C v128,v184,v185
M a[20:23],a[204:207],v[84:87],a[20:23]
E v187,v187
M a[24:27],a[204:207],v[92:95],a[24:27]
T a[216:217],v205
C v129,v186,v187
M a[24:27],a[200:203],v[88:91],a[24:27]
B v219,s[44:47],s61 offen offset:1024 nt lds
v_smfmac_f32_16x16x64_bf16 a[16:19],a[96:99],v[80:87],v253
M v[184:187],a[160:163],v[48:51],v[172:175]
E v180,v180
M v[184:187],a[164:167],v[52:55],v[184:187]
E v181,v181
M v[184:187],a[168:171],v[56:59],v[184:187]
T a[218:219],v205 offset:4096
B v220,s[44:47],s61 offen offset:2048 nt lds
M v[184:187],a[172:175],v[60:63],v[184:187]
E v182,v182
M a[28:31],a[200:203],v[96:99],a[28:31]
T a[220:221],v205 offset:8192
C v136,v180,v181
M a[28:31],a[204:207],v[100:103],a[28:31]
W lgkmcnt(3)
E v183,v183
M a[32:35],a[204:207],v[108:111],a[32:35]
T a[222:223],v205 offset:12288
C v137,v182,v183
M a[32:35],a[200:203],v[104:107],a[32:35]
B v221,s[44:47],s61 offen offset:3072 nt lds
s_add_u32 s61,s61,s75
s_and_b32 s61,s61,s74
M v[180:183],a[176:179],v[0:3],v[160:163]
E v184,v184
M v[180:183],a[180:183],v[4:7],v[180:183]
s_mov_b32 m0,0x0
E v185,v185
M v[180:183],a[184:187],v[8:11],v[180:183]
T a[192:193],v206
B v214,s[40:43],s60 offen nt lds
M v[180:183],a[188:191],v[12:15],v[180:183]
E v186,v186
M a[40:43],a[208:211],v[80:83],a[40:43]
T a[194:195],v206 offset:4096
C v144,v184,v185
M a[40:43],a[212:215],v[84:87],a[40:43]
E v187,v187
M a[44:47],a[212:215],v[92:95],a[44:47]
T a[196:197],v206 offset:8192
C v145,v186,v187
M a[44:47],a[208:211],v[88:91],a[44:47]
B v215,s[40:43],s60 offen offset:1024 nt lds
M v[184:187],a[176:179],v[16:19],v[164:167]
E v180,v180
M v[184:187],a[180:183],v[20:23],v[184:187]
E v181,v181
M v[184:187],a[184:187],v[24:27],v[184:187]
T a[198:199],v206 offset:12288
B v216,s[40:43],s60 offen offset:2048 nt lds
M v[184:187],a[188:191],v[28:31],v[184:187]
W lgkmcnt(4)
E v182,v182
M a[48:51],a[208:211],v[96:99],a[48:51]
R a[160:163],v198 offset:24576
C v122,v180,v181
M a[48:51],a[212:215],v[100:103],a[48:51]
E v183,v183
M a[52:55],a[212:215],v[108:111],a[52:55]
R a[164:167],v199 offset:24576
C v123,v182,v183
M a[52:55],a[208:211],v[104:107],a[52:55]
B v217,s[40:43],s60 offen offset:3072 nt lds
s_add_u32 s60,s60,s76
M v[180:183],a[176:179],v[32:35],v[168:171]
E v184,v184
M v[180:183],a[180:183],v[36:39],v[180:183]
s_mov_b32 m0,0x1000
E v185,v185
M v[180:183],a[184:187],v[40:43],v[180:183]
R a[168:171],v200 offset:24576
B v214,s[40:43],s60 offen nt lds
M v[180:183],a[188:191],v[44:47],v[180:183]
E v186,v186
M a[60:63],a[216:219],v[80:83],a[60:63]
R a[172:175],v201 offset:24576
C v130,v184,v185
M a[60:63],a[220:223],v[84:87],a[60:63]
E v187,v187
M a[64:67],a[220:223],v[92:95],a[64:67]
T a[200:201],v207
C v131,v186,v187
M a[64:67],a[216:219],v[88:91],a[64:67]
B v215,s[40:43],s60 offen offset:1024 nt lds
v_smfmac_f32_16x16x64_bf16 a[36:39],a[96:99],v[88:95],v253
M v[184:187],a[176:179],v[48:51],v[172:175]
E v180,v180
M v[184:187],a[180:183],v[52:55],v[184:187]
E v181,v181
M v[184:187],a[184:187],v[56:59],v[184:187]
T a[202:203],v207 offset:4096
B v216,s[40:43],s60 offen offset:2048 nt lds
M v[184:187],a[188:191],v[60:63],v[184:187]
E v182,v182
M a[68:71],a[216:219],v[96:99],a[68:71]
T a[204:205],v207 offset:8192
C v138,v180,v181
M a[68:71],a[220:223],v[100:103],a[68:71]
W lgkmcnt(3)
E v183,v183
M a[72:75],a[220:223],v[108:111],a[72:75]
T a[206:207],v207 offset:12288
C v139,v182,v183
M a[72:75],a[216:219],v[104:107],a[72:75]
B v217,s[40:43],s60 offen offset:3072 nt lds
s_add_u32 s60,s60,s76
M v[180:183],a[160:163],v[0:3],v[160:163]
E v184,v184
M v[180:183],a[164:167],v[4:7],v[180:183]
s_mov_b32 m0,0x2000
E v185,v185
M v[180:183],a[168:171],v[8:11],v[180:183]
T a[208:209],v208
B v214,s[40:43],s60 offen nt lds
M v[180:183],a[172:175],v[12:15],v[180:183]
E v186,v186
M a[80:83],a[192:195],v[80:83],a[80:83]
T a[210:211],v208 offset:4096
C v146,v184,v185
M a[80:83],a[196:199],v[84:87],a[80:83]
E v187,v187
M a[84:87],a[196:199],v[92:95],a[84:87]
T a[212:213],v208 offset:8192
C v147,v186,v187
M a[84:87],a[192:195],v[88:91],a[84:87]
B v215,s[40:43],s60 offen offset:1024 nt lds
M v[184:187],a[160:163],v[16:19],v[164:167]
E v180,v180
M v[184:187],a[164:167],v[20:23],v[184:187]
E v181,v181
M v[184:187],a[168:171],v[24:27],v[184:187]
T a[214:215],v208 offset:12288
B v216,s[40:43],s60 offen offset:2048 nt lds
M v[184:187],a[172:175],v[28:31],v[184:187]
W lgkmcnt(4)
E v182,v182
M a[88:91],a[192:195],v[96:99],a[88:91]
R a[176:179],v198 offset:28672
C v124,v180,v181
M a[88:91],a[196:199],v[100:103],a[88:91]
E v183,v183
M a[92:95],a[196:199],v[108:111],a[92:95]
R a[180:183],v199 offset:28672
C v125,v182,v183
M a[92:95],a[192:195],v[104:107],a[92:95]
B v217,s[40:43],s60 offen offset:3072 nt lds
s_add_u32 s60,s60,s76
M v[180:183],a[160:163],v[32:35],v[168:171]
E v184,v184
M v[180:183],a[164:167],v[36:39],v[180:183]
s_mov_b32 m0,0x3000
E v185,v185
M v[180:183],a[168:171],v[40:43],v[180:183]
R a[184:187],v200 offset:28672
B v214,s[40:43],s60 offen nt lds
M v[180:183],a[172:175],v[44:47],v[180:183]
E v186,v186
M a[100:103],a[200:203],v[80:83],a[100:103]
R a[188:191],v201 offset:28672
C v132,v184,v185
M a[100:103],a[204:207],v[84:87],a[100:103]
E v187,v187
M a[104:107],a[204:207],v[92:95],a[104:107]
T a[216:217],v209
C v133,v186,v187
M a[104:107],a[200:203],v[88:91],a[104:107]
B v215,s[40:43],s60 offen offset:1024 nt lds
v_smfmac_f32_16x16x64_bf16 a[56:59],a[96:99],v[96:103],v253
M v[184:187],a[160:163],v[48:51],v[172:175]
E v180,v180
M v[184:187],a[164:167],v[52:55],v[184:187]
E v181,v181
M v[184:187],a[168:171],v[56:59],v[184:187]
T a[218:219],v209 offset:4096
B v216,s[40:43],s60 offen offset:2048 nt lds
M v[184:187],a[172:175],v[60:63],v[184:187]
E v182,v182
M a[108:111],a[200:203],v[96:99],a[108:111]
T a[220:221],v209 offset:8192
C v140,v180,v181
M a[108:111],a[204:207],v[100:103],a[108:111]
W lgkmcnt(3)
E v183,v183
M a[112:115],a[204:207],v[108:111],a[112:115]
T a[222:223],v209 offset:12288
C v141,v182,v183
M a[112:115],a[200:203],v[104:107],a[112:115]
B v217,s[40:43],s60 offen offset:3072 nt lds
s_add_u32 s60,s60,s77
M v[180:183],a[176:179],v[0:3],v[160:163]
W vmcnt(48)
s_barrier
E v184,v184
M v[180:183],a[180:183],v[4:7],v[180:183]
s_mov_b32 m0,0x1c000
E v185,v185
M v[180:183],a[184:187],v[8:11],v[180:183]
)ASM"
R"ASM(
T a[192:193],v202 offset:16384
B v218,s[44:47],s61 offen nt lds
M v[180:183],a[188:191],v[12:15],v[180:183]
E v186,v186
M a[120:123],a[208:211],v[80:83],a[120:123]
T a[194:195],v202 offset:20480
C v148,v184,v185
M a[120:123],a[212:215],v[84:87],a[120:123]
E v187,v187
M a[124:127],a[212:215],v[92:95],a[124:127]
T a[196:197],v202 offset:24576
C v149,v186,v187
M a[124:127],a[208:211],v[88:91],a[124:127]
B v219,s[44:47],s61 offen offset:1024 nt lds
M v[184:187],a[176:179],v[16:19],v[164:167]
E v180,v180
M v[184:187],a[180:183],v[20:23],v[184:187]
E v181,v181
M v[184:187],a[184:187],v[24:27],v[184:187]
T a[198:199],v202 offset:28672
B v220,s[44:47],s61 offen offset:2048 nt lds
M v[184:187],a[188:191],v[28:31],v[184:187]
W lgkmcnt(4)
E v182,v182
M a[128:131],a[208:211],v[96:99],a[128:131]
R a[160:163],v198 offset:32768
C v126,v180,v181
M a[128:131],a[212:215],v[100:103],a[128:131]
E v183,v183
M a[132:135],a[212:215],v[108:111],a[132:135]
R a[164:167],v199 offset:32768
C v127,v182,v183
M a[132:135],a[208:211],v[104:107],a[132:135]
B v221,s[44:47],s61 offen offset:3072 nt lds
s_add_u32 s61,s61,s62
M v[180:183],a[176:179],v[32:35],v[168:171]
E v184,v184
M v[180:183],a[180:183],v[36:39],v[180:183]
s_mov_b32 m0,0x1d000
E v185,v185
M v[180:183],a[184:187],v[40:43],v[180:183]
R a[168:171],v200 offset:32768
B v218,s[44:47],s61 offen nt lds
M v[180:183],a[188:191],v[44:47],v[180:183]
E v186,v186
M a[140:143],a[216:219],v[80:83],a[140:143]
R a[172:175],v201 offset:32768
C v134,v184,v185
M a[140:143],a[220:223],v[84:87],a[140:143]
E v187,v187
M a[144:147],a[220:223],v[92:95],a[144:147]
T a[200:201],v203 offset:16384
C v135,v186,v187
M a[144:147],a[216:219],v[88:91],a[144:147]
B v219,s[44:47],s61 offen offset:1024 nt lds
v_smfmac_f32_16x16x64_bf16 a[76:79],a[96:99],v[104:111],v253
M v[184:187],a[176:179],v[48:51],v[172:175]
E v180,v180
M v[184:187],a[180:183],v[52:55],v[184:187]
E v181,v181
M v[184:187],a[184:187],v[56:59],v[184:187]
T a[202:203],v203 offset:20480
B v220,s[44:47],s61 offen offset:2048 nt lds
M v[184:187],a[188:191],v[60:63],v[184:187]
E v182,v182
M a[148:151],a[216:219],v[96:99],a[148:151]
T a[204:205],v203 offset:24576
C v142,v180,v181
M a[148:151],a[220:223],v[100:103],a[148:151]
W lgkmcnt(3)
E v183,v183
M a[152:155],a[220:223],v[108:111],a[152:155]
T a[206:207],v203 offset:28672
C v143,v182,v183
M a[152:155],a[216:219],v[104:107],a[152:155]
B v221,s[44:47],s61 offen offset:3072 nt lds
s_add_u32 s61,s61,s63
LCIT1b_%=:
M v[180:183],a[160:163],v[0:3],v[160:163]
E v184,v184
M v[180:183],a[164:167],v[4:7],v[180:183]
s_mov_b32 m0,0x1e000
E v185,v185
M v[180:183],a[168:171],v[8:11],v[180:183]
T a[208:209],v204 offset:16384
B v218,s[44:47],s61 offen nt lds
M v[180:183],a[172:175],v[12:15],v[180:183]
E v186,v186
M a[0:3],a[192:195],v[120:123],a[0:3]
T a[210:211],v204 offset:20480
C v150,v184,v185
M a[0:3],a[196:199],v[124:127],a[0:3]
E v187,v187
M a[4:7],a[196:199],v[132:135],a[4:7]
T a[212:213],v204 offset:24576
C v151,v186,v187
M a[4:7],a[192:195],v[128:131],a[4:7]
B v219,s[44:47],s61 offen offset:1024 nt lds
M v[184:187],a[160:163],v[16:19],v[164:167]
E v180,v180
M v[184:187],a[164:167],v[20:23],v[184:187]
E v181,v181
M v[184:187],a[168:171],v[24:27],v[184:187]
T a[214:215],v204 offset:28672
B v220,s[44:47],s61 offen offset:2048 nt lds
M v[184:187],a[172:175],v[28:31],v[184:187]
W lgkmcnt(4)
E v182,v182
M a[8:11],a[192:195],v[136:139],a[8:11]
R a[176:179],v198 offset:36864
C v80,v180,v181
M a[8:11],a[196:199],v[140:143],a[8:11]
E v183,v183
M a[12:15],a[196:199],v[148:151],a[12:15]
R a[180:183],v199 offset:36864
C v81,v182,v183
M a[12:15],a[192:195],v[144:147],a[12:15]
B v221,s[44:47],s61 offen offset:3072 nt lds
s_add_u32 s61,s61,s62
M v[180:183],a[160:163],v[32:35],v[168:171]
E v184,v184
M v[180:183],a[164:167],v[36:39],v[180:183]
s_mov_b32 m0,0x1f000
E v185,v185
M v[180:183],a[168:171],v[40:43],v[180:183]
R a[184:187],v200 offset:36864
B v218,s[44:47],s61 offen nt lds
M v[180:183],a[172:175],v[44:47],v[180:183]
E v186,v186
M a[20:23],a[200:203],v[120:123],a[20:23]
R a[188:191],v201 offset:36864
C v88,v184,v185
M a[20:23],a[204:207],v[124:127],a[20:23]
E v187,v187
M a[24:27],a[204:207],v[132:135],a[24:27]
T a[216:217],v205 offset:16384
C v89,v186,v187
M a[24:27],a[200:203],v[128:131],a[24:27]
B v219,s[44:47],s61 offen offset:1024 nt lds
v_smfmac_f32_16x16x64_bf16 a[16:19],a[96:99],v[120:127],v253
M v[184:187],a[160:163],v[48:51],v[172:175]
E v180,v180
M v[184:187],a[164:167],v[52:55],v[184:187]
E v181,v181
M v[184:187],a[168:171],v[56:59],v[184:187]
T a[218:219],v205 offset:20480
B v220,s[44:47],s61 offen offset:2048 nt lds
M v[184:187],a[172:175],v[60:63],v[184:187]
E v182,v182
M a[28:31],a[200:203],v[136:139],a[28:31]
T a[220:221],v205 offset:24576
C v96,v180,v181
M a[28:31],a[204:207],v[140:143],a[28:31]
W lgkmcnt(3)
E v183,v183
M a[32:35],a[204:207],v[148:151],a[32:35]
T a[222:223],v205 offset:28672
C v97,v182,v183
M a[32:35],a[200:203],v[144:147],a[32:35]
B v221,s[44:47],s61 offen offset:3072 nt lds
s_add_u32 s61,s61,s75
s_and_b32 s61,s61,s74
M v[180:183],a[176:179],v[0:3],v[160:163]
E v184,v184
M v[180:183],a[180:183],v[4:7],v[180:183]
s_mov_b32 m0,0x4000
E v185,v185
M v[180:183],a[184:187],v[8:11],v[180:183]
T a[192:193],v206 offset:16384
B v214,s[40:43],s60 offen nt lds
M v[180:183],a[188:191],v[12:15],v[180:183]
E v186,v186
M a[40:43],a[208:211],v[120:123],a[40:43]
T a[194:195],v206 offset:20480
C v104,v184,v185
M a[40:43],a[212:215],v[124:127],a[40:43]
E v187,v187
M a[44:47],a[212:215],v[132:135],a[44:47]
T a[196:197],v206 offset:24576
C v105,v186,v187
M a[44:47],a[208:211],v[128:131],a[44:47]
B v215,s[40:43],s60 offen offset:1024 nt lds
M v[184:187],a[176:179],v[16:19],v[164:167]
E v180,v180
M v[184:187],a[180:183],v[20:23],v[184:187]
E v181,v181
M v[184:187],a[184:187],v[24:27],v[184:187]
T a[198:199],v206 offset:28672
B v216,s[40:43],s60 offen offset:2048 nt lds
M v[184:187],a[188:191],v[28:31],v[184:187]
W lgkmcnt(4)
E v182,v182
M a[48:51],a[208:211],v[136:139],a[48:51]
R a[160:163],v198 offset:40960
C v82,v180,v181
M a[48:51],a[212:215],v[140:143],a[48:51]
E v183,v183
M a[52:55],a[212:215],v[148:151],a[52:55]
R a[164:167],v199 offset:40960
C v83,v182,v183
M a[52:55],a[208:211],v[144:147],a[52:55]
B v217,s[40:43],s60 offen offset:3072 nt lds
s_add_u32 s60,s60,s76
M v[180:183],a[176:179],v[32:35],v[168:171]
E v184,v184
M v[180:183],a[180:183],v[36:39],v[180:183]
s_mov_b32 m0,0x5000
E v185,v185
M v[180:183],a[184:187],v[40:43],v[180:183]
R a[168:171],v200 offset:40960
B v214,s[40:43],s60 offen nt lds
M v[180:183],a[188:191],v[44:47],v[180:183]
E v186,v186
M a[60:63],a[216:219],v[120:123],a[60:63]
R a[172:175],v201 offset:40960
C v90,v184,v185
M a[60:63],a[220:223],v[124:127],a[60:63]
E v187,v187
M a[64:67],a[220:223],v[132:135],a[64:67]
T a[200:201],v207 offset:16384
C v91,v186,v187
M a[64:67],a[216:219],v[128:131],a[64:67]
B v215,s[40:43],s60 offen offset:1024 nt lds
v_smfmac_f32_16x16x64_bf16 a[36:39],a[96:99],v[128:135],v253
M v[184:187],a[176:179],v[48:51],v[172:175]
E v180,v180
M v[184:187],a[180:183],v[52:55],v[184:187]
E v181,v181
M v[184:187],a[184:187],v[56:59],v[184:187]
T a[202:203],v207 offset:20480
B v216,s[40:43],s60 offen offset:2048 nt lds
M v[184:187],a[188:191],v[60:63],v[184:187]
E v182,v182
M a[68:71],a[216:219],v[136:139],a[68:71]
T a[204:205],v207 offset:24576
C v98,v180,v181
M a[68:71],a[220:223],v[140:143],a[68:71]
W lgkmcnt(3)
E v183,v183
M a[72:75],a[220:223],v[148:151],a[72:75]
T a[206:207],v207 offset:28672
C v99,v182,v183
M a[72:75],a[216:219],v[144:147],a[72:75]
B v217,s[40:43],s60 offen offset:3072 nt lds
s_add_u32 s60,s60,s76
s_branch LCSWV_%=
LCSWVR_%=:
M v[180:183],a[160:163],v[0:3],v[160:163]
E v184,v184
M v[180:183],a[164:167],v[4:7],v[180:183]
s_mov_b32 m0,0x6000
E v185,v185
M v[180:183],a[168:171],v[8:11],v[180:183]
T a[208:209],v208 offset:16384
B v214,s[40:43],s60 offen nt lds
M v[180:183],a[172:175],v[12:15],v[180:183]
E v186,v186
M a[80:83],a[192:195],v[120:123],a[80:83]
T a[210:211],v208 offset:20480
C v106,v184,v185
M a[80:83],a[196:199],v[124:127],a[80:83]
E v187,v187
M a[84:87],a[196:199],v[132:135],a[84:87]
T a[212:213],v208 offset:24576
C v107,v186,v187
M a[84:87],a[192:195],v[128:131],a[84:87]
B v215,s[40:43],s60 offen offset:1024 nt lds
M v[184:187],a[160:163],v[16:19],v[164:167]
E v180,v180
M v[184:187],a[164:167],v[20:23],v[184:187]
E v181,v181
M v[184:187],a[168:171],v[24:27],v[184:187]
T a[214:215],v208 offset:28672
B v216,s[40:43],s60 offen offset:2048 nt lds
M v[184:187],a[172:175],v[28:31],v[184:187]
W lgkmcnt(4)
E v182,v182
M a[88:91],a[192:195],v[136:139],a[88:91]
R a[176:179],v198 offset:45056
C v84,v180,v181
M a[88:91],a[196:199],v[140:143],a[88:91]
E v183,v183
M a[92:95],a[196:199],v[148:151],a[92:95]
R a[180:183],v199 offset:45056
C v85,v182,v183
M a[92:95],a[192:195],v[144:147],a[92:95]
B v217,s[40:43],s60 offen offset:3072 nt lds
s_add_u32 s60,s60,s76
M v[180:183],a[160:163],v[32:35],v[168:171]
E v184,v184
M v[180:183],a[164:167],v[36:39],v[180:183]
s_mov_b32 m0,0x7000
E v185,v185
M v[180:183],a[168:171],v[40:43],v[180:183]
R a[184:187],v200 offset:45056
B v214,s[40:43],s60 offen nt lds
M v[180:183],a[172:175],v[44:47],v[180:183]
E v186,v186
M a[100:103],a[200:203],v[120:123],a[100:103]
R a[188:191],v201 offset:45056
)ASM"
R"ASM(
C v92,v184,v185
M a[100:103],a[204:207],v[124:127],a[100:103]
E v187,v187
M a[104:107],a[204:207],v[132:135],a[104:107]
T a[216:217],v209 offset:16384
C v93,v186,v187
M a[104:107],a[200:203],v[128:131],a[104:107]
B v215,s[40:43],s60 offen offset:1024 nt lds
v_smfmac_f32_16x16x64_bf16 a[56:59],a[96:99],v[136:143],v253
M v[184:187],a[160:163],v[48:51],v[172:175]
E v180,v180
M v[184:187],a[164:167],v[52:55],v[184:187]
E v181,v181
M v[184:187],a[168:171],v[56:59],v[184:187]
T a[218:219],v209 offset:20480
B v216,s[40:43],s60 offen offset:2048 nt lds
M v[184:187],a[172:175],v[60:63],v[184:187]
E v182,v182
M a[108:111],a[200:203],v[136:139],a[108:111]
T a[220:221],v209 offset:24576
C v100,v180,v181
M a[108:111],a[204:207],v[140:143],a[108:111]
W lgkmcnt(3)
E v183,v183
M a[112:115],a[204:207],v[148:151],a[112:115]
T a[222:223],v209 offset:28672
C v101,v182,v183
M a[112:115],a[200:203],v[144:147],a[112:115]
B v217,s[40:43],s60 offen offset:3072 nt lds
s_add_u32 s60,s60,s77
M v[180:183],a[176:179],v[0:3],v[160:163]
W vmcnt(48)
s_barrier
E v184,v184
M v[180:183],a[180:183],v[4:7],v[180:183]
s_mov_b32 m0,0x10000
E v185,v185
M v[180:183],a[184:187],v[8:11],v[180:183]
T a[192:193],v202 offset:32768
B v218,s[44:47],s61 offen lds
M v[180:183],a[188:191],v[12:15],v[180:183]
E v186,v186
M a[120:123],a[208:211],v[120:123],a[120:123]
T a[194:195],v202 offset:36864
C v108,v184,v185
M a[120:123],a[212:215],v[124:127],a[120:123]
E v187,v187
M a[124:127],a[212:215],v[132:135],a[124:127]
T a[196:197],v202 offset:40960
C v109,v186,v187
M a[124:127],a[208:211],v[128:131],a[124:127]
B v219,s[44:47],s61 offen offset:1024 lds
M v[184:187],a[176:179],v[16:19],v[164:167]
E v180,v180
M v[184:187],a[180:183],v[20:23],v[184:187]
E v181,v181
M v[184:187],a[184:187],v[24:27],v[184:187]
T a[198:199],v202 offset:45056
B v220,s[44:47],s61 offen offset:2048 lds
M v[184:187],a[188:191],v[28:31],v[184:187]
W lgkmcnt(4)
E v182,v182
M a[128:131],a[208:211],v[136:139],a[128:131]
R a[160:163],v198 offset:49152
C v86,v180,v181
M a[128:131],a[212:215],v[140:143],a[128:131]
E v183,v183
M a[132:135],a[212:215],v[148:151],a[132:135]
R a[164:167],v199 offset:49152
C v87,v182,v183
M a[132:135],a[208:211],v[144:147],a[132:135]
B v221,s[44:47],s61 offen offset:3072 lds
s_add_u32 s61,s61,s62
M v[180:183],a[176:179],v[32:35],v[168:171]
E v184,v184
M v[180:183],a[180:183],v[36:39],v[180:183]
s_mov_b32 m0,0x11000
E v185,v185
M v[180:183],a[184:187],v[40:43],v[180:183]
R a[168:171],v200 offset:49152
B v218,s[44:47],s61 offen lds
M v[180:183],a[188:191],v[44:47],v[180:183]
E v186,v186
M a[140:143],a[216:219],v[120:123],a[140:143]
R a[172:175],v201 offset:49152
C v94,v184,v185
M a[140:143],a[220:223],v[124:127],a[140:143]
E v187,v187
M a[144:147],a[220:223],v[132:135],a[144:147]
T a[200:201],v203 offset:32768
C v95,v186,v187
M a[144:147],a[216:219],v[128:131],a[144:147]
B v219,s[44:47],s61 offen offset:1024 lds
v_smfmac_f32_16x16x64_bf16 a[76:79],a[96:99],v[144:151],v253
M v[184:187],a[176:179],v[48:51],v[172:175]
E v180,v180
M v[184:187],a[180:183],v[52:55],v[184:187]
E v181,v181
M v[184:187],a[184:187],v[56:59],v[184:187]
T a[202:203],v203 offset:36864
B v220,s[44:47],s61 offen offset:2048 lds
M v[184:187],a[188:191],v[60:63],v[184:187]
E v182,v182
M a[148:151],a[216:219],v[136:139],a[148:151]
T a[204:205],v203 offset:40960
C v102,v180,v181
M a[148:151],a[220:223],v[140:143],a[148:151]
W lgkmcnt(3)
E v183,v183
M a[152:155],a[220:223],v[148:151],a[152:155]
T a[206:207],v203 offset:45056
C v103,v182,v183
M a[152:155],a[216:219],v[144:147],a[152:155]
B v221,s[44:47],s61 offen offset:3072 lds
s_add_u32 s61,s61,s63
M v[180:183],a[160:163],v[0:3],v[160:163]
E v184,v184
M v[180:183],a[164:167],v[4:7],v[180:183]
s_mov_b32 m0,0x12000
E v185,v185
M v[180:183],a[168:171],v[8:11],v[180:183]
T a[208:209],v204 offset:32768
B v218,s[44:47],s61 offen lds
M v[180:183],a[172:175],v[12:15],v[180:183]
E v186,v186
M a[0:3],a[192:195],v[80:83],a[0:3]
T a[210:211],v204 offset:36864
C v110,v184,v185
M a[0:3],a[196:199],v[84:87],a[0:3]
E v187,v187
M a[4:7],a[196:199],v[92:95],a[4:7]
T a[212:213],v204 offset:40960
C v111,v186,v187
M a[4:7],a[192:195],v[88:91],a[4:7]
B v219,s[44:47],s61 offen offset:1024 lds
M v[184:187],a[160:163],v[16:19],v[164:167]
E v180,v180
M v[184:187],a[164:167],v[20:23],v[184:187]
E v181,v181
M v[184:187],a[168:171],v[24:27],v[184:187]
T a[214:215],v204 offset:45056
B v220,s[44:47],s61 offen offset:2048 lds
M v[184:187],a[172:175],v[28:31],v[184:187]
W lgkmcnt(4)
E v182,v182
M a[8:11],a[192:195],v[96:99],a[8:11]
R a[176:179],v198 offset:53248
C v120,v180,v181
M a[8:11],a[196:199],v[100:103],a[8:11]
E v183,v183
M a[12:15],a[196:199],v[108:111],a[12:15]
R a[180:183],v199 offset:53248
C v121,v182,v183
M a[12:15],a[192:195],v[104:107],a[12:15]
B v221,s[44:47],s61 offen offset:3072 lds
s_add_u32 s61,s61,s62
M v[180:183],a[160:163],v[32:35],v[168:171]
E v184,v184
M v[180:183],a[164:167],v[36:39],v[180:183]
s_mov_b32 m0,0x13000
E v185,v185
M v[180:183],a[168:171],v[40:43],v[180:183]
R a[184:187],v200 offset:53248
B v218,s[44:47],s61 offen lds
M v[180:183],a[172:175],v[44:47],v[180:183]
E v186,v186
M a[20:23],a[200:203],v[80:83],a[20:23]
R a[188:191],v201 offset:53248
C v128,v184,v185
M a[20:23],a[204:207],v[84:87],a[20:23]
E v187,v187
M a[24:27],a[204:207],v[92:95],a[24:27]
T a[216:217],v205 offset:32768
C v129,v186,v187
M a[24:27],a[200:203],v[88:91],a[24:27]
B v219,s[44:47],s61 offen offset:1024 lds
v_smfmac_f32_16x16x64_bf16 a[16:19],a[96:99],v[80:87],v253
M v[184:187],a[160:163],v[48:51],v[172:175]
E v180,v180
M v[184:187],a[164:167],v[52:55],v[184:187]
E v181,v181
M v[184:187],a[168:171],v[56:59],v[184:187]
T a[218:219],v205 offset:36864
B v220,s[44:47],s61 offen offset:2048 lds
M v[184:187],a[172:175],v[60:63],v[184:187]
E v182,v182
M a[28:31],a[200:203],v[96:99],a[28:31]
T a[220:221],v205 offset:40960
C v136,v180,v181
M a[28:31],a[204:207],v[100:103],a[28:31]
W lgkmcnt(3)
E v183,v183
M a[32:35],a[204:207],v[108:111],a[32:35]
T a[222:223],v205 offset:45056
C v137,v182,v183
M a[32:35],a[200:203],v[104:107],a[32:35]
B v221,s[44:47],s61 offen offset:3072 lds
s_add_u32 s61,s61,s75
s_and_b32 s61,s61,s74
M v[180:183],a[176:179],v[0:3],v[160:163]
E v184,v184
M v[180:183],a[180:183],v[4:7],v[180:183]
s_mov_b32 m0,0x8000
E v185,v185
M v[180:183],a[184:187],v[8:11],v[180:183]
T a[192:193],v206 offset:32768
B v214,s[40:43],s60 offen nt lds
M v[180:183],a[188:191],v[12:15],v[180:183]
E v186,v186
M a[40:43],a[208:211],v[80:83],a[40:43]
T a[194:195],v206 offset:36864
C v144,v184,v185
M a[40:43],a[212:215],v[84:87],a[40:43]
E v187,v187
M a[44:47],a[212:215],v[92:95],a[44:47]
T a[196:197],v206 offset:40960
C v145,v186,v187
M a[44:47],a[208:211],v[88:91],a[44:47]
B v215,s[40:43],s60 offen offset:1024 nt lds
M v[184:187],a[176:179],v[16:19],v[164:167]
E v180,v180
M v[184:187],a[180:183],v[20:23],v[184:187]
E v181,v181
M v[184:187],a[184:187],v[24:27],v[184:187]
T a[198:199],v206 offset:45056
B v216,s[40:43],s60 offen offset:2048 nt lds
M v[184:187],a[188:191],v[28:31],v[184:187]
W lgkmcnt(4)
E v182,v182
M a[48:51],a[208:211],v[96:99],a[48:51]
R a[160:163],v198 offset:57344
C v122,v180,v181
M a[48:51],a[212:215],v[100:103],a[48:51]
E v183,v183
M a[52:55],a[212:215],v[108:111],a[52:55]
R a[164:167],v199 offset:57344
C v123,v182,v183
M a[52:55],a[208:211],v[104:107],a[52:55]
B v217,s[40:43],s60 offen offset:3072 nt lds
s_add_u32 s60,s60,s76
M v[180:183],a[176:179],v[32:35],v[168:171]
E v184,v184
M v[180:183],a[180:183],v[36:39],v[180:183]
s_mov_b32 m0,0x9000
E v185,v185
M v[180:183],a[184:187],v[40:43],v[180:183]
R a[168:171],v200 offset:57344
B v214,s[40:43],s60 offen nt lds
M v[180:183],a[188:191],v[44:47],v[180:183]
E v186,v186
M a[60:63],a[216:219],v[80:83],a[60:63]
R a[172:175],v201 offset:57344
C v130,v184,v185
M a[60:63],a[220:223],v[84:87],a[60:63]
E v187,v187
M a[64:67],a[220:223],v[92:95],a[64:67]
T a[200:201],v207 offset:32768
C v131,v186,v187
M a[64:67],a[216:219],v[88:91],a[64:67]
B v215,s[40:43],s60 offen offset:1024 nt lds
v_smfmac_f32_16x16x64_bf16 a[36:39],a[96:99],v[88:95],v253
M v[184:187],a[176:179],v[48:51],v[172:175]
E v180,v180
M v[184:187],a[180:183],v[52:55],v[184:187]
E v181,v181
M v[184:187],a[184:187],v[56:59],v[184:187]
T a[202:203],v207 offset:36864
B v216,s[40:43],s60 offen offset:2048 nt lds
M v[184:187],a[188:191],v[60:63],v[184:187]
E v182,v182
M a[68:71],a[216:219],v[96:99],a[68:71]
T a[204:205],v207 offset:40960
C v138,v180,v181
M a[68:71],a[220:223],v[100:103],a[68:71]
W lgkmcnt(3)
E v183,v183
M a[72:75],a[220:223],v[108:111],a[72:75]
T a[206:207],v207 offset:45056
C v139,v182,v183
M a[72:75],a[216:219],v[104:107],a[72:75]
B v217,s[40:43],s60 offen offset:3072 nt lds
s_add_u32 s60,s60,s76
M v[180:183],a[160:163],v[0:3],v[160:163]
E v184,v184
M v[180:183],a[164:167],v[4:7],v[180:183]
s_mov_b32 m0,0xa000
E v185,v185
M v[180:183],a[168:171],v[8:11],v[180:183]
T a[208:209],v208 offset:32768
B v214,s[40:43],s60 offen nt lds
M v[180:183],a[172:175],v[12:15],v[180:183]
E v186,v186
M a[80:83],a[192:195],v[80:83],a[80:83]
T a[210:211],v208 offset:36864
C v146,v184,v185
M a[80:83],a[196:199],v[84:87],a[80:83]
E v187,v187
M a[84:87],a[196:199],v[92:95],a[84:87]
T a[212:213],v208 offset:40960
C v147,v186,v187
)ASM"
R"ASM(
M a[84:87],a[192:195],v[88:91],a[84:87]
B v215,s[40:43],s60 offen offset:1024 nt lds
M v[184:187],a[160:163],v[16:19],v[164:167]
E v180,v180
M v[184:187],a[164:167],v[20:23],v[184:187]
E v181,v181
M v[184:187],a[168:171],v[24:27],v[184:187]
T a[214:215],v208 offset:45056
B v216,s[40:43],s60 offen offset:2048 nt lds
M v[184:187],a[172:175],v[28:31],v[184:187]
W lgkmcnt(4)
E v182,v182
M a[88:91],a[192:195],v[96:99],a[88:91]
R a[176:179],v198 offset:61440
C v124,v180,v181
M a[88:91],a[196:199],v[100:103],a[88:91]
E v183,v183
M a[92:95],a[196:199],v[108:111],a[92:95]
R a[180:183],v199 offset:61440
C v125,v182,v183
M a[92:95],a[192:195],v[104:107],a[92:95]
B v217,s[40:43],s60 offen offset:3072 nt lds
s_add_u32 s60,s60,s76
M v[180:183],a[160:163],v[32:35],v[168:171]
E v184,v184
M v[180:183],a[164:167],v[36:39],v[180:183]
s_mov_b32 m0,0xb000
E v185,v185
M v[180:183],a[168:171],v[40:43],v[180:183]
R a[184:187],v200 offset:61440
B v214,s[40:43],s60 offen nt lds
M v[180:183],a[172:175],v[44:47],v[180:183]
E v186,v186
M a[100:103],a[200:203],v[80:83],a[100:103]
R a[188:191],v201 offset:61440
C v132,v184,v185
M a[100:103],a[204:207],v[84:87],a[100:103]
E v187,v187
M a[104:107],a[204:207],v[92:95],a[104:107]
T a[216:217],v209 offset:32768
C v133,v186,v187
M a[104:107],a[200:203],v[88:91],a[104:107]
B v215,s[40:43],s60 offen offset:1024 nt lds
v_smfmac_f32_16x16x64_bf16 a[56:59],a[96:99],v[96:103],v253
M v[184:187],a[160:163],v[48:51],v[172:175]
E v180,v180
M v[184:187],a[164:167],v[52:55],v[184:187]
E v181,v181
M v[184:187],a[168:171],v[56:59],v[184:187]
T a[218:219],v209 offset:36864
B v216,s[40:43],s60 offen offset:2048 nt lds
M v[184:187],a[172:175],v[60:63],v[184:187]
E v182,v182
M a[108:111],a[200:203],v[96:99],a[108:111]
T a[220:221],v209 offset:40960
C v140,v180,v181
M a[108:111],a[204:207],v[100:103],a[108:111]
W lgkmcnt(3)
E v183,v183
M a[112:115],a[204:207],v[108:111],a[112:115]
T a[222:223],v209 offset:45056
C v141,v182,v183
M a[112:115],a[200:203],v[104:107],a[112:115]
B v217,s[40:43],s60 offen offset:3072 nt lds
s_add_u32 s60,s60,s77
M v[180:183],a[176:179],v[0:3],v[160:163]
W vmcnt(48)
s_barrier
E v184,v184
M v[180:183],a[180:183],v[4:7],v[180:183]
s_mov_b32 m0,0x14000
E v185,v185
M v[180:183],a[184:187],v[8:11],v[180:183]
T a[192:193],v202 offset:49152
B v218,s[44:47],s61 offen lds
M v[180:183],a[188:191],v[12:15],v[180:183]
E v186,v186
M a[120:123],a[208:211],v[80:83],a[120:123]
T a[194:195],v202 offset:53248
C v148,v184,v185
M a[120:123],a[212:215],v[84:87],a[120:123]
E v187,v187
M a[124:127],a[212:215],v[92:95],a[124:127]
T a[196:197],v202 offset:57344
C v149,v186,v187
M a[124:127],a[208:211],v[88:91],a[124:127]
B v219,s[44:47],s61 offen offset:1024 lds
M v[184:187],a[176:179],v[16:19],v[164:167]
E v180,v180
M v[184:187],a[180:183],v[20:23],v[184:187]
E v181,v181
M v[184:187],a[184:187],v[24:27],v[184:187]
T a[198:199],v202 offset:61440
B v220,s[44:47],s61 offen offset:2048 lds
M v[184:187],a[188:191],v[28:31],v[184:187]
W lgkmcnt(4)
E v182,v182
M a[128:131],a[208:211],v[96:99],a[128:131]
R a[160:163],v198
C v126,v180,v181
M a[128:131],a[212:215],v[100:103],a[128:131]
E v183,v183
M a[132:135],a[212:215],v[108:111],a[132:135]
R a[164:167],v199
C v127,v182,v183
M a[132:135],a[208:211],v[104:107],a[132:135]
B v221,s[44:47],s61 offen offset:3072 lds
s_add_u32 s61,s61,s62
M v[180:183],a[176:179],v[32:35],v[168:171]
E v184,v184
M v[180:183],a[180:183],v[36:39],v[180:183]
s_mov_b32 m0,0x15000
E v185,v185
M v[180:183],a[184:187],v[40:43],v[180:183]
R a[168:171],v200
B v218,s[44:47],s61 offen lds
M v[180:183],a[188:191],v[44:47],v[180:183]
E v186,v186
M a[140:143],a[216:219],v[80:83],a[140:143]
R a[172:175],v201
C v134,v184,v185
M a[140:143],a[220:223],v[84:87],a[140:143]
E v187,v187
M a[144:147],a[220:223],v[92:95],a[144:147]
T a[200:201],v203 offset:49152
C v135,v186,v187
M a[144:147],a[216:219],v[88:91],a[144:147]
B v219,s[44:47],s61 offen offset:1024 lds
v_smfmac_f32_16x16x64_bf16 a[76:79],a[96:99],v[104:111],v253
M v[184:187],a[176:179],v[48:51],v[172:175]
E v180,v180
M v[184:187],a[180:183],v[52:55],v[184:187]
E v181,v181
M v[184:187],a[184:187],v[56:59],v[184:187]
T a[202:203],v203 offset:53248
B v220,s[44:47],s61 offen offset:2048 lds
M v[184:187],a[188:191],v[60:63],v[184:187]
E v182,v182
M a[148:151],a[216:219],v[96:99],a[148:151]
T a[204:205],v203 offset:57344
C v142,v180,v181
M a[148:151],a[220:223],v[100:103],a[148:151]
W lgkmcnt(3)
E v183,v183
M a[152:155],a[220:223],v[108:111],a[152:155]
T a[206:207],v203 offset:61440
C v143,v182,v183
M a[152:155],a[216:219],v[104:107],a[152:155]
B v221,s[44:47],s61 offen offset:3072 lds
s_add_u32 s61,s61,s63
s_cmp_eq_u32 s69,3
s_cbranch_scc1 LEXITb_%=
M v[180:183],a[160:163],v[0:3],v[160:163]
E v184,v184
M v[180:183],a[164:167],v[4:7],v[180:183]
s_mov_b32 m0,0x16000
E v185,v185
M v[180:183],a[168:171],v[8:11],v[180:183]
T a[208:209],v204 offset:49152
B v218,s[44:47],s61 offen nt lds
M v[180:183],a[172:175],v[12:15],v[180:183]
E v186,v186
M a[0:3],a[192:195],v[120:123],a[0:3]
T a[210:211],v204 offset:53248
C v150,v184,v185
M a[0:3],a[196:199],v[124:127],a[0:3]
E v187,v187
M a[4:7],a[196:199],v[132:135],a[4:7]
T a[212:213],v204 offset:57344
C v151,v186,v187
M a[4:7],a[192:195],v[128:131],a[4:7]
B v219,s[44:47],s61 offen offset:1024 nt lds
M v[184:187],a[160:163],v[16:19],v[164:167]
E v180,v180
M v[184:187],a[164:167],v[20:23],v[184:187]
E v181,v181
M v[184:187],a[168:171],v[24:27],v[184:187]
T a[214:215],v204 offset:61440
B v220,s[44:47],s61 offen offset:2048 nt lds
M v[184:187],a[172:175],v[28:31],v[184:187]
W lgkmcnt(4)
E v182,v182
M a[8:11],a[192:195],v[136:139],a[8:11]
R a[176:179],v198 offset:4096
C v80,v180,v181
M a[8:11],a[196:199],v[140:143],a[8:11]
E v183,v183
M a[12:15],a[196:199],v[148:151],a[12:15]
R a[180:183],v199 offset:4096
C v81,v182,v183
M a[12:15],a[192:195],v[144:147],a[12:15]
B v221,s[44:47],s61 offen offset:3072 nt lds
s_add_u32 s61,s61,s62
M v[180:183],a[160:163],v[32:35],v[168:171]
E v184,v184
M v[180:183],a[164:167],v[36:39],v[180:183]
s_mov_b32 m0,0x17000
E v185,v185
M v[180:183],a[168:171],v[40:43],v[180:183]
R a[184:187],v200 offset:4096
B v218,s[44:47],s61 offen nt lds
M v[180:183],a[172:175],v[44:47],v[180:183]
E v186,v186
M a[20:23],a[200:203],v[120:123],a[20:23]
R a[188:191],v201 offset:4096
C v88,v184,v185
M a[20:23],a[204:207],v[124:127],a[20:23]
E v187,v187
M a[24:27],a[204:207],v[132:135],a[24:27]
T a[216:217],v205 offset:49152
C v89,v186,v187
M a[24:27],a[200:203],v[128:131],a[24:27]
B v219,s[44:47],s61 offen offset:1024 nt lds
v_smfmac_f32_16x16x64_bf16 a[16:19],a[96:99],v[120:127],v253
M v[184:187],a[160:163],v[48:51],v[172:175]
E v180,v180
M v[184:187],a[164:167],v[52:55],v[184:187]
E v181,v181
M v[184:187],a[168:171],v[56:59],v[184:187]
T a[218:219],v205 offset:53248
B v220,s[44:47],s61 offen offset:2048 nt lds
M v[184:187],a[172:175],v[60:63],v[184:187]
E v182,v182
M a[28:31],a[200:203],v[136:139],a[28:31]
T a[220:221],v205 offset:57344
C v96,v180,v181
M a[28:31],a[204:207],v[140:143],a[28:31]
W lgkmcnt(3)
E v183,v183
M a[32:35],a[204:207],v[148:151],a[32:35]
T a[222:223],v205 offset:61440
C v97,v182,v183
M a[32:35],a[200:203],v[144:147],a[32:35]
B v221,s[44:47],s61 offen offset:3072 nt lds
s_add_u32 s61,s61,s75
s_and_b32 s61,s61,s74
M v[180:183],a[176:179],v[0:3],v[160:163]
E v184,v184
M v[180:183],a[180:183],v[4:7],v[180:183]
s_mov_b32 m0,0xc000
E v185,v185
M v[180:183],a[184:187],v[8:11],v[180:183]
T a[192:193],v206 offset:49152
B v214,s[40:43],s60 offen nt lds
M v[180:183],a[188:191],v[12:15],v[180:183]
E v186,v186
M a[40:43],a[208:211],v[120:123],a[40:43]
T a[194:195],v206 offset:53248
C v104,v184,v185
M a[40:43],a[212:215],v[124:127],a[40:43]
E v187,v187
M a[44:47],a[212:215],v[132:135],a[44:47]
T a[196:197],v206 offset:57344
C v105,v186,v187
M a[44:47],a[208:211],v[128:131],a[44:47]
B v215,s[40:43],s60 offen offset:1024 nt lds
M v[184:187],a[176:179],v[16:19],v[164:167]
E v180,v180
M v[184:187],a[180:183],v[20:23],v[184:187]
E v181,v181
M v[184:187],a[184:187],v[24:27],v[184:187]
T a[198:199],v206 offset:61440
B v216,s[40:43],s60 offen offset:2048 nt lds
M v[184:187],a[188:191],v[28:31],v[184:187]
W lgkmcnt(4)
E v182,v182
M a[48:51],a[208:211],v[136:139],a[48:51]
R a[160:163],v198 offset:8192
C v82,v180,v181
M a[48:51],a[212:215],v[140:143],a[48:51]
E v183,v183
M a[52:55],a[212:215],v[148:151],a[52:55]
R a[164:167],v199 offset:8192
C v83,v182,v183
M a[52:55],a[208:211],v[144:147],a[52:55]
B v217,s[40:43],s60 offen offset:3072 nt lds
s_add_u32 s60,s60,s76
M v[180:183],a[176:179],v[32:35],v[168:171]
E v184,v184
M v[180:183],a[180:183],v[36:39],v[180:183]
s_mov_b32 m0,0xd000
E v185,v185
M v[180:183],a[184:187],v[40:43],v[180:183]
R a[168:171],v200 offset:8192
B v214,s[40:43],s60 offen nt lds
M v[180:183],a[188:191],v[44:47],v[180:183]
E v186,v186
M a[60:63],a[216:219],v[120:123],a[60:63]
R a[172:175],v201 offset:8192
C v90,v184,v185
M a[60:63],a[220:223],v[124:127],a[60:63]
E v187,v187
M a[64:67],a[220:223],v[132:135],a[64:67]
T a[200:201],v207 offset:49152
C v91,v186,v187
M a[64:67],a[216:219],v[128:131],a[64:67]
B v215,s[40:43],s60 offen offset:1024 nt lds
v_smfmac_f32_16x16x64_bf16 a[36:39],a[96:99],v[128:135],v253
M v[184:187],a[176:179],v[48:51],v[172:175]
E v180,v180
)ASM"
R"ASM(
M v[184:187],a[180:183],v[52:55],v[184:187]
E v181,v181
M v[184:187],a[184:187],v[56:59],v[184:187]
T a[202:203],v207 offset:53248
B v216,s[40:43],s60 offen offset:2048 nt lds
M v[184:187],a[188:191],v[60:63],v[184:187]
E v182,v182
M a[68:71],a[216:219],v[136:139],a[68:71]
T a[204:205],v207 offset:57344
C v98,v180,v181
M a[68:71],a[220:223],v[140:143],a[68:71]
W lgkmcnt(3)
E v183,v183
M a[72:75],a[220:223],v[148:151],a[72:75]
T a[206:207],v207 offset:61440
C v99,v182,v183
M a[72:75],a[216:219],v[144:147],a[72:75]
B v217,s[40:43],s60 offen offset:3072 nt lds
s_add_u32 s60,s60,s76
M v[180:183],a[160:163],v[0:3],v[160:163]
E v184,v184
M v[180:183],a[164:167],v[4:7],v[180:183]
s_mov_b32 m0,0xe000
E v185,v185
M v[180:183],a[168:171],v[8:11],v[180:183]
T a[208:209],v208 offset:49152
B v214,s[40:43],s60 offen nt lds
M v[180:183],a[172:175],v[12:15],v[180:183]
E v186,v186
M a[80:83],a[192:195],v[120:123],a[80:83]
T a[210:211],v208 offset:53248
C v106,v184,v185
M a[80:83],a[196:199],v[124:127],a[80:83]
E v187,v187
M a[84:87],a[196:199],v[132:135],a[84:87]
T a[212:213],v208 offset:57344
C v107,v186,v187
M a[84:87],a[192:195],v[128:131],a[84:87]
B v215,s[40:43],s60 offen offset:1024 nt lds
M v[184:187],a[160:163],v[16:19],v[164:167]
E v180,v180
M v[184:187],a[164:167],v[20:23],v[184:187]
E v181,v181
M v[184:187],a[168:171],v[24:27],v[184:187]
T a[214:215],v208 offset:61440
B v216,s[40:43],s60 offen offset:2048 nt lds
M v[184:187],a[172:175],v[28:31],v[184:187]
W lgkmcnt(4)
E v182,v182
M a[88:91],a[192:195],v[136:139],a[88:91]
R a[176:179],v198 offset:12288
C v84,v180,v181
M a[88:91],a[196:199],v[140:143],a[88:91]
E v183,v183
M a[92:95],a[196:199],v[148:151],a[92:95]
R a[180:183],v199 offset:12288
C v85,v182,v183
M a[92:95],a[192:195],v[144:147],a[92:95]
B v217,s[40:43],s60 offen offset:3072 nt lds
s_add_u32 s60,s60,s76
M v[180:183],a[160:163],v[32:35],v[168:171]
E v184,v184
M v[180:183],a[164:167],v[36:39],v[180:183]
s_mov_b32 m0,0xf000
E v185,v185
M v[180:183],a[168:171],v[40:43],v[180:183]
R a[184:187],v200 offset:12288
B v214,s[40:43],s60 offen nt lds
M v[180:183],a[172:175],v[44:47],v[180:183]
E v186,v186
M a[100:103],a[200:203],v[120:123],a[100:103]
R a[188:191],v201 offset:12288
C v92,v184,v185
M a[100:103],a[204:207],v[124:127],a[100:103]
E v187,v187
M a[104:107],a[204:207],v[132:135],a[104:107]
T a[216:217],v209 offset:49152
C v93,v186,v187
M a[104:107],a[200:203],v[128:131],a[104:107]
B v215,s[40:43],s60 offen offset:1024 nt lds
v_smfmac_f32_16x16x64_bf16 a[56:59],a[96:99],v[136:143],v253
M v[184:187],a[160:163],v[48:51],v[172:175]
E v180,v180
M v[184:187],a[164:167],v[52:55],v[184:187]
E v181,v181
M v[184:187],a[168:171],v[56:59],v[184:187]
T a[218:219],v209 offset:53248
B v216,s[40:43],s60 offen offset:2048 nt lds
M v[184:187],a[172:175],v[60:63],v[184:187]
E v182,v182
M a[108:111],a[200:203],v[136:139],a[108:111]
T a[220:221],v209 offset:57344
C v100,v180,v181
M a[108:111],a[204:207],v[140:143],a[108:111]
W lgkmcnt(3)
E v183,v183
M a[112:115],a[204:207],v[148:151],a[112:115]
T a[222:223],v209 offset:61440
C v101,v182,v183
M a[112:115],a[200:203],v[144:147],a[112:115]
B v217,s[40:43],s60 offen offset:3072 nt lds
s_add_u32 s60,s60,s77
M v[180:183],a[176:179],v[0:3],v[160:163]
W vmcnt(48)
s_barrier
E v184,v184
M v[180:183],a[180:183],v[4:7],v[180:183]
s_mov_b32 m0,0x18000
E v185,v185
M v[180:183],a[184:187],v[8:11],v[180:183]
T a[192:193],v202
B v218,s[44:47],s61 offen nt lds
M v[180:183],a[188:191],v[12:15],v[180:183]
E v186,v186
M a[120:123],a[208:211],v[120:123],a[120:123]
T a[194:195],v202 offset:4096
C v108,v184,v185
M a[120:123],a[212:215],v[124:127],a[120:123]
E v187,v187
M a[124:127],a[212:215],v[132:135],a[124:127]
T a[196:197],v202 offset:8192
C v109,v186,v187
M a[124:127],a[208:211],v[128:131],a[124:127]
B v219,s[44:47],s61 offen offset:1024 nt lds
M v[184:187],a[176:179],v[16:19],v[164:167]
E v180,v180
M v[184:187],a[180:183],v[20:23],v[184:187]
E v181,v181
M v[184:187],a[184:187],v[24:27],v[184:187]
T a[198:199],v202 offset:12288
B v220,s[44:47],s61 offen offset:2048 nt lds
M v[184:187],a[188:191],v[28:31],v[184:187]
W lgkmcnt(4)
E v182,v182
M a[128:131],a[208:211],v[136:139],a[128:131]
R a[160:163],v198 offset:16384
C v86,v180,v181
M a[128:131],a[212:215],v[140:143],a[128:131]
E v183,v183
M a[132:135],a[212:215],v[148:151],a[132:135]
R a[164:167],v199 offset:16384
C v87,v182,v183
M a[132:135],a[208:211],v[144:147],a[132:135]
B v221,s[44:47],s61 offen offset:3072 nt lds
s_add_u32 s61,s61,s62
M v[180:183],a[176:179],v[32:35],v[168:171]
E v184,v184
M v[180:183],a[180:183],v[36:39],v[180:183]
s_mov_b32 m0,0x19000
E v185,v185
M v[180:183],a[184:187],v[40:43],v[180:183]
R a[168:171],v200 offset:16384
B v218,s[44:47],s61 offen nt lds
M v[180:183],a[188:191],v[44:47],v[180:183]
E v186,v186
M a[140:143],a[216:219],v[120:123],a[140:143]
R a[172:175],v201 offset:16384
C v94,v184,v185
M a[140:143],a[220:223],v[124:127],a[140:143]
E v187,v187
M a[144:147],a[220:223],v[132:135],a[144:147]
T a[200:201],v203
C v95,v186,v187
M a[144:147],a[216:219],v[128:131],a[144:147]
B v219,s[44:47],s61 offen offset:1024 nt lds
v_smfmac_f32_16x16x64_bf16 a[76:79],a[96:99],v[144:151],v253
M v[184:187],a[176:179],v[48:51],v[172:175]
E v180,v180
M v[184:187],a[180:183],v[52:55],v[184:187]
E v181,v181
M v[184:187],a[184:187],v[56:59],v[184:187]
T a[202:203],v203 offset:4096
B v220,s[44:47],s61 offen offset:2048 nt lds
M v[184:187],a[188:191],v[60:63],v[184:187]
E v182,v182
M a[148:151],a[216:219],v[136:139],a[148:151]
T a[204:205],v203 offset:8192
C v102,v180,v181
M a[148:151],a[220:223],v[140:143],a[148:151]
W lgkmcnt(3)
E v183,v183
M a[152:155],a[220:223],v[148:151],a[152:155]
T a[206:207],v203 offset:12288
C v103,v182,v183
M a[152:155],a[216:219],v[144:147],a[152:155]
s_add_u32 s69,s69,1
B v221,s[44:47],s61 offen offset:3072 nt lds
s_add_u32 s61,s61,s63
s_branch LCLOOPb_%=
LCSWK_%=:
s_lshl_b32 s95,s70,2
s_add_u32 s95,s95,s69
s_cmp_eq_u32 s95,3
s_cbranch_scc1 LCSK03_%=
s_cmp_eq_u32 s95,5
s_cbranch_scc1 LCSK11_%=
s_cmp_eq_u32 s95,6
s_cbranch_scc1 LCSK12_%=
s_cmp_eq_u32 s95,7
s_cbranch_scc1 LCSK13_%=
s_branch LCSWKR_%=
LCSK03_%=:
s_add_u32 s60,s82,4194304
s_branch LCSWKR_%=
LCSK11_%=:
v_lshrrev_b32 v250,1,v245
v_and_b32 v250,0xf8,v250
s_add_u32 s95,s86,0
buffer_load_dwordx2 v[248:249],v250,s[52:55],s95 offen sc1
s_branch LCSWKR_%=
LCSK12_%=:
W vmcnt(63)
s_mov_b32 s94,0
LCPAa_%=:
v_cmp_ne_u64 vcc,s[72:73],v[248:249]
s_nop 3
s_cmp_eq_u64 vcc,0
s_cbranch_scc1 LCPAb_%=
s_add_u32 s94,s94,1
s_cmp_gt_u32 s94,262144
s_cbranch_scc1 LCPAb_%=
s_sleep 4
v_lshrrev_b32 v250,1,v245
v_and_b32 v250,0xf8,v250
s_add_u32 s95,s86,0
buffer_load_dwordx2 v[248:249],v250,s[52:55],s95 offen sc1
W vmcnt(0)
s_branch LCPAa_%=
LCPAb_%=:
v_lshlrev_b32 v250,10,v227
v_lshl_add_u32 v250,v244,3,v250
s_and_b32 s95,s85,0xfffff000
buffer_load_dwordx2 v[248:249],v250,s[52:55],s95 offen sc1
s_branch LCSWKR_%=
LCSK13_%=:
s_cmp_eq_u32 s90,0x7fffff00
s_cbranch_scc1 LCNOE_%=
W vmcnt(63)
s_mov_b32 s94,0
LCPEa_%=:
v_cmp_ne_u64 vcc,s[72:73],v[248:249]
s_nop 3
s_cmp_eq_u64 vcc,0
s_cbranch_scc1 LCPEb_%=
s_add_u32 s94,s94,1
s_cmp_gt_u32 s94,262144
s_cbranch_scc1 LCPEb_%=
s_sleep 4
v_lshlrev_b32 v250,10,v227
v_lshl_add_u32 v250,v244,3,v250
s_and_b32 s95,s85,0xfffff000
buffer_load_dwordx2 v[248:249],v250,s[52:55],s95 offen sc1
W vmcnt(0)
s_branch LCPEa_%=
LCPEb_%=:
v_mov_b32 v248,s72
v_mov_b32 v249,s73
s_mov_b64 exec,1
buffer_store_dwordx2 v[248:249],off,s[52:55],s90 sc1
s_mov_b64 exec,-1
LCNOE_%=:
s_mov_b32 s40,s16
s_and_b32 s41,s17,0xffff
s_mov_b32 s42,0x400000
s_mov_b32 s43,0x20000
s_mov_b32 s95,1024
v_mad_u32_u24 v214,v227,s95,v246
v_mad_u32_u24 v215,v227,s95,v247
v_add_u32 v215,3072,v215
v_mad_u32_u24 v216,v227,s95,v246
v_add_u32 v216,6144,v216
v_mad_u32_u24 v217,v227,s95,v247
v_add_u32 v217,9216,v217
s_mov_b32 s60,s88
s_mov_b32 s76,16384
s_mov_b32 s77,212992
s_mov_b32 s20,s16
s_and_b32 s21,s17,0xffff
s_mov_b32 s22,0x400000
s_mov_b32 s23,0x20000
v_lshlrev_b32 v250,3,v245
s_and_b32 s94,s30,3
s_lshl_b32 s94,s94,5
s_add_u32 s94,s94,s29
s_lshl_b32 s94,s94,14
s_add_u32 s95,s94,2097152
buffer_load_dword v254,v250,s[20:23],s95 offen sc1
s_add_u32 s95,s94,2105344
buffer_load_dword v254,v250,s[20:23],s95 offen sc1
s_branch LCSWKR_%=
LCSWV_%=:
s_lshl_b32 s95,s70,2
s_add_u32 s95,s95,s69
s_cmp_eq_u32 s95,0
s_cbranch_scc1 LCSV00_%=
s_cmp_eq_u32 s95,3
s_cbranch_scc1 LCSV03_%=
s_cmp_eq_u32 s95,5
s_cbranch_scc1 LCSV11_%=
s_cmp_eq_u32 s95,6
)ASM"
R"ASM(
s_cbranch_scc1 LCSV12_%=
s_cmp_eq_u32 s95,7
s_cbranch_scc1 LCSV13_%=
s_branch LCSWVR_%=
LCSV00_%=:
v_lshlrev_b32 v250,3,v245
s_mov_b32 exec_hi,0xff
buffer_load_dword v254,v250,s[36:39],s92 offen sc1
s_mov_b32 exec_hi,-1
s_branch LCSWVR_%=
LCSV03_%=:
s_mov_b32 s44,s14
s_and_b32 s45,s15,0xffff
s_mov_b32 s46,0x400000
s_mov_b32 s47,0x20000
s_mov_b32 s95,8192
v_mad_u32_u24 v218,v227,s95,v246
v_mad_u32_u24 v219,v227,s95,v247
v_add_u32 v219,64512,v219
v_mad_u32_u24 v220,v227,s95,v246
v_add_u32 v220,129024,v220
v_mad_u32_u24 v221,v227,s95,v247
v_add_u32 v221,193536,v221
s_add_u32 s61,s83,4096
s_mov_b32 s62,32768
s_mov_b32 s63,229376
s_mov_b32 s75,229376
s_mov_b32 s74,0x3fffff
v_lshlrev_b32 v250,3,v245
s_mov_b32 exec_hi,0xff
buffer_load_dword v254,v250,s[36:39],s92 offen sc1
s_mov_b32 exec_hi,-1
s_sub_u32 s94,384,s14
s_and_b32 s94,s94,0x300
v_lshlrev_b32 v250,13,v244
v_lshl_add_u32 v250,v227,10,v250
s_lshl_b32 s95,s29,17
s_add_u32 s95,s95,s94
s_add_u32 s95,s95,4096
buffer_load_dword v254,v250,s[44:47],s95 offen sc1
buffer_load_dword v254,v250,s[44:47],s95 offen offset:128 sc1
s_branch LCSWVR_%=
LCSV11_%=:
v_lshlrev_b32 v250,3,v245
s_mov_b32 s95,648
v_mul_u32_u24 v251,s95,v245
s_and_b32 s94,s85,0xfffffc00
s_sub_u32 s95,s6,20864
s_mov_b64 exec,15
buffer_load_dword v254,v250,s[52:55],s94 offen offset:512 sc1
buffer_load_dword v254,v251,s[52:55],s95 offen sc1
s_mov_b64 exec,-1
s_branch LCSWVR_%=
LCSV12_%=:
s_mov_b32 s20,s16
s_and_b32 s21,s17,0xffff
s_mov_b32 s22,0x400000
s_mov_b32 s23,0x20000
v_lshlrev_b32 v250,3,v245
s_and_b32 s94,s30,3
s_lshl_b32 s94,s94,5
s_add_u32 s94,s94,s29
s_lshl_b32 s94,s94,14
s_add_u32 s95,s94,0
buffer_load_dword v254,v250,s[20:23],s95 offen sc1
s_add_u32 s95,s94,8192
buffer_load_dword v254,v250,s[20:23],s95 offen sc1
s_branch LCSWVR_%=
LCSV13_%=:
s_mov_b32 s44,s16
s_and_b32 s45,s17,0xffff
s_mov_b32 s46,0x400000
s_mov_b32 s47,0x20000
s_mov_b32 s95,1024
v_mad_u32_u24 v218,v227,s95,v246
v_mad_u32_u24 v219,v227,s95,v247
v_add_u32 v219,3072,v219
v_mad_u32_u24 v220,v227,s95,v246
v_add_u32 v220,6144,v220
v_mad_u32_u24 v221,v227,s95,v247
v_add_u32 v221,9216,v221
s_add_u32 s61,s88,65536
s_mov_b32 s62,16384
s_mov_b32 s63,16384
s_mov_b32 s75,212992
s_mov_b32 s74,-1
v_lshlrev_b32 v250,3,v245
s_mov_b32 exec_hi,0xff
buffer_load_dword v254,v250,s[36:39],s92 offen sc1
s_mov_b32 exec_hi,-1
s_branch LCSWVR_%=
)ASM"
R"ASM(
LOP5_%=:
s_mov_b32 s69,0
T v[0:1],v160
T v[8:9],v161
T v[16:17],v162
T v[24:25],v163
T v[32:33],v164
T v[40:41],v165
T v[48:49],v166
T v[56:57],v167
T v[2:3],v160 offset:4096
T v[10:11],v161 offset:4096
T v[18:19],v162 offset:4096
T v[26:27],v163 offset:4096
AR v168,a0
AR v169,a1
AR v170,a2
AR v171,a3
AR v172,a20
AR v173,a21
AR v174,a22
AR v175,a23
v_pk_mul_f32 v[168:169],v[168:169],v[188:189] op_sel_hi:[1,0]
v_pk_mul_f32 v[170:171],v[170:171],v[188:189] op_sel_hi:[1,0]
v_pk_mul_f32 v[172:173],v[172:173],v[188:189] op_sel_hi:[1,0]
v_pk_mul_f32 v[174:175],v[174:175],v[188:189] op_sel_hi:[1,0]
C v64,v168,v169
C v65,v170,v171
C v66,v172,v173
C v67,v174,v175
T v[34:35],v164 offset:4096
T v[42:43],v165 offset:4096
T v[50:51],v166 offset:4096
T v[58:59],v167 offset:4096
T v[4:5],v160 offset:8192
T v[12:13],v161 offset:8192
T v[20:21],v162 offset:8192
T v[28:29],v163 offset:8192
s_add_u32 s94,s84,0
buffer_store_dwordx4 v[64:67],v245,s[52:55],s94 offen sc1
AR v176,a40
AR v177,a41
AR v178,a42
AR v179,a43
AR v180,a60
AR v181,a61
AR v182,a62
AR v183,a63
v_pk_mul_f32 v[176:177],v[176:177],v[188:189] op_sel_hi:[1,0]
v_pk_mul_f32 v[178:179],v[178:179],v[188:189] op_sel_hi:[1,0]
v_pk_mul_f32 v[180:181],v[180:181],v[188:189] op_sel_hi:[1,0]
v_pk_mul_f32 v[182:183],v[182:183],v[188:189] op_sel_hi:[1,0]
C v68,v176,v177
C v69,v178,v179
C v70,v180,v181
C v71,v182,v183
T v[36:37],v164 offset:8192
T v[44:45],v165 offset:8192
T v[52:53],v166 offset:8192
T v[60:61],v167 offset:8192
T v[6:7],v160 offset:12288
T v[14:15],v161 offset:12288
T v[22:23],v162 offset:12288
T v[30:31],v163 offset:12288
s_add_u32 s94,s84,5120
buffer_store_dwordx4 v[68:71],v245,s[52:55],s94 offen sc1
AR v168,a80
AR v169,a81
AR v170,a82
AR v171,a83
AR v172,a100
AR v173,a101
AR v174,a102
AR v175,a103
v_pk_mul_f32 v[168:169],v[168:169],v[188:189] op_sel_hi:[1,0]
v_pk_mul_f32 v[170:171],v[170:171],v[188:189] op_sel_hi:[1,0]
v_pk_mul_f32 v[172:173],v[172:173],v[188:189] op_sel_hi:[1,0]
v_pk_mul_f32 v[174:175],v[174:175],v[188:189] op_sel_hi:[1,0]
C v72,v168,v169
C v73,v170,v171
C v74,v172,v173
C v75,v174,v175
T v[38:39],v164 offset:12288
T v[46:47],v165 offset:12288
T v[54:55],v166 offset:12288
T v[62:63],v167 offset:12288
AR v176,a120
AR v177,a121
AR v178,a122
AR v179,a123
AR v180,a140
AR v181,a141
AR v182,a142
AR v183,a143
v_pk_mul_f32 v[176:177],v[176:177],v[188:189] op_sel_hi:[1,0]
v_pk_mul_f32 v[178:179],v[178:179],v[188:189] op_sel_hi:[1,0]
v_pk_mul_f32 v[180:181],v[180:181],v[188:189] op_sel_hi:[1,0]
v_pk_mul_f32 v[182:183],v[182:183],v[188:189] op_sel_hi:[1,0]
C v76,v176,v177
C v77,v178,v179
C v78,v180,v181
C v79,v182,v183
)ASM"
R"ASM(
W lgkmcnt(4)
W vmcnt(20)
M a[0:3],v[0:3],a[224:227],0
W vmcnt(63)
s_barrier
AR v168,a4
AR v169,a5
AR v170,a6
AR v171,a7
AR v172,a24
M a[0:3],v[4:7],a[228:231],a[0:3]
T a[160:161],v160 offset:16384
AR v173,a25
AR v174,a26
AR v175,a27
U v168,v168,v190
U v169,v169,v190
M a[20:23],v[12:15],a[228:231],0
U v170,v170,v190
U v171,v171,v190
U v172,v172,v190
U v173,v173,v190
U v174,v174,v190
M a[20:23],v[8:11],a[224:227],a[20:23]
s_add_u32 m0,s78,s5
T a[168:169],v161 offset:16384
U v175,v175,v190
C v120,v168,v169
C v121,v170,v171
C v122,v172,v173
C v123,v174,v175
M a[40:43],v[16:19],a[224:227],0
B v218,s[44:47],s80 offen lds
AR v176,a44
AR v177,a45
AR v178,a46
AR v179,a47
AR v180,a64
M a[40:43],v[20:23],a[228:231],a[40:43]
s_add_u32 s94,s84,10240
T a[176:177],v162 offset:16384
AR v181,a65
AR v182,a66
AR v183,a67
U v176,v176,v190
U v177,v177,v190
M a[60:63],v[28:31],a[228:231],0
buffer_store_dwordx4 v[72:75],v245,s[52:55],s94 offen sc1
U v178,v178,v190
U v179,v179,v190
U v180,v180,v190
U v181,v181,v190
U v182,v182,v190
M a[60:63],v[24:27],a[224:227],a[60:63]
W lgkmcnt(3)
T a[184:185],v163 offset:16384
U v183,v183,v190
C v124,v176,v177
C v125,v178,v179
C v126,v180,v181
C v127,v182,v183
M a[80:83],v[32:35],a[224:227],0
AR v168,a84
AR v169,a85
AR v170,a86
AR v171,a87
AR v172,a104
M a[80:83],v[36:39],a[228:231],a[80:83]
T a[192:193],v164 offset:16384
AR v173,a105
AR v174,a106
AR v175,a107
U v168,v168,v190
U v169,v169,v190
M a[100:103],v[44:47],a[228:231],0
U v170,v170,v190
U v171,v171,v190
U v172,v172,v190
U v173,v173,v190
U v174,v174,v190
M a[100:103],v[40:43],a[224:227],a[100:103]
s_add_u32 s94,s84,15360
T a[200:201],v165 offset:16384
U v175,v175,v190
C v128,v168,v169
C v129,v170,v171
C v130,v172,v173
C v131,v174,v175
M a[120:123],v[48:51],a[224:227],0
buffer_store_dwordx4 v[76:79],v245,s[52:55],s94 offen sc1
AR v176,a124
AR v177,a125
AR v178,a126
AR v179,a127
AR v180,a144
M a[120:123],v[52:55],a[228:231],a[120:123]
T a[208:209],v166 offset:16384
AR v181,a145
AR v182,a146
AR v183,a147
U v176,v176,v190
U v177,v177,v190
M a[140:143],v[60:63],a[228:231],0
B v219,s[44:47],s80 offen offset:1024 lds
U v178,v178,v190
U v179,v179,v190
U v180,v180,v190
U v181,v181,v190
U v182,v182,v190
M a[140:143],v[56:59],a[224:227],a[140:143]
W vmcnt(22)
T a[216:217],v167 offset:16384
U v183,v183,v190
C v132,v176,v177
C v133,v178,v179
C v134,v180,v181
C v135,v182,v183
M a[4:7],v[0:3],a[232:235],0
AR v168,a8
AR v169,a9
AR v170,a10
AR v171,a11
AR v172,a28
M a[4:7],v[4:7],a[236:239],a[4:7]
AR v173,a29
AR v174,a30
AR v175,a31
U v168,v168,v192
U v169,v169,v192
M a[24:27],v[12:15],a[236:239],0
U v170,v170,v192
U v171,v171,v192
U v172,v172,v192
U v173,v173,v192
U v174,v174,v192
M a[24:27],v[8:11],a[232:235],a[24:27]
s_add_u32 s94,s84,1024
U v175,v175,v192
C v136,v168,v169
C v137,v170,v171
C v138,v172,v173
C v139,v174,v175
M a[44:47],v[16:19],a[232:235],0
buffer_store_dwordx4 v[120:123],v245,s[52:55],s94 offen sc1
AR v176,a48
AR v177,a49
AR v178,a50
AR v179,a51
AR v180,a68
M a[44:47],v[20:23],a[236:239],a[44:47]
T a[162:163],v160 offset:20480
AR v181,a69
AR v182,a70
AR v183,a71
U v176,v176,v192
U v177,v177,v192
M a[64:67],v[28:31],a[236:239],0
B v220,s[44:47],s80 offen offset:2048 lds
U v178,v178,v192
U v179,v179,v192
U v180,v180,v192
U v181,v181,v192
U v182,v182,v192
M a[64:67],v[24:27],a[232:235],a[64:67]
T a[170:171],v161 offset:20480
U v183,v183,v192
C v140,v176,v177
C v141,v178,v179
C v142,v180,v181
C v143,v182,v183
M a[84:87],v[32:35],a[232:235],0
AR v168,a88
AR v169,a89
AR v170,a90
AR v171,a91
AR v172,a108
M a[84:87],v[36:39],a[236:239],a[84:87]
T a[178:179],v162 offset:20480
AR v173,a109
AR v174,a110
AR v175,a111
U v168,v168,v192
U v169,v169,v192
M a[104:107],v[44:47],a[236:239],0
U v170,v170,v192
U v171,v171,v192
U v172,v172,v192
U v173,v173,v192
U v174,v174,v192
M a[104:107],v[40:43],a[232:235],a[104:107]
s_add_u32 s94,s84,6144
T a[186:187],v163 offset:20480
U v175,v175,v192
C v144,v168,v169
C v145,v170,v171
C v146,v172,v173
C v147,v174,v175
M a[124:127],v[48:51],a[232:235],0
buffer_store_dwordx4 v[124:127],v245,s[52:55],s94 offen sc1
AR v176,a128
AR v177,a129
AR v178,a130
AR v179,a131
AR v180,a148
M a[124:127],v[52:55],a[236:239],a[124:127]
T a[194:195],v164 offset:20480
AR v181,a149
AR v182,a150
AR v183,a151
U v176,v176,v192
U v177,v177,v192
M a[144:147],v[60:63],a[236:239],0
U v178,v178,v192
U v179,v179,v192
U v180,v180,v192
U v181,v181,v192
U v182,v182,v192
M a[144:147],v[56:59],a[232:235],a[144:147]
W vmcnt(23)
T a[202:203],v165 offset:20480
U v183,v183,v192
C v148,v176,v177
C v149,v178,v179
C v150,v180,v181
C v151,v182,v183
M a[8:11],v[0:3],a[240:243],0
B v221,s[44:47],s80 offen offset:3072 lds
s_add_u32 s80,s80,16384
AR v168,a12
AR v169,a13
AR v170,a14
AR v171,a15
AR v172,a32
M a[8:11],v[4:7],a[244:247],a[8:11]
T a[210:211],v166 offset:20480
AR v173,a33
AR v174,a34
AR v175,a35
U v168,v168,v194
U v169,v169,v194
M a[28:31],v[12:15],a[244:247],0
U v170,v170,v194
U v171,v171,v194
U v172,v172,v194
U v173,v173,v194
U v174,v174,v194
M a[28:31],v[8:11],a[240:243],a[28:31]
s_add_u32 s94,s84,11264
T a[218:219],v167 offset:20480
U v175,v175,v194
C v152,v168,v169
C v153,v170,v171
C v154,v172,v173
C v155,v174,v175
M a[48:51],v[16:19],a[240:243],0
buffer_store_dwordx4 v[128:131],v245,s[52:55],s94 offen sc1
AR v176,a52
AR v177,a53
AR v178,a54
AR v179,a55
AR v180,a72
M a[48:51],v[20:23],a[244:247],a[48:51]
AR v181,a73
AR v182,a74
AR v183,a75
U v176,v176,v194
U v177,v177,v194
M a[68:71],v[28:31],a[244:247],0
U v178,v178,v194
U v179,v179,v194
U v180,v180,v194
U v181,v181,v194
U v182,v182,v194
M a[68:71],v[24:27],a[240:243],a[68:71]
s_add_u32 m0,m0,4096
U v183,v183,v194
C v156,v176,v177
C v157,v178,v179
C v158,v180,v181
C v159,v182,v183
M a[88:91],v[32:35],a[240:243],0
B v218,s[44:47],s80 offen lds
AR v168,a92
AR v169,a93
AR v170,a94
AR v171,a95
AR v172,a112
M a[88:91],v[36:39],a[244:247],a[88:91]
T a[164:165],v160 offset:24576
AR v173,a113
AR v174,a114
AR v175,a115
U v168,v168,v194
U v169,v169,v194
M a[108:111],v[44:47],a[244:247],0
U v170,v170,v194
U v171,v171,v194
U v172,v172,v194
U v173,v173,v194
U v174,v174,v194
)ASM"
R"ASM(
M a[108:111],v[40:43],a[240:243],a[108:111]
s_add_u32 s94,s84,16384
T a[172:173],v161 offset:24576
U v175,v175,v194
C v198,v168,v169
C v199,v170,v171
C v200,v172,v173
C v201,v174,v175
M a[128:131],v[48:51],a[240:243],0
buffer_store_dwordx4 v[132:135],v245,s[52:55],s94 offen sc1
AR v176,a132
AR v177,a133
AR v178,a134
AR v179,a135
AR v180,a152
M a[128:131],v[52:55],a[244:247],a[128:131]
T a[180:181],v162 offset:24576
AR v181,a153
AR v182,a154
AR v183,a155
U v176,v176,v194
U v177,v177,v194
M a[148:151],v[60:63],a[244:247],0
U v178,v178,v194
U v179,v179,v194
U v180,v180,v194
U v181,v181,v194
U v182,v182,v194
M a[148:151],v[56:59],a[240:243],a[148:151]
W vmcnt(25)
T a[188:189],v163 offset:24576
U v183,v183,v194
C v202,v176,v177
C v203,v178,v179
C v204,v180,v181
C v205,v182,v183
M a[12:15],v[0:3],a[248:251],0
AR v168,a16
AR v169,a17
AR v170,a18
AR v171,a19
AR v172,a36
M a[12:15],v[4:7],a[252:255],a[12:15]
T a[196:197],v164 offset:24576
AR v173,a37
AR v174,a38
AR v175,a39
U v168,v168,v196
U v169,v169,v196
M a[32:35],v[12:15],a[252:255],0
B v219,s[44:47],s80 offen offset:1024 lds
U v170,v170,v196
U v171,v171,v196
U v172,v172,v196
U v173,v173,v196
U v174,v174,v196
M a[32:35],v[8:11],a[248:251],a[32:35]
s_add_u32 s94,s84,2048
T a[204:205],v165 offset:24576
U v175,v175,v196
C v206,v168,v169
C v207,v170,v171
C v208,v172,v173
C v209,v174,v175
M a[52:55],v[16:19],a[248:251],0
buffer_store_dwordx4 v[136:139],v245,s[52:55],s94 offen sc1
AR v176,a56
AR v177,a57
AR v178,a58
AR v179,a59
AR v180,a76
M a[52:55],v[20:23],a[252:255],a[52:55]
T a[212:213],v166 offset:24576
AR v181,a77
AR v182,a78
AR v183,a79
U v176,v176,v196
U v177,v177,v196
M a[72:75],v[28:31],a[252:255],0
U v178,v178,v196
U v179,v179,v196
U v180,v180,v196
U v181,v181,v196
U v182,v182,v196
M a[72:75],v[24:27],a[248:251],a[72:75]
T a[220:221],v167 offset:24576
U v183,v183,v196
C v210,v176,v177
C v211,v178,v179
C v212,v180,v181
C v213,v182,v183
M a[92:95],v[32:35],a[248:251],0
AR v168,a96
AR v169,a97
AR v170,a98
AR v171,a99
AR v172,a116
M a[92:95],v[36:39],a[252:255],a[92:95]
AR v173,a117
AR v174,a118
AR v175,a119
U v168,v168,v196
U v169,v169,v196
M a[112:115],v[44:47],a[252:255],0
B v220,s[44:47],s80 offen offset:2048 lds
U v170,v170,v196
U v171,v171,v196
U v172,v172,v196
U v173,v173,v196
U v174,v174,v196
M a[112:115],v[40:43],a[248:251],a[112:115]
s_add_u32 s94,s84,7168
U v175,v175,v196
C v214,v168,v169
C v215,v170,v171
C v216,v172,v173
C v217,v174,v175
M a[132:135],v[48:51],a[248:251],0
buffer_store_dwordx4 v[140:143],v245,s[52:55],s94 offen sc1
AR v176,a136
AR v177,a137
AR v178,a138
AR v179,a139
AR v180,a156
M a[132:135],v[52:55],a[252:255],a[132:135]
T a[166:167],v160 offset:28672
AR v181,a157
AR v182,a158
AR v183,a159
U v176,v176,v196
U v177,v177,v196
M a[152:155],v[60:63],a[252:255],0
U v178,v178,v196
U v179,v179,v196
U v180,v180,v196
U v181,v181,v196
U v182,v182,v196
M a[152:155],v[56:59],a[248:251],a[152:155]
W vmcnt(27)
T a[174:175],v161 offset:28672
U v183,v183,v196
C v236,v176,v177
C v237,v178,v179
C v238,v180,v181
C v239,v182,v183
M a[16:19],v[0:3],v[228:231],0
M a[16:19],v[4:7],v[232:235],a[16:19]
T a[182:183],v162 offset:28672
M a[36:39],v[12:15],v[232:235],0
M a[36:39],v[8:11],v[228:231],a[36:39]
T a[190:191],v163 offset:28672
M a[56:59],v[16:19],v[228:231],0
B v221,s[44:47],s80 offen offset:3072 lds
s_add_u32 s80,s80,245760
s_add_u32 s5,s5,16384
s_and_b32 s5,s5,0xc000
M a[56:59],v[20:23],v[232:235],a[56:59]
s_add_u32 s94,s84,12288
T a[198:199],v164 offset:28672
M a[76:79],v[28:31],v[232:235],0
buffer_store_dwordx4 v[144:147],v245,s[52:55],s94 offen sc1
M a[76:79],v[24:27],v[228:231],a[76:79]
T a[206:207],v165 offset:28672
M a[96:99],v[32:35],v[228:231],0
M a[96:99],v[36:39],v[232:235],a[96:99]
s_add_u32 s94,s84,17408
T a[214:215],v166 offset:28672
M a[116:119],v[44:47],v[232:235],0
buffer_store_dwordx4 v[148:151],v245,s[52:55],s94 offen sc1
M a[116:119],v[40:43],v[228:231],a[116:119]
T a[222:223],v167 offset:28672
M a[136:139],v[48:51],v[228:231],0
M a[136:139],v[52:55],v[232:235],a[136:139]
M a[156:159],v[60:63],v[232:235],0
M a[156:159],v[56:59],v[228:231],a[156:159]
)ASM"
R"ASM(
W lgkmcnt(4)
W vmcnt(28)
M a[0:3],a[160:163],v[80:83],a[0:3]
W vmcnt(20)
s_barrier
v_xor_b32 v160,0x8000,v160
v_xor_b32 v161,0x8000,v161
M a[0:3],a[164:167],v[84:87],a[0:3]
T v[0:1],v160
M a[20:23],a[172:175],v[84:87],a[20:23]
B a[224:227],v245,s[96:99],s79 offen sc1
v_xor_b32 v162,0x8000,v162
M a[20:23],a[168:171],v[80:83],a[20:23]
s_add_u32 m0,s78,s5
T v[8:9],v161
M a[40:43],a[176:179],v[80:83],a[40:43]
B v218,s[44:47],s80 offen lds
v_xor_b32 v163,0x8000,v163
M a[40:43],a[180:183],v[84:87],a[40:43]
T v[16:17],v162
M a[60:63],a[188:191],v[84:87],a[60:63]
v_xor_b32 v164,0x8000,v164
M a[60:63],a[184:187],v[80:83],a[60:63]
W lgkmcnt(3)
T v[24:25],v163
M a[80:83],a[192:195],v[80:83],a[80:83]
B a[228:231],v240,s[96:99],s79 offen offset:1024 sc1
v_xor_b32 v165,0x8000,v165
M a[80:83],a[196:199],v[84:87],a[80:83]
s_add_u32 s94,s84,3072
T v[32:33],v164
M a[100:103],a[204:207],v[84:87],a[100:103]
buffer_store_dwordx4 v[152:155],v245,s[52:55],s94 offen sc1
v_xor_b32 v166,0x8000,v166
M a[100:103],a[200:203],v[80:83],a[100:103]
T v[40:41],v165
M a[120:123],a[208:211],v[80:83],a[120:123]
v_xor_b32 v167,0x8000,v167
M a[120:123],a[212:215],v[84:87],a[120:123]
T v[48:49],v166
M a[140:143],a[220:223],v[84:87],a[140:143]
B v219,s[44:47],s80 offen offset:1024 lds
M a[140:143],a[216:219],v[80:83],a[140:143]
W vmcnt(31)
T v[56:57],v167
M a[4:7],a[160:163],v[88:91],a[4:7]
M a[4:7],a[164:167],v[92:95],a[4:7]
M a[24:27],a[172:175],v[92:95],a[24:27]
B a[232:235],v245,s[96:99],s79 offen offset:1024 sc1
M a[24:27],a[168:171],v[88:91],a[24:27]
M a[44:47],a[176:179],v[88:91],a[44:47]
M a[44:47],a[180:183],v[92:95],a[44:47]
T v[2:3],v160 offset:4096
M a[64:67],a[188:191],v[92:95],a[64:67]
B v220,s[44:47],s80 offen offset:2048 lds
M a[64:67],a[184:187],v[88:91],a[64:67]
T v[10:11],v161 offset:4096
M a[84:87],a[192:195],v[88:91],a[84:87]
B a[236:239],v240,s[96:99],s79 offen offset:2048 sc1
M a[84:87],a[196:199],v[92:95],a[84:87]
T v[18:19],v162 offset:4096
M a[104:107],a[204:207],v[92:95],a[104:107]
M a[104:107],a[200:203],v[88:91],a[104:107]
T v[26:27],v163 offset:4096
M a[124:127],a[208:211],v[88:91],a[124:127]
M a[124:127],a[212:215],v[92:95],a[124:127]
s_add_u32 s94,s84,8192
T v[34:35],v164 offset:4096
M a[144:147],a[220:223],v[92:95],a[144:147]
buffer_store_dwordx4 v[156:159],v245,s[52:55],s94 offen sc1
M a[144:147],a[216:219],v[88:91],a[144:147]
W vmcnt(33)
T v[42:43],v165 offset:4096
M a[8:11],a[160:163],v[96:99],a[8:11]
B v221,s[44:47],s80 offen offset:3072 lds
s_add_u32 s80,s80,16384
M a[8:11],a[164:167],v[100:103],a[8:11]
T v[50:51],v166 offset:4096
M a[28:31],a[172:175],v[100:103],a[28:31]
B a[240:243],v245,s[96:99],s79 offen offset:2048 sc1
M a[28:31],a[168:171],v[96:99],a[28:31]
T v[58:59],v167 offset:4096
M a[48:51],a[176:179],v[96:99],a[48:51]
M a[48:51],a[180:183],v[100:103],a[48:51]
M a[68:71],a[188:191],v[100:103],a[68:71]
M a[68:71],a[184:187],v[96:99],a[68:71]
s_add_u32 m0,m0,4096
M a[88:91],a[192:195],v[96:99],a[88:91]
B v218,s[44:47],s80 offen lds
M a[88:91],a[196:199],v[100:103],a[88:91]
T v[4:5],v160 offset:8192
M a[108:111],a[204:207],v[100:103],a[108:111]
B a[244:247],v240,s[96:99],s79 offen offset:3072 sc1
M a[108:111],a[200:203],v[96:99],a[108:111]
T v[12:13],v161 offset:8192
M a[128:131],a[208:211],v[96:99],a[128:131]
M a[128:131],a[212:215],v[100:103],a[128:131]
T v[20:21],v162 offset:8192
M a[148:151],a[220:223],v[100:103],a[148:151]
M a[148:151],a[216:219],v[96:99],a[148:151]
W vmcnt(35)
T v[28:29],v163 offset:8192
M a[12:15],a[160:163],v[104:107],a[12:15]
M a[12:15],a[164:167],v[108:111],a[12:15]
T v[36:37],v164 offset:8192
M a[32:35],a[172:175],v[108:111],a[32:35]
B v219,s[44:47],s80 offen offset:1024 lds
M a[32:35],a[168:171],v[104:107],a[32:35]
T v[44:45],v165 offset:8192
M a[52:55],a[176:179],v[104:107],a[52:55]
B a[248:251],v245,s[96:99],s79 offen offset:3072 sc1
s_add_u32 s94,s84,13312
M a[52:55],a[180:183],v[108:111],a[52:55]
T v[52:53],v166 offset:8192
buffer_store_dwordx4 v[198:201],v245,s[52:55],s94 offen sc1
M a[72:75],a[188:191],v[108:111],a[72:75]
M a[72:75],a[184:187],v[104:107],a[72:75]
T v[60:61],v167 offset:8192
M a[92:95],a[192:195],v[104:107],a[92:95]
B a[252:255],v241,s[96:99],s79 offen sc1
M a[92:95],a[196:199],v[108:111],a[92:95]
M a[112:115],a[204:207],v[108:111],a[112:115]
B v220,s[44:47],s80 offen offset:2048 lds
M a[112:115],a[200:203],v[104:107],a[112:115]
M a[132:135],a[208:211],v[104:107],a[132:135]
M a[132:135],a[212:215],v[108:111],a[132:135]
T v[6:7],v160 offset:12288
M a[152:155],a[220:223],v[108:111],a[152:155]
M a[152:155],a[216:219],v[104:107],a[152:155]
W vmcnt(38)
T v[14:15],v161 offset:12288
M a[16:19],a[160:163],v[112:115],a[16:19]
M a[16:19],a[164:167],v[116:119],a[16:19]
T v[22:23],v162 offset:12288
M a[36:39],a[172:175],v[116:119],a[36:39]
B v[228:231],v240,s[96:99],s79 offen sc1
M a[36:39],a[168:171],v[112:115],a[36:39]
T v[30:31],v163 offset:12288
M a[56:59],a[176:179],v[112:115],a[56:59]
B v221,s[44:47],s80 offen offset:3072 lds
s_add_u32 s80,s80,245760
s_add_u32 s5,s5,16384
s_and_b32 s5,s5,0xc000
M a[56:59],a[180:183],v[116:119],a[56:59]
s_add_u32 s94,s84,18432
T v[38:39],v164 offset:12288
M a[76:79],a[188:191],v[116:119],a[76:79]
buffer_store_dwordx4 v[202:205],v245,s[52:55],s94 offen sc1
M a[76:79],a[184:187],v[112:115],a[76:79]
T v[46:47],v165 offset:12288
M a[96:99],a[192:195],v[112:115],a[96:99]
B v[232:235],v241,s[96:99],s79 offen offset:1024 sc1
s_add_u32 s79,s79,40960
M a[96:99],a[196:199],v[116:119],a[96:99]
T v[54:55],v166 offset:12288
M a[116:119],a[204:207],v[116:119],a[116:119]
M a[116:119],a[200:203],v[112:115],a[116:119]
T v[62:63],v167 offset:12288
M a[136:139],a[208:211],v[112:115],a[136:139]
M a[136:139],a[212:215],v[116:119],a[136:139]
M a[156:159],a[220:223],v[116:119],a[156:159]
M a[156:159],a[216:219],v[112:115],a[156:159]
)ASM"
R"ASM(
W lgkmcnt(4)
W vmcnt(19)
M a[0:3],v[0:3],a[224:227],a[0:3]
W vmcnt(63)
s_barrier
M a[0:3],v[4:7],a[228:231],a[0:3]
T a[160:161],v160 offset:16384
M a[20:23],v[12:15],a[228:231],a[20:23]
B v[80:83],v245,s[96:99],s79 offen sc1
M a[20:23],v[8:11],a[224:227],a[20:23]
s_add_u32 m0,s78,s5
T a[168:169],v161 offset:16384
M a[40:43],v[16:19],a[224:227],a[40:43]
B v218,s[44:47],s80 offen lds
M a[40:43],v[20:23],a[228:231],a[40:43]
T a[176:177],v162 offset:16384
M a[60:63],v[28:31],a[228:231],a[60:63]
M a[60:63],v[24:27],a[224:227],a[60:63]
W lgkmcnt(3)
T a[184:185],v163 offset:16384
M a[80:83],v[32:35],a[224:227],a[80:83]
B v[84:87],v240,s[96:99],s79 offen offset:1024 sc1
M a[80:83],v[36:39],a[228:231],a[80:83]
s_add_u32 s94,s33,4096
T a[192:193],v164 offset:16384
M a[100:103],v[44:47],a[228:231],a[100:103]
buffer_store_dwordx4 v[206:209],v245,s[52:55],s94 offen sc1
M a[100:103],v[40:43],a[224:227],a[100:103]
T a[200:201],v165 offset:16384
M a[120:123],v[48:51],a[224:227],a[120:123]
M a[120:123],v[52:55],a[228:231],a[120:123]
T a[208:209],v166 offset:16384
M a[140:143],v[60:63],a[228:231],a[140:143]
B v219,s[44:47],s80 offen offset:1024 lds
M a[140:143],v[56:59],a[224:227],a[140:143]
W vmcnt(19)
T a[216:217],v167 offset:16384
M a[4:7],v[0:3],a[232:235],a[4:7]
M a[4:7],v[4:7],a[236:239],a[4:7]
M a[24:27],v[12:15],a[236:239],a[24:27]
B v[88:91],v245,s[96:99],s79 offen offset:1024 sc1
M a[24:27],v[8:11],a[232:235],a[24:27]
M a[44:47],v[16:19],a[232:235],a[44:47]
M a[44:47],v[20:23],a[236:239],a[44:47]
T a[162:163],v160 offset:20480
M a[64:67],v[28:31],a[236:239],a[64:67]
B v220,s[44:47],s80 offen offset:2048 lds
M a[64:67],v[24:27],a[232:235],a[64:67]
T a[170:171],v161 offset:20480
M a[84:87],v[32:35],a[232:235],a[84:87]
B v[92:95],v240,s[96:99],s79 offen offset:2048 sc1
M a[84:87],v[36:39],a[236:239],a[84:87]
T a[178:179],v162 offset:20480
M a[104:107],v[44:47],a[236:239],a[104:107]
M a[104:107],v[40:43],a[232:235],a[104:107]
T a[186:187],v163 offset:20480
M a[124:127],v[48:51],a[232:235],a[124:127]
M a[124:127],v[52:55],a[236:239],a[124:127]
s_add_u32 s94,s33,9216
T a[194:195],v164 offset:20480
M a[144:147],v[60:63],a[236:239],a[144:147]
buffer_store_dwordx4 v[210:213],v245,s[52:55],s94 offen sc1
M a[144:147],v[56:59],a[232:235],a[144:147]
W vmcnt(18)
T a[202:203],v165 offset:20480
M a[8:11],v[0:3],a[240:243],a[8:11]
B v221,s[44:47],s80 offen offset:3072 lds
s_add_u32 s80,s80,16384
M a[8:11],v[4:7],a[244:247],a[8:11]
T a[210:211],v166 offset:20480
M a[28:31],v[12:15],a[244:247],a[28:31]
B v[96:99],v245,s[96:99],s79 offen offset:2048 sc1
M a[28:31],v[8:11],a[240:243],a[28:31]
T a[218:219],v167 offset:20480
M a[48:51],v[16:19],a[240:243],a[48:51]
M a[48:51],v[20:23],a[244:247],a[48:51]
M a[68:71],v[28:31],a[244:247],a[68:71]
M a[68:71],v[24:27],a[240:243],a[68:71]
s_add_u32 m0,m0,4096
M a[88:91],v[32:35],a[240:243],a[88:91]
B v218,s[44:47],s80 offen lds
M a[88:91],v[36:39],a[244:247],a[88:91]
T a[164:165],v160 offset:24576
M a[108:111],v[44:47],a[244:247],a[108:111]
B v[100:103],v240,s[96:99],s79 offen offset:3072 sc1
M a[108:111],v[40:43],a[240:243],a[108:111]
T a[172:173],v161 offset:24576
M a[128:131],v[48:51],a[240:243],a[128:131]
M a[128:131],v[52:55],a[244:247],a[128:131]
T a[180:181],v162 offset:24576
M a[148:151],v[60:63],a[244:247],a[148:151]
M a[148:151],v[56:59],a[240:243],a[148:151]
W vmcnt(18)
T a[188:189],v163 offset:24576
M a[12:15],v[0:3],a[248:251],a[12:15]
M a[12:15],v[4:7],a[252:255],a[12:15]
T a[196:197],v164 offset:24576
M a[32:35],v[12:15],a[252:255],a[32:35]
B v219,s[44:47],s80 offen offset:1024 lds
M a[32:35],v[8:11],a[248:251],a[32:35]
T a[204:205],v165 offset:24576
M a[52:55],v[16:19],a[248:251],a[52:55]
B v[104:107],v245,s[96:99],s79 offen offset:3072 sc1
s_add_u32 s94,s33,14336
M a[52:55],v[20:23],a[252:255],a[52:55]
T a[212:213],v166 offset:24576
buffer_store_dwordx4 v[214:217],v245,s[52:55],s94 offen sc1
M a[72:75],v[28:31],a[252:255],a[72:75]
M a[72:75],v[24:27],a[248:251],a[72:75]
T a[220:221],v167 offset:24576
M a[92:95],v[32:35],a[248:251],a[92:95]
B v[108:111],v241,s[96:99],s79 offen sc1
M a[92:95],v[36:39],a[252:255],a[92:95]
M a[112:115],v[44:47],a[252:255],a[112:115]
B v220,s[44:47],s80 offen offset:2048 lds
M a[112:115],v[40:43],a[248:251],a[112:115]
M a[132:135],v[48:51],a[248:251],a[132:135]
M a[132:135],v[52:55],a[252:255],a[132:135]
T a[166:167],v160 offset:28672
M a[152:155],v[60:63],a[252:255],a[152:155]
M a[152:155],v[56:59],a[248:251],a[152:155]
W vmcnt(18)
T a[174:175],v161 offset:28672
M a[16:19],v[0:3],v[228:231],a[16:19]
M a[16:19],v[4:7],v[232:235],a[16:19]
T a[182:183],v162 offset:28672
M a[36:39],v[12:15],v[232:235],a[36:39]
B v[112:115],v240,s[96:99],s79 offen sc1
M a[36:39],v[8:11],v[228:231],a[36:39]
T a[190:191],v163 offset:28672
M a[56:59],v[16:19],v[228:231],a[56:59]
B v221,s[44:47],s80 offen offset:3072 lds
s_add_u32 s80,s80,245760
s_add_u32 s5,s5,16384
s_and_b32 s5,s5,0xc000
M a[56:59],v[20:23],v[232:235],a[56:59]
s_add_u32 s94,s33,19456
T a[198:199],v164 offset:28672
M a[76:79],v[28:31],v[232:235],a[76:79]
buffer_store_dwordx4 v[236:239],v245,s[52:55],s94 offen sc1
M a[76:79],v[24:27],v[228:231],a[76:79]
T a[206:207],v165 offset:28672
M a[96:99],v[32:35],v[228:231],a[96:99]
B v[116:119],v241,s[96:99],s79 offen offset:1024 sc1
s_add_u32 s79,s79,40960
M a[96:99],v[36:39],v[232:235],a[96:99]
T a[214:215],v166 offset:28672
M a[116:119],v[44:47],v[232:235],a[116:119]
M a[116:119],v[40:43],v[228:231],a[116:119]
T a[222:223],v167 offset:28672
M a[136:139],v[48:51],v[228:231],a[136:139]
M a[136:139],v[52:55],v[232:235],a[136:139]
M a[156:159],v[60:63],v[232:235],a[156:159]
M a[156:159],v[56:59],v[228:231],a[156:159]
)ASM"
R"ASM(
.p2align 4
LOPL5_%=:
s_cmp_eq_u32 s69,2
s_cbranch_scc1 LOPA75_%=
LOPN5_%=:
)ASM"
R"ASM(
W lgkmcnt(4)
W vmcnt(15)
M a[0:3],a[160:163],v[80:83],a[0:3]
W vmcnt(63)
s_barrier
v_xor_b32 v160,0x8000,v160
v_xor_b32 v161,0x8000,v161
M a[0:3],a[164:167],v[84:87],a[0:3]
T v[0:1],v160
M a[20:23],a[172:175],v[84:87],a[20:23]
B a[224:227],v245,s[96:99],s79 offen sc1
v_xor_b32 v162,0x8000,v162
M a[20:23],a[168:171],v[80:83],a[20:23]
s_add_u32 m0,s78,s5
T v[8:9],v161
M a[40:43],a[176:179],v[80:83],a[40:43]
B v218,s[44:47],s80 offen lds
v_xor_b32 v163,0x8000,v163
M a[40:43],a[180:183],v[84:87],a[40:43]
T v[16:17],v162
M a[60:63],a[188:191],v[84:87],a[60:63]
v_xor_b32 v164,0x8000,v164
M a[60:63],a[184:187],v[80:83],a[60:63]
W lgkmcnt(3)
T v[24:25],v163
M a[80:83],a[192:195],v[80:83],a[80:83]
B a[228:231],v240,s[96:99],s79 offen offset:1024 sc1
v_xor_b32 v165,0x8000,v165
M a[80:83],a[196:199],v[84:87],a[80:83]
T v[32:33],v164
M a[100:103],a[204:207],v[84:87],a[100:103]
v_xor_b32 v166,0x8000,v166
M a[100:103],a[200:203],v[80:83],a[100:103]
T v[40:41],v165
M a[120:123],a[208:211],v[80:83],a[120:123]
v_xor_b32 v167,0x8000,v167
M a[120:123],a[212:215],v[84:87],a[120:123]
T v[48:49],v166
M a[140:143],a[220:223],v[84:87],a[140:143]
B v219,s[44:47],s80 offen offset:1024 lds
M a[140:143],a[216:219],v[80:83],a[140:143]
W vmcnt(15)
T v[56:57],v167
M a[4:7],a[160:163],v[88:91],a[4:7]
M a[4:7],a[164:167],v[92:95],a[4:7]
M a[24:27],a[172:175],v[92:95],a[24:27]
B a[232:235],v245,s[96:99],s79 offen offset:1024 sc1
M a[24:27],a[168:171],v[88:91],a[24:27]
M a[44:47],a[176:179],v[88:91],a[44:47]
M a[44:47],a[180:183],v[92:95],a[44:47]
T v[2:3],v160 offset:4096
M a[64:67],a[188:191],v[92:95],a[64:67]
B v220,s[44:47],s80 offen offset:2048 lds
M a[64:67],a[184:187],v[88:91],a[64:67]
T v[10:11],v161 offset:4096
M a[84:87],a[192:195],v[88:91],a[84:87]
B a[236:239],v240,s[96:99],s79 offen offset:2048 sc1
M a[84:87],a[196:199],v[92:95],a[84:87]
T v[18:19],v162 offset:4096
M a[104:107],a[204:207],v[92:95],a[104:107]
M a[104:107],a[200:203],v[88:91],a[104:107]
T v[26:27],v163 offset:4096
M a[124:127],a[208:211],v[88:91],a[124:127]
M a[124:127],a[212:215],v[92:95],a[124:127]
T v[34:35],v164 offset:4096
M a[144:147],a[220:223],v[92:95],a[144:147]
M a[144:147],a[216:219],v[88:91],a[144:147]
W vmcnt(14)
T v[42:43],v165 offset:4096
M a[8:11],a[160:163],v[96:99],a[8:11]
B v221,s[44:47],s80 offen offset:3072 lds
s_add_u32 s80,s80,16384
M a[8:11],a[164:167],v[100:103],a[8:11]
T v[50:51],v166 offset:4096
M a[28:31],a[172:175],v[100:103],a[28:31]
B a[240:243],v245,s[96:99],s79 offen offset:2048 sc1
M a[28:31],a[168:171],v[96:99],a[28:31]
T v[58:59],v167 offset:4096
M a[48:51],a[176:179],v[96:99],a[48:51]
M a[48:51],a[180:183],v[100:103],a[48:51]
M a[68:71],a[188:191],v[100:103],a[68:71]
M a[68:71],a[184:187],v[96:99],a[68:71]
s_add_u32 m0,m0,4096
M a[88:91],a[192:195],v[96:99],a[88:91]
B v218,s[44:47],s80 offen lds
M a[88:91],a[196:199],v[100:103],a[88:91]
T v[4:5],v160 offset:8192
M a[108:111],a[204:207],v[100:103],a[108:111]
B a[244:247],v240,s[96:99],s79 offen offset:3072 sc1
M a[108:111],a[200:203],v[96:99],a[108:111]
T v[12:13],v161 offset:8192
M a[128:131],a[208:211],v[96:99],a[128:131]
M a[128:131],a[212:215],v[100:103],a[128:131]
T v[20:21],v162 offset:8192
M a[148:151],a[220:223],v[100:103],a[148:151]
M a[148:151],a[216:219],v[96:99],a[148:151]
W vmcnt(15)
T v[28:29],v163 offset:8192
M a[12:15],a[160:163],v[104:107],a[12:15]
M a[12:15],a[164:167],v[108:111],a[12:15]
T v[36:37],v164 offset:8192
M a[32:35],a[172:175],v[108:111],a[32:35]
B v219,s[44:47],s80 offen offset:1024 lds
M a[32:35],a[168:171],v[104:107],a[32:35]
T v[44:45],v165 offset:8192
M a[52:55],a[176:179],v[104:107],a[52:55]
B a[248:251],v245,s[96:99],s79 offen offset:3072 sc1
M a[52:55],a[180:183],v[108:111],a[52:55]
T v[52:53],v166 offset:8192
M a[72:75],a[188:191],v[108:111],a[72:75]
M a[72:75],a[184:187],v[104:107],a[72:75]
T v[60:61],v167 offset:8192
M a[92:95],a[192:195],v[104:107],a[92:95]
B a[252:255],v241,s[96:99],s79 offen sc1
M a[92:95],a[196:199],v[108:111],a[92:95]
M a[112:115],a[204:207],v[108:111],a[112:115]
B v220,s[44:47],s80 offen offset:2048 lds
M a[112:115],a[200:203],v[104:107],a[112:115]
M a[132:135],a[208:211],v[104:107],a[132:135]
M a[132:135],a[212:215],v[108:111],a[132:135]
T v[6:7],v160 offset:12288
M a[152:155],a[220:223],v[108:111],a[152:155]
M a[152:155],a[216:219],v[104:107],a[152:155]
W vmcnt(15)
T v[14:15],v161 offset:12288
M a[16:19],a[160:163],v[112:115],a[16:19]
M a[16:19],a[164:167],v[116:119],a[16:19]
T v[22:23],v162 offset:12288
M a[36:39],a[172:175],v[116:119],a[36:39]
B v[228:231],v240,s[96:99],s79 offen sc1
M a[36:39],a[168:171],v[112:115],a[36:39]
T v[30:31],v163 offset:12288
M a[56:59],a[176:179],v[112:115],a[56:59]
B v221,s[44:47],s80 offen offset:3072 lds
s_add_u32 s80,s80,245760
s_add_u32 s5,s5,16384
s_and_b32 s5,s5,0xc000
M a[56:59],a[180:183],v[116:119],a[56:59]
T v[38:39],v164 offset:12288
M a[76:79],a[188:191],v[116:119],a[76:79]
M a[76:79],a[184:187],v[112:115],a[76:79]
T v[46:47],v165 offset:12288
M a[96:99],a[192:195],v[112:115],a[96:99]
B v[232:235],v241,s[96:99],s79 offen offset:1024 sc1
s_add_u32 s79,s79,40960
M a[96:99],a[196:199],v[116:119],a[96:99]
T v[54:55],v166 offset:12288
M a[116:119],a[204:207],v[116:119],a[116:119]
M a[116:119],a[200:203],v[112:115],a[116:119]
T v[62:63],v167 offset:12288
M a[136:139],a[208:211],v[112:115],a[136:139]
M a[136:139],a[212:215],v[116:119],a[136:139]
M a[156:159],a[220:223],v[116:119],a[156:159]
M a[156:159],a[216:219],v[112:115],a[156:159]
s_cmp_eq_u32 s69,0
s_cbranch_scc1 LOPA45_%=
s_cmp_eq_u32 s69,1
s_cbranch_scc1 LOPA65_%=
LOPR5_%=:
)ASM"
R"ASM(
W lgkmcnt(4)
W vmcnt(15)
M a[0:3],v[0:3],a[224:227],a[0:3]
W vmcnt(63)
s_barrier
M a[0:3],v[4:7],a[228:231],a[0:3]
T a[160:161],v160 offset:16384
M a[20:23],v[12:15],a[228:231],a[20:23]
B v[80:83],v245,s[96:99],s79 offen sc1
M a[20:23],v[8:11],a[224:227],a[20:23]
s_add_u32 m0,s78,s5
T a[168:169],v161 offset:16384
M a[40:43],v[16:19],a[224:227],a[40:43]
B v218,s[44:47],s80 offen lds
M a[40:43],v[20:23],a[228:231],a[40:43]
T a[176:177],v162 offset:16384
M a[60:63],v[28:31],a[228:231],a[60:63]
M a[60:63],v[24:27],a[224:227],a[60:63]
W lgkmcnt(3)
T a[184:185],v163 offset:16384
M a[80:83],v[32:35],a[224:227],a[80:83]
B v[84:87],v240,s[96:99],s79 offen offset:1024 sc1
M a[80:83],v[36:39],a[228:231],a[80:83]
T a[192:193],v164 offset:16384
M a[100:103],v[44:47],a[228:231],a[100:103]
M a[100:103],v[40:43],a[224:227],a[100:103]
T a[200:201],v165 offset:16384
M a[120:123],v[48:51],a[224:227],a[120:123]
M a[120:123],v[52:55],a[228:231],a[120:123]
T a[208:209],v166 offset:16384
M a[140:143],v[60:63],a[228:231],a[140:143]
B v219,s[44:47],s80 offen offset:1024 lds
M a[140:143],v[56:59],a[224:227],a[140:143]
W vmcnt(15)
T a[216:217],v167 offset:16384
M a[4:7],v[0:3],a[232:235],a[4:7]
M a[4:7],v[4:7],a[236:239],a[4:7]
M a[24:27],v[12:15],a[236:239],a[24:27]
B v[88:91],v245,s[96:99],s79 offen offset:1024 sc1
M a[24:27],v[8:11],a[232:235],a[24:27]
M a[44:47],v[16:19],a[232:235],a[44:47]
M a[44:47],v[20:23],a[236:239],a[44:47]
T a[162:163],v160 offset:20480
M a[64:67],v[28:31],a[236:239],a[64:67]
B v220,s[44:47],s80 offen offset:2048 lds
M a[64:67],v[24:27],a[232:235],a[64:67]
T a[170:171],v161 offset:20480
M a[84:87],v[32:35],a[232:235],a[84:87]
B v[92:95],v240,s[96:99],s79 offen offset:2048 sc1
M a[84:87],v[36:39],a[236:239],a[84:87]
T a[178:179],v162 offset:20480
M a[104:107],v[44:47],a[236:239],a[104:107]
M a[104:107],v[40:43],a[232:235],a[104:107]
T a[186:187],v163 offset:20480
M a[124:127],v[48:51],a[232:235],a[124:127]
M a[124:127],v[52:55],a[236:239],a[124:127]
T a[194:195],v164 offset:20480
M a[144:147],v[60:63],a[236:239],a[144:147]
M a[144:147],v[56:59],a[232:235],a[144:147]
W vmcnt(14)
T a[202:203],v165 offset:20480
M a[8:11],v[0:3],a[240:243],a[8:11]
B v221,s[44:47],s80 offen offset:3072 lds
s_add_u32 s80,s80,16384
M a[8:11],v[4:7],a[244:247],a[8:11]
T a[210:211],v166 offset:20480
M a[28:31],v[12:15],a[244:247],a[28:31]
B v[96:99],v245,s[96:99],s79 offen offset:2048 sc1
M a[28:31],v[8:11],a[240:243],a[28:31]
T a[218:219],v167 offset:20480
M a[48:51],v[16:19],a[240:243],a[48:51]
M a[48:51],v[20:23],a[244:247],a[48:51]
M a[68:71],v[28:31],a[244:247],a[68:71]
M a[68:71],v[24:27],a[240:243],a[68:71]
s_add_u32 m0,m0,4096
M a[88:91],v[32:35],a[240:243],a[88:91]
B v218,s[44:47],s80 offen lds
M a[88:91],v[36:39],a[244:247],a[88:91]
T a[164:165],v160 offset:24576
M a[108:111],v[44:47],a[244:247],a[108:111]
B v[100:103],v240,s[96:99],s79 offen offset:3072 sc1
M a[108:111],v[40:43],a[240:243],a[108:111]
T a[172:173],v161 offset:24576
M a[128:131],v[48:51],a[240:243],a[128:131]
M a[128:131],v[52:55],a[244:247],a[128:131]
T a[180:181],v162 offset:24576
M a[148:151],v[60:63],a[244:247],a[148:151]
M a[148:151],v[56:59],a[240:243],a[148:151]
W vmcnt(15)
T a[188:189],v163 offset:24576
M a[12:15],v[0:3],a[248:251],a[12:15]
M a[12:15],v[4:7],a[252:255],a[12:15]
T a[196:197],v164 offset:24576
M a[32:35],v[12:15],a[252:255],a[32:35]
B v219,s[44:47],s80 offen offset:1024 lds
M a[32:35],v[8:11],a[248:251],a[32:35]
T a[204:205],v165 offset:24576
M a[52:55],v[16:19],a[248:251],a[52:55]
B v[104:107],v245,s[96:99],s79 offen offset:3072 sc1
M a[52:55],v[20:23],a[252:255],a[52:55]
T a[212:213],v166 offset:24576
M a[72:75],v[28:31],a[252:255],a[72:75]
M a[72:75],v[24:27],a[248:251],a[72:75]
T a[220:221],v167 offset:24576
M a[92:95],v[32:35],a[248:251],a[92:95]
B v[108:111],v241,s[96:99],s79 offen sc1
M a[92:95],v[36:39],a[252:255],a[92:95]
M a[112:115],v[44:47],a[252:255],a[112:115]
B v220,s[44:47],s80 offen offset:2048 lds
M a[112:115],v[40:43],a[248:251],a[112:115]
M a[132:135],v[48:51],a[248:251],a[132:135]
M a[132:135],v[52:55],a[252:255],a[132:135]
T a[166:167],v160 offset:28672
M a[152:155],v[60:63],a[252:255],a[152:155]
M a[152:155],v[56:59],a[248:251],a[152:155]
W vmcnt(15)
T a[174:175],v161 offset:28672
M a[16:19],v[0:3],v[228:231],a[16:19]
M a[16:19],v[4:7],v[232:235],a[16:19]
T a[182:183],v162 offset:28672
M a[36:39],v[12:15],v[232:235],a[36:39]
B v[112:115],v240,s[96:99],s79 offen sc1
M a[36:39],v[8:11],v[228:231],a[36:39]
T a[190:191],v163 offset:28672
M a[56:59],v[16:19],v[228:231],a[56:59]
B v221,s[44:47],s80 offen offset:3072 lds
s_add_u32 s80,s80,245760
s_add_u32 s5,s5,16384
s_and_b32 s5,s5,0xc000
M a[56:59],v[20:23],v[232:235],a[56:59]
T a[198:199],v164 offset:28672
M a[76:79],v[28:31],v[232:235],a[76:79]
M a[76:79],v[24:27],v[228:231],a[76:79]
T a[206:207],v165 offset:28672
M a[96:99],v[32:35],v[228:231],a[96:99]
B v[116:119],v241,s[96:99],s79 offen offset:1024 sc1
s_add_u32 s79,s79,40960
M a[96:99],v[36:39],v[232:235],a[96:99]
T a[214:215],v166 offset:28672
M a[116:119],v[44:47],v[232:235],a[116:119]
M a[116:119],v[40:43],v[228:231],a[116:119]
T a[222:223],v167 offset:28672
M a[136:139],v[48:51],v[228:231],a[136:139]
M a[136:139],v[52:55],v[232:235],a[136:139]
M a[156:159],v[60:63],v[232:235],a[156:159]
M a[156:159],v[56:59],v[228:231],a[156:159]
s_add_u32 s69,s69,1
s_cmp_lt_u32 s69,5
s_cbranch_scc1 LOPL5_%=
)ASM"
R"ASM(
W lgkmcnt(4)
W vmcnt(15)
M a[0:3],a[160:163],v[80:83],a[0:3]
W vmcnt(63)
s_barrier
v_xor_b32 v160,0x8000,v160
v_xor_b32 v161,0x8000,v161
M a[0:3],a[164:167],v[84:87],a[0:3]
T v[0:1],v160
M a[20:23],a[172:175],v[84:87],a[20:23]
s_add_u32 s34,s79,40960
B a[224:227],v245,s[96:99],s79 offen sc1
v_xor_b32 v162,0x8000,v162
M a[20:23],a[168:171],v[80:83],a[20:23]
T v[8:9],v161
M a[40:43],a[176:179],v[80:83],a[40:43]
v_xor_b32 v163,0x8000,v163
M a[40:43],a[180:183],v[84:87],a[40:43]
T v[16:17],v162
M a[60:63],a[188:191],v[84:87],a[60:63]
B a[228:231],v240,s[96:99],s79 offen offset:1024 sc1
v_xor_b32 v164,0x8000,v164
M a[60:63],a[184:187],v[80:83],a[60:63]
W lgkmcnt(3)
T v[24:25],v163
M a[80:83],a[192:195],v[80:83],a[80:83]
v_xor_b32 v165,0x8000,v165
M a[80:83],a[196:199],v[84:87],a[80:83]
T v[32:33],v164
M a[100:103],a[204:207],v[84:87],a[100:103]
B a[232:235],v245,s[96:99],s79 offen offset:1024 sc1
v_xor_b32 v166,0x8000,v166
M a[100:103],a[200:203],v[80:83],a[100:103]
T v[40:41],v165
M a[120:123],a[208:211],v[80:83],a[120:123]
v_xor_b32 v167,0x8000,v167
M a[120:123],a[212:215],v[84:87],a[120:123]
T v[48:49],v166
M a[140:143],a[220:223],v[84:87],a[140:143]
B a[236:239],v240,s[96:99],s79 offen offset:2048 sc1
M a[140:143],a[216:219],v[80:83],a[140:143]
W vmcnt(15)
T v[56:57],v167
M a[4:7],a[160:163],v[88:91],a[4:7]
M a[4:7],a[164:167],v[92:95],a[4:7]
M a[24:27],a[172:175],v[92:95],a[24:27]
B a[240:243],v245,s[96:99],s79 offen offset:2048 sc1
M a[24:27],a[168:171],v[88:91],a[24:27]
M a[44:47],a[176:179],v[88:91],a[44:47]
M a[44:47],a[180:183],v[92:95],a[44:47]
T v[2:3],v160 offset:4096
M a[64:67],a[188:191],v[92:95],a[64:67]
B a[244:247],v240,s[96:99],s79 offen offset:3072 sc1
M a[64:67],a[184:187],v[88:91],a[64:67]
T v[10:11],v161 offset:4096
M a[84:87],a[192:195],v[88:91],a[84:87]
M a[84:87],a[196:199],v[92:95],a[84:87]
T v[18:19],v162 offset:4096
M a[104:107],a[204:207],v[92:95],a[104:107]
B a[248:251],v245,s[96:99],s79 offen offset:3072 sc1
M a[104:107],a[200:203],v[88:91],a[104:107]
T v[26:27],v163 offset:4096
M a[124:127],a[208:211],v[88:91],a[124:127]
M a[124:127],a[212:215],v[92:95],a[124:127]
T v[34:35],v164 offset:4096
M a[144:147],a[220:223],v[92:95],a[144:147]
B a[252:255],v241,s[96:99],s79 offen sc1
M a[144:147],a[216:219],v[88:91],a[144:147]
W vmcnt(15)
T v[42:43],v165 offset:4096
M a[8:11],a[160:163],v[96:99],a[8:11]
M a[8:11],a[164:167],v[100:103],a[8:11]
T v[50:51],v166 offset:4096
M a[28:31],a[172:175],v[100:103],a[28:31]
B v[228:231],v240,s[96:99],s79 offen sc1
M a[28:31],a[168:171],v[96:99],a[28:31]
T v[58:59],v167 offset:4096
M a[48:51],a[176:179],v[96:99],a[48:51]
M a[48:51],a[180:183],v[100:103],a[48:51]
M a[68:71],a[188:191],v[100:103],a[68:71]
B v[232:235],v241,s[96:99],s79 offen offset:1024 sc1
s_add_u32 s79,s79,40960
M a[68:71],a[184:187],v[96:99],a[68:71]
M a[88:91],a[192:195],v[96:99],a[88:91]
B v[80:83],v245,s[96:99],s34 offen sc1
M a[88:91],a[196:199],v[100:103],a[88:91]
T v[4:5],v160 offset:8192
M a[108:111],a[204:207],v[100:103],a[108:111]
M a[108:111],a[200:203],v[96:99],a[108:111]
T v[12:13],v161 offset:8192
M a[128:131],a[208:211],v[96:99],a[128:131]
B v[84:87],v240,s[96:99],s34 offen offset:1024 sc1
M a[128:131],a[212:215],v[100:103],a[128:131]
T v[20:21],v162 offset:8192
M a[148:151],a[220:223],v[100:103],a[148:151]
M a[148:151],a[216:219],v[96:99],a[148:151]
W vmcnt(16)
T v[28:29],v163 offset:8192
M a[12:15],a[160:163],v[104:107],a[12:15]
B v[88:91],v245,s[96:99],s34 offen offset:1024 sc1
M a[12:15],a[164:167],v[108:111],a[12:15]
T v[36:37],v164 offset:8192
M a[32:35],a[172:175],v[108:111],a[32:35]
M a[32:35],a[168:171],v[104:107],a[32:35]
T v[44:45],v165 offset:8192
M a[52:55],a[176:179],v[104:107],a[52:55]
B v[92:95],v240,s[96:99],s34 offen offset:2048 sc1
M a[52:55],a[180:183],v[108:111],a[52:55]
T v[52:53],v166 offset:8192
M a[72:75],a[188:191],v[108:111],a[72:75]
M a[72:75],a[184:187],v[104:107],a[72:75]
T v[60:61],v167 offset:8192
M a[92:95],a[192:195],v[104:107],a[92:95]
B v[96:99],v245,s[96:99],s34 offen offset:2048 sc1
M a[92:95],a[196:199],v[108:111],a[92:95]
M a[112:115],a[204:207],v[108:111],a[112:115]
M a[112:115],a[200:203],v[104:107],a[112:115]
M a[132:135],a[208:211],v[104:107],a[132:135]
B v[100:103],v240,s[96:99],s34 offen offset:3072 sc1
M a[132:135],a[212:215],v[108:111],a[132:135]
T v[6:7],v160 offset:12288
M a[152:155],a[220:223],v[108:111],a[152:155]
M a[152:155],a[216:219],v[104:107],a[152:155]
W vmcnt(16)
T v[14:15],v161 offset:12288
M a[16:19],a[160:163],v[112:115],a[16:19]
B v[104:107],v245,s[96:99],s34 offen offset:3072 sc1
M a[16:19],a[164:167],v[116:119],a[16:19]
T v[22:23],v162 offset:12288
M a[36:39],a[172:175],v[116:119],a[36:39]
M a[36:39],a[168:171],v[112:115],a[36:39]
T v[30:31],v163 offset:12288
M a[56:59],a[176:179],v[112:115],a[56:59]
B v[108:111],v241,s[96:99],s34 offen sc1
M a[56:59],a[180:183],v[116:119],a[56:59]
T v[38:39],v164 offset:12288
M a[76:79],a[188:191],v[116:119],a[76:79]
B v[208:211],v240,s[96:99],s34 offen sc1
M a[76:79],a[184:187],v[112:115],a[76:79]
T v[46:47],v165 offset:12288
M a[96:99],a[192:195],v[112:115],a[96:99]
M a[96:99],a[196:199],v[116:119],a[96:99]
T v[54:55],v166 offset:12288
M a[116:119],a[204:207],v[116:119],a[116:119]
B v[212:215],v241,s[96:99],s34 offen offset:1024 sc1
M a[116:119],a[200:203],v[112:115],a[116:119]
T v[62:63],v167 offset:12288
M a[136:139],a[208:211],v[112:115],a[136:139]
M a[136:139],a[212:215],v[116:119],a[136:139]
M a[156:159],a[220:223],v[116:119],a[156:159]
M a[156:159],a[216:219],v[112:115],a[156:159]
)ASM"
R"ASM(
W lgkmcnt(0)
W vmcnt(18)
M a[120:123],v[48:51],a[224:227],a[120:123]
W vmcnt(63)
s_barrier
M a[120:123],v[52:55],a[228:231],a[120:123]
s_lshl_b32 s95,s87,15
T a[208:209],v166 offset:16384
v_add_u32 v200,s95,v245
M a[140:143],v[60:63],a[228:231],a[140:143]
T a[210:211],v166 offset:20480
M a[140:143],v[56:59],a[224:227],a[140:143]
W vmcnt(16)
T a[212:213],v166 offset:24576
v_xor_b32 v201,0x8000,v200
M a[124:127],v[48:51],a[232:235],a[124:127]
T a[214:215],v166 offset:28672
M a[124:127],v[52:55],a[236:239],a[124:127]
T a[216:217],v167 offset:16384
v_lshlrev_b32 v251,11,v244
M a[144:147],v[60:63],a[236:239],a[144:147]
T a[218:219],v167 offset:20480
M a[144:147],v[56:59],a[232:235],a[144:147]
W vmcnt(14)
T a[220:221],v167 offset:24576
v_lshl_add_u32 v251,v227,4,v251
M a[128:131],v[48:51],a[240:243],a[128:131]
T a[222:223],v167 offset:28672
M a[128:131],v[52:55],a[244:247],a[128:131]
T a[176:177],v162 offset:16384
v_mov_b32 v202,s72
M a[148:151],v[60:63],a[244:247],a[148:151]
T a[178:179],v162 offset:20480
M a[148:151],v[56:59],a[240:243],a[148:151]
W vmcnt(12)
T a[180:181],v162 offset:24576
v_mov_b32 v203,s73
M a[132:135],v[48:51],a[248:251],a[132:135]
T a[182:183],v162 offset:28672
M a[132:135],v[52:55],a[252:255],a[132:135]
T a[184:185],v163 offset:16384
M a[152:155],v[60:63],a[252:255],a[152:155]
T a[186:187],v163 offset:20480
M a[152:155],v[56:59],a[248:251],a[152:155]
W vmcnt(10)
T a[188:189],v163 offset:24576
M a[136:139],v[48:51],v[228:231],a[136:139]
T a[190:191],v163 offset:28672
M a[136:139],v[52:55],v[232:235],a[136:139]
T a[192:193],v164 offset:16384
M a[156:159],v[60:63],v[232:235],a[156:159]
T a[194:195],v164 offset:20480
M a[156:159],v[56:59],v[228:231],a[156:159]
W vmcnt(8)
W lgkmcnt(10)
T a[196:197],v164 offset:24576
M a[120:123],a[208:211],v[80:83],a[120:123]
T a[198:199],v164 offset:28672
M a[120:123],a[212:215],v[84:87],a[120:123]
T a[200:201],v165 offset:16384
M a[140:143],a[220:223],v[84:87],a[140:143]
T a[202:203],v165 offset:20480
M a[140:143],a[216:219],v[80:83],a[140:143]
W vmcnt(6)
T a[204:205],v165 offset:24576
M a[124:127],a[208:211],v[88:91],a[124:127]
T a[206:207],v165 offset:28672
M a[124:127],a[212:215],v[92:95],a[124:127]
T a[160:161],v160 offset:16384
M a[144:147],a[220:223],v[92:95],a[144:147]
T a[162:163],v160 offset:20480
M a[144:147],a[216:219],v[88:91],a[144:147]
W vmcnt(4)
T a[164:165],v160 offset:24576
M a[128:131],a[208:211],v[96:99],a[128:131]
T a[166:167],v160 offset:28672
M a[128:131],a[212:215],v[100:103],a[128:131]
T a[168:169],v161 offset:16384
M a[148:151],a[220:223],v[100:103],a[148:151]
T a[170:171],v161 offset:20480
M a[148:151],a[216:219],v[96:99],a[148:151]
W vmcnt(2)
T a[172:173],v161 offset:24576
M a[132:135],a[208:211],v[104:107],a[132:135]
T a[174:175],v161 offset:28672
M a[132:135],a[212:215],v[108:111],a[132:135]
ds_write_b128 v200,a[120:123]
M a[152:155],a[220:223],v[108:111],a[152:155]
ds_write_b128 v200,a[140:143] offset:1024
M a[152:155],a[216:219],v[104:107],a[152:155]
W vmcnt(0)
ds_write_b128 v200,a[124:127] offset:2048
M a[136:139],a[208:211],v[208:211],a[136:139]
ds_write_b128 v200,a[144:147] offset:3072
M a[136:139],a[212:215],v[212:215],a[136:139]
ds_write_b128 v200,a[128:131] offset:4096
M a[156:159],a[220:223],v[212:215],a[156:159]
M a[156:159],a[216:219],v[208:211],a[156:159]
W lgkmcnt(15)
ds_write_b128 v200,a[148:151] offset:5120
M a[40:43],v[16:19],a[224:227],a[40:43]
M a[40:43],v[20:23],a[228:231],a[40:43]
ds_write_b128 v200,a[132:135] offset:6144
M a[40:43],a[176:179],v[80:83],a[40:43]
M a[40:43],a[180:183],v[84:87],a[40:43]
ds_write_b128 v200,a[152:155] offset:7168
M a[60:63],a[188:191],v[84:87],a[60:63]
M a[60:63],a[184:187],v[80:83],a[60:63]
ds_write_b128 v200,a[136:139] offset:8192
M a[60:63],v[28:31],a[228:231],a[60:63]
M a[60:63],v[24:27],a[224:227],a[60:63]
ds_write_b128 v200,a[156:159] offset:9216
M a[44:47],v[16:19],a[232:235],a[44:47]
M a[44:47],v[20:23],a[236:239],a[44:47]
M a[44:47],a[176:179],v[88:91],a[44:47]
W lgkmcnt(0)
s_barrier
M a[44:47],a[180:183],v[92:95],a[44:47]
R v[120:123],v201
M a[64:67],a[188:191],v[92:95],a[64:67]
R v[124:127],v201 offset:1024
M a[64:67],a[184:187],v[88:91],a[64:67]
R v[128:131],v201 offset:2048
M a[64:67],v[28:31],a[236:239],a[64:67]
R v[132:135],v201 offset:3072
M a[64:67],v[24:27],a[232:235],a[64:67]
R v[136:139],v201 offset:4096
M a[48:51],v[16:19],a[240:243],a[48:51]
R v[140:143],v201 offset:5120
M a[48:51],v[20:23],a[244:247],a[48:51]
R v[144:147],v201 offset:6144
M a[48:51],a[176:179],v[96:99],a[48:51]
W lgkmcnt(5)
R v[148:151],v201 offset:7168
AR v160,a40
AR v161,a41
AR v162,a42
AR v163,a43
M a[48:51],a[180:183],v[100:103],a[48:51]
R v[152:155],v201 offset:8192
AR v164,a60
AR v165,a61
AR v166,a62
AR v167,a63
M a[68:71],a[188:191],v[100:103],a[68:71]
R v[156:159],v201 offset:9216
F v160,v160,v120
F v161,v161,v121
F v162,v162,v122
F v163,v163,v123
M a[68:71],a[184:187],v[96:99],a[68:71]
F v164,v164,v124
F v165,v165,v125
F v166,v166,v126
F v167,v167,v127
M a[68:71],v[28:31],a[244:247],a[68:71]
C v160,v160,v161
C v161,v162,v163
C v162,v164,v165
C v163,v166,v167
M a[68:71],v[24:27],a[240:243],a[68:71]
W lgkmcnt(6)
buffer_store_dwordx4 v[160:163],v245,s[52:55],s6 offen sc1
AR v168,a44
AR v169,a45
AR v170,a46
AR v171,a47
M a[52:55],v[16:19],a[248:251],a[52:55]
AR v172,a64
AR v173,a65
AR v174,a66
AR v175,a67
M a[52:55],v[20:23],a[252:255],a[52:55]
F v168,v168,v128
F v169,v169,v129
F v170,v170,v130
F v171,v171,v131
M a[52:55],a[176:179],v[104:107],a[52:55]
F v172,v172,v132
F v173,v173,v133
F v174,v174,v134
F v175,v175,v135
M a[52:55],a[180:183],v[108:111],a[52:55]
C v168,v168,v169
C v169,v170,v171
C v170,v172,v173
C v171,v174,v175
M a[72:75],a[188:191],v[108:111],a[72:75]
buffer_store_dwordx4 v[168:171],v245,s[52:55],s6 offen offset:1024 sc1
M a[72:75],a[184:187],v[104:107],a[72:75]
M a[72:75],v[28:31],a[252:255],a[72:75]
M a[72:75],v[24:27],a[248:251],a[72:75]
W lgkmcnt(4)
AR v176,a48
AR v177,a49
AR v178,a50
AR v179,a51
M a[56:59],v[16:19],v[228:231],a[56:59]
AR v180,a68
AR v181,a69
AR v182,a70
AR v183,a71
M a[56:59],v[20:23],v[232:235],a[56:59]
F v176,v176,v136
F v177,v177,v137
F v178,v178,v138
F v179,v179,v139
M a[56:59],a[176:179],v[208:211],a[56:59]
F v180,v180,v140
F v181,v181,v141
F v182,v182,v142
F v183,v183,v143
M a[56:59],a[180:183],v[212:215],a[56:59]
C v176,v176,v177
C v177,v178,v179
C v178,v180,v181
C v179,v182,v183
M a[76:79],a[188:191],v[212:215],a[76:79]
buffer_store_dwordx4 v[176:179],v245,s[52:55],s6 offen offset:2048 sc1
M a[76:79],a[184:187],v[208:211],a[76:79]
M a[76:79],v[28:31],v[232:235],a[76:79]
M a[76:79],v[24:27],v[228:231],a[76:79]
W lgkmcnt(2)
AR v184,a52
AR v185,a53
AR v186,a54
AR v187,a55
M a[80:83],v[32:35],a[224:227],a[80:83]
AR v188,a72
AR v189,a73
AR v190,a74
AR v191,a75
M a[80:83],v[36:39],a[228:231],a[80:83]
F v184,v184,v144
F v185,v185,v145
F v186,v186,v146
F v187,v187,v147
M a[80:83],a[192:195],v[80:83],a[80:83]
F v188,v188,v148
F v189,v189,v149
F v190,v190,v150
F v191,v191,v151
M a[80:83],a[196:199],v[84:87],a[80:83]
C v184,v184,v185
C v185,v186,v187
C v186,v188,v189
C v187,v190,v191
M a[100:103],a[204:207],v[84:87],a[100:103]
buffer_store_dwordx4 v[184:187],v245,s[52:55],s6 offen offset:3072 sc1
M a[100:103],a[200:203],v[80:83],a[100:103]
M a[100:103],v[44:47],a[228:231],a[100:103]
M a[100:103],v[40:43],a[224:227],a[100:103]
W lgkmcnt(0)
AR v192,a56
AR v193,a57
AR v194,a58
AR v195,a59
M a[84:87],v[32:35],a[232:235],a[84:87]
AR v196,a76
AR v197,a77
AR v198,a78
AR v199,a79
M a[84:87],v[36:39],a[236:239],a[84:87]
F v192,v192,v152
F v193,v193,v153
F v194,v194,v154
F v195,v195,v155
M a[84:87],a[192:195],v[88:91],a[84:87]
F v196,v196,v156
F v197,v197,v157
F v198,v198,v158
F v199,v199,v159
M a[84:87],a[196:199],v[92:95],a[84:87]
ds_write_b128 v200,a[80:83] offset:10240
C v192,v192,v193
C v193,v194,v195
C v194,v196,v197
C v195,v198,v199
M a[104:107],a[204:207],v[92:95],a[104:107]
buffer_store_dwordx4 v[192:195],v240,s[52:55],s6 offen sc1
M a[104:107],a[200:203],v[88:91],a[104:107]
M a[104:107],v[44:47],a[236:239],a[104:107]
M a[104:107],v[40:43],a[232:235],a[104:107]
ds_write_b128 v200,a[100:103] offset:11264
M a[88:91],v[32:35],a[240:243],a[88:91]
M a[88:91],v[36:39],a[244:247],a[88:91]
M a[88:91],a[192:195],v[96:99],a[88:91]
M a[88:91],a[196:199],v[100:103],a[88:91]
ds_write_b128 v200,a[84:87] offset:12288
M a[108:111],a[204:207],v[100:103],a[108:111]
M a[108:111],a[200:203],v[96:99],a[108:111]
M a[108:111],v[44:47],a[244:247],a[108:111]
M a[108:111],v[40:43],a[240:243],a[108:111]
ds_write_b128 v200,a[104:107] offset:13312
M a[92:95],v[32:35],a[248:251],a[92:95]
M a[92:95],v[36:39],a[252:255],a[92:95]
M a[92:95],a[192:195],v[104:107],a[92:95]
M a[92:95],a[196:199],v[108:111],a[92:95]
ds_write_b128 v200,a[88:91] offset:14336
M a[112:115],a[204:207],v[108:111],a[112:115]
)ASM"
R"ASM(
M a[112:115],a[200:203],v[104:107],a[112:115]
M a[112:115],v[44:47],a[252:255],a[112:115]
M a[112:115],v[40:43],a[248:251],a[112:115]
ds_write_b128 v200,a[108:111] offset:15360
M a[96:99],v[32:35],v[228:231],a[96:99]
M a[96:99],v[36:39],v[232:235],a[96:99]
M a[96:99],a[192:195],v[208:211],a[96:99]
M a[96:99],a[196:199],v[212:215],a[96:99]
ds_write_b128 v200,a[92:95] offset:16384
M a[116:119],a[204:207],v[212:215],a[116:119]
M a[116:119],a[200:203],v[208:211],a[116:119]
M a[116:119],v[44:47],v[232:235],a[116:119]
M a[116:119],v[40:43],v[228:231],a[116:119]
ds_write_b128 v200,a[112:115] offset:17408
M a[0:3],v[0:3],a[224:227],a[0:3]
M a[0:3],v[4:7],a[228:231],a[0:3]
M a[0:3],a[160:163],v[80:83],a[0:3]
M a[0:3],a[164:167],v[84:87],a[0:3]
ds_write_b128 v200,a[96:99] offset:18432
M a[20:23],a[172:175],v[84:87],a[20:23]
s_add_u32 s94,s6,10240
s_add_u32 s95,s7,10240
W vmcnt(0)
s_mov_b64 exec,1
buffer_store_dwordx2 v[202:203],off,s[52:55],s94 sc1
s_mov_b64 exec,-1
M a[20:23],a[168:171],v[80:83],a[20:23]
M a[20:23],v[12:15],a[228:231],a[20:23]
M a[20:23],v[8:11],a[224:227],a[20:23]
ds_write_b128 v200,a[116:119] offset:19456
M a[4:7],v[0:3],a[232:235],a[4:7]
M a[4:7],v[4:7],a[236:239],a[4:7]
M a[4:7],a[160:163],v[88:91],a[4:7]
W lgkmcnt(0)
s_barrier
M a[4:7],a[164:167],v[92:95],a[4:7]
R v[16:19],v201 offset:10240
M a[24:27],a[172:175],v[92:95],a[24:27]
R v[20:23],v201 offset:11264
M a[24:27],a[168:171],v[88:91],a[24:27]
R v[24:27],v201 offset:12288
M a[24:27],v[12:15],a[236:239],a[24:27]
R v[28:31],v201 offset:13312
M a[24:27],v[8:11],a[232:235],a[24:27]
R v[32:35],v201 offset:14336
AR v120,a0
AR v121,a1
M a[8:11],v[0:3],a[240:243],a[8:11]
R v[36:39],v201 offset:15360
AR v122,a2
AR v123,a3
M a[8:11],v[4:7],a[244:247],a[8:11]
R v[40:43],v201 offset:16384
AR v124,a20
AR v125,a21
M a[8:11],a[160:163],v[96:99],a[8:11]
R v[44:47],v201 offset:17408
AR v126,a22
AR v127,a23
M a[8:11],a[164:167],v[100:103],a[8:11]
R v[48:51],v201 offset:18432
M a[28:31],a[172:175],v[100:103],a[28:31]
R v[52:55],v201 offset:19456
s_mov_b64 exec,1
buffer_load_dwordx2 v[248:249],off,s[52:55],s95 sc1
s_mov_b64 exec,-1
M a[28:31],a[168:171],v[96:99],a[28:31]
M a[28:31],v[12:15],a[244:247],a[28:31]
M a[28:31],v[8:11],a[240:243],a[28:31]
AR v128,a4
AR v129,a5
M a[12:15],v[0:3],a[248:251],a[12:15]
AR v130,a6
AR v131,a7
M a[12:15],v[4:7],a[252:255],a[12:15]
AR v132,a24
AR v133,a25
M a[12:15],a[160:163],v[104:107],a[12:15]
AR v134,a26
AR v135,a27
M a[12:15],a[164:167],v[108:111],a[12:15]
M a[32:35],a[172:175],v[108:111],a[32:35]
M a[32:35],a[168:171],v[104:107],a[32:35]
M a[32:35],v[12:15],a[252:255],a[32:35]
M a[32:35],v[8:11],a[248:251],a[32:35]
AR v136,a8
AR v137,a9
M a[16:19],v[0:3],v[228:231],a[16:19]
AR v138,a10
AR v139,a11
M a[16:19],v[4:7],v[232:235],a[16:19]
AR v140,a28
AR v141,a29
M a[16:19],a[160:163],v[208:211],a[16:19]
AR v142,a30
AR v143,a31
M a[16:19],a[164:167],v[212:215],a[16:19]
M a[36:39],a[172:175],v[212:215],a[36:39]
M a[36:39],a[168:171],v[208:211],a[36:39]
M a[36:39],v[12:15],v[232:235],a[36:39]
M a[36:39],v[8:11],v[228:231],a[36:39]
AR v144,a12
AR v145,a13
)ASM"
R"ASM(
s_nop 7
W vmcnt(0)
s_mov_b64 exec,1
s_mov_b32 s94,0
LPX5a_%=:
v_cmp_ne_u64 vcc,s[72:73],v[248:249]
s_nop 3
s_cmp_eq_u64 vcc,0
s_cbranch_scc1 LPX5b_%=
s_add_u32 s94,s94,1
s_cmp_gt_u32 s94,262144
s_cbranch_scc1 LPX5b_%=
s_sleep 1
buffer_load_dwordx2 v[248:249],off,s[52:55],s95 sc1
W vmcnt(0)
s_branch LPX5a_%=
LPX5b_%=:
s_mov_b64 exec,-1
B v[64:67],v245,s[52:55],s7 offen sc1
B v[68:71],v245,s[52:55],s7 offen offset:1024 sc1
B v[72:75],v245,s[52:55],s7 offen offset:2048 sc1
B v[76:79],v245,s[52:55],s7 offen offset:3072 sc1
B v[204:207],v240,s[52:55],s7 offen sc1
AR v146,a14
AR v147,a15
AR v148,a32
AR v149,a33
AR v150,a34
AR v151,a35
AR v152,a16
AR v153,a17
AR v154,a18
AR v155,a19
AR v156,a36
AR v157,a37
AR v158,a38
AR v159,a39
W lgkmcnt(0)
v_pk_add_f32 v[120:121],v[120:121],v[16:17]
v_pk_add_f32 v[122:123],v[122:123],v[18:19]
v_pk_add_f32 v[124:125],v[124:125],v[20:21]
v_pk_add_f32 v[126:127],v[126:127],v[22:23]
v_pk_add_f32 v[128:129],v[128:129],v[24:25]
v_pk_add_f32 v[130:131],v[130:131],v[26:27]
v_pk_add_f32 v[132:133],v[132:133],v[28:29]
v_pk_add_f32 v[134:135],v[134:135],v[30:31]
v_pk_add_f32 v[136:137],v[136:137],v[32:33]
v_pk_add_f32 v[138:139],v[138:139],v[34:35]
v_pk_add_f32 v[140:141],v[140:141],v[36:37]
v_pk_add_f32 v[142:143],v[142:143],v[38:39]
v_pk_add_f32 v[144:145],v[144:145],v[40:41]
v_pk_add_f32 v[146:147],v[146:147],v[42:43]
v_pk_add_f32 v[148:149],v[148:149],v[44:45]
v_pk_add_f32 v[150:151],v[150:151],v[46:47]
v_pk_add_f32 v[152:153],v[152:153],v[48:49]
v_pk_add_f32 v[154:155],v[154:155],v[50:51]
v_pk_add_f32 v[156:157],v[156:157],v[52:53]
v_pk_add_f32 v[158:159],v[158:159],v[54:55]
W vmcnt(4)
v_lshlrev_b32 v80,16,v64
v_and_b32 v81,0xffff0000,v64
v_lshlrev_b32 v82,16,v65
v_and_b32 v83,0xffff0000,v65
v_lshlrev_b32 v84,16,v66
v_and_b32 v85,0xffff0000,v66
v_lshlrev_b32 v86,16,v67
v_and_b32 v87,0xffff0000,v67
v_pk_add_f32 v[120:121],v[120:121],v[80:81]
v_pk_add_f32 v[122:123],v[122:123],v[82:83]
v_pk_add_f32 v[124:125],v[124:125],v[84:85]
v_pk_add_f32 v[126:127],v[126:127],v[86:87]
C v160,v120,v121
C v161,v122,v123
C v162,v124,v125
C v163,v126,v127
s_nop 0
v_permlane32_swap_b32 v160,v162
v_permlane32_swap_b32 v161,v163
s_nop 0
v_permlane16_swap_b32 v160,v162
v_permlane16_swap_b32 v161,v163
s_add_u32 s94,s89,0
buffer_store_dwordx4 v[160:163],v251,s[56:59],s94 offen sc1
W vmcnt(4)
v_lshlrev_b32 v88,16,v68
v_and_b32 v89,0xffff0000,v68
v_lshlrev_b32 v90,16,v69
v_and_b32 v91,0xffff0000,v69
v_lshlrev_b32 v92,16,v70
v_and_b32 v93,0xffff0000,v70
v_lshlrev_b32 v94,16,v71
v_and_b32 v95,0xffff0000,v71
v_pk_add_f32 v[128:129],v[128:129],v[88:89]
v_pk_add_f32 v[130:131],v[130:131],v[90:91]
v_pk_add_f32 v[132:133],v[132:133],v[92:93]
v_pk_add_f32 v[134:135],v[134:135],v[94:95]
C v164,v128,v129
C v165,v130,v131
C v166,v132,v133
C v167,v134,v135
s_nop 0
v_permlane32_swap_b32 v164,v166
v_permlane32_swap_b32 v165,v167
s_nop 0
v_permlane16_swap_b32 v164,v166
v_permlane16_swap_b32 v165,v167
s_add_u32 s94,s89,32768
buffer_store_dwordx4 v[164:167],v251,s[56:59],s94 offen sc1
W vmcnt(4)
v_lshlrev_b32 v96,16,v72
v_and_b32 v97,0xffff0000,v72
v_lshlrev_b32 v98,16,v73
v_and_b32 v99,0xffff0000,v73
v_lshlrev_b32 v100,16,v74
v_and_b32 v101,0xffff0000,v74
v_lshlrev_b32 v102,16,v75
v_and_b32 v103,0xffff0000,v75
v_pk_add_f32 v[136:137],v[136:137],v[96:97]
v_pk_add_f32 v[138:139],v[138:139],v[98:99]
v_pk_add_f32 v[140:141],v[140:141],v[100:101]
v_pk_add_f32 v[142:143],v[142:143],v[102:103]
C v168,v136,v137
C v169,v138,v139
C v170,v140,v141
C v171,v142,v143
s_nop 0
v_permlane32_swap_b32 v168,v170
v_permlane32_swap_b32 v169,v171
s_nop 0
v_permlane16_swap_b32 v168,v170
v_permlane16_swap_b32 v169,v171
s_add_u32 s94,s89,65536
buffer_store_dwordx4 v[168:171],v251,s[56:59],s94 offen sc1
W vmcnt(4)
v_lshlrev_b32 v104,16,v76
v_and_b32 v105,0xffff0000,v76
v_lshlrev_b32 v106,16,v77
v_and_b32 v107,0xffff0000,v77
v_lshlrev_b32 v108,16,v78
v_and_b32 v109,0xffff0000,v78
v_lshlrev_b32 v110,16,v79
v_and_b32 v111,0xffff0000,v79
v_pk_add_f32 v[144:145],v[144:145],v[104:105]
v_pk_add_f32 v[146:147],v[146:147],v[106:107]
v_pk_add_f32 v[148:149],v[148:149],v[108:109]
v_pk_add_f32 v[150:151],v[150:151],v[110:111]
C v172,v144,v145
C v173,v146,v147
C v174,v148,v149
C v175,v150,v151
s_nop 0
v_permlane32_swap_b32 v172,v174
v_permlane32_swap_b32 v173,v175
s_nop 0
v_permlane16_swap_b32 v172,v174
v_permlane16_swap_b32 v173,v175
s_add_u32 s94,s89,98304
buffer_store_dwordx4 v[172:175],v251,s[56:59],s94 offen sc1
W vmcnt(4)
v_lshlrev_b32 v112,16,v204
v_and_b32 v113,0xffff0000,v204
v_lshlrev_b32 v114,16,v205
v_and_b32 v115,0xffff0000,v205
v_lshlrev_b32 v116,16,v206
v_and_b32 v117,0xffff0000,v206
v_lshlrev_b32 v118,16,v207
v_and_b32 v119,0xffff0000,v207
v_pk_add_f32 v[152:153],v[152:153],v[112:113]
v_pk_add_f32 v[154:155],v[154:155],v[114:115]
v_pk_add_f32 v[156:157],v[156:157],v[116:117]
v_pk_add_f32 v[158:159],v[158:159],v[118:119]
C v176,v152,v153
C v177,v154,v155
C v178,v156,v157
C v179,v158,v159
s_nop 0
v_permlane32_swap_b32 v176,v178
v_permlane32_swap_b32 v177,v179
s_nop 0
v_permlane16_swap_b32 v176,v178
v_permlane16_swap_b32 v177,v179
s_add_u32 s94,s89,131072
buffer_store_dwordx4 v[176:179],v251,s[56:59],s94 offen sc1
s_branch LEND_%=
)ASM"
R"ASM(
LOPA75_%=:
W vmcnt(18)
s_mov_b32 s94,0
LPB5a_%=:
v_cmp_ne_u64 vcc,s[72:73],v[248:249]
s_nop 3
s_cmp_eq_u64 vcc,0
s_cbranch_scc1 LPB5b_%=
s_add_u32 s94,s94,1
s_cmp_gt_u32 s94,262144
s_cbranch_scc1 LPB5b_%=
s_sleep 4
v_lshrrev_b32 v250,1,v245
v_and_b32 v250,0xf8,v250
s_add_u32 s95,s86,512
buffer_load_dwordx2 v[248:249],v250,s[52:55],s95 offen sc1
W vmcnt(0)
s_branch LPB5a_%=
LPB5b_%=:
s_branch LOPN5_%=
)ASM"
R"ASM(
LOPA45_%=:
W vmcnt(63)
v_mov_b32 v248,s72
v_mov_b32 v249,s73
s_mov_b64 exec,1
buffer_store_dwordx2 v[248:249],off,s[52:55],s85 offset:512 sc1
s_mov_b64 exec,-1
s_branch LOPR5_%=
)ASM"
R"ASM(
LOPA65_%=:
v_lshrrev_b32 v250,1,v245
v_and_b32 v250,0xf8,v250
s_add_u32 s95,s86,512
buffer_load_dwordx2 v[248:249],v250,s[52:55],s95 offen sc1
s_branch LOPR5_%=
)ASM"
R"ASM(
LOP4_%=:
s_mov_b32 s69,0
T v[0:1],v160
T v[8:9],v161
T v[16:17],v162
T v[24:25],v163
T v[32:33],v164
T v[40:41],v165
T v[48:49],v166
T v[56:57],v167
T v[2:3],v160 offset:4096
T v[10:11],v161 offset:4096
T v[18:19],v162 offset:4096
T v[26:27],v163 offset:4096
AR v168,a0
AR v169,a1
AR v170,a2
AR v171,a3
AR v172,a20
AR v173,a21
AR v174,a22
AR v175,a23
v_pk_mul_f32 v[168:169],v[168:169],v[188:189] op_sel_hi:[1,0]
v_pk_mul_f32 v[170:171],v[170:171],v[188:189] op_sel_hi:[1,0]
v_pk_mul_f32 v[172:173],v[172:173],v[188:189] op_sel_hi:[1,0]
v_pk_mul_f32 v[174:175],v[174:175],v[188:189] op_sel_hi:[1,0]
C v64,v168,v169
C v65,v170,v171
C v66,v172,v173
C v67,v174,v175
T v[34:35],v164 offset:4096
T v[42:43],v165 offset:4096
T v[50:51],v166 offset:4096
T v[58:59],v167 offset:4096
T v[4:5],v160 offset:8192
T v[12:13],v161 offset:8192
T v[20:21],v162 offset:8192
T v[28:29],v163 offset:8192
s_add_u32 s94,s84,0
buffer_store_dwordx4 v[64:67],v245,s[52:55],s94 offen sc1
AR v176,a40
AR v177,a41
AR v178,a42
AR v179,a43
AR v180,a60
AR v181,a61
AR v182,a62
AR v183,a63
v_pk_mul_f32 v[176:177],v[176:177],v[188:189] op_sel_hi:[1,0]
v_pk_mul_f32 v[178:179],v[178:179],v[188:189] op_sel_hi:[1,0]
v_pk_mul_f32 v[180:181],v[180:181],v[188:189] op_sel_hi:[1,0]
v_pk_mul_f32 v[182:183],v[182:183],v[188:189] op_sel_hi:[1,0]
C v68,v176,v177
C v69,v178,v179
C v70,v180,v181
C v71,v182,v183
T v[36:37],v164 offset:8192
T v[44:45],v165 offset:8192
T v[52:53],v166 offset:8192
T v[60:61],v167 offset:8192
T v[6:7],v160 offset:12288
T v[14:15],v161 offset:12288
T v[22:23],v162 offset:12288
T v[30:31],v163 offset:12288
s_add_u32 s94,s84,5120
buffer_store_dwordx4 v[68:71],v245,s[52:55],s94 offen sc1
AR v168,a80
AR v169,a81
AR v170,a82
AR v171,a83
AR v172,a100
AR v173,a101
AR v174,a102
AR v175,a103
v_pk_mul_f32 v[168:169],v[168:169],v[188:189] op_sel_hi:[1,0]
v_pk_mul_f32 v[170:171],v[170:171],v[188:189] op_sel_hi:[1,0]
v_pk_mul_f32 v[172:173],v[172:173],v[188:189] op_sel_hi:[1,0]
v_pk_mul_f32 v[174:175],v[174:175],v[188:189] op_sel_hi:[1,0]
C v72,v168,v169
C v73,v170,v171
C v74,v172,v173
C v75,v174,v175
T v[38:39],v164 offset:12288
T v[46:47],v165 offset:12288
T v[54:55],v166 offset:12288
T v[62:63],v167 offset:12288
AR v176,a120
AR v177,a121
AR v178,a122
AR v179,a123
AR v180,a140
AR v181,a141
AR v182,a142
AR v183,a143
v_pk_mul_f32 v[176:177],v[176:177],v[188:189] op_sel_hi:[1,0]
v_pk_mul_f32 v[178:179],v[178:179],v[188:189] op_sel_hi:[1,0]
v_pk_mul_f32 v[180:181],v[180:181],v[188:189] op_sel_hi:[1,0]
v_pk_mul_f32 v[182:183],v[182:183],v[188:189] op_sel_hi:[1,0]
C v76,v176,v177
C v77,v178,v179
C v78,v180,v181
C v79,v182,v183
)ASM"
R"ASM(
W lgkmcnt(4)
W vmcnt(20)
M a[0:3],v[0:3],a[224:227],0
W vmcnt(63)
s_barrier
AR v168,a4
AR v169,a5
AR v170,a6
AR v171,a7
AR v172,a24
M a[0:3],v[4:7],a[228:231],a[0:3]
T a[160:161],v160 offset:16384
AR v173,a25
AR v174,a26
AR v175,a27
U v168,v168,v190
U v169,v169,v190
M a[20:23],v[12:15],a[228:231],0
U v170,v170,v190
U v171,v171,v190
U v172,v172,v190
U v173,v173,v190
U v174,v174,v190
M a[20:23],v[8:11],a[224:227],a[20:23]
s_add_u32 s94,s84,10240
T a[168:169],v161 offset:16384
U v175,v175,v190
C v120,v168,v169
C v121,v170,v171
C v122,v172,v173
C v123,v174,v175
M a[40:43],v[16:19],a[224:227],0
buffer_store_dwordx4 v[72:75],v245,s[52:55],s94 offen sc1
AR v176,a44
AR v177,a45
AR v178,a46
AR v179,a47
AR v180,a64
M a[40:43],v[20:23],a[228:231],a[40:43]
s_add_u32 m0,s78,s5
T a[176:177],v162 offset:16384
AR v181,a65
AR v182,a66
AR v183,a67
U v176,v176,v190
U v177,v177,v190
M a[60:63],v[28:31],a[228:231],0
B v218,s[44:47],s80 offen lds
U v178,v178,v190
U v179,v179,v190
U v180,v180,v190
U v181,v181,v190
U v182,v182,v190
M a[60:63],v[24:27],a[224:227],a[60:63]
W lgkmcnt(3)
T a[184:185],v163 offset:16384
U v183,v183,v190
C v124,v176,v177
C v125,v178,v179
C v126,v180,v181
C v127,v182,v183
M a[80:83],v[32:35],a[224:227],0
AR v168,a84
AR v169,a85
AR v170,a86
AR v171,a87
AR v172,a104
M a[80:83],v[36:39],a[228:231],a[80:83]
s_add_u32 s94,s84,15360
T a[192:193],v164 offset:16384
AR v173,a105
AR v174,a106
AR v175,a107
U v168,v168,v190
U v169,v169,v190
M a[100:103],v[44:47],a[228:231],0
buffer_store_dwordx4 v[76:79],v245,s[52:55],s94 offen sc1
U v170,v170,v190
U v171,v171,v190
U v172,v172,v190
U v173,v173,v190
U v174,v174,v190
M a[100:103],v[40:43],a[224:227],a[100:103]
T a[200:201],v165 offset:16384
U v175,v175,v190
C v128,v168,v169
C v129,v170,v171
C v130,v172,v173
C v131,v174,v175
M a[120:123],v[48:51],a[224:227],0
B v219,s[44:47],s80 offen offset:1024 lds
AR v176,a124
AR v177,a125
AR v178,a126
AR v179,a127
AR v180,a144
M a[120:123],v[52:55],a[228:231],a[120:123]
T a[208:209],v166 offset:16384
AR v181,a145
AR v182,a146
AR v183,a147
U v176,v176,v190
U v177,v177,v190
M a[140:143],v[60:63],a[228:231],0
U v178,v178,v190
U v179,v179,v190
U v180,v180,v190
U v181,v181,v190
U v182,v182,v190
M a[140:143],v[56:59],a[224:227],a[140:143]
W vmcnt(22)
s_add_u32 s94,s84,1024
T a[216:217],v167 offset:16384
U v183,v183,v190
C v132,v176,v177
C v133,v178,v179
C v134,v180,v181
C v135,v182,v183
M a[4:7],v[0:3],a[232:235],0
buffer_store_dwordx4 v[120:123],v245,s[52:55],s94 offen sc1
AR v168,a8
AR v169,a9
AR v170,a10
AR v171,a11
AR v172,a28
M a[4:7],v[4:7],a[236:239],a[4:7]
T a[162:163],v160 offset:20480
AR v173,a29
AR v174,a30
AR v175,a31
U v168,v168,v192
U v169,v169,v192
M a[24:27],v[12:15],a[236:239],0
B v220,s[44:47],s80 offen offset:2048 lds
U v170,v170,v192
U v171,v171,v192
U v172,v172,v192
U v173,v173,v192
U v174,v174,v192
M a[24:27],v[8:11],a[232:235],a[24:27]
T a[170:171],v161 offset:20480
U v175,v175,v192
C v136,v168,v169
C v137,v170,v171
C v138,v172,v173
C v139,v174,v175
M a[44:47],v[16:19],a[232:235],0
AR v176,a48
AR v177,a49
AR v178,a50
AR v179,a51
AR v180,a68
M a[44:47],v[20:23],a[236:239],a[44:47]
s_add_u32 s94,s84,6144
T a[178:179],v162 offset:20480
AR v181,a69
AR v182,a70
AR v183,a71
U v176,v176,v192
U v177,v177,v192
M a[64:67],v[28:31],a[236:239],0
buffer_store_dwordx4 v[124:127],v245,s[52:55],s94 offen sc1
U v178,v178,v192
U v179,v179,v192
U v180,v180,v192
U v181,v181,v192
U v182,v182,v192
M a[64:67],v[24:27],a[232:235],a[64:67]
T a[186:187],v163 offset:20480
U v183,v183,v192
C v140,v176,v177
C v141,v178,v179
C v142,v180,v181
C v143,v182,v183
M a[84:87],v[32:35],a[232:235],0
AR v168,a88
AR v169,a89
AR v170,a90
AR v171,a91
AR v172,a108
M a[84:87],v[36:39],a[236:239],a[84:87]
T a[194:195],v164 offset:20480
AR v173,a109
AR v174,a110
AR v175,a111
U v168,v168,v192
U v169,v169,v192
M a[104:107],v[44:47],a[236:239],0
B v221,s[44:47],s80 offen offset:3072 lds
s_add_u32 s80,s80,16384
U v170,v170,v192
U v171,v171,v192
U v172,v172,v192
U v173,v173,v192
U v174,v174,v192
M a[104:107],v[40:43],a[232:235],a[104:107]
s_add_u32 s94,s84,11264
T a[202:203],v165 offset:20480
U v175,v175,v192
C v144,v168,v169
C v145,v170,v171
C v146,v172,v173
C v147,v174,v175
M a[124:127],v[48:51],a[232:235],0
buffer_store_dwordx4 v[128:131],v245,s[52:55],s94 offen sc1
AR v176,a128
AR v177,a129
AR v178,a130
AR v179,a131
AR v180,a148
M a[124:127],v[52:55],a[236:239],a[124:127]
T a[210:211],v166 offset:20480
AR v181,a149
AR v182,a150
AR v183,a151
U v176,v176,v192
U v177,v177,v192
M a[144:147],v[60:63],a[236:239],0
U v178,v178,v192
U v179,v179,v192
U v180,v180,v192
U v181,v181,v192
U v182,v182,v192
M a[144:147],v[56:59],a[232:235],a[144:147]
W vmcnt(25)
s_add_u32 m0,m0,4096
T a[218:219],v167 offset:20480
U v183,v183,v192
C v148,v176,v177
C v149,v178,v179
C v150,v180,v181
C v151,v182,v183
M a[8:11],v[0:3],a[240:243],0
B v218,s[44:47],s80 offen lds
AR v168,a12
AR v169,a13
AR v170,a14
AR v171,a15
AR v172,a32
M a[8:11],v[4:7],a[244:247],a[8:11]
T a[164:165],v160 offset:24576
AR v173,a33
AR v174,a34
AR v175,a35
U v168,v168,v194
U v169,v169,v194
M a[28:31],v[12:15],a[244:247],0
U v170,v170,v194
U v171,v171,v194
U v172,v172,v194
U v173,v173,v194
U v174,v174,v194
M a[28:31],v[8:11],a[240:243],a[28:31]
s_add_u32 s94,s84,16384
T a[172:173],v161 offset:24576
U v175,v175,v194
C v152,v168,v169
C v153,v170,v171
C v154,v172,v173
C v155,v174,v175
M a[48:51],v[16:19],a[240:243],0
buffer_store_dwordx4 v[132:135],v245,s[52:55],s94 offen sc1
AR v176,a52
AR v177,a53
AR v178,a54
AR v179,a55
AR v180,a72
M a[48:51],v[20:23],a[244:247],a[48:51]
T a[180:181],v162 offset:24576
AR v181,a73
AR v182,a74
AR v183,a75
U v176,v176,v194
U v177,v177,v194
M a[68:71],v[28:31],a[244:247],0
U v178,v178,v194
U v179,v179,v194
U v180,v180,v194
U v181,v181,v194
U v182,v182,v194
M a[68:71],v[24:27],a[240:243],a[68:71]
T a[188:189],v163 offset:24576
U v183,v183,v194
C v156,v176,v177
C v157,v178,v179
C v158,v180,v181
C v159,v182,v183
M a[88:91],v[32:35],a[240:243],0
B v219,s[44:47],s80 offen offset:1024 lds
AR v168,a92
AR v169,a93
AR v170,a94
AR v171,a95
AR v172,a112
M a[88:91],v[36:39],a[244:247],a[88:91]
s_add_u32 s94,s84,2048
T a[196:197],v164 offset:24576
AR v173,a113
AR v174,a114
AR v175,a115
)ASM"
R"ASM(
U v168,v168,v194
U v169,v169,v194
M a[108:111],v[44:47],a[244:247],0
buffer_store_dwordx4 v[136:139],v245,s[52:55],s94 offen sc1
U v170,v170,v194
U v171,v171,v194
U v172,v172,v194
U v173,v173,v194
U v174,v174,v194
M a[108:111],v[40:43],a[240:243],a[108:111]
T a[204:205],v165 offset:24576
U v175,v175,v194
C v198,v168,v169
C v199,v170,v171
C v200,v172,v173
C v201,v174,v175
M a[128:131],v[48:51],a[240:243],0
AR v176,a132
AR v177,a133
AR v178,a134
AR v179,a135
AR v180,a152
M a[128:131],v[52:55],a[244:247],a[128:131]
T a[212:213],v166 offset:24576
AR v181,a153
AR v182,a154
AR v183,a155
U v176,v176,v194
U v177,v177,v194
M a[148:151],v[60:63],a[244:247],0
B v220,s[44:47],s80 offen offset:2048 lds
U v178,v178,v194
U v179,v179,v194
U v180,v180,v194
U v181,v181,v194
U v182,v182,v194
M a[148:151],v[56:59],a[240:243],a[148:151]
W vmcnt(28)
s_add_u32 s94,s84,7168
T a[220:221],v167 offset:24576
U v183,v183,v194
C v202,v176,v177
C v203,v178,v179
C v204,v180,v181
C v205,v182,v183
M a[12:15],v[0:3],a[248:251],0
buffer_store_dwordx4 v[140:143],v245,s[52:55],s94 offen sc1
AR v168,a16
AR v169,a17
AR v170,a18
AR v171,a19
AR v172,a36
M a[12:15],v[4:7],a[252:255],a[12:15]
T a[166:167],v160 offset:28672
AR v173,a37
AR v174,a38
AR v175,a39
U v168,v168,v196
U v169,v169,v196
M a[32:35],v[12:15],a[252:255],0
U v170,v170,v196
U v171,v171,v196
U v172,v172,v196
U v173,v173,v196
U v174,v174,v196
M a[32:35],v[8:11],a[248:251],a[32:35]
T a[174:175],v161 offset:28672
U v175,v175,v196
C v206,v168,v169
C v207,v170,v171
C v208,v172,v173
C v209,v174,v175
M a[52:55],v[16:19],a[248:251],0
AR v176,a56
AR v177,a57
AR v178,a58
AR v179,a59
AR v180,a76
M a[52:55],v[20:23],a[252:255],a[52:55]
T a[182:183],v162 offset:28672
AR v181,a77
AR v182,a78
AR v183,a79
U v176,v176,v196
U v177,v177,v196
M a[72:75],v[28:31],a[252:255],0
B v221,s[44:47],s80 offen offset:3072 lds
s_add_u32 s80,s80,245760
s_add_u32 s5,s5,16384
s_and_b32 s5,s5,0xc000
U v178,v178,v196
U v179,v179,v196
U v180,v180,v196
U v181,v181,v196
U v182,v182,v196
M a[72:75],v[24:27],a[248:251],a[72:75]
s_add_u32 s94,s84,12288
T a[190:191],v163 offset:28672
U v183,v183,v196
C v210,v176,v177
C v211,v178,v179
C v212,v180,v181
C v213,v182,v183
M a[92:95],v[32:35],a[248:251],0
buffer_store_dwordx4 v[144:147],v245,s[52:55],s94 offen sc1
AR v168,a96
AR v169,a97
AR v170,a98
AR v171,a99
AR v172,a116
M a[92:95],v[36:39],a[252:255],a[92:95]
s_add_u32 s94,s84,17408
T a[198:199],v164 offset:28672
AR v173,a117
AR v174,a118
AR v175,a119
U v168,v168,v196
U v169,v169,v196
M a[112:115],v[44:47],a[252:255],0
buffer_store_dwordx4 v[148:151],v245,s[52:55],s94 offen sc1
U v170,v170,v196
U v171,v171,v196
U v172,v172,v196
U v173,v173,v196
U v174,v174,v196
M a[112:115],v[40:43],a[248:251],a[112:115]
T a[206:207],v165 offset:28672
U v175,v175,v196
C v214,v168,v169
C v215,v170,v171
C v216,v172,v173
C v217,v174,v175
M a[132:135],v[48:51],a[248:251],0
AR v176,a136
AR v177,a137
AR v178,a138
AR v179,a139
AR v180,a156
M a[132:135],v[52:55],a[252:255],a[132:135]
T a[214:215],v166 offset:28672
AR v181,a157
AR v182,a158
AR v183,a159
U v176,v176,v196
U v177,v177,v196
M a[152:155],v[60:63],a[252:255],0
U v178,v178,v196
U v179,v179,v196
U v180,v180,v196
U v181,v181,v196
U v182,v182,v196
M a[152:155],v[56:59],a[248:251],a[152:155]
T a[222:223],v167 offset:28672
U v183,v183,v196
C v236,v176,v177
C v237,v178,v179
C v238,v180,v181
C v239,v182,v183
)ASM"
R"ASM(
W lgkmcnt(4)
W vmcnt(28)
M a[0:3],a[160:163],v[80:83],a[0:3]
W vmcnt(20)
s_barrier
v_xor_b32 v160,0x8000,v160
v_xor_b32 v161,0x8000,v161
M a[0:3],a[164:167],v[84:87],a[0:3]
T v[0:1],v160
M a[20:23],a[172:175],v[84:87],a[20:23]
B a[224:227],v245,s[96:99],s79 offen sc1
v_xor_b32 v162,0x8000,v162
M a[20:23],a[168:171],v[80:83],a[20:23]
s_add_u32 m0,s78,s5
T v[8:9],v161
M a[40:43],a[176:179],v[80:83],a[40:43]
B v218,s[44:47],s80 offen lds
v_xor_b32 v163,0x8000,v163
M a[40:43],a[180:183],v[84:87],a[40:43]
T v[16:17],v162
M a[60:63],a[188:191],v[84:87],a[60:63]
v_xor_b32 v164,0x8000,v164
M a[60:63],a[184:187],v[80:83],a[60:63]
W lgkmcnt(3)
T v[24:25],v163
M a[80:83],a[192:195],v[80:83],a[80:83]
B a[228:231],v240,s[96:99],s79 offen offset:1024 sc1
v_xor_b32 v165,0x8000,v165
M a[80:83],a[196:199],v[84:87],a[80:83]
s_add_u32 s94,s84,3072
T v[32:33],v164
M a[100:103],a[204:207],v[84:87],a[100:103]
buffer_store_dwordx4 v[152:155],v245,s[52:55],s94 offen sc1
v_xor_b32 v166,0x8000,v166
M a[100:103],a[200:203],v[80:83],a[100:103]
T v[40:41],v165
M a[120:123],a[208:211],v[80:83],a[120:123]
B v219,s[44:47],s80 offen offset:1024 lds
v_xor_b32 v167,0x8000,v167
M a[120:123],a[212:215],v[84:87],a[120:123]
T v[48:49],v166
M a[140:143],a[220:223],v[84:87],a[140:143]
M a[140:143],a[216:219],v[80:83],a[140:143]
W vmcnt(31)
T v[56:57],v167
M a[4:7],a[160:163],v[88:91],a[4:7]
M a[4:7],a[164:167],v[92:95],a[4:7]
T v[2:3],v160 offset:4096
M a[24:27],a[172:175],v[92:95],a[24:27]
B v220,s[44:47],s80 offen offset:2048 lds
M a[24:27],a[168:171],v[88:91],a[24:27]
T v[10:11],v161 offset:4096
M a[44:47],a[176:179],v[88:91],a[44:47]
B a[232:235],v245,s[96:99],s79 offen offset:1024 sc1
M a[44:47],a[180:183],v[92:95],a[44:47]
T v[18:19],v162 offset:4096
M a[64:67],a[188:191],v[92:95],a[64:67]
M a[64:67],a[184:187],v[88:91],a[64:67]
T v[26:27],v163 offset:4096
M a[84:87],a[192:195],v[88:91],a[84:87]
B a[236:239],v240,s[96:99],s79 offen offset:2048 sc1
M a[84:87],a[196:199],v[92:95],a[84:87]
s_add_u32 s94,s84,8192
T v[34:35],v164 offset:4096
M a[104:107],a[204:207],v[92:95],a[104:107]
buffer_store_dwordx4 v[156:159],v245,s[52:55],s94 offen sc1
M a[104:107],a[200:203],v[88:91],a[104:107]
T v[42:43],v165 offset:4096
M a[124:127],a[208:211],v[88:91],a[124:127]
B v221,s[44:47],s80 offen offset:3072 lds
s_add_u32 s80,s80,16384
M a[124:127],a[212:215],v[92:95],a[124:127]
T v[50:51],v166 offset:4096
M a[144:147],a[220:223],v[92:95],a[144:147]
M a[144:147],a[216:219],v[88:91],a[144:147]
W vmcnt(34)
s_add_u32 m0,m0,4096
T v[58:59],v167 offset:4096
M a[8:11],a[160:163],v[96:99],a[8:11]
B v218,s[44:47],s80 offen lds
M a[8:11],a[164:167],v[100:103],a[8:11]
T v[4:5],v160 offset:8192
M a[28:31],a[172:175],v[100:103],a[28:31]
B a[240:243],v245,s[96:99],s79 offen offset:2048 sc1
M a[28:31],a[168:171],v[96:99],a[28:31]
T v[12:13],v161 offset:8192
M a[48:51],a[176:179],v[96:99],a[48:51]
M a[48:51],a[180:183],v[100:103],a[48:51]
T v[20:21],v162 offset:8192
M a[68:71],a[188:191],v[100:103],a[68:71]
M a[68:71],a[184:187],v[96:99],a[68:71]
T v[28:29],v163 offset:8192
M a[88:91],a[192:195],v[96:99],a[88:91]
B v219,s[44:47],s80 offen offset:1024 lds
M a[88:91],a[196:199],v[100:103],a[88:91]
T v[36:37],v164 offset:8192
M a[108:111],a[204:207],v[100:103],a[108:111]
B a[244:247],v240,s[96:99],s79 offen offset:3072 sc1
s_add_u32 s94,s84,13312
M a[108:111],a[200:203],v[96:99],a[108:111]
T v[44:45],v165 offset:8192
buffer_store_dwordx4 v[198:201],v245,s[52:55],s94 offen sc1
M a[128:131],a[208:211],v[96:99],a[128:131]
M a[128:131],a[212:215],v[100:103],a[128:131]
T v[52:53],v166 offset:8192
M a[148:151],a[220:223],v[100:103],a[148:151]
B v220,s[44:47],s80 offen offset:2048 lds
M a[148:151],a[216:219],v[96:99],a[148:151]
W vmcnt(38)
T v[60:61],v167 offset:8192
M a[12:15],a[160:163],v[104:107],a[12:15]
M a[12:15],a[164:167],v[108:111],a[12:15]
T v[6:7],v160 offset:12288
M a[32:35],a[172:175],v[108:111],a[32:35]
B a[248:251],v245,s[96:99],s79 offen offset:3072 sc1
M a[32:35],a[168:171],v[104:107],a[32:35]
T v[14:15],v161 offset:12288
M a[52:55],a[176:179],v[104:107],a[52:55]
M a[52:55],a[180:183],v[108:111],a[52:55]
T v[22:23],v162 offset:12288
M a[72:75],a[188:191],v[108:111],a[72:75]
B v221,s[44:47],s80 offen offset:3072 lds
s_add_u32 s80,s80,245760
s_add_u32 s5,s5,16384
s_and_b32 s5,s5,0xc000
M a[72:75],a[184:187],v[104:107],a[72:75]
T v[30:31],v163 offset:12288
M a[92:95],a[192:195],v[104:107],a[92:95]
B a[252:255],v241,s[96:99],s79 offen sc1
s_add_u32 s79,s79,40960
M a[92:95],a[196:199],v[108:111],a[92:95]
s_add_u32 s94,s84,18432
T v[38:39],v164 offset:12288
M a[112:115],a[204:207],v[108:111],a[112:115]
buffer_store_dwordx4 v[202:205],v245,s[52:55],s94 offen sc1
M a[112:115],a[200:203],v[104:107],a[112:115]
T v[46:47],v165 offset:12288
M a[132:135],a[208:211],v[104:107],a[132:135]
M a[132:135],a[212:215],v[108:111],a[132:135]
T v[54:55],v166 offset:12288
M a[152:155],a[220:223],v[108:111],a[152:155]
M a[152:155],a[216:219],v[104:107],a[152:155]
T v[62:63],v167 offset:12288
)ASM"
R"ASM(
W lgkmcnt(4)
W vmcnt(17)
M a[0:3],v[0:3],a[224:227],a[0:3]
W vmcnt(63)
s_barrier
M a[0:3],v[4:7],a[228:231],a[0:3]
T a[160:161],v160 offset:16384
M a[20:23],v[12:15],a[228:231],a[20:23]
B v[80:83],v245,s[96:99],s79 offen sc1
M a[20:23],v[8:11],a[224:227],a[20:23]
s_add_u32 m0,s78,s5
T a[168:169],v161 offset:16384
M a[40:43],v[16:19],a[224:227],a[40:43]
B v218,s[44:47],s80 offen lds
M a[40:43],v[20:23],a[228:231],a[40:43]
T a[176:177],v162 offset:16384
M a[60:63],v[28:31],a[228:231],a[60:63]
M a[60:63],v[24:27],a[224:227],a[60:63]
W lgkmcnt(3)
T a[184:185],v163 offset:16384
M a[80:83],v[32:35],a[224:227],a[80:83]
B v[84:87],v240,s[96:99],s79 offen offset:1024 sc1
M a[80:83],v[36:39],a[228:231],a[80:83]
s_add_u32 s94,s33,4096
T a[192:193],v164 offset:16384
M a[100:103],v[44:47],a[228:231],a[100:103]
buffer_store_dwordx4 v[206:209],v245,s[52:55],s94 offen sc1
M a[100:103],v[40:43],a[224:227],a[100:103]
T a[200:201],v165 offset:16384
M a[120:123],v[48:51],a[224:227],a[120:123]
B v219,s[44:47],s80 offen offset:1024 lds
M a[120:123],v[52:55],a[228:231],a[120:123]
T a[208:209],v166 offset:16384
M a[140:143],v[60:63],a[228:231],a[140:143]
M a[140:143],v[56:59],a[224:227],a[140:143]
W vmcnt(17)
T a[216:217],v167 offset:16384
M a[4:7],v[0:3],a[232:235],a[4:7]
M a[4:7],v[4:7],a[236:239],a[4:7]
T a[162:163],v160 offset:20480
M a[24:27],v[12:15],a[236:239],a[24:27]
B v220,s[44:47],s80 offen offset:2048 lds
M a[24:27],v[8:11],a[232:235],a[24:27]
T a[170:171],v161 offset:20480
M a[44:47],v[16:19],a[232:235],a[44:47]
B v[88:91],v245,s[96:99],s79 offen offset:1024 sc1
M a[44:47],v[20:23],a[236:239],a[44:47]
T a[178:179],v162 offset:20480
M a[64:67],v[28:31],a[236:239],a[64:67]
M a[64:67],v[24:27],a[232:235],a[64:67]
T a[186:187],v163 offset:20480
M a[84:87],v[32:35],a[232:235],a[84:87]
B v[92:95],v240,s[96:99],s79 offen offset:2048 sc1
M a[84:87],v[36:39],a[236:239],a[84:87]
s_add_u32 s94,s33,9216
T a[194:195],v164 offset:20480
M a[104:107],v[44:47],a[236:239],a[104:107]
buffer_store_dwordx4 v[210:213],v245,s[52:55],s94 offen sc1
M a[104:107],v[40:43],a[232:235],a[104:107]
T a[202:203],v165 offset:20480
M a[124:127],v[48:51],a[232:235],a[124:127]
B v221,s[44:47],s80 offen offset:3072 lds
s_add_u32 s80,s80,16384
M a[124:127],v[52:55],a[236:239],a[124:127]
T a[210:211],v166 offset:20480
M a[144:147],v[60:63],a[236:239],a[144:147]
M a[144:147],v[56:59],a[232:235],a[144:147]
W vmcnt(16)
s_add_u32 m0,m0,4096
T a[218:219],v167 offset:20480
M a[8:11],v[0:3],a[240:243],a[8:11]
B v218,s[44:47],s80 offen lds
M a[8:11],v[4:7],a[244:247],a[8:11]
T a[164:165],v160 offset:24576
M a[28:31],v[12:15],a[244:247],a[28:31]
B v[96:99],v245,s[96:99],s79 offen offset:2048 sc1
M a[28:31],v[8:11],a[240:243],a[28:31]
T a[172:173],v161 offset:24576
M a[48:51],v[16:19],a[240:243],a[48:51]
M a[48:51],v[20:23],a[244:247],a[48:51]
T a[180:181],v162 offset:24576
M a[68:71],v[28:31],a[244:247],a[68:71]
M a[68:71],v[24:27],a[240:243],a[68:71]
T a[188:189],v163 offset:24576
M a[88:91],v[32:35],a[240:243],a[88:91]
B v219,s[44:47],s80 offen offset:1024 lds
M a[88:91],v[36:39],a[244:247],a[88:91]
T a[196:197],v164 offset:24576
M a[108:111],v[44:47],a[244:247],a[108:111]
B v[100:103],v240,s[96:99],s79 offen offset:3072 sc1
s_add_u32 s94,s33,14336
M a[108:111],v[40:43],a[240:243],a[108:111]
T a[204:205],v165 offset:24576
buffer_store_dwordx4 v[214:217],v245,s[52:55],s94 offen sc1
M a[128:131],v[48:51],a[240:243],a[128:131]
M a[128:131],v[52:55],a[244:247],a[128:131]
T a[212:213],v166 offset:24576
M a[148:151],v[60:63],a[244:247],a[148:151]
B v220,s[44:47],s80 offen offset:2048 lds
M a[148:151],v[56:59],a[240:243],a[148:151]
W vmcnt(17)
T a[220:221],v167 offset:24576
M a[12:15],v[0:3],a[248:251],a[12:15]
M a[12:15],v[4:7],a[252:255],a[12:15]
T a[166:167],v160 offset:28672
M a[32:35],v[12:15],a[252:255],a[32:35]
B v[104:107],v245,s[96:99],s79 offen offset:3072 sc1
M a[32:35],v[8:11],a[248:251],a[32:35]
T a[174:175],v161 offset:28672
M a[52:55],v[16:19],a[248:251],a[52:55]
M a[52:55],v[20:23],a[252:255],a[52:55]
T a[182:183],v162 offset:28672
M a[72:75],v[28:31],a[252:255],a[72:75]
B v221,s[44:47],s80 offen offset:3072 lds
s_add_u32 s80,s80,245760
s_add_u32 s5,s5,16384
s_and_b32 s5,s5,0xc000
M a[72:75],v[24:27],a[248:251],a[72:75]
T a[190:191],v163 offset:28672
M a[92:95],v[32:35],a[248:251],a[92:95]
B v[108:111],v241,s[96:99],s79 offen sc1
s_add_u32 s79,s79,40960
M a[92:95],v[36:39],a[252:255],a[92:95]
s_add_u32 s94,s33,19456
T a[198:199],v164 offset:28672
M a[112:115],v[44:47],a[252:255],a[112:115]
buffer_store_dwordx4 v[236:239],v245,s[52:55],s94 offen sc1
M a[112:115],v[40:43],a[248:251],a[112:115]
T a[206:207],v165 offset:28672
M a[132:135],v[48:51],a[248:251],a[132:135]
M a[132:135],v[52:55],a[252:255],a[132:135]
T a[214:215],v166 offset:28672
M a[152:155],v[60:63],a[252:255],a[152:155]
M a[152:155],v[56:59],a[248:251],a[152:155]
T a[222:223],v167 offset:28672
)ASM"
R"ASM(
.p2align 4
LOPL4_%=:
s_cmp_eq_u32 s69,2
s_cbranch_scc1 LOPA74_%=
LOPN4_%=:
)ASM"
R"ASM(
W lgkmcnt(4)
W vmcnt(13)
M a[0:3],a[160:163],v[80:83],a[0:3]
W vmcnt(63)
s_barrier
v_xor_b32 v160,0x8000,v160
v_xor_b32 v161,0x8000,v161
M a[0:3],a[164:167],v[84:87],a[0:3]
T v[0:1],v160
M a[20:23],a[172:175],v[84:87],a[20:23]
B a[224:227],v245,s[96:99],s79 offen sc1
v_xor_b32 v162,0x8000,v162
M a[20:23],a[168:171],v[80:83],a[20:23]
s_add_u32 m0,s78,s5
T v[8:9],v161
M a[40:43],a[176:179],v[80:83],a[40:43]
B v218,s[44:47],s80 offen lds
v_xor_b32 v163,0x8000,v163
M a[40:43],a[180:183],v[84:87],a[40:43]
T v[16:17],v162
M a[60:63],a[188:191],v[84:87],a[60:63]
v_xor_b32 v164,0x8000,v164
M a[60:63],a[184:187],v[80:83],a[60:63]
W lgkmcnt(3)
T v[24:25],v163
M a[80:83],a[192:195],v[80:83],a[80:83]
B a[228:231],v240,s[96:99],s79 offen offset:1024 sc1
v_xor_b32 v165,0x8000,v165
M a[80:83],a[196:199],v[84:87],a[80:83]
T v[32:33],v164
M a[100:103],a[204:207],v[84:87],a[100:103]
v_xor_b32 v166,0x8000,v166
M a[100:103],a[200:203],v[80:83],a[100:103]
T v[40:41],v165
M a[120:123],a[208:211],v[80:83],a[120:123]
B v219,s[44:47],s80 offen offset:1024 lds
v_xor_b32 v167,0x8000,v167
M a[120:123],a[212:215],v[84:87],a[120:123]
T v[48:49],v166
M a[140:143],a[220:223],v[84:87],a[140:143]
M a[140:143],a[216:219],v[80:83],a[140:143]
W vmcnt(13)
T v[56:57],v167
M a[4:7],a[160:163],v[88:91],a[4:7]
M a[4:7],a[164:167],v[92:95],a[4:7]
T v[2:3],v160 offset:4096
M a[24:27],a[172:175],v[92:95],a[24:27]
B v220,s[44:47],s80 offen offset:2048 lds
M a[24:27],a[168:171],v[88:91],a[24:27]
T v[10:11],v161 offset:4096
M a[44:47],a[176:179],v[88:91],a[44:47]
B a[232:235],v245,s[96:99],s79 offen offset:1024 sc1
M a[44:47],a[180:183],v[92:95],a[44:47]
T v[18:19],v162 offset:4096
M a[64:67],a[188:191],v[92:95],a[64:67]
M a[64:67],a[184:187],v[88:91],a[64:67]
T v[26:27],v163 offset:4096
M a[84:87],a[192:195],v[88:91],a[84:87]
B a[236:239],v240,s[96:99],s79 offen offset:2048 sc1
M a[84:87],a[196:199],v[92:95],a[84:87]
T v[34:35],v164 offset:4096
M a[104:107],a[204:207],v[92:95],a[104:107]
B v221,s[44:47],s80 offen offset:3072 lds
s_add_u32 s80,s80,16384
M a[104:107],a[200:203],v[88:91],a[104:107]
T v[42:43],v165 offset:4096
M a[124:127],a[208:211],v[88:91],a[124:127]
M a[124:127],a[212:215],v[92:95],a[124:127]
T v[50:51],v166 offset:4096
M a[144:147],a[220:223],v[92:95],a[144:147]
M a[144:147],a[216:219],v[88:91],a[144:147]
W vmcnt(12)
s_add_u32 m0,m0,4096
T v[58:59],v167 offset:4096
M a[8:11],a[160:163],v[96:99],a[8:11]
B v218,s[44:47],s80 offen lds
M a[8:11],a[164:167],v[100:103],a[8:11]
T v[4:5],v160 offset:8192
M a[28:31],a[172:175],v[100:103],a[28:31]
B a[240:243],v245,s[96:99],s79 offen offset:2048 sc1
M a[28:31],a[168:171],v[96:99],a[28:31]
T v[12:13],v161 offset:8192
M a[48:51],a[176:179],v[96:99],a[48:51]
M a[48:51],a[180:183],v[100:103],a[48:51]
T v[20:21],v162 offset:8192
M a[68:71],a[188:191],v[100:103],a[68:71]
M a[68:71],a[184:187],v[96:99],a[68:71]
T v[28:29],v163 offset:8192
M a[88:91],a[192:195],v[96:99],a[88:91]
B v219,s[44:47],s80 offen offset:1024 lds
M a[88:91],a[196:199],v[100:103],a[88:91]
T v[36:37],v164 offset:8192
M a[108:111],a[204:207],v[100:103],a[108:111]
B a[244:247],v240,s[96:99],s79 offen offset:3072 sc1
M a[108:111],a[200:203],v[96:99],a[108:111]
T v[44:45],v165 offset:8192
M a[128:131],a[208:211],v[96:99],a[128:131]
M a[128:131],a[212:215],v[100:103],a[128:131]
T v[52:53],v166 offset:8192
M a[148:151],a[220:223],v[100:103],a[148:151]
B v220,s[44:47],s80 offen offset:2048 lds
M a[148:151],a[216:219],v[96:99],a[148:151]
W vmcnt(13)
T v[60:61],v167 offset:8192
M a[12:15],a[160:163],v[104:107],a[12:15]
M a[12:15],a[164:167],v[108:111],a[12:15]
T v[6:7],v160 offset:12288
M a[32:35],a[172:175],v[108:111],a[32:35]
B a[248:251],v245,s[96:99],s79 offen offset:3072 sc1
M a[32:35],a[168:171],v[104:107],a[32:35]
T v[14:15],v161 offset:12288
M a[52:55],a[176:179],v[104:107],a[52:55]
M a[52:55],a[180:183],v[108:111],a[52:55]
T v[22:23],v162 offset:12288
M a[72:75],a[188:191],v[108:111],a[72:75]
B v221,s[44:47],s80 offen offset:3072 lds
s_add_u32 s80,s80,245760
s_add_u32 s5,s5,16384
s_and_b32 s5,s5,0xc000
M a[72:75],a[184:187],v[104:107],a[72:75]
T v[30:31],v163 offset:12288
M a[92:95],a[192:195],v[104:107],a[92:95]
B a[252:255],v241,s[96:99],s79 offen sc1
s_add_u32 s79,s79,40960
M a[92:95],a[196:199],v[108:111],a[92:95]
T v[38:39],v164 offset:12288
M a[112:115],a[204:207],v[108:111],a[112:115]
M a[112:115],a[200:203],v[104:107],a[112:115]
T v[46:47],v165 offset:12288
M a[132:135],a[208:211],v[104:107],a[132:135]
M a[132:135],a[212:215],v[108:111],a[132:135]
T v[54:55],v166 offset:12288
M a[152:155],a[220:223],v[108:111],a[152:155]
M a[152:155],a[216:219],v[104:107],a[152:155]
T v[62:63],v167 offset:12288
s_cmp_eq_u32 s69,0
s_cbranch_scc1 LOPA44_%=
s_cmp_eq_u32 s69,1
s_cbranch_scc1 LOPA64_%=
LOPR4_%=:
)ASM"
R"ASM(
W lgkmcnt(4)
W vmcnt(13)
M a[0:3],v[0:3],a[224:227],a[0:3]
W vmcnt(63)
s_barrier
M a[0:3],v[4:7],a[228:231],a[0:3]
T a[160:161],v160 offset:16384
M a[20:23],v[12:15],a[228:231],a[20:23]
B v[80:83],v245,s[96:99],s79 offen sc1
M a[20:23],v[8:11],a[224:227],a[20:23]
s_add_u32 m0,s78,s5
T a[168:169],v161 offset:16384
M a[40:43],v[16:19],a[224:227],a[40:43]
B v218,s[44:47],s80 offen lds
M a[40:43],v[20:23],a[228:231],a[40:43]
T a[176:177],v162 offset:16384
M a[60:63],v[28:31],a[228:231],a[60:63]
M a[60:63],v[24:27],a[224:227],a[60:63]
W lgkmcnt(3)
T a[184:185],v163 offset:16384
M a[80:83],v[32:35],a[224:227],a[80:83]
B v[84:87],v240,s[96:99],s79 offen offset:1024 sc1
M a[80:83],v[36:39],a[228:231],a[80:83]
T a[192:193],v164 offset:16384
M a[100:103],v[44:47],a[228:231],a[100:103]
M a[100:103],v[40:43],a[224:227],a[100:103]
T a[200:201],v165 offset:16384
M a[120:123],v[48:51],a[224:227],a[120:123]
B v219,s[44:47],s80 offen offset:1024 lds
M a[120:123],v[52:55],a[228:231],a[120:123]
T a[208:209],v166 offset:16384
M a[140:143],v[60:63],a[228:231],a[140:143]
M a[140:143],v[56:59],a[224:227],a[140:143]
W vmcnt(13)
T a[216:217],v167 offset:16384
M a[4:7],v[0:3],a[232:235],a[4:7]
M a[4:7],v[4:7],a[236:239],a[4:7]
T a[162:163],v160 offset:20480
M a[24:27],v[12:15],a[236:239],a[24:27]
B v220,s[44:47],s80 offen offset:2048 lds
M a[24:27],v[8:11],a[232:235],a[24:27]
T a[170:171],v161 offset:20480
M a[44:47],v[16:19],a[232:235],a[44:47]
B v[88:91],v245,s[96:99],s79 offen offset:1024 sc1
M a[44:47],v[20:23],a[236:239],a[44:47]
T a[178:179],v162 offset:20480
M a[64:67],v[28:31],a[236:239],a[64:67]
M a[64:67],v[24:27],a[232:235],a[64:67]
T a[186:187],v163 offset:20480
M a[84:87],v[32:35],a[232:235],a[84:87]
B v[92:95],v240,s[96:99],s79 offen offset:2048 sc1
M a[84:87],v[36:39],a[236:239],a[84:87]
T a[194:195],v164 offset:20480
M a[104:107],v[44:47],a[236:239],a[104:107]
B v221,s[44:47],s80 offen offset:3072 lds
s_add_u32 s80,s80,16384
M a[104:107],v[40:43],a[232:235],a[104:107]
T a[202:203],v165 offset:20480
M a[124:127],v[48:51],a[232:235],a[124:127]
M a[124:127],v[52:55],a[236:239],a[124:127]
T a[210:211],v166 offset:20480
M a[144:147],v[60:63],a[236:239],a[144:147]
M a[144:147],v[56:59],a[232:235],a[144:147]
W vmcnt(12)
s_add_u32 m0,m0,4096
T a[218:219],v167 offset:20480
M a[8:11],v[0:3],a[240:243],a[8:11]
B v218,s[44:47],s80 offen lds
M a[8:11],v[4:7],a[244:247],a[8:11]
T a[164:165],v160 offset:24576
M a[28:31],v[12:15],a[244:247],a[28:31]
B v[96:99],v245,s[96:99],s79 offen offset:2048 sc1
M a[28:31],v[8:11],a[240:243],a[28:31]
T a[172:173],v161 offset:24576
M a[48:51],v[16:19],a[240:243],a[48:51]
M a[48:51],v[20:23],a[244:247],a[48:51]
T a[180:181],v162 offset:24576
M a[68:71],v[28:31],a[244:247],a[68:71]
M a[68:71],v[24:27],a[240:243],a[68:71]
T a[188:189],v163 offset:24576
M a[88:91],v[32:35],a[240:243],a[88:91]
B v219,s[44:47],s80 offen offset:1024 lds
M a[88:91],v[36:39],a[244:247],a[88:91]
T a[196:197],v164 offset:24576
M a[108:111],v[44:47],a[244:247],a[108:111]
B v[100:103],v240,s[96:99],s79 offen offset:3072 sc1
M a[108:111],v[40:43],a[240:243],a[108:111]
T a[204:205],v165 offset:24576
M a[128:131],v[48:51],a[240:243],a[128:131]
M a[128:131],v[52:55],a[244:247],a[128:131]
T a[212:213],v166 offset:24576
M a[148:151],v[60:63],a[244:247],a[148:151]
B v220,s[44:47],s80 offen offset:2048 lds
M a[148:151],v[56:59],a[240:243],a[148:151]
W vmcnt(13)
T a[220:221],v167 offset:24576
M a[12:15],v[0:3],a[248:251],a[12:15]
M a[12:15],v[4:7],a[252:255],a[12:15]
T a[166:167],v160 offset:28672
M a[32:35],v[12:15],a[252:255],a[32:35]
B v[104:107],v245,s[96:99],s79 offen offset:3072 sc1
M a[32:35],v[8:11],a[248:251],a[32:35]
T a[174:175],v161 offset:28672
M a[52:55],v[16:19],a[248:251],a[52:55]
M a[52:55],v[20:23],a[252:255],a[52:55]
T a[182:183],v162 offset:28672
M a[72:75],v[28:31],a[252:255],a[72:75]
B v221,s[44:47],s80 offen offset:3072 lds
s_add_u32 s80,s80,245760
s_add_u32 s5,s5,16384
s_and_b32 s5,s5,0xc000
M a[72:75],v[24:27],a[248:251],a[72:75]
T a[190:191],v163 offset:28672
M a[92:95],v[32:35],a[248:251],a[92:95]
B v[108:111],v241,s[96:99],s79 offen sc1
s_add_u32 s79,s79,40960
M a[92:95],v[36:39],a[252:255],a[92:95]
T a[198:199],v164 offset:28672
M a[112:115],v[44:47],a[252:255],a[112:115]
M a[112:115],v[40:43],a[248:251],a[112:115]
T a[206:207],v165 offset:28672
M a[132:135],v[48:51],a[248:251],a[132:135]
M a[132:135],v[52:55],a[252:255],a[132:135]
T a[214:215],v166 offset:28672
M a[152:155],v[60:63],a[252:255],a[152:155]
M a[152:155],v[56:59],a[248:251],a[152:155]
T a[222:223],v167 offset:28672
s_add_u32 s69,s69,1
s_cmp_lt_u32 s69,5
s_cbranch_scc1 LOPL4_%=
)ASM"
R"ASM(
W lgkmcnt(4)
W vmcnt(13)
M a[0:3],a[160:163],v[80:83],a[0:3]
W vmcnt(63)
s_barrier
v_xor_b32 v160,0x8000,v160
v_xor_b32 v161,0x8000,v161
M a[0:3],a[164:167],v[84:87],a[0:3]
T v[0:1],v160
M a[20:23],a[172:175],v[84:87],a[20:23]
s_add_u32 s34,s79,40960
B a[224:227],v245,s[96:99],s79 offen sc1
v_xor_b32 v162,0x8000,v162
M a[20:23],a[168:171],v[80:83],a[20:23]
T v[8:9],v161
M a[40:43],a[176:179],v[80:83],a[40:43]
v_xor_b32 v163,0x8000,v163
M a[40:43],a[180:183],v[84:87],a[40:43]
T v[16:17],v162
M a[60:63],a[188:191],v[84:87],a[60:63]
B a[228:231],v240,s[96:99],s79 offen offset:1024 sc1
v_xor_b32 v164,0x8000,v164
M a[60:63],a[184:187],v[80:83],a[60:63]
W lgkmcnt(3)
T v[24:25],v163
M a[80:83],a[192:195],v[80:83],a[80:83]
v_xor_b32 v165,0x8000,v165
M a[80:83],a[196:199],v[84:87],a[80:83]
T v[32:33],v164
M a[100:103],a[204:207],v[84:87],a[100:103]
B a[232:235],v245,s[96:99],s79 offen offset:1024 sc1
v_xor_b32 v166,0x8000,v166
M a[100:103],a[200:203],v[80:83],a[100:103]
T v[40:41],v165
M a[120:123],a[208:211],v[80:83],a[120:123]
v_xor_b32 v167,0x8000,v167
M a[120:123],a[212:215],v[84:87],a[120:123]
T v[48:49],v166
M a[140:143],a[220:223],v[84:87],a[140:143]
B a[236:239],v240,s[96:99],s79 offen offset:2048 sc1
M a[140:143],a[216:219],v[80:83],a[140:143]
W vmcnt(13)
T v[56:57],v167
M a[4:7],a[160:163],v[88:91],a[4:7]
M a[4:7],a[164:167],v[92:95],a[4:7]
T v[2:3],v160 offset:4096
M a[24:27],a[172:175],v[92:95],a[24:27]
B a[240:243],v245,s[96:99],s79 offen offset:2048 sc1
M a[24:27],a[168:171],v[88:91],a[24:27]
T v[10:11],v161 offset:4096
M a[44:47],a[176:179],v[88:91],a[44:47]
M a[44:47],a[180:183],v[92:95],a[44:47]
T v[18:19],v162 offset:4096
M a[64:67],a[188:191],v[92:95],a[64:67]
B a[244:247],v240,s[96:99],s79 offen offset:3072 sc1
M a[64:67],a[184:187],v[88:91],a[64:67]
T v[26:27],v163 offset:4096
M a[84:87],a[192:195],v[88:91],a[84:87]
M a[84:87],a[196:199],v[92:95],a[84:87]
T v[34:35],v164 offset:4096
M a[104:107],a[204:207],v[92:95],a[104:107]
B a[248:251],v245,s[96:99],s79 offen offset:3072 sc1
M a[104:107],a[200:203],v[88:91],a[104:107]
T v[42:43],v165 offset:4096
M a[124:127],a[208:211],v[88:91],a[124:127]
M a[124:127],a[212:215],v[92:95],a[124:127]
T v[50:51],v166 offset:4096
M a[144:147],a[220:223],v[92:95],a[144:147]
B a[252:255],v241,s[96:99],s79 offen sc1
s_add_u32 s79,s79,40960
M a[144:147],a[216:219],v[88:91],a[144:147]
W vmcnt(12)
T v[58:59],v167 offset:4096
M a[8:11],a[160:163],v[96:99],a[8:11]
B v[80:83],v245,s[96:99],s34 offen sc1
M a[8:11],a[164:167],v[100:103],a[8:11]
T v[4:5],v160 offset:8192
M a[28:31],a[172:175],v[100:103],a[28:31]
M a[28:31],a[168:171],v[96:99],a[28:31]
T v[12:13],v161 offset:8192
M a[48:51],a[176:179],v[96:99],a[48:51]
B v[84:87],v240,s[96:99],s34 offen offset:1024 sc1
M a[48:51],a[180:183],v[100:103],a[48:51]
T v[20:21],v162 offset:8192
M a[68:71],a[188:191],v[100:103],a[68:71]
M a[68:71],a[184:187],v[96:99],a[68:71]
T v[28:29],v163 offset:8192
M a[88:91],a[192:195],v[96:99],a[88:91]
B v[88:91],v245,s[96:99],s34 offen offset:1024 sc1
M a[88:91],a[196:199],v[100:103],a[88:91]
T v[36:37],v164 offset:8192
M a[108:111],a[204:207],v[100:103],a[108:111]
M a[108:111],a[200:203],v[96:99],a[108:111]
T v[44:45],v165 offset:8192
M a[128:131],a[208:211],v[96:99],a[128:131]
B v[92:95],v240,s[96:99],s34 offen offset:2048 sc1
M a[128:131],a[212:215],v[100:103],a[128:131]
T v[52:53],v166 offset:8192
M a[148:151],a[220:223],v[100:103],a[148:151]
M a[148:151],a[216:219],v[96:99],a[148:151]
W vmcnt(12)
T v[60:61],v167 offset:8192
M a[12:15],a[160:163],v[104:107],a[12:15]
B v[96:99],v245,s[96:99],s34 offen offset:2048 sc1
M a[12:15],a[164:167],v[108:111],a[12:15]
T v[6:7],v160 offset:12288
M a[32:35],a[172:175],v[108:111],a[32:35]
M a[32:35],a[168:171],v[104:107],a[32:35]
T v[14:15],v161 offset:12288
M a[52:55],a[176:179],v[104:107],a[52:55]
B v[100:103],v240,s[96:99],s34 offen offset:3072 sc1
M a[52:55],a[180:183],v[108:111],a[52:55]
T v[22:23],v162 offset:12288
M a[72:75],a[188:191],v[108:111],a[72:75]
B v[208:211],v245,s[96:99],s34 offen offset:3072 sc1
M a[72:75],a[184:187],v[104:107],a[72:75]
T v[30:31],v163 offset:12288
M a[92:95],a[192:195],v[104:107],a[92:95]
M a[92:95],a[196:199],v[108:111],a[92:95]
T v[38:39],v164 offset:12288
M a[112:115],a[204:207],v[108:111],a[112:115]
B v[212:215],v241,s[96:99],s34 offen sc1
M a[112:115],a[200:203],v[104:107],a[112:115]
T v[46:47],v165 offset:12288
M a[132:135],a[208:211],v[104:107],a[132:135]
M a[132:135],a[212:215],v[108:111],a[132:135]
T v[54:55],v166 offset:12288
M a[152:155],a[220:223],v[108:111],a[152:155]
M a[152:155],a[216:219],v[104:107],a[152:155]
T v[62:63],v167 offset:12288
)ASM"
R"ASM(
W lgkmcnt(0)
W vmcnt(14)
M a[120:123],v[48:51],a[224:227],a[120:123]
W vmcnt(63)
s_barrier
M a[120:123],v[52:55],a[228:231],a[120:123]
s_lshl_b32 s95,s87,15
T a[208:209],v166 offset:16384
v_add_u32 v200,s95,v245
M a[140:143],v[60:63],a[228:231],a[140:143]
T a[210:211],v166 offset:20480
M a[140:143],v[56:59],a[224:227],a[140:143]
W vmcnt(12)
T a[212:213],v166 offset:24576
v_xor_b32 v201,0x8000,v200
M a[124:127],v[48:51],a[232:235],a[124:127]
T a[214:215],v166 offset:28672
M a[124:127],v[52:55],a[236:239],a[124:127]
T a[216:217],v167 offset:16384
v_lshlrev_b32 v251,11,v244
M a[144:147],v[60:63],a[236:239],a[144:147]
T a[218:219],v167 offset:20480
M a[144:147],v[56:59],a[232:235],a[144:147]
W vmcnt(10)
T a[220:221],v167 offset:24576
v_lshl_add_u32 v251,v227,4,v251
M a[128:131],v[48:51],a[240:243],a[128:131]
T a[222:223],v167 offset:28672
M a[128:131],v[52:55],a[244:247],a[128:131]
T a[176:177],v162 offset:16384
v_mov_b32 v202,s72
M a[148:151],v[60:63],a[244:247],a[148:151]
T a[178:179],v162 offset:20480
M a[148:151],v[56:59],a[240:243],a[148:151]
W vmcnt(8)
T a[180:181],v162 offset:24576
v_mov_b32 v203,s73
M a[132:135],v[48:51],a[248:251],a[132:135]
T a[182:183],v162 offset:28672
M a[132:135],v[52:55],a[252:255],a[132:135]
T a[184:185],v163 offset:16384
M a[152:155],v[60:63],a[252:255],a[152:155]
T a[186:187],v163 offset:20480
M a[152:155],v[56:59],a[248:251],a[152:155]
W vmcnt(6)
W lgkmcnt(6)
T a[188:189],v163 offset:24576
M a[120:123],a[208:211],v[80:83],a[120:123]
T a[190:191],v163 offset:28672
M a[120:123],a[212:215],v[84:87],a[120:123]
T a[192:193],v164 offset:16384
M a[140:143],a[220:223],v[84:87],a[140:143]
T a[194:195],v164 offset:20480
M a[140:143],a[216:219],v[80:83],a[140:143]
W vmcnt(4)
T a[196:197],v164 offset:24576
M a[124:127],a[208:211],v[88:91],a[124:127]
T a[198:199],v164 offset:28672
M a[124:127],a[212:215],v[92:95],a[124:127]
T a[200:201],v165 offset:16384
M a[144:147],a[220:223],v[92:95],a[144:147]
T a[202:203],v165 offset:20480
M a[144:147],a[216:219],v[88:91],a[144:147]
W vmcnt(2)
T a[204:205],v165 offset:24576
M a[128:131],a[208:211],v[96:99],a[128:131]
T a[206:207],v165 offset:28672
M a[128:131],a[212:215],v[100:103],a[128:131]
T a[160:161],v160 offset:16384
M a[148:151],a[220:223],v[100:103],a[148:151]
T a[162:163],v160 offset:20480
M a[148:151],a[216:219],v[96:99],a[148:151]
W vmcnt(0)
T a[164:165],v160 offset:24576
M a[132:135],a[208:211],v[208:211],a[132:135]
T a[166:167],v160 offset:28672
M a[132:135],a[212:215],v[212:215],a[132:135]
T a[168:169],v161 offset:16384
M a[152:155],a[220:223],v[212:215],a[152:155]
T a[170:171],v161 offset:20480
M a[152:155],a[216:219],v[208:211],a[152:155]
W lgkmcnt(14)
T a[172:173],v161 offset:24576
M a[40:43],v[16:19],a[224:227],a[40:43]
T a[174:175],v161 offset:28672
M a[40:43],v[20:23],a[228:231],a[40:43]
ds_write_b128 v200,a[120:123]
M a[40:43],a[176:179],v[80:83],a[40:43]
ds_write_b128 v200,a[140:143] offset:1024
M a[40:43],a[180:183],v[84:87],a[40:43]
ds_write_b128 v200,a[124:127] offset:2048
M a[60:63],a[188:191],v[84:87],a[60:63]
ds_write_b128 v200,a[144:147] offset:3072
M a[60:63],a[184:187],v[80:83],a[60:63]
ds_write_b128 v200,a[128:131] offset:4096
M a[60:63],v[28:31],a[228:231],a[60:63]
ds_write_b128 v200,a[148:151] offset:5120
M a[60:63],v[24:27],a[224:227],a[60:63]
ds_write_b128 v200,a[132:135] offset:6144
M a[44:47],v[16:19],a[232:235],a[44:47]
ds_write_b128 v200,a[152:155] offset:7168
M a[44:47],v[20:23],a[236:239],a[44:47]
M a[44:47],a[176:179],v[88:91],a[44:47]
M a[44:47],a[180:183],v[92:95],a[44:47]
W lgkmcnt(0)
s_barrier
M a[64:67],a[188:191],v[92:95],a[64:67]
R v[120:123],v201
M a[64:67],a[184:187],v[88:91],a[64:67]
R v[124:127],v201 offset:1024
M a[64:67],v[28:31],a[236:239],a[64:67]
R v[128:131],v201 offset:2048
M a[64:67],v[24:27],a[232:235],a[64:67]
R v[132:135],v201 offset:3072
M a[48:51],v[16:19],a[240:243],a[48:51]
R v[136:139],v201 offset:4096
M a[48:51],v[20:23],a[244:247],a[48:51]
R v[140:143],v201 offset:5120
M a[48:51],a[176:179],v[96:99],a[48:51]
R v[144:147],v201 offset:6144
M a[48:51],a[180:183],v[100:103],a[48:51]
W lgkmcnt(5)
R v[148:151],v201 offset:7168
AR v160,a40
AR v161,a41
AR v162,a42
AR v163,a43
M a[68:71],a[188:191],v[100:103],a[68:71]
AR v164,a60
AR v165,a61
AR v166,a62
AR v167,a63
M a[68:71],a[184:187],v[96:99],a[68:71]
F v160,v160,v120
F v161,v161,v121
F v162,v162,v122
F v163,v163,v123
M a[68:71],v[28:31],a[244:247],a[68:71]
F v164,v164,v124
F v165,v165,v125
F v166,v166,v126
F v167,v167,v127
M a[68:71],v[24:27],a[240:243],a[68:71]
C v160,v160,v161
C v161,v162,v163
C v162,v164,v165
C v163,v166,v167
M a[52:55],v[16:19],a[248:251],a[52:55]
W lgkmcnt(4)
buffer_store_dwordx4 v[160:163],v245,s[52:55],s6 offen sc1
AR v168,a44
AR v169,a45
AR v170,a46
AR v171,a47
M a[52:55],v[20:23],a[252:255],a[52:55]
AR v172,a64
AR v173,a65
AR v174,a66
AR v175,a67
M a[52:55],a[176:179],v[208:211],a[52:55]
F v168,v168,v128
F v169,v169,v129
F v170,v170,v130
F v171,v171,v131
M a[52:55],a[180:183],v[212:215],a[52:55]
F v172,v172,v132
F v173,v173,v133
F v174,v174,v134
F v175,v175,v135
M a[72:75],a[188:191],v[212:215],a[72:75]
C v168,v168,v169
C v169,v170,v171
C v170,v172,v173
C v171,v174,v175
M a[72:75],a[184:187],v[208:211],a[72:75]
buffer_store_dwordx4 v[168:171],v245,s[52:55],s6 offen offset:1024 sc1
M a[72:75],v[28:31],a[252:255],a[72:75]
M a[72:75],v[24:27],a[248:251],a[72:75]
W lgkmcnt(2)
AR v176,a48
AR v177,a49
AR v178,a50
AR v179,a51
M a[80:83],v[32:35],a[224:227],a[80:83]
AR v180,a68
AR v181,a69
AR v182,a70
AR v183,a71
M a[80:83],v[36:39],a[228:231],a[80:83]
F v176,v176,v136
F v177,v177,v137
F v178,v178,v138
F v179,v179,v139
M a[80:83],a[192:195],v[80:83],a[80:83]
F v180,v180,v140
F v181,v181,v141
F v182,v182,v142
F v183,v183,v143
M a[80:83],a[196:199],v[84:87],a[80:83]
C v176,v176,v177
C v177,v178,v179
C v178,v180,v181
C v179,v182,v183
M a[100:103],a[204:207],v[84:87],a[100:103]
buffer_store_dwordx4 v[176:179],v245,s[52:55],s6 offen offset:2048 sc1
M a[100:103],a[200:203],v[80:83],a[100:103]
M a[100:103],v[44:47],a[228:231],a[100:103]
M a[100:103],v[40:43],a[224:227],a[100:103]
W lgkmcnt(0)
AR v184,a52
AR v185,a53
AR v186,a54
AR v187,a55
M a[84:87],v[32:35],a[232:235],a[84:87]
AR v188,a72
AR v189,a73
AR v190,a74
AR v191,a75
M a[84:87],v[36:39],a[236:239],a[84:87]
F v184,v184,v144
F v185,v185,v145
F v186,v186,v146
F v187,v187,v147
M a[84:87],a[192:195],v[88:91],a[84:87]
F v188,v188,v148
F v189,v189,v149
F v190,v190,v150
F v191,v191,v151
M a[84:87],a[196:199],v[92:95],a[84:87]
ds_write_b128 v200,a[80:83] offset:10240
C v184,v184,v185
C v185,v186,v187
C v186,v188,v189
C v187,v190,v191
M a[104:107],a[204:207],v[92:95],a[104:107]
buffer_store_dwordx4 v[184:187],v245,s[52:55],s6 offen offset:3072 sc1
M a[104:107],a[200:203],v[88:91],a[104:107]
M a[104:107],v[44:47],a[236:239],a[104:107]
M a[104:107],v[40:43],a[232:235],a[104:107]
ds_write_b128 v200,a[100:103] offset:11264
M a[88:91],v[32:35],a[240:243],a[88:91]
M a[88:91],v[36:39],a[244:247],a[88:91]
M a[88:91],a[192:195],v[96:99],a[88:91]
M a[88:91],a[196:199],v[100:103],a[88:91]
ds_write_b128 v200,a[84:87] offset:12288
M a[108:111],a[204:207],v[100:103],a[108:111]
M a[108:111],a[200:203],v[96:99],a[108:111]
M a[108:111],v[44:47],a[244:247],a[108:111]
M a[108:111],v[40:43],a[240:243],a[108:111]
ds_write_b128 v200,a[104:107] offset:13312
M a[92:95],v[32:35],a[248:251],a[92:95]
M a[92:95],v[36:39],a[252:255],a[92:95]
M a[92:95],a[192:195],v[208:211],a[92:95]
M a[92:95],a[196:199],v[212:215],a[92:95]
ds_write_b128 v200,a[88:91] offset:14336
M a[112:115],a[204:207],v[212:215],a[112:115]
M a[112:115],a[200:203],v[208:211],a[112:115]
M a[112:115],v[44:47],a[252:255],a[112:115]
M a[112:115],v[40:43],a[248:251],a[112:115]
ds_write_b128 v200,a[108:111] offset:15360
M a[0:3],v[0:3],a[224:227],a[0:3]
M a[0:3],v[4:7],a[228:231],a[0:3]
M a[0:3],a[160:163],v[80:83],a[0:3]
M a[0:3],a[164:167],v[84:87],a[0:3]
ds_write_b128 v200,a[92:95] offset:16384
M a[20:23],a[172:175],v[84:87],a[20:23]
M a[20:23],a[168:171],v[80:83],a[20:23]
M a[20:23],v[12:15],a[228:231],a[20:23]
M a[20:23],v[8:11],a[224:227],a[20:23]
ds_write_b128 v200,a[112:115] offset:17408
M a[4:7],v[0:3],a[232:235],a[4:7]
M a[4:7],v[4:7],a[236:239],a[4:7]
M a[4:7],a[160:163],v[88:91],a[4:7]
W lgkmcnt(0)
s_barrier
M a[4:7],a[164:167],v[92:95],a[4:7]
R v[16:19],v201 offset:10240
M a[24:27],a[172:175],v[92:95],a[24:27]
R v[20:23],v201 offset:11264
s_add_u32 s94,s6,10240
s_add_u32 s95,s7,10240
W vmcnt(0)
s_mov_b64 exec,1
buffer_store_dwordx2 v[202:203],off,s[52:55],s94 sc1
s_mov_b64 exec,-1
M a[24:27],a[168:171],v[88:91],a[24:27]
R v[24:27],v201 offset:12288
M a[24:27],v[12:15],a[236:239],a[24:27]
R v[28:31],v201 offset:13312
M a[24:27],v[8:11],a[232:235],a[24:27]
R v[32:35],v201 offset:14336
AR v120,a0
AR v121,a1
M a[8:11],v[0:3],a[240:243],a[8:11]
R v[36:39],v201 offset:15360
AR v122,a2
AR v123,a3
M a[8:11],v[4:7],a[244:247],a[8:11]
R v[40:43],v201 offset:16384
AR v124,a20
)ASM"
R"ASM(
AR v125,a21
M a[8:11],a[160:163],v[96:99],a[8:11]
R v[44:47],v201 offset:17408
AR v126,a22
AR v127,a23
M a[8:11],a[164:167],v[100:103],a[8:11]
M a[28:31],a[172:175],v[100:103],a[28:31]
M a[28:31],a[168:171],v[96:99],a[28:31]
M a[28:31],v[12:15],a[244:247],a[28:31]
M a[28:31],v[8:11],a[240:243],a[28:31]
AR v128,a4
AR v129,a5
M a[12:15],v[0:3],a[248:251],a[12:15]
AR v130,a6
AR v131,a7
M a[12:15],v[4:7],a[252:255],a[12:15]
AR v132,a24
AR v133,a25
M a[12:15],a[160:163],v[208:211],a[12:15]
AR v134,a26
AR v135,a27
M a[12:15],a[164:167],v[212:215],a[12:15]
M a[32:35],a[172:175],v[212:215],a[32:35]
s_mov_b64 exec,1
buffer_load_dwordx2 v[248:249],off,s[52:55],s95 sc1
s_mov_b64 exec,-1
M a[32:35],a[168:171],v[208:211],a[32:35]
M a[32:35],v[12:15],a[252:255],a[32:35]
M a[32:35],v[8:11],a[248:251],a[32:35]
AR v136,a8
AR v137,a9
)ASM"
R"ASM(
s_nop 7
W vmcnt(0)
s_mov_b64 exec,1
s_mov_b32 s94,0
LPX4a_%=:
v_cmp_ne_u64 vcc,s[72:73],v[248:249]
s_nop 3
s_cmp_eq_u64 vcc,0
s_cbranch_scc1 LPX4b_%=
s_add_u32 s94,s94,1
s_cmp_gt_u32 s94,262144
s_cbranch_scc1 LPX4b_%=
s_sleep 1
buffer_load_dwordx2 v[248:249],off,s[52:55],s95 sc1
W vmcnt(0)
s_branch LPX4a_%=
LPX4b_%=:
s_mov_b64 exec,-1
B v[64:67],v245,s[52:55],s7 offen sc1
B v[68:71],v245,s[52:55],s7 offen offset:1024 sc1
B v[72:75],v245,s[52:55],s7 offen offset:2048 sc1
B v[76:79],v245,s[52:55],s7 offen offset:3072 sc1
AR v138,a10
AR v139,a11
AR v140,a28
AR v141,a29
AR v142,a30
AR v143,a31
AR v144,a12
AR v145,a13
AR v146,a14
AR v147,a15
AR v148,a32
AR v149,a33
AR v150,a34
AR v151,a35
W lgkmcnt(0)
v_pk_add_f32 v[120:121],v[120:121],v[16:17]
v_pk_add_f32 v[122:123],v[122:123],v[18:19]
v_pk_add_f32 v[124:125],v[124:125],v[20:21]
v_pk_add_f32 v[126:127],v[126:127],v[22:23]
v_pk_add_f32 v[128:129],v[128:129],v[24:25]
v_pk_add_f32 v[130:131],v[130:131],v[26:27]
v_pk_add_f32 v[132:133],v[132:133],v[28:29]
v_pk_add_f32 v[134:135],v[134:135],v[30:31]
v_pk_add_f32 v[136:137],v[136:137],v[32:33]
v_pk_add_f32 v[138:139],v[138:139],v[34:35]
v_pk_add_f32 v[140:141],v[140:141],v[36:37]
v_pk_add_f32 v[142:143],v[142:143],v[38:39]
v_pk_add_f32 v[144:145],v[144:145],v[40:41]
v_pk_add_f32 v[146:147],v[146:147],v[42:43]
v_pk_add_f32 v[148:149],v[148:149],v[44:45]
v_pk_add_f32 v[150:151],v[150:151],v[46:47]
W vmcnt(3)
v_lshlrev_b32 v80,16,v64
v_and_b32 v81,0xffff0000,v64
v_lshlrev_b32 v82,16,v65
v_and_b32 v83,0xffff0000,v65
v_lshlrev_b32 v84,16,v66
v_and_b32 v85,0xffff0000,v66
v_lshlrev_b32 v86,16,v67
v_and_b32 v87,0xffff0000,v67
v_pk_add_f32 v[120:121],v[120:121],v[80:81]
v_pk_add_f32 v[122:123],v[122:123],v[82:83]
v_pk_add_f32 v[124:125],v[124:125],v[84:85]
v_pk_add_f32 v[126:127],v[126:127],v[86:87]
C v160,v120,v121
C v161,v122,v123
C v162,v124,v125
C v163,v126,v127
s_nop 0
v_permlane32_swap_b32 v160,v162
v_permlane32_swap_b32 v161,v163
s_nop 0
v_permlane16_swap_b32 v160,v162
v_permlane16_swap_b32 v161,v163
s_add_u32 s94,s89,0
buffer_store_dwordx4 v[160:163],v251,s[56:59],s94 offen sc1
W vmcnt(3)
v_lshlrev_b32 v88,16,v68
v_and_b32 v89,0xffff0000,v68
v_lshlrev_b32 v90,16,v69
v_and_b32 v91,0xffff0000,v69
v_lshlrev_b32 v92,16,v70
v_and_b32 v93,0xffff0000,v70
v_lshlrev_b32 v94,16,v71
v_and_b32 v95,0xffff0000,v71
v_pk_add_f32 v[128:129],v[128:129],v[88:89]
v_pk_add_f32 v[130:131],v[130:131],v[90:91]
v_pk_add_f32 v[132:133],v[132:133],v[92:93]
v_pk_add_f32 v[134:135],v[134:135],v[94:95]
C v164,v128,v129
C v165,v130,v131
C v166,v132,v133
C v167,v134,v135
s_nop 0
v_permlane32_swap_b32 v164,v166
v_permlane32_swap_b32 v165,v167
s_nop 0
v_permlane16_swap_b32 v164,v166
v_permlane16_swap_b32 v165,v167
s_add_u32 s94,s89,32768
buffer_store_dwordx4 v[164:167],v251,s[56:59],s94 offen sc1
W vmcnt(3)
v_lshlrev_b32 v96,16,v72
v_and_b32 v97,0xffff0000,v72
v_lshlrev_b32 v98,16,v73
v_and_b32 v99,0xffff0000,v73
v_lshlrev_b32 v100,16,v74
v_and_b32 v101,0xffff0000,v74
v_lshlrev_b32 v102,16,v75
v_and_b32 v103,0xffff0000,v75
v_pk_add_f32 v[136:137],v[136:137],v[96:97]
v_pk_add_f32 v[138:139],v[138:139],v[98:99]
v_pk_add_f32 v[140:141],v[140:141],v[100:101]
v_pk_add_f32 v[142:143],v[142:143],v[102:103]
C v168,v136,v137
C v169,v138,v139
C v170,v140,v141
C v171,v142,v143
s_nop 0
v_permlane32_swap_b32 v168,v170
v_permlane32_swap_b32 v169,v171
s_nop 0
v_permlane16_swap_b32 v168,v170
v_permlane16_swap_b32 v169,v171
s_add_u32 s94,s89,65536
buffer_store_dwordx4 v[168:171],v251,s[56:59],s94 offen sc1
W vmcnt(3)
v_lshlrev_b32 v104,16,v76
v_and_b32 v105,0xffff0000,v76
v_lshlrev_b32 v106,16,v77
v_and_b32 v107,0xffff0000,v77
v_lshlrev_b32 v108,16,v78
v_and_b32 v109,0xffff0000,v78
v_lshlrev_b32 v110,16,v79
v_and_b32 v111,0xffff0000,v79
v_pk_add_f32 v[144:145],v[144:145],v[104:105]
v_pk_add_f32 v[146:147],v[146:147],v[106:107]
v_pk_add_f32 v[148:149],v[148:149],v[108:109]
v_pk_add_f32 v[150:151],v[150:151],v[110:111]
C v172,v144,v145
C v173,v146,v147
C v174,v148,v149
C v175,v150,v151
s_nop 0
v_permlane32_swap_b32 v172,v174
v_permlane32_swap_b32 v173,v175
s_nop 0
v_permlane16_swap_b32 v172,v174
v_permlane16_swap_b32 v173,v175
s_add_u32 s94,s89,98304
buffer_store_dwordx4 v[172:175],v251,s[56:59],s94 offen sc1
s_branch LEND_%=
)ASM"
R"ASM(
LOPA74_%=:
W vmcnt(16)
s_mov_b32 s94,0
LPB4a_%=:
v_cmp_ne_u64 vcc,s[72:73],v[248:249]
s_nop 3
s_cmp_eq_u64 vcc,0
s_cbranch_scc1 LPB4b_%=
s_add_u32 s94,s94,1
s_cmp_gt_u32 s94,262144
s_cbranch_scc1 LPB4b_%=
s_sleep 4
v_lshrrev_b32 v250,1,v245
v_and_b32 v250,0xf8,v250
s_add_u32 s95,s86,512
buffer_load_dwordx2 v[248:249],v250,s[52:55],s95 offen sc1
W vmcnt(0)
s_branch LPB4a_%=
LPB4b_%=:
s_branch LOPN4_%=
)ASM"
R"ASM(
LOPA44_%=:
W vmcnt(16)
v_mov_b32 v248,s72
v_mov_b32 v249,s73
s_mov_b64 exec,1
buffer_store_dwordx2 v[248:249],off,s[52:55],s85 offset:512 sc1
s_mov_b64 exec,-1
s_branch LOPR4_%=
)ASM"
R"ASM(
LOPA64_%=:
v_lshrrev_b32 v250,1,v245
v_and_b32 v250,0xf8,v250
s_add_u32 s95,s86,512
buffer_load_dwordx2 v[248:249],v250,s[52:55],s95 offen sc1
s_branch LOPR4_%=
)ASM"
R"ASM(
LFBb_%=:
s_mov_b32 s71,1
W vmcnt(0)
W lgkmcnt(0)
s_barrier
s_mov_b32 s69,0
s_mov_b32 s40,s10
s_and_b32 s41,s11,0xffff
s_mov_b32 s42,0x4000000
s_mov_b32 s43,0x20000
v_add_u32 v250,0,v227
v_xor_b32 v250,v250,v244
v_lshlrev_b32 v250,4,v250
v_lshl_add_u32 v214,v227,8,v250
v_add_u32 v250,4,v227
v_xor_b32 v250,v250,v244
v_lshlrev_b32 v250,4,v250
v_lshl_add_u32 v215,v227,8,v250
v_add_u32 v250,8,v227
v_xor_b32 v250,v250,v244
v_lshlrev_b32 v250,4,v250
v_lshl_add_u32 v216,v227,8,v250
v_add_u32 v250,12,v227
v_xor_b32 v250,v250,v244
v_lshlrev_b32 v250,4,v250
v_lshl_add_u32 v217,v227,8,v250
s_add_u32 s60,s82,0
s_mov_b32 s76,4096
s_mov_b32 s77,4096
s_mov_b32 s44,s12
s_and_b32 s45,s13,0xffff
s_mov_b32 s46,0x4000000
s_mov_b32 s47,0x20000
s_mov_b32 s95,256
v_mad_u32_u24 v218,v227,s95,v246
v_mad_u32_u24 v219,v227,s95,v247
v_mad_u32_u24 v220,v227,s95,v246
v_mad_u32_u24 v221,v227,s95,v247
s_mov_b32 s61,s82
s_mov_b32 s62,4096
s_mov_b32 s63,4096
s_mov_b32 s75,4096
s_mov_b32 s74,-1
)ASM"
R"ASM(
LFBLb_%=:
s_mov_b32 m0,0x0
s_nop 0
B v214,s[40:43],s60 offen nt lds
B v215,s[40:43],s60 offen offset:1024 nt lds
B v216,s[40:43],s60 offen offset:2048 nt lds
B v217,s[40:43],s60 offen offset:3072 nt lds
s_add_u32 s60,s60,s76
s_mov_b32 m0,0x1000
s_nop 0
B v214,s[40:43],s60 offen nt lds
B v215,s[40:43],s60 offen offset:1024 nt lds
B v216,s[40:43],s60 offen offset:2048 nt lds
B v217,s[40:43],s60 offen offset:3072 nt lds
s_add_u32 s60,s60,s76
s_mov_b32 m0,0x2000
s_nop 0
B v214,s[40:43],s60 offen nt lds
B v215,s[40:43],s60 offen offset:1024 nt lds
B v216,s[40:43],s60 offen offset:2048 nt lds
B v217,s[40:43],s60 offen offset:3072 nt lds
s_add_u32 s60,s60,s76
s_mov_b32 m0,0x3000
s_nop 0
B v214,s[40:43],s60 offen nt lds
B v215,s[40:43],s60 offen offset:1024 nt lds
B v216,s[40:43],s60 offen offset:2048 nt lds
B v217,s[40:43],s60 offen offset:3072 nt lds
s_add_u32 s60,s60,s77
W vmcnt(0)
s_barrier
R a[160:163],v198
R a[164:167],v199
R a[168:171],v200
R a[172:175],v201
W lgkmcnt(0)
M v[120:123],a[160:163],v[0:3],0
M v[120:123],a[164:167],v[4:7],v[120:123]
M v[120:123],a[168:171],v[8:11],v[120:123]
M v[120:123],a[172:175],v[12:15],v[120:123]
M v[124:127],a[160:163],v[16:19],0
M v[124:127],a[164:167],v[20:23],v[124:127]
M v[124:127],a[168:171],v[24:27],v[124:127]
M v[124:127],a[172:175],v[28:31],v[124:127]
M v[128:131],a[160:163],v[32:35],0
M v[128:131],a[164:167],v[36:39],v[128:131]
M v[128:131],a[168:171],v[40:43],v[128:131]
M v[128:131],a[172:175],v[44:47],v[128:131]
M v[132:135],a[160:163],v[48:51],0
M v[132:135],a[164:167],v[52:55],v[132:135]
M v[132:135],a[168:171],v[56:59],v[132:135]
M v[132:135],a[172:175],v[60:63],v[132:135]
s_nop 7
v_max3_f32 v222,v120,v121,v222
v_max3_f32 v222,v122,v123,v222
v_max3_f32 v223,v124,v125,v223
v_max3_f32 v223,v126,v127,v223
v_max3_f32 v224,v128,v129,v224
v_max3_f32 v224,v130,v131,v224
v_max3_f32 v225,v132,v133,v225
v_max3_f32 v225,v134,v135,v225
R a[176:179],v198 offset:4096
R a[180:183],v199 offset:4096
R a[184:187],v200 offset:4096
R a[188:191],v201 offset:4096
W lgkmcnt(0)
M v[120:123],a[176:179],v[0:3],0
M v[120:123],a[180:183],v[4:7],v[120:123]
M v[120:123],a[184:187],v[8:11],v[120:123]
M v[120:123],a[188:191],v[12:15],v[120:123]
M v[124:127],a[176:179],v[16:19],0
M v[124:127],a[180:183],v[20:23],v[124:127]
M v[124:127],a[184:187],v[24:27],v[124:127]
M v[124:127],a[188:191],v[28:31],v[124:127]
M v[128:131],a[176:179],v[32:35],0
M v[128:131],a[180:183],v[36:39],v[128:131]
M v[128:131],a[184:187],v[40:43],v[128:131]
M v[128:131],a[188:191],v[44:47],v[128:131]
M v[132:135],a[176:179],v[48:51],0
M v[132:135],a[180:183],v[52:55],v[132:135]
M v[132:135],a[184:187],v[56:59],v[132:135]
M v[132:135],a[188:191],v[60:63],v[132:135]
s_nop 7
v_max3_f32 v222,v120,v121,v222
v_max3_f32 v222,v122,v123,v222
v_max3_f32 v223,v124,v125,v223
v_max3_f32 v223,v126,v127,v223
v_max3_f32 v224,v128,v129,v224
v_max3_f32 v224,v130,v131,v224
v_max3_f32 v225,v132,v133,v225
v_max3_f32 v225,v134,v135,v225
R a[160:163],v198 offset:8192
R a[164:167],v199 offset:8192
R a[168:171],v200 offset:8192
R a[172:175],v201 offset:8192
W lgkmcnt(0)
M v[120:123],a[160:163],v[0:3],0
M v[120:123],a[164:167],v[4:7],v[120:123]
M v[120:123],a[168:171],v[8:11],v[120:123]
M v[120:123],a[172:175],v[12:15],v[120:123]
M v[124:127],a[160:163],v[16:19],0
M v[124:127],a[164:167],v[20:23],v[124:127]
M v[124:127],a[168:171],v[24:27],v[124:127]
M v[124:127],a[172:175],v[28:31],v[124:127]
M v[128:131],a[160:163],v[32:35],0
M v[128:131],a[164:167],v[36:39],v[128:131]
M v[128:131],a[168:171],v[40:43],v[128:131]
M v[128:131],a[172:175],v[44:47],v[128:131]
M v[132:135],a[160:163],v[48:51],0
M v[132:135],a[164:167],v[52:55],v[132:135]
M v[132:135],a[168:171],v[56:59],v[132:135]
M v[132:135],a[172:175],v[60:63],v[132:135]
s_nop 7
v_max3_f32 v222,v120,v121,v222
v_max3_f32 v222,v122,v123,v222
v_max3_f32 v223,v124,v125,v223
v_max3_f32 v223,v126,v127,v223
v_max3_f32 v224,v128,v129,v224
v_max3_f32 v224,v130,v131,v224
v_max3_f32 v225,v132,v133,v225
v_max3_f32 v225,v134,v135,v225
R a[176:179],v198 offset:12288
R a[180:183],v199 offset:12288
R a[184:187],v200 offset:12288
R a[188:191],v201 offset:12288
W lgkmcnt(0)
M v[120:123],a[176:179],v[0:3],0
M v[120:123],a[180:183],v[4:7],v[120:123]
M v[120:123],a[184:187],v[8:11],v[120:123]
M v[120:123],a[188:191],v[12:15],v[120:123]
M v[124:127],a[176:179],v[16:19],0
M v[124:127],a[180:183],v[20:23],v[124:127]
M v[124:127],a[184:187],v[24:27],v[124:127]
M v[124:127],a[188:191],v[28:31],v[124:127]
M v[128:131],a[176:179],v[32:35],0
M v[128:131],a[180:183],v[36:39],v[128:131]
M v[128:131],a[184:187],v[40:43],v[128:131]
M v[128:131],a[188:191],v[44:47],v[128:131]
M v[132:135],a[176:179],v[48:51],0
M v[132:135],a[180:183],v[52:55],v[132:135]
M v[132:135],a[184:187],v[56:59],v[132:135]
M v[132:135],a[188:191],v[60:63],v[132:135]
s_nop 7
v_max3_f32 v222,v120,v121,v222
v_max3_f32 v222,v122,v123,v222
v_max3_f32 v223,v124,v125,v223
v_max3_f32 v223,v126,v127,v223
v_max3_f32 v224,v128,v129,v224
v_max3_f32 v224,v130,v131,v224
v_max3_f32 v225,v132,v133,v225
v_max3_f32 v225,v134,v135,v225
s_barrier
s_add_u32 s69,s69,1
s_cmp_lt_u32 s69,16
s_cbranch_scc1 LFBLb_%=
)ASM"
R"ASM(
s_mov_b32 s60,s82
s_mov_b32 m0,0x0
s_nop 0
B v214,s[40:43],s60 offen nt lds
B v215,s[40:43],s60 offen offset:1024 nt lds
B v216,s[40:43],s60 offen offset:2048 nt lds
B v217,s[40:43],s60 offen offset:3072 nt lds
s_add_u32 s60,s60,s76
s_mov_b32 m0,0x1000
s_nop 0
B v214,s[40:43],s60 offen nt lds
B v215,s[40:43],s60 offen offset:1024 nt lds
B v216,s[40:43],s60 offen offset:2048 nt lds
B v217,s[40:43],s60 offen offset:3072 nt lds
s_add_u32 s60,s60,s76
s_mov_b32 m0,0x2000
s_nop 0
B v214,s[40:43],s60 offen nt lds
B v215,s[40:43],s60 offen offset:1024 nt lds
B v216,s[40:43],s60 offen offset:2048 nt lds
B v217,s[40:43],s60 offen offset:3072 nt lds
s_add_u32 s60,s60,s76
s_mov_b32 m0,0x3000
s_nop 0
B v214,s[40:43],s60 offen nt lds
B v215,s[40:43],s60 offen offset:1024 nt lds
B v216,s[40:43],s60 offen offset:2048 nt lds
B v217,s[40:43],s60 offen offset:3072 nt lds
s_add_u32 s60,s60,s77
s_mov_b32 m0,0x4000
s_nop 0
B v214,s[40:43],s60 offen nt lds
B v215,s[40:43],s60 offen offset:1024 nt lds
B v216,s[40:43],s60 offen offset:2048 nt lds
B v217,s[40:43],s60 offen offset:3072 nt lds
s_add_u32 s60,s60,s76
s_mov_b32 m0,0x5000
s_nop 0
B v214,s[40:43],s60 offen nt lds
B v215,s[40:43],s60 offen offset:1024 nt lds
B v216,s[40:43],s60 offen offset:2048 nt lds
B v217,s[40:43],s60 offen offset:3072 nt lds
s_add_u32 s60,s60,s76
s_mov_b32 m0,0x6000
s_nop 0
B v214,s[40:43],s60 offen nt lds
B v215,s[40:43],s60 offen offset:1024 nt lds
B v216,s[40:43],s60 offen offset:2048 nt lds
B v217,s[40:43],s60 offen offset:3072 nt lds
s_add_u32 s60,s60,s76
s_mov_b32 m0,0x7000
s_nop 0
B v214,s[40:43],s60 offen nt lds
B v215,s[40:43],s60 offen offset:1024 nt lds
B v216,s[40:43],s60 offen offset:2048 nt lds
B v217,s[40:43],s60 offen offset:3072 nt lds
s_add_u32 s60,s60,s77
s_mov_b32 m0,0x10000
s_nop 0
B v218,s[44:47],s61 offen lds
B v219,s[44:47],s61 offen offset:1024 lds
B v220,s[44:47],s61 offen offset:2048 lds
B v221,s[44:47],s61 offen offset:3072 lds
s_add_u32 s61,s61,s62
s_mov_b32 m0,0x11000
s_nop 0
B v218,s[44:47],s61 offen lds
B v219,s[44:47],s61 offen offset:1024 lds
B v220,s[44:47],s61 offen offset:2048 lds
B v221,s[44:47],s61 offen offset:3072 lds
s_add_u32 s61,s61,s63
s_mov_b32 m0,0x12000
s_nop 0
B v218,s[44:47],s61 offen lds
B v219,s[44:47],s61 offen offset:1024 lds
B v220,s[44:47],s61 offen offset:2048 lds
B v221,s[44:47],s61 offen offset:3072 lds
s_add_u32 s61,s61,s62
s_mov_b32 m0,0x13000
s_nop 0
B v218,s[44:47],s61 offen lds
B v219,s[44:47],s61 offen offset:1024 lds
B v220,s[44:47],s61 offen offset:2048 lds
B v221,s[44:47],s61 offen offset:3072 lds
s_add_u32 s61,s61,s75
s_and_b32 s61,s61,s74
W vmcnt(0)
s_mov_b32 m0,0x8000
s_nop 0
B v214,s[40:43],s60 offen nt lds
B v215,s[40:43],s60 offen offset:1024 nt lds
B v216,s[40:43],s60 offen offset:2048 nt lds
B v217,s[40:43],s60 offen offset:3072 nt lds
s_add_u32 s60,s60,s76
s_mov_b32 m0,0x9000
s_nop 0
B v214,s[40:43],s60 offen nt lds
B v215,s[40:43],s60 offen offset:1024 nt lds
B v216,s[40:43],s60 offen offset:2048 nt lds
B v217,s[40:43],s60 offen offset:3072 nt lds
s_add_u32 s60,s60,s76
s_mov_b32 m0,0xa000
s_nop 0
B v214,s[40:43],s60 offen nt lds
B v215,s[40:43],s60 offen offset:1024 nt lds
B v216,s[40:43],s60 offen offset:2048 nt lds
B v217,s[40:43],s60 offen offset:3072 nt lds
s_add_u32 s60,s60,s76
s_mov_b32 m0,0xb000
s_nop 0
B v214,s[40:43],s60 offen nt lds
B v215,s[40:43],s60 offen offset:1024 nt lds
B v216,s[40:43],s60 offen offset:2048 nt lds
B v217,s[40:43],s60 offen offset:3072 nt lds
s_add_u32 s60,s60,s77
s_mov_b32 m0,0x14000
s_nop 0
B v218,s[44:47],s61 offen lds
B v219,s[44:47],s61 offen offset:1024 lds
B v220,s[44:47],s61 offen offset:2048 lds
B v221,s[44:47],s61 offen offset:3072 lds
s_add_u32 s61,s61,s62
s_mov_b32 m0,0x15000
s_nop 0
B v218,s[44:47],s61 offen lds
B v219,s[44:47],s61 offen offset:1024 lds
B v220,s[44:47],s61 offen offset:2048 lds
B v221,s[44:47],s61 offen offset:3072 lds
s_add_u32 s61,s61,s63
s_barrier
R a[160:163],v198
R a[164:167],v199
R a[168:171],v200
R a[172:175],v201
s_branch LPREb_%=
)ASM"
R"ASM(
LEND_%=:
W vmcnt(0)
W lgkmcnt(0)
)ASM"
      :
      : "s"(__builtin_amdgcn_kernarg_segment_ptr()), "s"(blockIdx.x), "s"(w)
      : "v0","v1","v2","v3","v4","v5","v6","v7","v8","v9","v10","v11","v12","v13","v14","v15","v16","v17","v18","v19","v20","v21","v22","v23","v24","v25","v26","v27","v28","v29","v30","v31","v32","v33","v34","v35","v36","v37","v38","v39","v40","v41","v42","v43","v44","v45","v46","v47","v48","v49","v50","v51","v52","v53","v54","v55","v56","v57","v58","v59","v60","v61","v62","v63","v64","v65","v66","v67","v68","v69","v70","v71","v72","v73","v74","v75","v76","v77","v78","v79","v80","v81","v82","v83","v84","v85","v86","v87","v88","v89","v90","v91","v92","v93","v94","v95","v96","v97","v98","v99","v100","v101","v102","v103","v104","v105","v106","v107","v108","v109","v110","v111","v112","v113","v114","v115","v116","v117","v118","v119","v120","v121","v122","v123","v124","v125","v126","v127","v128","v129","v130","v131","v132","v133","v134","v135","v136","v137","v138","v139","v140","v141","v142","v143","v144","v145","v146","v147","v148","v149","v150","v151","v152","v153","v154","v155","v156","v157","v158","v159","v160","v161","v162","v163","v164","v165","v166","v167","v168","v169","v170","v171","v172","v173","v174","v175","v176","v177","v178","v179","v180","v181","v182","v183","v184","v185","v186","v187","v188","v189","v190","v191","v192","v193","v194","v195","v196","v197","v198","v199","v200","v201","v202","v203","v204","v205","v206","v207","v208","v209","v210","v211","v212","v213","v214","v215","v216","v217","v218","v219","v220","v221","v222","v223","v224","v225","v226","v227","v228","v229","v230","v231","v232","v233","v234","v235","v236","v237","v238","v239","v240","v241","v242","v243","v244","v245","v246","v247","v248","v249","v250","v251","v252","v253","v254","v255","a0","a1","a2","a3","a4","a5","a6","a7","a8","a9","a10","a11","a12","a13","a14","a15","a16","a17","a18","a19","a20","a21","a22","a23","a24","a25","a26","a27","a28","a29","a30","a31","a32","a33","a34","a35","a36","a37","a38","a39","a40","a41","a42","a43","a44","a45","a46","a47","a48","a49","a50","a51","a52","a53","a54","a55","a56","a57","a58","a59","a60","a61","a62","a63","a64","a65","a66","a67","a68","a69","a70","a71","a72","a73","a74","a75","a76","a77","a78","a79","a80","a81","a82","a83","a84","a85","a86","a87","a88","a89","a90","a91","a92","a93","a94","a95","a96","a97","a98","a99","a100","a101","a102","a103","a104","a105","a106","a107","a108","a109","a110","a111","a112","a113","a114","a115","a116","a117","a118","a119","a120","a121","a122","a123","a124","a125","a126","a127","a128","a129","a130","a131","a132","a133","a134","a135","a136","a137","a138","a139","a140","a141","a142","a143","a144","a145","a146","a147","a148","a149","a150","a151","a152","a153","a154","a155","a156","a157","a158","a159","a160","a161","a162","a163","a164","a165","a166","a167","a168","a169","a170","a171","a172","a173","a174","a175","a176","a177","a178","a179","a180","a181","a182","a183","a184","a185","a186","a187","a188","a189","a190","a191","a192","a193","a194","a195","a196","a197","a198","a199","a200","a201","a202","a203","a204","a205","a206","a207","a208","a209","a210","a211","a212","a213","a214","a215","a216","a217","a218","a219","a220","a221","a222","a223","a224","a225","a226","a227","a228","a229","a230","a231","a232","a233","a234","a235","a236","a237","a238","a239","a240","a241","a242","a243","a244","a245","a246","a247","a248","a249","a250","a251","a252","a253","a254","a255","s4","s5","s6","s7","s8","s9","s10","s11","s12","s13","s14","s15","s16","s17","s18","s19","s20","s21","s22","s23","s24","s25","s26","s27","s28","s29","s30","s31","s32","s33","s34","s35","s36","s37","s38","s39","s40","s41","s42","s43","s44","s45","s46","s47","s48","s49","s50","s51","s52","s53","s54","s55","s56","s57","s58","s59","s60","s61","s62","s63","s64","s65","s66","s67","s68","s69","s70","s71","s72","s73","s74","s75","s76","s77","s78","s79","s80","s81","s82","s83","s84","s85","s86","s87","s88","s89","s90","s91","s92","s93","s94","s95","s96","s97","s98","s99","s100","s101","vcc","scc","memory");
}

extern "C" void kernel_launch(void* const* d_in, const int* in_sizes, int n_in, void* d_out, int out_size, void* d_ws, size_t ws_size,
                              hipStream_t stream) {
  Params p;
  for (int br = 0; br < 2; ++br) {
    for (int i = 0; i < 5; ++i) p.p[br][i] = d_in[5 * br + i];
    p.p[br][5] = d_ws; p.p[br][6] = d_out; p.p[br][7] = d_ws;
  }
  (void)hipFuncSetAttribute((const void*)fused_attn_kernel, hipFuncAttributeMaxDynamicSharedMemorySize, 163840);
  fused_attn_kernel<<<256, 256, 163840, stream>>>(p);
}
```
